```python
import torch

def _softmax(x, axis=-1):
    return torch.softmax(x, dim=axis)
def _gelu(x):
    return torch.nn.functional.gelu(x, approximate="tanh")
def _silu(x):
    return torch.nn.functional.silu(x)
def _sigmoid(x):
    return torch.sigmoid(x)
def _layernorm(x, axis=-1, eps=1e-5):
    m = x.mean(dim=axis, keepdim=True)
    v = x.var(dim=axis, keepdim=True, unbiased=False)
    return (x - m) / torch.sqrt(v + eps)
def _scatter_add(tgt, idx, src, axis=0):
    return tgt.clone().index_add_(axis, idx, src)
def _segment_sum(src, seg, n):
    out = torch.zeros((n,) + src.shape[1:], dtype=src.dtype)
    return out.index_add_(0, seg, src)
def _pad(x, pw):
    flat = tuple(v for pair in reversed(pw) for v in pair)
    return torch.nn.functional.pad(x, flat)

kernel_name = 'concat_heads__mha_block_cached__mha_block_cached__near_ridge'

def setup_inputs():

    _w = (lambda a: (lambda t: t.to(torch.bfloat16) if t.is_floating_point() else t)(torch.as_tensor(a)))
    def _A_setup():
        B, Sq, D = 8, 160, 448
        H, Sk, Dh = 16, 1024, 256
        return {
            "x": _w((lambda *s: torch.randn(*s, dtype=torch.bfloat16) * 0.8)(B, Sq, D)),
            "k_cache": _w((lambda *s: torch.randn(*s, dtype=torch.bfloat16) * 0.8)(B, H, Sk, Dh)),
            "v_cache": _w((lambda *s: torch.randn(*s, dtype=torch.bfloat16) * 0.05)(B, H, Sk, Dh)),
            "wq": _w((lambda *s: torch.randn(*s, dtype=torch.bfloat16) * 0.8)(D, H * Dh) * (D ** -0.5)),
            "wo": _w((lambda *s: torch.randn(*s, dtype=torch.bfloat16) * 0.05)(H * Dh, D) * ((H * Dh) ** -0.5)),
        }
    _A_in = _A_setup()

    def _B_setup():
        B, Sq, D = 8, 160, 448
        H, Sk, Dh = 16, 1024, 256
        return {
            "x": _w((lambda *s: torch.randn(*s, dtype=torch.bfloat16) * 0.8)(B, Sq, D)),
            "k_cache": _w((lambda *s: torch.randn(*s, dtype=torch.bfloat16) * 0.8)(B, H, Sk, Dh)),
            "v_cache": _w((lambda *s: torch.randn(*s, dtype=torch.bfloat16) * 0.05)(B, H, Sk, Dh)),
            "wq": _w((lambda *s: torch.randn(*s, dtype=torch.bfloat16) * 0.8)(D, H * Dh) * (D ** -0.5)),
            "wo": _w((lambda *s: torch.randn(*s, dtype=torch.bfloat16) * 0.05)(H * Dh, D) * ((H * Dh) ** -0.5)),
        }
    _B_in = _B_setup()

    return {
        "A_x": _A_in["x"],
        "A_k_cache": _A_in["k_cache"],
        "A_v_cache": _A_in["v_cache"],
        "A_wq": _A_in["wq"],
        "A_wo": _A_in["wo"],
        "B_x": _B_in["x"],
        "B_k_cache": _B_in["k_cache"],
        "B_v_cache": _B_in["v_cache"],
        "B_wq": _B_in["wq"],
        "B_wo": _B_in["wo"],
    }

def reference(A_x, A_k_cache, A_v_cache, A_wq, A_wo, B_x, B_k_cache, B_v_cache, B_wq, B_wo):
    def _A_ref(x, k_cache, v_cache, wq, wo):
        B, Sq, D = x.shape
        H, Dh = 16, 256
        q = (lambda x, *d: x.permute(*d))((x @ wq).reshape(B, Sq, H, Dh), 0, 2, 1, 3)
        scores = torch.einsum("bhqd,bhkd->bhqk", q, k_cache) * (Dh ** -0.5)
        p = _softmax(scores, axis=-1)
        o = torch.einsum("bhqk,bhkd->bhqd", p, v_cache)
        return (lambda x, *d: x.permute(*d))(o, 0, 2, 1, 3).reshape(B, Sq, H * Dh) @ wo

    def _B_ref(x, k_cache, v_cache, wq, wo):
        B, Sq, D = x.shape
        H, Dh = 16, 256
        q = (lambda x, *d: x.permute(*d))((x @ wq).reshape(B, Sq, H, Dh), 0, 2, 1, 3)
        scores = torch.einsum("bhqd,bhkd->bhqk", q, k_cache) * (Dh ** -0.5)
        p = _softmax(scores, axis=-1)
        o = torch.einsum("bhqk,bhkd->bhqd", p, v_cache)
        return (lambda x, *d: x.permute(*d))(o, 0, 2, 1, 3).reshape(B, Sq, H * Dh) @ wo

    a = _A_ref(A_x, A_k_cache, A_v_cache, A_wq, A_wo)
    b = _B_ref(B_x, B_k_cache, B_v_cache, B_wq, B_wo)
    return torch.concatenate([a, b], axis=-1)
```

```cpp
#include <hip/hip_runtime.h>
#include <hip/hip_bf16.h>
#include <stdint.h>
typedef unsigned short u16;
__device__ __forceinline__ float bf2f(u16 x){ return __uint_as_float(((unsigned)x)<<16); }
__device__ __forceinline__ u16 f2bf(float f){ unsigned u=__float_as_uint(f); u += 0x7fff + ((u>>16)&1); return (u16)(u>>16); }
constexpr int B=8,SQ=160,D=448,H=16,SK=1024,DH=256;
__global__ void qproj(const u16* xA,const u16* wA,const u16* xB,const u16* wB,u16* q){
  int br=blockIdx.z; const u16* x=br?xB:xA; const u16* w=br?wB:wA;
  int row=blockIdx.y;
  int col=blockIdx.x*256+threadIdx.x;
  float acc=0; for(int k=0;k<D;k++) acc+=bf2f(x[row*D+k])*bf2f(w[(size_t)k*4096+col]);
  int b=row/SQ,s=row%SQ,h=col/DH,d=col%DH;
  q[((((size_t)br*B+b)*H+h)*SQ+s)*DH+d]=f2bf(acc);
}
__global__ void attn(const u16* q,const u16* kA,const u16* vA,const u16* kB,const u16* vB,u16* o){
  int s=blockIdx.x; int bh=blockIdx.y; int br=blockIdx.z;
  const u16* k=(br?kB:kA)+(size_t)bh*SK*DH; const u16* v=(br?vB:vA)+(size_t)bh*SK*DH;
  __shared__ float qs[DH]; __shared__ float p[SK]; __shared__ float red[256];
  int t=threadIdx.x;
  qs[t]=bf2f(q[(((size_t)br*B*H+bh)*SQ+s)*DH+t]); __syncthreads();
  float mx=-1e30f;
  for(int j=t;j<SK;j+=256){ float a=0; for(int d=0;d<DH;d++) a+=qs[d]*bf2f(k[(size_t)j*DH+d]); a=bf2f(f2bf(a))*0.0625f; p[j]=a; mx=fmaxf(mx,a);}
  red[t]=mx; __syncthreads(); for(int o2=128;o2>0;o2>>=1){ if(t<o2) red[t]=fmaxf(red[t],red[t+o2]); __syncthreads(); }
  mx=red[0]; __syncthreads();
  float sum=0; for(int j=t;j<SK;j+=256){ float e=__expf(p[j]-mx); p[j]=e; sum+=e; }
  red[t]=sum; __syncthreads(); for(int o2=128;o2>0;o2>>=1){ if(t<o2) red[t]+=red[t+o2]; __syncthreads(); }
  sum=red[0]; __syncthreads();
  float acc=0; for(int j=0;j<SK;j++) acc+=bf2f(f2bf(p[j]/sum))*bf2f(v[(size_t)j*DH+t]);
  int b=bh/H,h=bh%H;
  o[(((size_t)br*B+b)*SQ+s)*4096+h*DH+t]=f2bf(acc);
}
__global__ void oproj(const u16* o,const u16* wA,const u16* wB,u16* out){
  int br=blockIdx.z; const u16* w=br?wB:wA; int row=blockIdx.y; int col=blockIdx.x*64+threadIdx.x;
  float acc=0; for(int k=0;k<4096;k++) acc+=bf2f(o[((size_t)br*1280+row)*4096+k])*bf2f(w[(size_t)k*D+col]);
  out[(size_t)row*896+br*D+col]=f2bf(acc);
}
extern "C" void kernel_launch(void* const* d_in, const int* in_sizes, int n_in, void* d_out, int out_size, void* d_ws, size_t ws_size, hipStream_t stream) {
  const u16* xA=(const u16*)d_in[0],*kA=(const u16*)d_in[1],*vA=(const u16*)d_in[2],*wqA=(const u16*)d_in[3],*woA=(const u16*)d_in[4];
  const u16* xB=(const u16*)d_in[5],*kB=(const u16*)d_in[6],*vB=(const u16*)d_in[7],*wqB=(const u16*)d_in[8],*woB=(const u16*)d_in[9];
  u16* q=(u16*)d_ws; u16* o=q+(size_t)2*B*H*SQ*DH;
  qproj<<<dim3(16,1280,2),256,0,stream>>>(xA,wqA,xB,wqB,q);
  attn<<<dim3(SQ,B*H,2),256,0,stream>>>(q,kA,vA,kB,vB,o);
  oproj<<<dim3(7,1280,2),64,0,stream>>>(o,woA,woB,(u16*)d_out);
}
```

```cpp
#include <hip/hip_runtime.h>
#include <stdint.h>
typedef unsigned short u16;
typedef int i32x4 __attribute__((ext_vector_type(4)));
typedef unsigned u32x4 __attribute__((ext_vector_type(4)));
typedef unsigned u32x2 __attribute__((ext_vector_type(2)));
__device__ __forceinline__ unsigned rfl(unsigned x){ return __builtin_amdgcn_readfirstlane(x); }
__device__ __forceinline__ i32x4 mk(const void* p, unsigned sz){ i32x4 s; unsigned long long a=(unsigned long long)p; s[0]=rfl((unsigned)a); s[1]=rfl((unsigned)(a>>32))&0xffff; s[2]=rfl(sz); s[3]=0x00020000; return s; }

__global__ __launch_bounds__(256,1) void mha_main(const char* xA,const char* kA,const char* vA,const char* wqA,const char* woA,
    const char* xB,const char* kB,const char* vB,const char* wqB,const char* woB,char* part,char* out,char* dbg){
  const unsigned wid=rfl(threadIdx.x>>6);
  const unsigned wg=blockIdx.x;
  const unsigned xcd=wg&7, idx=wg>>3, br=xcd>>2, hh=idx>>3, head=(xcd&3)*4+hh, b=idx&7;
  const size_t bh=b*16+head;
  i32x4 sx=mk((br?xB:xA)+(size_t)b*143360,143360);
  i32x4 swq=mk((br?wqB:wqA)+head*512,3670016-head*512);
  i32x4 sk=mk((br?kB:kA)+bh*524288,524288);
  i32x4 sv=mk((br?vB:vA)+bh*524288,524288);
  i32x4 swo=mk((br?woB:woA)+(size_t)head*229376,229376);
  const unsigned gb=(br*128+b)*143360u;
  i32x4 sp=mk(part+gb,256u*143360u+32768u-gb);
  i32x4 so=mk(out+b*286720+br*896,286720-br*896);
  const unsigned flg=rfl(256u*143360u-gb+(br*8+b)*2048u);

  asm volatile(
R"ASM(.macro m d,a,b,c
v_mfma_f32_16x16x32_bf16 \d,\a,\b,\c
.endm
.macro t d,a,o
ds_read_b64_tr_b16 \d,\a offset:\o
.endm
.macro r d,a,o
ds_read_b128 \d,\a offset:\o
.endm
.macro dn v,s,f,o
buffer_load_dwordx4 \v,\s,\f offen offset:\o nt lds
.endm
.macro dt v,s,f,o
buffer_load_dwordx4 \v,\s,\f offen offset:\o lds
.endm
.macro ar d,a
v_accvgpr_read_b32 \d,\a
.endm
.macro cv d,a,b
v_cvt_pk_bf16_f32 \d,\a,\b
.endm
Lentry%=:
v_mbcnt_lo_u32_b32 v214,-1,0
v_mbcnt_hi_u32_b32 v214,-1,v214
s_lshl_b32 s92,%6,12
s_mul_i32 s95,%6,0x7000
v_lshrrev_b32 v236,3,v214
v_lshrrev_b32 v237,4,v214
v_and_b32 v238,7,v214
v_xor_b32 v237,v237,v238
v_lshlrev_b32 v237,4,v237
v_mul_u32_u24 v236,0x380,v236
v_add_u32 v220,v236,v237
v_xor_b32 v238,64,v220
v_add_u32 v221,0x1800,v238
v_add_u32 v222,0x3000,v220
v_add_u32 v223,0x4800,v238
v_lshrrev_b32 v236,3,v214
v_and_b32 v237,2,v236
v_and_b32 v238,7,v214
v_xor_b32 v237,v237,v238
v_lshlrev_b32 v237,4,v237
v_lshlrev_b32 v236,13,v236
v_add_u32 v244,v236,v237
v_xor_b32 v238,64,v244
v_add_u32 v245,0xfc00,v238
v_add_u32 v246,0x1f800,v244
v_add_u32 v247,0x2f400,v238
s_and_b32 s46,%6,1
s_lshl_b32 s46,s46,6
v_xor_b32 v248,s46,v220
s_lshr_b32 s59,%6,1
s_lshl_b32 s59,s59,18
s_lshl_b32 s46,s46,1
s_add_u32 s59,s59,s46
s_mul_i32 s46,%6,0x1c00
s_lshl_b32 s55,%6,10
s_mul_i32 s45,%7,1
s_mul_i32 s42,%8,1
s_add_u32 s45,s45,s42
s_lshr_b32 s42,%11,2
s_mul_i32 s42,s42,1
s_add_u32 s45,s45,s42
s_mul_i32 s42,s45,0x56
s_lshr_b32 s42,s42,8
s_mul_i32 s42,s42,3
s_sub_u32 s45,s45,s42
s_mul_i32 s45,s45,2
s_lshl_b32 s85,s45,19
s_add_u32 m0,s92,0x1c000
s_add_u32 s45,s59,s85
dn v244,%1,s45,0
dn v245,%1,s45,1024
dn v246,%1,s45,2048
dn v247,%1,s45,3072
s_add_u32 m0,s92,0x12000
s_lshr_b32 s42,s85,12
s_add_u32 s45,s95,s42
dt v220,%0,s45,0
dt v221,%0,s45,1024
dt v222,%0,s45,2048
dt v223,%0,s45,3072
s_add_u32 m0,s55,0x16000
s_add_u32 s45,s46,s42
s_add_u32 s45,s45,0x1c000
dt v248,%0,s45,0
s_add_u32 s86,s85,0x80000
s_cmp_eq_u32 s86,0x380000
s_cselect_b32 s86,0,s86
s_add_u32 s87,s86,0x80000
s_cmp_eq_u32 s87,0x380000
s_cselect_b32 s87,0,s87
s_add_u32 s88,s87,0x80000
s_cmp_eq_u32 s88,0x380000
s_cselect_b32 s88,0,s88
s_add_u32 s89,s88,0x80000
s_cmp_eq_u32 s89,0x380000
s_cselect_b32 s89,0,s89
s_add_u32 s90,s89,0x80000
s_cmp_eq_u32 s90,0x380000
s_cselect_b32 s90,0,s90
s_add_u32 s91,s90,0x80000
s_cmp_eq_u32 s91,0x380000
s_cselect_b32 s91,0,s91
s_mov_b32 s47,0x3db8aa3b
s_mov_b32 s48,0x8000
s_mov_b32 s49,0xfffe0000
s_mov_b32 s40,0x20000
s_mov_b32 s41,0x18000
s_mul_i32 s58,%6,48
s_lshl_b32 s54,%6,5
s_add_u32 s54,s54,32
s_cmp_lt_u32 %6,2
s_cselect_b32 s58,s58,s54
s_lshl_b32 s54,s58,7
s_lshr_b32 s45,%11,2
s_mul_i32 s45,s45,0x460000
s_add_u32 s54,s54,s45
s_and_b32 s45,%11,3
s_mul_i32 s45,s45,0x118000
s_add_u32 s54,s54,s45
v_lshrrev_b32 v236,4,v214
v_bfe_u32 v237,v214,2,2
v_and_b32 v238,3,v214
v_and_b32 v239,15,v214
v_lshlrev_b32 v238,3,v238
v_lshl_add_u32 v240,v236,3,v237
v_lshlrev_b32 v240,7,v240
v_lshrrev_b32 v241,1,v237
v_and_b32 v242,1,v236
v_lshl_add_u32 v241,v242,1,v241
v_lshl_add_u32 v240,v241,5,v240
v_add_u32 v240,v240,v238
v_xor_b32 v204,0,v240
v_add_u32 v204,0x18000,v204
v_xor_b32 v205,32,v240
v_add_u32 v205,0x18000,v205
v_xor_b32 v206,64,v240
v_add_u32 v206,0x18000,v206
v_xor_b32 v207,0x60,v240
v_add_u32 v207,0x18000,v207
v_add_u32 v240,s58,v239
v_lshlrev_b32 v240,7,v240
v_lshrrev_b32 v241,1,v239
v_xor_b32 v241,v241,v236
v_lshl_add_u32 v240,v241,4,v240
v_add_u32 v212,0x8000,v240
v_xor_b32 v240,64,v240
v_add_u32 v213,0x8000,v240
v_xor_b32 v241,v236,v239
v_lshlrev_b32 v241,4,v241
v_lshl_add_u32 v240,v239,9,v241
v_xor_b32 v192,0,v240
v_add_u32 v192,0x20000,v192
v_xor_b32 v193,64,v240
v_add_u32 v193,0x20000,v193
v_xor_b32 v194,0x80,v240
v_add_u32 v194,0x20000,v194
v_xor_b32 v195,0xc0,v240
v_add_u32 v195,0x20000,v195
v_lshl_add_u32 v240,v236,2,v237
v_and_b32 v241,7,v240
v_lshlrev_b32 v242,9,v240
v_lshl_add_u32 v242,v241,5,v242
v_add_u32 v242,v242,v238
v_xor_b32 v196,0,v242
v_add_u32 v196,0x24000,v196
v_xor_b32 v197,32,v242
v_add_u32 v197,0x24000,v197
v_xor_b32 v198,64,v242
v_add_u32 v198,0x24000,v198
v_xor_b32 v199,0x60,v242
v_add_u32 v199,0x24000,v199
v_xor_b32 v200,0x80,v242
v_add_u32 v200,0x24000,v200
v_xor_b32 v201,0xa0,v242
v_add_u32 v201,0x24000,v201
v_xor_b32 v202,0xc0,v242
v_add_u32 v202,0x24000,v202
v_xor_b32 v203,0xe0,v242
v_add_u32 v203,0x24000,v203
v_bfe_u32 v241,v240,1,2
v_lshlrev_b32 v242,7,v240
v_lshl_add_u32 v242,v241,5,v242
v_add_u32 v242,v242,v238
v_xor_b32 v208,0,v242
v_add_u32 v208,0x10000,v208
v_xor_b32 v209,32,v242
v_add_u32 v209,0x10000,v209
v_xor_b32 v210,64,v242
v_add_u32 v210,0x10000,v210
v_xor_b32 v211,0x60,v242
v_add_u32 v211,0x10000,v211
v_lshlrev_b32 v215,4,v214
s_add_u32 m0,s92,0x24000
s_add_u32 s45,s59,s86
dn v244,%1,s45,0
dn v245,%1,s45,1024
dn v246,%1,s45,2048
dn v247,%1,s45,3072
s_add_u32 m0,s92,0x8000
s_lshr_b32 s42,s86,12
s_add_u32 s45,s95,s42
dt v220,%0,s45,0
dt v221,%0,s45,1024
dt v222,%0,s45,2048
dt v223,%0,s45,3072
s_add_u32 m0,s55,0xc000
s_add_u32 s45,s46,s42
s_add_u32 s45,s45,0x1c000
dt v248,%0,s45,0
s_cmp_eq_u32 %6,3
s_cbranch_scc0 Lnp%=
s_getpc_b64 s[56:57]
Lpc%=:
s_sub_u32 s56,s56,Lpc%=-Lentry%=
s_subb_u32 s57,s57,0
s_lshl_b32 s53,%9,13
v_lshlrev_b32 v242,7,v214
v_add_u32 v242,s53,v242
v_min_u32 v242,Lend%=-Lentry%=-64,v242
global_load_dword v255,v242,s[56:57]
Lnp%=:
s_cmp_eq_u32 %6,2
s_cbranch_scc1 LB_start%=
s_cmp_eq_u32 %6,3
s_cbranch_scc1 LC_start%=
LA_start%=:
s_lshl_b32 s93,%6,7
s_lshl_b32 s94,%6,18
s_waitcnt lgkmcnt(0)
s_waitcnt vmcnt(9)
s_barrier
r a[0:3],v212,40960
r a[56:59],v212,43008
r a[112:115],v212,45056
t a[204:205],v204,16384
t a[206:207],v204,16896
t a[208:209],v205,16384
t a[210:211],v205,16896
t a[212:213],v206,16384
t a[214:215],v206,16896
t a[216:217],v207,16384
t a[218:219],v207,16896
s_waitcnt lgkmcnt(0)
m v[0:3],a[204:207],a[0:3],0
t a[220:221],v204,20480
t a[222:223],v204,20992
m v[16:19],a[204:207],a[56:59],0
t a[224:225],v205,20480
t a[226:227],v205,20992
m v[32:35],a[204:207],a[112:115],0
t a[228:229],v206,20480
t a[230:231],v206,20992
m v[4:7],a[208:211],a[0:3],0
t a[232:233],v207,20480
t a[234:235],v207,20992
m v[20:23],a[208:211],a[56:59],0
v_lshrrev_b32 v236,5,v214
v_and_b32 v237,31,v214
m v[36:39],a[208:211],a[112:115],0
v_xor_b32 v237,v237,v236
v_lshlrev_b32 v237,4,v237
m v[8:11],a[212:215],a[0:3],0
v_lshlrev_b32 v236,9,v236
v_add_u32 v224,v236,v237
m v[24:27],a[212:215],a[56:59],0
v_xor_b32 v225,32,v224
v_xor_b32 v226,64,v224
m v[40:43],a[212:215],a[112:115],0
v_xor_b32 v227,0x60,v224
m v[12:15],a[216:219],a[0:3],0
m v[28:31],a[216:219],a[56:59],0
m v[44:47],a[216:219],a[112:115],0
s_waitcnt lgkmcnt(0)
m v[48:51],a[220:223],a[0:3],0
r a[4:7],v213,40960
r a[60:63],v213,43008
m v[64:67],a[220:223],a[56:59],0
r a[116:119],v213,45056
t a[204:205],v204,24576
m v[80:83],a[220:223],a[112:115],0
t a[206:207],v204,25088
t a[208:209],v205,24576
m v[52:55],a[224:227],a[0:3],0
t a[210:211],v205,25088
t a[212:213],v206,24576
m v[68:71],a[224:227],a[56:59],0
t a[214:215],v206,25088
t a[216:217],v207,24576
m v[84:87],a[224:227],a[112:115],0
t a[218:219],v207,25088
v_xor_b32 v228,0x80,v224
m v[56:59],a[228:231],a[0:3],0
v_xor_b32 v229,0xa0,v224
v_xor_b32 v230,0xc0,v224
m v[72:75],a[228:231],a[56:59],0
v_xor_b32 v231,0xe0,v224
v_lshrrev_b32 v236,5,v214
m v[88:91],a[228:231],a[112:115],0
v_and_b32 v237,31,v214
v_lshlrev_b32 v238,1,v236
m v[60:63],a[232:235],a[0:3],0
v_xor_b32 v237,v237,v238
v_lshlrev_b32 v237,4,v237
m v[76:79],a[232:235],a[56:59],0
m v[92:95],a[232:235],a[112:115],0
s_waitcnt lgkmcnt(0)
m v[0:3],a[204:207],a[4:7],v[0:3]
t a[220:221],v204,28672
t a[222:223],v204,29184
m v[16:19],a[204:207],a[60:63],v[16:19]
t a[224:225],v205,28672
t a[226:227],v205,29184
m v[32:35],a[204:207],a[116:119],v[32:35]
t a[228:229],v206,28672
t a[230:231],v206,29184
m v[4:7],a[208:211],a[4:7],v[4:7]
t a[232:233],v207,28672
t a[234:235],v207,29184
m v[20:23],a[208:211],a[60:63],v[20:23]
v_lshlrev_b32 v236,9,v236
v_add_u32 v232,v236,v237
m v[36:39],a[208:211],a[116:119],v[36:39]
v_xor_b32 v233,64,v232
v_xor_b32 v234,0x80,v232
m v[8:11],a[212:215],a[4:7],v[8:11]
v_xor_b32 v235,0xc0,v232
v_xor_b32 v224,s93,v224
m v[24:27],a[212:215],a[60:63],v[24:27]
v_xor_b32 v225,s93,v225
v_xor_b32 v226,s93,v226
m v[40:43],a[212:215],a[116:119],v[40:43]
v_xor_b32 v227,s93,v227
m v[12:15],a[216:219],a[4:7],v[12:15]
m v[28:31],a[216:219],a[60:63],v[28:31]
m v[44:47],a[216:219],a[116:119],v[44:47]
s_waitcnt lgkmcnt(0)
s_waitcnt vmcnt(0)
s_barrier
m v[48:51],a[220:223],a[4:7],v[48:51]
r a[8:11],v212,0
r a[64:67],v212,2048
m v[64:67],a[220:223],a[60:63],v[64:67]
r a[120:123],v212,4096
t a[204:205],v204,49152
m v[80:83],a[220:223],a[116:119],v[80:83]
t a[206:207],v204,49664
t a[208:209],v205,49152
m v[52:55],a[224:227],a[4:7],v[52:55]
t a[210:211],v205,49664
t a[212:213],v206,49152
m v[68:71],a[224:227],a[60:63],v[68:71]
t a[214:215],v206,49664
t a[216:217],v207,49152
m v[84:87],a[224:227],a[116:119],v[84:87]
t a[218:219],v207,49664
m v[56:59],a[228:231],a[4:7],v[56:59]
m v[72:75],a[228:231],a[60:63],v[72:75]
m v[88:91],a[228:231],a[116:119],v[88:91]
m v[60:63],a[232:235],a[4:7],v[60:63]
m v[76:79],a[232:235],a[60:63],v[76:79]
m v[92:95],a[232:235],a[116:119],v[92:95]
s_waitcnt lgkmcnt(0)
m v[0:3],a[204:207],a[8:11],v[0:3]
t a[220:221],v204,53248
t a[222:223],v204,53760
m v[16:19],a[204:207],a[64:67],v[16:19]
t a[224:225],v205,53248
t a[226:227],v205,53760
m v[32:35],a[204:207],a[120:123],v[32:35]
t a[228:229],v206,53248
t a[230:231],v206,53760
m v[4:7],a[208:211],a[8:11],v[4:7]
t a[232:233],v207,53248
t a[234:235],v207,53760
m v[20:23],a[208:211],a[64:67],v[20:23]
m v[36:39],a[208:211],a[120:123],v[36:39]
m v[8:11],a[212:215],a[8:11],v[8:11]
m v[24:27],a[212:215],a[64:67],v[24:27]
m v[40:43],a[212:215],a[120:123],v[40:43]
m v[12:15],a[216:219],a[8:11],v[12:15]
m v[28:31],a[216:219],a[64:67],v[28:31]
m v[44:47],a[216:219],a[120:123],v[44:47]
s_waitcnt lgkmcnt(0)
m v[48:51],a[220:223],a[8:11],v[48:51]
r a[12:15],v213,0
r a[68:71],v213,2048
m v[64:67],a[220:223],a[64:67],v[64:67]
r a[124:127],v213,4096
t a[204:205],v204,57344
m v[80:83],a[220:223],a[120:123],v[80:83]
t a[206:207],v204,57856
t a[208:209],v205,57344
m v[52:55],a[224:227],a[8:11],v[52:55]
t a[210:211],v205,57856
t a[212:213],v206,57344
m v[68:71],a[224:227],a[64:67],v[68:71]
t a[214:215],v206,57856
t a[216:217],v207,57344
m v[84:87],a[224:227],a[120:123],v[84:87]
t a[218:219],v207,57856
m v[56:59],a[228:231],a[8:11],v[56:59]
m v[72:75],a[228:231],a[64:67],v[72:75]
m v[88:91],a[228:231],a[120:123],v[88:91]
m v[60:63],a[232:235],a[8:11],v[60:63]
m v[76:79],a[232:235],a[64:67],v[76:79]
m v[92:95],a[232:235],a[120:123],v[92:95]
s_waitcnt lgkmcnt(0)
m v[0:3],a[204:207],a[12:15],v[0:3]
t a[220:221],v204,61440
t a[222:223],v204,61952
m v[16:19],a[204:207],a[68:71],v[16:19]
t a[224:225],v205,61440
t a[226:227],v205,61952
m v[32:35],a[204:207],a[124:127],v[32:35]
t a[228:229],v206,61440
t a[230:231],v206,61952
m v[4:7],a[208:211],a[12:15],v[4:7]
t a[232:233],v207,61440
t a[234:235],v207,61952
m v[20:23],a[208:211],a[68:71],v[20:23]
m v[36:39],a[208:211],a[124:127],v[36:39]
m v[8:11],a[212:215],a[12:15],v[8:11]
m v[24:27],a[212:215],a[68:71],v[24:27]
m v[40:43],a[212:215],a[124:127],v[40:43]
m v[12:15],a[216:219],a[12:15],v[12:15]
m v[28:31],a[216:219],a[68:71],v[28:31]
m v[44:47],a[216:219],a[124:127],v[44:47]
s_waitcnt lgkmcnt(0)
s_barrier
m v[48:51],a[220:223],a[12:15],v[48:51]
r a[16:19],v212,20480
r a[72:75],v212,22528
m v[64:67],a[220:223],a[68:71],v[64:67]
r a[128:131],v212,24576
t a[204:205],v204,0
m v[80:83],a[220:223],a[124:127],v[80:83]
t a[206:207],v204,512
t a[208:209],v205,0
m v[52:55],a[224:227],a[12:15],v[52:55]
t a[210:211],v205,512
t a[212:213],v206,0
m v[68:71],a[224:227],a[68:71],v[68:71]
t a[214:215],v206,512
t a[216:217],v207,0
m v[84:87],a[224:227],a[124:127],v[84:87]
t a[218:219],v207,512
m v[56:59],a[228:231],a[12:15],v[56:59]
m v[72:75],a[228:231],a[68:71],v[72:75]
m v[88:91],a[228:231],a[124:127],v[88:91]
m v[60:63],a[232:235],a[12:15],v[60:63]
m v[76:79],a[232:235],a[68:71],v[76:79]
m v[92:95],a[232:235],a[124:127],v[92:95]
s_waitcnt lgkmcnt(0)
m v[0:3],a[204:207],a[16:19],v[0:3]
t a[220:221],v204,4096
t a[222:223],v204,4608
m v[16:19],a[204:207],a[72:75],v[16:19]
t a[224:225],v205,4096
t a[226:227],v205,4608
m v[32:35],a[204:207],a[128:131],v[32:35]
t a[228:229],v206,4096
t a[230:231],v206,4608
m v[4:7],a[208:211],a[16:19],v[4:7]
t a[232:233],v207,4096
t a[234:235],v207,4608
m v[20:23],a[208:211],a[72:75],v[20:23]
m v[36:39],a[208:211],a[128:131],v[36:39]
m v[8:11],a[212:215],a[16:19],v[8:11]
m v[24:27],a[212:215],a[72:75],v[24:27]
m v[40:43],a[212:215],a[128:131],v[40:43]
m v[12:15],a[216:219],a[16:19],v[12:15]
m v[28:31],a[216:219],a[72:75],v[28:31]
m v[44:47],a[216:219],a[128:131],v[44:47]
s_waitcnt lgkmcnt(0)
m v[48:51],a[220:223],a[16:19],v[48:51]
r a[20:23],v213,20480
r a[76:79],v213,22528
m v[64:67],a[220:223],a[72:75],v[64:67]
r a[132:135],v213,24576
t a[204:205],v204,8192
m v[80:83],a[220:223],a[128:131],v[80:83]
t a[206:207],v204,8704
t a[208:209],v205,8192
m v[52:55],a[224:227],a[16:19],v[52:55]
t a[210:211],v205,8704
t a[212:213],v206,8192
m v[68:71],a[224:227],a[72:75],v[68:71]
t a[214:215],v206,8704
t a[216:217],v207,8192
m v[84:87],a[224:227],a[128:131],v[84:87]
t a[218:219],v207,8704
m v[56:59],a[228:231],a[16:19],v[56:59]
m v[72:75],a[228:231],a[72:75],v[72:75]
m v[88:91],a[228:231],a[128:131],v[88:91]
m v[60:63],a[232:235],a[16:19],v[60:63]
m v[76:79],a[232:235],a[72:75],v[76:79]
m v[92:95],a[232:235],a[128:131],v[92:95]
s_waitcnt lgkmcnt(0)
m v[0:3],a[204:207],a[20:23],v[0:3]
t a[220:221],v204,12288
t a[222:223],v204,12800
m v[16:19],a[204:207],a[76:79],v[16:19]
t a[224:225],v205,12288
t a[226:227],v205,12800
m v[32:35],a[204:207],a[132:135],v[32:35]
t a[228:229],v206,12288
t a[230:231],v206,12800
m v[4:7],a[208:211],a[20:23],v[4:7]
t a[232:233],v207,12288
t a[234:235],v207,12800
m v[20:23],a[208:211],a[76:79],v[20:23]
m v[36:39],a[208:211],a[132:135],v[36:39]
m v[8:11],a[212:215],a[20:23],v[8:11]
m v[24:27],a[212:215],a[76:79],v[24:27]
m v[40:43],a[212:215],a[132:135],v[40:43]
m v[12:15],a[216:219],a[20:23],v[12:15]
m v[28:31],a[216:219],a[76:79],v[28:31]
m v[44:47],a[216:219],a[132:135],v[44:47]
s_waitcnt lgkmcnt(0)
s_barrier
m v[48:51],a[220:223],a[20:23],v[48:51]
r a[24:27],v212,40960
r a[80:83],v212,43008
m v[64:67],a[220:223],a[76:79],v[64:67]
r a[136:139],v212,45056
t a[204:205],v204,16384
m v[80:83],a[220:223],a[132:135],v[80:83]
t a[206:207],v204,16896
t a[208:209],v205,16384
m v[52:55],a[224:227],a[20:23],v[52:55]
t a[210:211],v205,16896
t a[212:213],v206,16384
m v[68:71],a[224:227],a[76:79],v[68:71]
t a[214:215],v206,16896
t a[216:217],v207,16384
m v[84:87],a[224:227],a[132:135],v[84:87]
t a[218:219],v207,16896
m v[56:59],a[228:231],a[20:23],v[56:59]
m v[72:75],a[228:231],a[76:79],v[72:75]
m v[88:91],a[228:231],a[132:135],v[88:91]
m v[60:63],a[232:235],a[20:23],v[60:63]
m v[76:79],a[232:235],a[76:79],v[76:79]
m v[92:95],a[232:235],a[132:135],v[92:95]
s_waitcnt lgkmcnt(0)
m v[0:3],a[204:207],a[24:27],v[0:3]
t a[220:221],v204,20480
t a[222:223],v204,20992
m v[16:19],a[204:207],a[80:83],v[16:19]
t a[224:225],v205,20480
t a[226:227],v205,20992
m v[32:35],a[204:207],a[136:139],v[32:35]
t a[228:229],v206,20480
t a[230:231],v206,20992
m v[4:7],a[208:211],a[24:27],v[4:7]
t a[232:233],v207,20480
t a[234:235],v207,20992
m v[20:23],a[208:211],a[80:83],v[20:23]
m v[36:39],a[208:211],a[136:139],v[36:39]
m v[8:11],a[212:215],a[24:27],v[8:11]
m v[24:27],a[212:215],a[80:83],v[24:27]
m v[40:43],a[212:215],a[136:139],v[40:43]
m v[12:15],a[216:219],a[24:27],v[12:15]
m v[28:31],a[216:219],a[80:83],v[28:31]
m v[44:47],a[216:219],a[136:139],v[44:47]
s_waitcnt lgkmcnt(0)
m v[48:51],a[220:223],a[24:27],v[48:51]
r a[28:31],v213,40960
r a[84:87],v213,43008
m v[64:67],a[220:223],a[80:83],v[64:67]
r a[140:143],v213,45056
t a[204:205],v204,24576
m v[80:83],a[220:223],a[136:139],v[80:83]
t a[206:207],v204,25088
t a[208:209],v205,24576
m v[52:55],a[224:227],a[24:27],v[52:55]
t a[210:211],v205,25088
t a[212:213],v206,24576
m v[68:71],a[224:227],a[80:83],v[68:71]
t a[214:215],v206,25088
t a[216:217],v207,24576
m v[84:87],a[224:227],a[136:139],v[84:87]
t a[218:219],v207,25088
m v[56:59],a[228:231],a[24:27],v[56:59]
m v[72:75],a[228:231],a[80:83],v[72:75]
m v[88:91],a[228:231],a[136:139],v[88:91]
m v[60:63],a[232:235],a[24:27],v[60:63]
m v[76:79],a[232:235],a[80:83],v[76:79]
m v[92:95],a[232:235],a[136:139],v[92:95]
s_waitcnt lgkmcnt(0)
m v[0:3],a[204:207],a[28:31],v[0:3]
t a[220:221],v204,28672
t a[222:223],v204,29184
m v[16:19],a[204:207],a[84:87],v[16:19]
t a[224:225],v205,28672
t a[226:227],v205,29184
m v[32:35],a[204:207],a[140:143],v[32:35]
t a[228:229],v206,28672
t a[230:231],v206,29184
m v[4:7],a[208:211],a[28:31],v[4:7]
t a[232:233],v207,28672
t a[234:235],v207,29184
m v[20:23],a[208:211],a[84:87],v[20:23]
m v[36:39],a[208:211],a[140:143],v[36:39]
m v[8:11],a[212:215],a[28:31],v[8:11]
m v[24:27],a[212:215],a[84:87],v[24:27]
m v[40:43],a[212:215],a[140:143],v[40:43]
m v[12:15],a[216:219],a[28:31],v[12:15]
m v[28:31],a[216:219],a[84:87],v[28:31]
m v[44:47],a[216:219],a[140:143],v[44:47]
s_waitcnt lgkmcnt(0)
s_barrier
m v[48:51],a[220:223],a[28:31],v[48:51]
r a[32:35],v212,0
r a[88:91],v212,2048
m v[64:67],a[220:223],a[84:87],v[64:67]
r a[144:147],v212,4096
t a[204:205],v204,49152
m v[80:83],a[220:223],a[140:143],v[80:83]
t a[206:207],v204,49664
t a[208:209],v205,49152
m v[52:55],a[224:227],a[28:31],v[52:55]
t a[210:211],v205,49664
t a[212:213],v206,49152
m v[68:71],a[224:227],a[84:87],v[68:71]
t a[214:215],v206,49664
t a[216:217],v207,49152
m v[84:87],a[224:227],a[140:143],v[84:87]
t a[218:219],v207,49664
m v[56:59],a[228:231],a[28:31],v[56:59]
m v[72:75],a[228:231],a[84:87],v[72:75]
m v[88:91],a[228:231],a[140:143],v[88:91]
m v[60:63],a[232:235],a[28:31],v[60:63]
m v[76:79],a[232:235],a[84:87],v[76:79]
m v[92:95],a[232:235],a[140:143],v[92:95]
s_waitcnt lgkmcnt(0)
m v[0:3],a[204:207],a[32:35],v[0:3]
t a[220:221],v204,53248
t a[222:223],v204,53760
m v[16:19],a[204:207],a[88:91],v[16:19]
t a[224:225],v205,53248
t a[226:227],v205,53760
m v[32:35],a[204:207],a[144:147],v[32:35]
t a[228:229],v206,53248
t a[230:231],v206,53760
m v[4:7],a[208:211],a[32:35],v[4:7]
t a[232:233],v207,53248
t a[234:235],v207,53760
m v[20:23],a[208:211],a[88:91],v[20:23]
m v[36:39],a[208:211],a[144:147],v[36:39]
m v[8:11],a[212:215],a[32:35],v[8:11]
m v[24:27],a[212:215],a[88:91],v[24:27]
m v[40:43],a[212:215],a[144:147],v[40:43]
m v[12:15],a[216:219],a[32:35],v[12:15]
m v[28:31],a[216:219],a[88:91],v[28:31]
m v[44:47],a[216:219],a[144:147],v[44:47]
s_waitcnt lgkmcnt(0)
m v[48:51],a[220:223],a[32:35],v[48:51]
r a[36:39],v213,0
r a[92:95],v213,2048
m v[64:67],a[220:223],a[88:91],v[64:67]
r a[148:151],v213,4096
t a[204:205],v204,57344
m v[80:83],a[220:223],a[144:147],v[80:83]
t a[206:207],v204,57856
t a[208:209],v205,57344
m v[52:55],a[224:227],a[32:35],v[52:55]
t a[210:211],v205,57856
t a[212:213],v206,57344
m v[68:71],a[224:227],a[88:91],v[68:71]
t a[214:215],v206,57856
t a[216:217],v207,57344
m v[84:87],a[224:227],a[144:147],v[84:87]
t a[218:219],v207,57856
v_lshlrev_b32 v251,2,v214
m v[56:59],a[228:231],a[32:35],v[56:59]
v_add_u32 v251,0x17000,v251
ds_read_b32 v219,v251
m v[72:75],a[228:231],a[88:91],v[72:75]
m v[88:91],a[228:231],a[144:147],v[88:91]
m v[60:63],a[232:235],a[32:35],v[60:63]
m v[76:79],a[232:235],a[88:91],v[76:79]
m v[92:95],a[232:235],a[144:147],v[92:95]
s_waitcnt lgkmcnt(0)
m v[0:3],a[204:207],a[36:39],v[0:3]
t a[220:221],v204,61440
t a[222:223],v204,61952
m v[16:19],a[204:207],a[92:95],v[16:19]
t a[224:225],v205,61440
t a[226:227],v205,61952
m v[32:35],a[204:207],a[148:151],v[32:35]
t a[228:229],v206,61440
t a[230:231],v206,61952
m v[4:7],a[208:211],a[36:39],v[4:7]
t a[232:233],v207,61440
t a[234:235],v207,61952
m v[20:23],a[208:211],a[92:95],v[20:23]
m v[36:39],a[208:211],a[148:151],v[36:39]
m v[8:11],a[212:215],a[36:39],v[8:11]
m v[24:27],a[212:215],a[92:95],v[24:27]
m v[40:43],a[212:215],a[148:151],v[40:43]
m v[12:15],a[216:219],a[36:39],v[12:15]
m v[28:31],a[216:219],a[92:95],v[28:31]
m v[44:47],a[216:219],a[148:151],v[44:47]
s_waitcnt lgkmcnt(0)
s_barrier
m v[48:51],a[220:223],a[36:39],v[48:51]
r a[40:43],v212,20480
r a[96:99],v212,22528
m v[64:67],a[220:223],a[92:95],v[64:67]
r a[152:155],v212,24576
t a[204:205],v204,0
m v[80:83],a[220:223],a[148:151],v[80:83]
t a[206:207],v204,512
t a[208:209],v205,0
m v[52:55],a[224:227],a[36:39],v[52:55]
t a[210:211],v205,512
t a[212:213],v206,0
m v[68:71],a[224:227],a[92:95],v[68:71]
t a[214:215],v206,512
t a[216:217],v207,0
m v[84:87],a[224:227],a[148:151],v[84:87]
t a[218:219],v207,512
m v[56:59],a[228:231],a[36:39],v[56:59]
m v[72:75],a[228:231],a[92:95],v[72:75]
m v[88:91],a[228:231],a[148:151],v[88:91]
m v[60:63],a[232:235],a[36:39],v[60:63]
m v[76:79],a[232:235],a[92:95],v[76:79]
m v[92:95],a[232:235],a[148:151],v[92:95]
s_waitcnt lgkmcnt(0)
m v[0:3],a[204:207],a[40:43],v[0:3]
t a[220:221],v204,4096
t a[222:223],v204,4608
m v[16:19],a[204:207],a[96:99],v[16:19]
t a[224:225],v205,4096
t a[226:227],v205,4608
m v[32:35],a[204:207],a[152:155],v[32:35]
t a[228:229],v206,4096
t a[230:231],v206,4608
m v[4:7],a[208:211],a[40:43],v[4:7]
t a[232:233],v207,4096
t a[234:235],v207,4608
m v[20:23],a[208:211],a[96:99],v[20:23]
m v[36:39],a[208:211],a[152:155],v[36:39]
m v[8:11],a[212:215],a[40:43],v[8:11]
m v[24:27],a[212:215],a[96:99],v[24:27]
m v[40:43],a[212:215],a[152:155],v[40:43]
m v[12:15],a[216:219],a[40:43],v[12:15]
m v[28:31],a[216:219],a[96:99],v[28:31]
m v[44:47],a[216:219],a[152:155],v[44:47]
s_waitcnt lgkmcnt(0)
m v[48:51],a[220:223],a[40:43],v[48:51]
r a[44:47],v213,20480
r a[100:103],v213,22528
m v[64:67],a[220:223],a[96:99],v[64:67]
r a[156:159],v213,24576
t a[204:205],v204,8192
m v[80:83],a[220:223],a[152:155],v[80:83]
t a[206:207],v204,8704
t a[208:209],v205,8192
m v[52:55],a[224:227],a[40:43],v[52:55]
t a[210:211],v205,8704
t a[212:213],v206,8192
m v[68:71],a[224:227],a[96:99],v[68:71]
t a[214:215],v206,8704
t a[216:217],v207,8192
m v[84:87],a[224:227],a[152:155],v[84:87]
t a[218:219],v207,8704
m v[56:59],a[228:231],a[40:43],v[56:59]
m v[72:75],a[228:231],a[96:99],v[72:75]
m v[88:91],a[228:231],a[152:155],v[88:91]
m v[60:63],a[232:235],a[40:43],v[60:63]
m v[76:79],a[232:235],a[96:99],v[76:79]
m v[92:95],a[232:235],a[152:155],v[92:95]
s_waitcnt lgkmcnt(0)
m v[0:3],a[204:207],a[44:47],v[0:3]
t a[220:221],v204,12288
t a[222:223],v204,12800
m v[16:19],a[204:207],a[100:103],v[16:19]
t a[224:225],v205,12288
t a[226:227],v205,12800
m v[32:35],a[204:207],a[156:159],v[32:35]
t a[228:229],v206,12288
t a[230:231],v206,12800
m v[4:7],a[208:211],a[44:47],v[4:7]
t a[232:233],v207,12288
t a[234:235],v207,12800
m v[20:23],a[208:211],a[100:103],v[20:23]
m v[36:39],a[208:211],a[156:159],v[36:39]
m v[8:11],a[212:215],a[44:47],v[8:11]
m v[24:27],a[212:215],a[100:103],v[24:27]
m v[40:43],a[212:215],a[156:159],v[40:43]
m v[12:15],a[216:219],a[44:47],v[12:15]
m v[28:31],a[216:219],a[100:103],v[28:31]
m v[44:47],a[216:219],a[156:159],v[44:47]
s_waitcnt lgkmcnt(0)
s_barrier
m v[48:51],a[220:223],a[44:47],v[48:51]
r a[48:51],v212,40960
r a[104:107],v212,43008
m v[64:67],a[220:223],a[100:103],v[64:67]
r a[160:163],v212,45056
t a[204:205],v204,16384
m v[80:83],a[220:223],a[156:159],v[80:83]
t a[206:207],v204,16896
t a[208:209],v205,16384
m v[52:55],a[224:227],a[44:47],v[52:55]
t a[210:211],v205,16896
t a[212:213],v206,16384
m v[68:71],a[224:227],a[100:103],v[68:71]
t a[214:215],v206,16896
t a[216:217],v207,16384
m v[84:87],a[224:227],a[156:159],v[84:87]
t a[218:219],v207,16896
m v[56:59],a[228:231],a[44:47],v[56:59]
m v[72:75],a[228:231],a[100:103],v[72:75]
m v[88:91],a[228:231],a[156:159],v[88:91]
m v[60:63],a[232:235],a[44:47],v[60:63]
m v[76:79],a[232:235],a[100:103],v[76:79]
m v[92:95],a[232:235],a[156:159],v[92:95]
s_waitcnt lgkmcnt(0)
m v[0:3],a[204:207],a[48:51],v[0:3]
t a[220:221],v204,20480
t a[222:223],v204,20992
m v[16:19],a[204:207],a[104:107],v[16:19]
t a[224:225],v205,20480
t a[226:227],v205,20992
m v[32:35],a[204:207],a[160:163],v[32:35]
t a[228:229],v206,20480
t a[230:231],v206,20992
m v[4:7],a[208:211],a[48:51],v[4:7]
t a[232:233],v207,20480
t a[234:235],v207,20992
m v[20:23],a[208:211],a[104:107],v[20:23]
m v[36:39],a[208:211],a[160:163],v[36:39]
m v[8:11],a[212:215],a[48:51],v[8:11]
m v[24:27],a[212:215],a[104:107],v[24:27]
m v[40:43],a[212:215],a[160:163],v[40:43]
m v[12:15],a[216:219],a[48:51],v[12:15]
m v[28:31],a[216:219],a[104:107],v[28:31]
m v[44:47],a[216:219],a[160:163],v[44:47]
s_waitcnt lgkmcnt(0)
m v[48:51],a[220:223],a[48:51],v[48:51]
r a[52:55],v213,40960
r a[108:111],v213,43008
m v[64:67],a[220:223],a[104:107],v[64:67]
r a[164:167],v213,45056
t a[204:205],v204,24576
m v[80:83],a[220:223],a[160:163],v[80:83]
t a[206:207],v204,25088
t a[208:209],v205,24576
m v[52:55],a[224:227],a[48:51],v[52:55]
t a[210:211],v205,25088
t a[212:213],v206,24576
m v[68:71],a[224:227],a[104:107],v[68:71]
t a[214:215],v206,25088
t a[216:217],v207,24576
m v[84:87],a[224:227],a[160:163],v[84:87]
t a[218:219],v207,25088
m v[56:59],a[228:231],a[48:51],v[56:59]
m v[72:75],a[228:231],a[104:107],v[72:75]
m v[88:91],a[228:231],a[160:163],v[88:91]
m v[60:63],a[232:235],a[48:51],v[60:63]
m v[76:79],a[232:235],a[104:107],v[76:79]
m v[92:95],a[232:235],a[160:163],v[92:95]
s_waitcnt lgkmcnt(0)
m v[0:3],a[204:207],a[52:55],v[0:3]
t a[220:221],v204,28672
t a[222:223],v204,29184
m v[16:19],a[204:207],a[108:111],v[16:19]
t a[224:225],v205,28672
t a[226:227],v205,29184
m v[32:35],a[204:207],a[164:167],v[32:35]
t a[228:229],v206,28672
t a[230:231],v206,29184
m v[4:7],a[208:211],a[52:55],v[4:7]
t a[232:233],v207,28672
t a[234:235],v207,29184
m v[20:23],a[208:211],a[108:111],v[20:23]
m v[36:39],a[208:211],a[164:167],v[36:39]
m v[8:11],a[212:215],a[52:55],v[8:11]
m v[24:27],a[212:215],a[108:111],v[24:27]
m v[40:43],a[212:215],a[164:167],v[40:43]
m v[12:15],a[216:219],a[52:55],v[12:15]
m v[28:31],a[216:219],a[108:111],v[28:31]
m v[44:47],a[216:219],a[164:167],v[44:47]
s_waitcnt lgkmcnt(0)
s_barrier
m v[48:51],a[220:223],a[52:55],v[48:51]
t a[204:205],v204,49152
t a[206:207],v204,49664
m v[64:67],a[220:223],a[108:111],v[64:67]
t a[208:209],v205,49152
t a[210:211],v205,49664
m v[80:83],a[220:223],a[164:167],v[80:83]
t a[212:213],v206,49152
t a[214:215],v206,49664
m v[52:55],a[224:227],a[52:55],v[52:55]
t a[216:217],v207,49152
t a[218:219],v207,49664
m v[68:71],a[224:227],a[108:111],v[68:71]
m v[84:87],a[224:227],a[164:167],v[84:87]
m v[56:59],a[228:231],a[52:55],v[56:59]
m v[72:75],a[228:231],a[108:111],v[72:75]
m v[88:91],a[228:231],a[164:167],v[88:91]
m v[60:63],a[232:235],a[52:55],v[60:63]
m v[76:79],a[232:235],a[108:111],v[76:79]
m v[92:95],a[232:235],a[164:167],v[92:95]
s_waitcnt lgkmcnt(0)
m v[96:99],a[204:207],a[0:3],0
t a[220:221],v204,53248
t a[222:223],v204,53760
m v[112:115],a[204:207],a[56:59],0
t a[224:225],v205,53248
t a[226:227],v205,53760
m v[128:131],a[204:207],a[112:115],0
t a[228:229],v206,53248
t a[230:231],v206,53760
m v[100:103],a[208:211],a[0:3],0
t a[232:233],v207,53248
t a[234:235],v207,53760
m v[116:119],a[208:211],a[56:59],0
m v[132:135],a[208:211],a[112:115],0
m v[104:107],a[212:215],a[0:3],0
m v[120:123],a[212:215],a[56:59],0
m v[136:139],a[212:215],a[112:115],0
m v[108:111],a[216:219],a[0:3],0
m v[124:127],a[216:219],a[56:59],0
m v[140:143],a[216:219],a[112:115],0
s_waitcnt lgkmcnt(0)
m v[96:99],a[220:223],a[4:7],v[96:99]
t a[204:205],v204,57344
t a[206:207],v204,57856
m v[112:115],a[220:223],a[60:63],v[112:115]
t a[208:209],v205,57344
t a[210:211],v205,57856
m v[128:131],a[220:223],a[116:119],v[128:131]
t a[212:213],v206,57344
t a[214:215],v206,57856
m v[100:103],a[224:227],a[4:7],v[100:103]
t a[216:217],v207,57344
t a[218:219],v207,57856
m v[116:119],a[224:227],a[60:63],v[116:119]
v_mul_f32 v0,s47,v0
v_mul_f32 v1,s47,v1
m v[132:135],a[224:227],a[116:119],v[132:135]
v_mul_f32 v2,s47,v2
v_mul_f32 v3,s47,v3
m v[104:107],a[228:231],a[4:7],v[104:107]
v_mul_f32 v4,s47,v4
v_mul_f32 v5,s47,v5
m v[120:123],a[228:231],a[60:63],v[120:123]
v_mul_f32 v6,s47,v6
v_mul_f32 v7,s47,v7
m v[136:139],a[228:231],a[116:119],v[136:139]
cv v0,v0,v1
cv v1,v2,v3
m v[108:111],a[232:235],a[4:7],v[108:111]
cv v2,v4,v5
cv v3,v6,v7
m v[124:127],a[232:235],a[60:63],v[124:127]
v_mul_f32 v8,s47,v8
v_mul_f32 v9,s47,v9
m v[140:143],a[232:235],a[116:119],v[140:143]
s_waitcnt lgkmcnt(0)
m v[96:99],a[204:207],a[8:11],v[96:99]
t a[220:221],v204,61440
t a[222:223],v204,61952
m v[112:115],a[204:207],a[64:67],v[112:115]
t a[224:225],v205,61440
t a[226:227],v205,61952
m v[128:131],a[204:207],a[120:123],v[128:131]
t a[228:229],v206,61440
t a[230:231],v206,61952
m v[100:103],a[208:211],a[8:11],v[100:103]
t a[232:233],v207,61440
t a[234:235],v207,61952
m v[116:119],a[208:211],a[64:67],v[116:119]
v_mul_f32 v10,s47,v10
v_mul_f32 v11,s47,v11
m v[132:135],a[208:211],a[120:123],v[132:135]
v_mul_f32 v12,s47,v12
v_mul_f32 v13,s47,v13
m v[104:107],a[212:215],a[8:11],v[104:107]
v_mul_f32 v14,s47,v14
v_mul_f32 v15,s47,v15
m v[120:123],a[212:215],a[64:67],v[120:123]
cv v8,v8,v9
cv v9,v10,v11
m v[136:139],a[212:215],a[120:123],v[136:139]
cv v10,v12,v13
cv v11,v14,v15
m v[108:111],a[216:219],a[8:11],v[108:111]
v_mul_f32 v16,s47,v16
v_mul_f32 v17,s47,v17
m v[124:127],a[216:219],a[64:67],v[124:127]
v_mul_f32 v18,s47,v18
v_mul_f32 v19,s47,v19
m v[140:143],a[216:219],a[120:123],v[140:143]
v_mul_f32 v20,s47,v20
s_waitcnt lgkmcnt(0)
s_barrier
m v[96:99],a[220:223],a[12:15],v[96:99]
t a[204:205],v204,0
t a[206:207],v204,512
m v[112:115],a[220:223],a[68:71],v[112:115]
t a[208:209],v205,0
t a[210:211],v205,512
m v[128:131],a[220:223],a[124:127],v[128:131]
t a[212:213],v206,0
t a[214:215],v206,512
m v[100:103],a[224:227],a[12:15],v[100:103]
t a[216:217],v207,0
t a[218:219],v207,512
m v[116:119],a[224:227],a[68:71],v[116:119]
v_mul_f32 v21,s47,v21
v_mul_f32 v22,s47,v22
m v[132:135],a[224:227],a[124:127],v[132:135]
v_mul_f32 v23,s47,v23
cv v16,v16,v17
m v[104:107],a[228:231],a[12:15],v[104:107]
cv v17,v18,v19
cv v18,v20,v21
m v[120:123],a[228:231],a[68:71],v[120:123]
cv v19,v22,v23
v_mul_f32 v24,s47,v24
m v[136:139],a[228:231],a[124:127],v[136:139]
v_mul_f32 v25,s47,v25
v_mul_f32 v26,s47,v26
m v[108:111],a[232:235],a[12:15],v[108:111]
v_mul_f32 v27,s47,v27
v_mul_f32 v28,s47,v28
m v[124:127],a[232:235],a[68:71],v[124:127]
v_mul_f32 v29,s47,v29
v_mul_f32 v30,s47,v30
m v[140:143],a[232:235],a[124:127],v[140:143]
v_mul_f32 v31,s47,v31
s_waitcnt lgkmcnt(0)
m v[96:99],a[204:207],a[16:19],v[96:99]
t a[220:221],v204,4096
t a[222:223],v204,4608
m v[112:115],a[204:207],a[72:75],v[112:115]
t a[224:225],v205,4096
t a[226:227],v205,4608
m v[128:131],a[204:207],a[128:131],v[128:131]
t a[228:229],v206,4096
t a[230:231],v206,4608
m v[100:103],a[208:211],a[16:19],v[100:103]
t a[232:233],v207,4096
t a[234:235],v207,4608
m v[116:119],a[208:211],a[72:75],v[116:119]
cv v24,v24,v25
cv v25,v26,v27
m v[132:135],a[208:211],a[128:131],v[132:135]
cv v26,v28,v29
cv v27,v30,v31
m v[104:107],a[212:215],a[16:19],v[104:107]
v_mul_f32 v32,s47,v32
v_mul_f32 v33,s47,v33
m v[120:123],a[212:215],a[72:75],v[120:123]
v_mul_f32 v34,s47,v34
v_mul_f32 v35,s47,v35
m v[136:139],a[212:215],a[128:131],v[136:139]
v_mul_f32 v36,s47,v36
v_mul_f32 v37,s47,v37
m v[108:111],a[216:219],a[16:19],v[108:111]
v_mul_f32 v38,s47,v38
v_mul_f32 v39,s47,v39
m v[124:127],a[216:219],a[72:75],v[124:127]
cv v32,v32,v33
cv v33,v34,v35
m v[140:143],a[216:219],a[128:131],v[140:143]
cv v34,v36,v37
s_waitcnt lgkmcnt(0)
m v[96:99],a[220:223],a[20:23],v[96:99]
t a[204:205],v204,8192
t a[206:207],v204,8704
m v[112:115],a[220:223],a[76:79],v[112:115]
t a[208:209],v205,8192
t a[210:211],v205,8704
m v[128:131],a[220:223],a[132:135],v[128:131]
t a[212:213],v206,8192
t a[214:215],v206,8704
m v[100:103],a[224:227],a[20:23],v[100:103]
t a[216:217],v207,8192
t a[218:219],v207,8704
m v[116:119],a[224:227],a[76:79],v[116:119]
cv v35,v38,v39
v_mul_f32 v40,s47,v40
m v[132:135],a[224:227],a[132:135],v[132:135]
v_mul_f32 v41,s47,v41
v_mul_f32 v42,s47,v42
m v[104:107],a[228:231],a[20:23],v[104:107]
v_mul_f32 v43,s47,v43
v_mul_f32 v44,s47,v44
m v[120:123],a[228:231],a[76:79],v[120:123]
v_mul_f32 v45,s47,v45
v_mul_f32 v46,s47,v46
m v[136:139],a[228:231],a[132:135],v[136:139]
v_mul_f32 v47,s47,v47
cv v40,v40,v41
m v[108:111],a[232:235],a[20:23],v[108:111]
cv v41,v42,v43
cv v42,v44,v45
m v[124:127],a[232:235],a[76:79],v[124:127]
cv v43,v46,v47
v_permlane32_swap_b32 v0,v2
m v[140:143],a[232:235],a[132:135],v[140:143]
s_waitcnt lgkmcnt(0)
m v[96:99],a[204:207],a[24:27],v[96:99]
t a[220:221],v204,12288
t a[222:223],v204,12800
m v[112:115],a[204:207],a[80:83],v[112:115]
t a[224:225],v205,12288
t a[226:227],v205,12800
m v[128:131],a[204:207],a[136:139],v[128:131]
t a[228:229],v206,12288
t a[230:231],v206,12800
m v[100:103],a[208:211],a[24:27],v[100:103]
t a[232:233],v207,12288
t a[234:235],v207,12800
m v[116:119],a[208:211],a[80:83],v[116:119]
v_permlane32_swap_b32 v1,v3
v_permlane32_swap_b32 v8,v10
m v[132:135],a[208:211],a[136:139],v[132:135]
v_permlane32_swap_b32 v9,v11
v_permlane32_swap_b32 v16,v18
m v[104:107],a[212:215],a[24:27],v[104:107]
v_permlane32_swap_b32 v17,v19
v_permlane32_swap_b32 v24,v26
m v[120:123],a[212:215],a[80:83],v[120:123]
v_permlane32_swap_b32 v25,v27
v_permlane32_swap_b32 v32,v34
m v[136:139],a[212:215],a[136:139],v[136:139]
v_permlane32_swap_b32 v33,v35
v_permlane32_swap_b32 v40,v42
m v[108:111],a[216:219],a[24:27],v[108:111]
v_permlane32_swap_b32 v41,v43
v_permlane16_swap_b32 v0,v2
m v[124:127],a[216:219],a[80:83],v[124:127]
v_permlane16_swap_b32 v1,v3
v_permlane16_swap_b32 v8,v10
m v[140:143],a[216:219],a[136:139],v[140:143]
v_permlane16_swap_b32 v9,v11
s_waitcnt lgkmcnt(0)
s_barrier
m v[96:99],a[220:223],a[28:31],v[96:99]
t a[204:205],v204,16384
t a[206:207],v204,16896
m v[112:115],a[220:223],a[84:87],v[112:115]
t a[208:209],v205,16384
t a[210:211],v205,16896
m v[128:131],a[220:223],a[140:143],v[128:131]
t a[212:213],v206,16384
t a[214:215],v206,16896
m v[100:103],a[224:227],a[28:31],v[100:103]
t a[216:217],v207,16384
t a[218:219],v207,16896
m v[116:119],a[224:227],a[84:87],v[116:119]
v_permlane16_swap_b32 v16,v18
v_permlane16_swap_b32 v17,v19
m v[132:135],a[224:227],a[140:143],v[132:135]
v_permlane16_swap_b32 v24,v26
v_permlane16_swap_b32 v25,v27
m v[104:107],a[228:231],a[28:31],v[104:107]
v_permlane16_swap_b32 v32,v34
v_permlane16_swap_b32 v33,v35
m v[120:123],a[228:231],a[84:87],v[120:123]
v_permlane16_swap_b32 v40,v42
v_permlane16_swap_b32 v41,v43
m v[136:139],a[228:231],a[140:143],v[136:139]
v_mul_f32 v48,s47,v48
v_mul_f32 v49,s47,v49
m v[108:111],a[232:235],a[28:31],v[108:111]
v_mul_f32 v50,s47,v50
v_mul_f32 v51,s47,v51
m v[124:127],a[232:235],a[84:87],v[124:127]
v_mul_f32 v52,s47,v52
v_mul_f32 v53,s47,v53
m v[140:143],a[232:235],a[140:143],v[140:143]
v_mul_f32 v54,s47,v54
s_waitcnt lgkmcnt(0)
m v[96:99],a[204:207],a[32:35],v[96:99]
t a[220:221],v204,20480
t a[222:223],v204,20992
m v[112:115],a[204:207],a[88:91],v[112:115]
t a[224:225],v205,20480
t a[226:227],v205,20992
m v[128:131],a[204:207],a[144:147],v[128:131]
t a[228:229],v206,20480
t a[230:231],v206,20992
m v[100:103],a[208:211],a[32:35],v[100:103]
t a[232:233],v207,20480
t a[234:235],v207,20992
m v[116:119],a[208:211],a[88:91],v[116:119]
v_mul_f32 v55,s47,v55
cv v48,v48,v49
m v[132:135],a[208:211],a[144:147],v[132:135]
cv v49,v50,v51
cv v50,v52,v53
m v[104:107],a[212:215],a[32:35],v[104:107]
cv v51,v54,v55
v_mul_f32 v56,s47,v56
m v[120:123],a[212:215],a[88:91],v[120:123]
v_mul_f32 v57,s47,v57
v_mul_f32 v58,s47,v58
m v[136:139],a[212:215],a[144:147],v[136:139]
v_mul_f32 v59,s47,v59
v_mul_f32 v60,s47,v60
m v[108:111],a[216:219],a[32:35],v[108:111]
v_mul_f32 v61,s47,v61
v_mul_f32 v62,s47,v62
m v[124:127],a[216:219],a[88:91],v[124:127]
v_mul_f32 v63,s47,v63
cv v56,v56,v57
m v[140:143],a[216:219],a[144:147],v[140:143]
cv v57,v58,v59
s_waitcnt lgkmcnt(0)
m v[96:99],a[220:223],a[36:39],v[96:99]
t a[204:205],v204,24576
t a[206:207],v204,25088
m v[112:115],a[220:223],a[92:95],v[112:115]
t a[208:209],v205,24576
t a[210:211],v205,25088
m v[128:131],a[220:223],a[148:151],v[128:131]
t a[212:213],v206,24576
t a[214:215],v206,25088
m v[100:103],a[224:227],a[36:39],v[100:103]
t a[216:217],v207,24576
t a[218:219],v207,25088
m v[116:119],a[224:227],a[92:95],v[116:119]
cv v58,v60,v61
cv v59,v62,v63
m v[132:135],a[224:227],a[148:151],v[132:135]
v_mul_f32 v64,s47,v64
v_mul_f32 v65,s47,v65
m v[104:107],a[228:231],a[36:39],v[104:107]
v_mul_f32 v66,s47,v66
v_mul_f32 v67,s47,v67
m v[120:123],a[228:231],a[92:95],v[120:123]
v_mul_f32 v68,s47,v68
v_mul_f32 v69,s47,v69
m v[136:139],a[228:231],a[148:151],v[136:139]
v_mul_f32 v70,s47,v70
v_mul_f32 v71,s47,v71
m v[108:111],a[232:235],a[36:39],v[108:111]
cv v64,v64,v65
cv v65,v66,v67
m v[124:127],a[232:235],a[92:95],v[124:127]
cv v66,v68,v69
cv v67,v70,v71
m v[140:143],a[232:235],a[148:151],v[140:143]
s_waitcnt lgkmcnt(0)
m v[96:99],a[204:207],a[40:43],v[96:99]
t a[220:221],v204,28672
t a[222:223],v204,29184
m v[112:115],a[204:207],a[96:99],v[112:115]
t a[224:225],v205,28672
t a[226:227],v205,29184
m v[128:131],a[204:207],a[152:155],v[128:131]
t a[228:229],v206,28672
t a[230:231],v206,29184
m v[100:103],a[208:211],a[40:43],v[100:103]
t a[232:233],v207,28672
t a[234:235],v207,29184
m v[116:119],a[208:211],a[96:99],v[116:119]
v_mul_f32 v72,s47,v72
v_mul_f32 v73,s47,v73
m v[132:135],a[208:211],a[152:155],v[132:135]
v_mul_f32 v74,s47,v74
v_mul_f32 v75,s47,v75
m v[104:107],a[212:215],a[40:43],v[104:107]
v_mul_f32 v76,s47,v76
v_mul_f32 v77,s47,v77
m v[120:123],a[212:215],a[96:99],v[120:123]
v_mul_f32 v78,s47,v78
v_mul_f32 v79,s47,v79
m v[136:139],a[212:215],a[152:155],v[136:139]
cv v72,v72,v73
cv v73,v74,v75
m v[108:111],a[216:219],a[40:43],v[108:111]
cv v74,v76,v77
cv v75,v78,v79
m v[124:127],a[216:219],a[96:99],v[124:127]
v_mul_f32 v80,s47,v80
v_mul_f32 v81,s47,v81
m v[140:143],a[216:219],a[152:155],v[140:143]
v_mul_f32 v82,s47,v82
s_waitcnt lgkmcnt(0)
s_barrier
m v[96:99],a[220:223],a[44:47],v[96:99]
t a[204:205],v204,49152
t a[206:207],v204,49664
m v[112:115],a[220:223],a[100:103],v[112:115]
t a[208:209],v205,49152
t a[210:211],v205,49664
m v[128:131],a[220:223],a[156:159],v[128:131]
t a[212:213],v206,49152
t a[214:215],v206,49664
m v[100:103],a[224:227],a[44:47],v[100:103]
t a[216:217],v207,49152
t a[218:219],v207,49664
m v[116:119],a[224:227],a[100:103],v[116:119]
v_mul_f32 v83,s47,v83
v_mul_f32 v84,s47,v84
m v[132:135],a[224:227],a[156:159],v[132:135]
v_mul_f32 v85,s47,v85
v_mul_f32 v86,s47,v86
m v[104:107],a[228:231],a[44:47],v[104:107]
v_mul_f32 v87,s47,v87
cv v80,v80,v81
m v[120:123],a[228:231],a[100:103],v[120:123]
cv v81,v82,v83
cv v82,v84,v85
m v[136:139],a[228:231],a[156:159],v[136:139]
cv v83,v86,v87
v_mul_f32 v88,s47,v88
m v[108:111],a[232:235],a[44:47],v[108:111]
v_mul_f32 v89,s47,v89
v_mul_f32 v90,s47,v90
m v[124:127],a[232:235],a[100:103],v[124:127]
v_mul_f32 v91,s47,v91
v_mul_f32 v92,s47,v92
m v[140:143],a[232:235],a[156:159],v[140:143]
v_mul_f32 v93,s47,v93
s_waitcnt lgkmcnt(0)
m v[96:99],a[204:207],a[48:51],v[96:99]
t a[220:221],v204,53248
t a[222:223],v204,53760
m v[112:115],a[204:207],a[104:107],v[112:115]
t a[224:225],v205,53248
t a[226:227],v205,53760
m v[128:131],a[204:207],a[160:163],v[128:131]
t a[228:229],v206,53248
t a[230:231],v206,53760
m v[100:103],a[208:211],a[48:51],v[100:103]
t a[232:233],v207,53248
t a[234:235],v207,53760
m v[116:119],a[208:211],a[104:107],v[116:119]
v_mul_f32 v94,s47,v94
v_mul_f32 v95,s47,v95
m v[132:135],a[208:211],a[160:163],v[132:135]
cv v88,v88,v89
cv v89,v90,v91
m v[104:107],a[212:215],a[48:51],v[104:107]
cv v90,v92,v93
cv v91,v94,v95
m v[120:123],a[212:215],a[104:107],v[120:123]
v_permlane32_swap_b32 v48,v50
v_permlane32_swap_b32 v49,v51
m v[136:139],a[212:215],a[160:163],v[136:139]
v_permlane32_swap_b32 v56,v58
v_permlane32_swap_b32 v57,v59
m v[108:111],a[216:219],a[48:51],v[108:111]
v_permlane32_swap_b32 v64,v66
v_permlane32_swap_b32 v65,v67
m v[124:127],a[216:219],a[104:107],v[124:127]
v_permlane32_swap_b32 v72,v74
v_permlane32_swap_b32 v73,v75
m v[140:143],a[216:219],a[160:163],v[140:143]
v_permlane32_swap_b32 v80,v82
s_waitcnt lgkmcnt(0)
m v[96:99],a[220:223],a[52:55],v[96:99]
t a[204:205],v204,57344
t a[206:207],v204,57856
m v[112:115],a[220:223],a[108:111],v[112:115]
t a[208:209],v205,57344
t a[210:211],v205,57856
m v[128:131],a[220:223],a[164:167],v[128:131]
t a[212:213],v206,57344
t a[214:215],v206,57856
m v[100:103],a[224:227],a[52:55],v[100:103]
t a[216:217],v207,57344
t a[218:219],v207,57856
m v[116:119],a[224:227],a[108:111],v[116:119]
v_permlane32_swap_b32 v81,v83
v_permlane32_swap_b32 v88,v90
m v[132:135],a[224:227],a[164:167],v[132:135]
v_permlane32_swap_b32 v89,v91
v_permlane16_swap_b32 v48,v50
m v[104:107],a[228:231],a[52:55],v[104:107]
v_permlane16_swap_b32 v49,v51
v_permlane16_swap_b32 v56,v58
m v[120:123],a[228:231],a[108:111],v[120:123]
v_permlane16_swap_b32 v57,v59
v_permlane16_swap_b32 v64,v66
m v[136:139],a[228:231],a[164:167],v[136:139]
v_permlane16_swap_b32 v65,v67
v_permlane16_swap_b32 v72,v74
m v[108:111],a[232:235],a[52:55],v[108:111]
v_permlane16_swap_b32 v73,v75
v_permlane16_swap_b32 v80,v82
m v[124:127],a[232:235],a[108:111],v[124:127]
v_permlane16_swap_b32 v81,v83
v_permlane16_swap_b32 v88,v90
m v[140:143],a[232:235],a[164:167],v[140:143]
v_permlane16_swap_b32 v89,v91
s_waitcnt lgkmcnt(0)
m v[144:147],a[204:207],a[0:3],0
t a[220:221],v204,61440
t a[222:223],v204,61952
m v[160:163],a[204:207],a[56:59],0
t a[224:225],v205,61440
t a[226:227],v205,61952
m v[176:179],a[204:207],a[112:115],0
t a[228:229],v206,61440
t a[230:231],v206,61952
m v[148:151],a[208:211],a[0:3],0
t a[232:233],v207,61440
t a[234:235],v207,61952
m v[164:167],a[208:211],a[56:59],0
m v[180:183],a[208:211],a[112:115],0
m v[152:155],a[212:215],a[0:3],0
m v[168:171],a[212:215],a[56:59],0
m v[184:187],a[212:215],a[112:115],0
m v[156:159],a[216:219],a[0:3],0
m v[172:175],a[216:219],a[56:59],0
m v[188:191],a[216:219],a[112:115],0
s_waitcnt lgkmcnt(0)
s_barrier
m v[144:147],a[220:223],a[4:7],v[144:147]
t a[204:205],v204,0
t a[206:207],v204,512
m v[160:163],a[220:223],a[60:63],v[160:163]
t a[208:209],v205,0
t a[210:211],v205,512
m v[176:179],a[220:223],a[116:119],v[176:179]
t a[212:213],v206,0
t a[214:215],v206,512
m v[148:151],a[224:227],a[4:7],v[148:151]
t a[216:217],v207,0
t a[218:219],v207,512
m v[164:167],a[224:227],a[60:63],v[164:167]
v_mul_f32 v96,s47,v96
v_mul_f32 v97,s47,v97
m v[180:183],a[224:227],a[116:119],v[180:183]
v_mul_f32 v98,s47,v98
v_mul_f32 v99,s47,v99
m v[152:155],a[228:231],a[4:7],v[152:155]
v_mul_f32 v100,s47,v100
v_mul_f32 v101,s47,v101
m v[168:171],a[228:231],a[60:63],v[168:171]
v_mul_f32 v102,s47,v102
m v[184:187],a[228:231],a[116:119],v[184:187]
m v[156:159],a[232:235],a[4:7],v[156:159]
m v[172:175],a[232:235],a[60:63],v[172:175]
m v[188:191],a[232:235],a[116:119],v[188:191]
s_waitcnt lgkmcnt(0)
m v[144:147],a[204:207],a[8:11],v[144:147]
t a[220:221],v204,4096
t a[222:223],v204,4608
m v[160:163],a[204:207],a[64:67],v[160:163]
t a[224:225],v205,4096
t a[226:227],v205,4608
m v[176:179],a[204:207],a[120:123],v[176:179]
t a[228:229],v206,4096
t a[230:231],v206,4608
m v[148:151],a[208:211],a[8:11],v[148:151]
t a[232:233],v207,4096
t a[234:235],v207,4608
m v[164:167],a[208:211],a[64:67],v[164:167]
v_mul_f32 v103,s47,v103
cv v96,v96,v97
m v[180:183],a[208:211],a[120:123],v[180:183]
cv v97,v98,v99
cv v98,v100,v101
m v[152:155],a[212:215],a[8:11],v[152:155]
cv v99,v102,v103
v_mul_f32 v104,s47,v104
m v[168:171],a[212:215],a[64:67],v[168:171]
v_mul_f32 v105,s47,v105
m v[184:187],a[212:215],a[120:123],v[184:187]
m v[156:159],a[216:219],a[8:11],v[156:159]
m v[172:175],a[216:219],a[64:67],v[172:175]
m v[188:191],a[216:219],a[120:123],v[188:191]
s_waitcnt lgkmcnt(0)
m v[144:147],a[220:223],a[12:15],v[144:147]
t a[204:205],v204,8192
t a[206:207],v204,8704
m v[160:163],a[220:223],a[68:71],v[160:163]
t a[208:209],v205,8192
t a[210:211],v205,8704
m v[176:179],a[220:223],a[124:127],v[176:179]
t a[212:213],v206,8192
t a[214:215],v206,8704
m v[148:151],a[224:227],a[12:15],v[148:151]
t a[216:217],v207,8192
t a[218:219],v207,8704
m v[164:167],a[224:227],a[68:71],v[164:167]
v_mul_f32 v106,s47,v106
v_mul_f32 v107,s47,v107
m v[180:183],a[224:227],a[124:127],v[180:183]
v_mul_f32 v108,s47,v108
v_mul_f32 v109,s47,v109
m v[152:155],a[228:231],a[12:15],v[152:155]
v_mul_f32 v110,s47,v110
v_mul_f32 v111,s47,v111
m v[168:171],a[228:231],a[68:71],v[168:171]
cv v104,v104,v105
cv v105,v106,v107
m v[184:187],a[228:231],a[124:127],v[184:187]
m v[156:159],a[232:235],a[12:15],v[156:159]
m v[172:175],a[232:235],a[68:71],v[172:175]
m v[188:191],a[232:235],a[124:127],v[188:191]
s_waitcnt lgkmcnt(0)
m v[144:147],a[204:207],a[16:19],v[144:147]
t a[220:221],v204,12288
t a[222:223],v204,12800
m v[160:163],a[204:207],a[72:75],v[160:163]
t a[224:225],v205,12288
t a[226:227],v205,12800
m v[176:179],a[204:207],a[128:131],v[176:179]
t a[228:229],v206,12288
t a[230:231],v206,12800
m v[148:151],a[208:211],a[16:19],v[148:151]
t a[232:233],v207,12288
t a[234:235],v207,12800
m v[164:167],a[208:211],a[72:75],v[164:167]
cv v106,v108,v109
cv v107,v110,v111
m v[180:183],a[208:211],a[128:131],v[180:183]
v_mul_f32 v112,s47,v112
v_mul_f32 v113,s47,v113
m v[152:155],a[212:215],a[16:19],v[152:155]
v_mul_f32 v114,s47,v114
v_mul_f32 v115,s47,v115
m v[168:171],a[212:215],a[72:75],v[168:171]
v_mul_f32 v116,s47,v116
m v[184:187],a[212:215],a[128:131],v[184:187]
m v[156:159],a[216:219],a[16:19],v[156:159]
m v[172:175],a[216:219],a[72:75],v[172:175]
m v[188:191],a[216:219],a[128:131],v[188:191]
s_waitcnt lgkmcnt(0)
s_barrier
m v[144:147],a[220:223],a[20:23],v[144:147]
t a[204:205],v204,16384
t a[206:207],v204,16896
m v[160:163],a[220:223],a[76:79],v[160:163]
t a[208:209],v205,16384
t a[210:211],v205,16896
m v[176:179],a[220:223],a[132:135],v[176:179]
t a[212:213],v206,16384
t a[214:215],v206,16896
m v[148:151],a[224:227],a[20:23],v[148:151]
t a[216:217],v207,16384
t a[218:219],v207,16896
m v[164:167],a[224:227],a[76:79],v[164:167]
v_mul_f32 v117,s47,v117
v_mul_f32 v118,s47,v118
m v[180:183],a[224:227],a[132:135],v[180:183]
v_mul_f32 v119,s47,v119
cv v112,v112,v113
m v[152:155],a[228:231],a[20:23],v[152:155]
cv v113,v114,v115
cv v114,v116,v117
m v[168:171],a[228:231],a[76:79],v[168:171]
cv v115,v118,v119
m v[184:187],a[228:231],a[132:135],v[184:187]
m v[156:159],a[232:235],a[20:23],v[156:159]
m v[172:175],a[232:235],a[76:79],v[172:175]
m v[188:191],a[232:235],a[132:135],v[188:191]
s_waitcnt lgkmcnt(0)
m v[144:147],a[204:207],a[24:27],v[144:147]
t a[220:221],v204,20480
t a[222:223],v204,20992
m v[160:163],a[204:207],a[80:83],v[160:163]
t a[224:225],v205,20480
t a[226:227],v205,20992
m v[176:179],a[204:207],a[136:139],v[176:179]
t a[228:229],v206,20480
t a[230:231],v206,20992
m v[148:151],a[208:211],a[24:27],v[148:151]
t a[232:233],v207,20480
t a[234:235],v207,20992
m v[164:167],a[208:211],a[80:83],v[164:167]
v_mul_f32 v120,s47,v120
v_mul_f32 v121,s47,v121
m v[180:183],a[208:211],a[136:139],v[180:183]
v_mul_f32 v122,s47,v122
v_mul_f32 v123,s47,v123
m v[152:155],a[212:215],a[24:27],v[152:155]
v_mul_f32 v124,s47,v124
v_mul_f32 v125,s47,v125
m v[168:171],a[212:215],a[80:83],v[168:171]
v_mul_f32 v126,s47,v126
v_mul_f32 v127,s47,v127
m v[184:187],a[212:215],a[136:139],v[184:187]
m v[156:159],a[216:219],a[24:27],v[156:159]
m v[172:175],a[216:219],a[80:83],v[172:175]
m v[188:191],a[216:219],a[136:139],v[188:191]
s_waitcnt lgkmcnt(0)
m v[144:147],a[220:223],a[28:31],v[144:147]
t a[204:205],v204,24576
t a[206:207],v204,25088
m v[160:163],a[220:223],a[84:87],v[160:163]
t a[208:209],v205,24576
t a[210:211],v205,25088
m v[176:179],a[220:223],a[140:143],v[176:179]
t a[212:213],v206,24576
t a[214:215],v206,25088
m v[148:151],a[224:227],a[28:31],v[148:151]
t a[216:217],v207,24576
t a[218:219],v207,25088
m v[164:167],a[224:227],a[84:87],v[164:167]
cv v120,v120,v121
cv v121,v122,v123
m v[180:183],a[224:227],a[140:143],v[180:183]
cv v122,v124,v125
cv v123,v126,v127
m v[152:155],a[228:231],a[28:31],v[152:155]
v_mul_f32 v128,s47,v128
v_mul_f32 v129,s47,v129
m v[168:171],a[228:231],a[84:87],v[168:171]
v_mul_f32 v130,s47,v130
m v[184:187],a[228:231],a[140:143],v[184:187]
m v[156:159],a[232:235],a[28:31],v[156:159]
m v[172:175],a[232:235],a[84:87],v[172:175]
m v[188:191],a[232:235],a[140:143],v[188:191]
s_waitcnt lgkmcnt(0)
m v[144:147],a[204:207],a[32:35],v[144:147]
t a[220:221],v204,28672
t a[222:223],v204,29184
m v[160:163],a[204:207],a[88:91],v[160:163]
t a[224:225],v205,28672
t a[226:227],v205,29184
m v[176:179],a[204:207],a[144:147],v[176:179]
t a[228:229],v206,28672
t a[230:231],v206,29184
m v[148:151],a[208:211],a[32:35],v[148:151]
t a[232:233],v207,28672
t a[234:235],v207,29184
m v[164:167],a[208:211],a[88:91],v[164:167]
v_mul_f32 v131,s47,v131
v_mul_f32 v132,s47,v132
m v[180:183],a[208:211],a[144:147],v[180:183]
v_mul_f32 v133,s47,v133
v_mul_f32 v134,s47,v134
m v[152:155],a[212:215],a[32:35],v[152:155]
v_mul_f32 v135,s47,v135
cv v128,v128,v129
m v[168:171],a[212:215],a[88:91],v[168:171]
cv v129,v130,v131
cv v130,v132,v133
m v[184:187],a[212:215],a[144:147],v[184:187]
m v[156:159],a[216:219],a[32:35],v[156:159]
m v[172:175],a[216:219],a[88:91],v[172:175]
m v[188:191],a[216:219],a[144:147],v[188:191]
s_waitcnt lgkmcnt(0)
s_barrier
m v[144:147],a[220:223],a[36:39],v[144:147]
t a[204:205],v204,49152
t a[206:207],v204,49664
m v[160:163],a[220:223],a[92:95],v[160:163]
t a[208:209],v205,49152
t a[210:211],v205,49664
m v[176:179],a[220:223],a[148:151],v[176:179]
t a[212:213],v206,49152
t a[214:215],v206,49664
m v[148:151],a[224:227],a[36:39],v[148:151]
t a[216:217],v207,49152
t a[218:219],v207,49664
m v[164:167],a[224:227],a[92:95],v[164:167]
cv v131,v134,v135
v_mul_f32 v136,s47,v136
m v[180:183],a[224:227],a[148:151],v[180:183]
v_mul_f32 v137,s47,v137
v_mul_f32 v138,s47,v138
m v[152:155],a[228:231],a[36:39],v[152:155]
v_mul_f32 v139,s47,v139
v_mul_f32 v140,s47,v140
m v[168:171],a[228:231],a[92:95],v[168:171]
v_mul_f32 v141,s47,v141
m v[184:187],a[228:231],a[148:151],v[184:187]
m v[156:159],a[232:235],a[36:39],v[156:159]
m v[172:175],a[232:235],a[92:95],v[172:175]
m v[188:191],a[232:235],a[148:151],v[188:191]
s_waitcnt lgkmcnt(0)
m v[144:147],a[204:207],a[40:43],v[144:147]
t a[220:221],v204,53248
t a[222:223],v204,53760
m v[160:163],a[204:207],a[96:99],v[160:163]
t a[224:225],v205,53248
t a[226:227],v205,53760
m v[176:179],a[204:207],a[152:155],v[176:179]
t a[228:229],v206,53248
t a[230:231],v206,53760
m v[148:151],a[208:211],a[40:43],v[148:151]
t a[232:233],v207,53248
t a[234:235],v207,53760
m v[164:167],a[208:211],a[96:99],v[164:167]
v_mul_f32 v142,s47,v142
v_mul_f32 v143,s47,v143
m v[180:183],a[208:211],a[152:155],v[180:183]
cv v136,v136,v137
cv v137,v138,v139
m v[152:155],a[212:215],a[40:43],v[152:155]
cv v138,v140,v141
cv v139,v142,v143
m v[168:171],a[212:215],a[96:99],v[168:171]
v_permlane32_swap_b32 v96,v98
m v[184:187],a[212:215],a[152:155],v[184:187]
m v[156:159],a[216:219],a[40:43],v[156:159]
m v[172:175],a[216:219],a[96:99],v[172:175]
m v[188:191],a[216:219],a[152:155],v[188:191]
s_waitcnt lgkmcnt(0)
m v[144:147],a[220:223],a[44:47],v[144:147]
t a[204:205],v204,57344
t a[206:207],v204,57856
m v[160:163],a[220:223],a[100:103],v[160:163]
t a[208:209],v205,57344
t a[210:211],v205,57856
m v[176:179],a[220:223],a[156:159],v[176:179]
t a[212:213],v206,57344
t a[214:215],v206,57856
m v[148:151],a[224:227],a[44:47],v[148:151]
t a[216:217],v207,57344
t a[218:219],v207,57856
m v[164:167],a[224:227],a[100:103],v[164:167]
v_permlane32_swap_b32 v97,v99
v_permlane32_swap_b32 v104,v106
m v[180:183],a[224:227],a[156:159],v[180:183]
v_permlane32_swap_b32 v105,v107
v_permlane32_swap_b32 v112,v114
m v[152:155],a[228:231],a[44:47],v[152:155]
v_permlane32_swap_b32 v113,v115
v_permlane32_swap_b32 v120,v122
m v[168:171],a[228:231],a[100:103],v[168:171]
v_permlane32_swap_b32 v121,v123
v_permlane32_swap_b32 v128,v130
m v[184:187],a[228:231],a[156:159],v[184:187]
m v[156:159],a[232:235],a[44:47],v[156:159]
m v[172:175],a[232:235],a[100:103],v[172:175]
m v[188:191],a[232:235],a[156:159],v[188:191]
s_waitcnt lgkmcnt(0)
m v[144:147],a[204:207],a[48:51],v[144:147]
t a[220:221],v204,61440
t a[222:223],v204,61952
m v[160:163],a[204:207],a[104:107],v[160:163]
t a[224:225],v205,61440
t a[226:227],v205,61952
m v[176:179],a[204:207],a[160:163],v[176:179]
t a[228:229],v206,61440
t a[230:231],v206,61952
m v[148:151],a[208:211],a[48:51],v[148:151]
t a[232:233],v207,61440
t a[234:235],v207,61952
m v[164:167],a[208:211],a[104:107],v[164:167]
v_permlane32_swap_b32 v129,v131
v_permlane32_swap_b32 v136,v138
m v[180:183],a[208:211],a[160:163],v[180:183]
v_permlane32_swap_b32 v137,v139
v_permlane16_swap_b32 v96,v98
m v[152:155],a[212:215],a[48:51],v[152:155]
v_permlane16_swap_b32 v97,v99
v_permlane16_swap_b32 v104,v106
m v[168:171],a[212:215],a[104:107],v[168:171]
v_permlane16_swap_b32 v105,v107
m v[184:187],a[212:215],a[160:163],v[184:187]
m v[156:159],a[216:219],a[48:51],v[156:159]
m v[172:175],a[216:219],a[104:107],v[172:175]
m v[188:191],a[216:219],a[160:163],v[188:191]
s_waitcnt lgkmcnt(0)
s_barrier
m v[144:147],a[220:223],a[52:55],v[144:147]
r a[204:207],v192,0
r a[208:211],v192,8192
m v[160:163],a[220:223],a[108:111],v[160:163]
r a[212:215],v193,0
r a[216:219],v193,8192
m v[176:179],a[220:223],a[164:167],v[176:179]
v_permlane16_swap_b32 v112,v114
v_permlane16_swap_b32 v113,v115
m v[148:151],a[224:227],a[52:55],v[148:151]
v_permlane16_swap_b32 v120,v122
v_permlane16_swap_b32 v121,v123
m v[164:167],a[224:227],a[108:111],v[164:167]
v_permlane16_swap_b32 v128,v130
v_permlane16_swap_b32 v129,v131
m v[180:183],a[224:227],a[164:167],v[180:183]
v_permlane16_swap_b32 v136,v138
v_permlane16_swap_b32 v137,v139
m v[152:155],a[228:231],a[52:55],v[152:155]
m v[168:171],a[228:231],a[108:111],v[168:171]
m v[184:187],a[228:231],a[164:167],v[184:187]
m v[156:159],a[232:235],a[52:55],v[156:159]
m v[172:175],a[232:235],a[108:111],v[172:175]
m v[188:191],a[232:235],a[164:167],v[188:191]
s_waitcnt lgkmcnt(0)
m v[4:7],a[204:207],v[0:3],0
r a[220:223],v194,0
r a[224:227],v194,8192
r a[228:231],v195,0
m v[20:23],a[204:207],v[16:19],0
r a[232:235],v195,8192
v_mul_f32 v144,s47,v144
v_mul_f32 v145,s47,v145
m v[36:39],a[204:207],v[32:35],0
v_mul_f32 v146,s47,v146
v_mul_f32 v147,s47,v147
v_mul_f32 v148,s47,v148
m v[12:15],a[208:211],v[0:3],0
v_mul_f32 v149,s47,v149
v_mul_f32 v150,s47,v150
v_mul_f32 v151,s47,v151
m v[28:31],a[208:211],v[16:19],0
cv v144,v144,v145
cv v145,v146,v147
cv v146,v148,v149
m v[44:47],a[208:211],v[32:35],0
cv v147,v150,v151
v_mul_f32 v152,s47,v152
v_mul_f32 v153,s47,v153
m v[4:7],a[212:215],v[8:11],v[4:7]
v_mul_f32 v154,s47,v154
v_mul_f32 v155,s47,v155
v_mul_f32 v156,s47,v156
m v[20:23],a[212:215],v[24:27],v[20:23]
v_mul_f32 v157,s47,v157
v_mul_f32 v158,s47,v158
v_mul_f32 v159,s47,v159
m v[36:39],a[212:215],v[40:43],v[36:39]
cv v152,v152,v153
cv v153,v154,v155
cv v154,v156,v157
m v[12:15],a[216:219],v[8:11],v[12:15]
cv v155,v158,v159
v_mul_f32 v160,s47,v160
v_mul_f32 v161,s47,v161
m v[28:31],a[216:219],v[24:27],v[28:31]
v_mul_f32 v162,s47,v162
v_mul_f32 v163,s47,v163
v_mul_f32 v164,s47,v164
m v[44:47],a[216:219],v[40:43],v[44:47]
v_mul_f32 v165,s47,v165
v_mul_f32 v166,s47,v166
v_mul_f32 v167,s47,v167
s_waitcnt lgkmcnt(0)
m v[4:7],a[220:223],v[48:51],v[4:7]
r a[204:207],v192,256
r a[208:211],v192,8448
r a[212:215],v193,256
m v[20:23],a[220:223],v[64:67],v[20:23]
r a[216:219],v193,8448
cv v160,v160,v161
cv v161,v162,v163
m v[36:39],a[220:223],v[80:83],v[36:39]
cv v162,v164,v165
cv v163,v166,v167
v_mul_f32 v168,s47,v168
m v[12:15],a[224:227],v[48:51],v[12:15]
v_mul_f32 v169,s47,v169
v_mul_f32 v170,s47,v170
v_mul_f32 v171,s47,v171
m v[28:31],a[224:227],v[64:67],v[28:31]
v_mul_f32 v172,s47,v172
v_mul_f32 v173,s47,v173
v_mul_f32 v174,s47,v174
m v[44:47],a[224:227],v[80:83],v[44:47]
v_mul_f32 v175,s47,v175
cv v168,v168,v169
cv v169,v170,v171
m v[4:7],a[228:231],v[56:59],v[4:7]
cv v170,v172,v173
cv v171,v174,v175
v_mul_f32 v176,s47,v176
m v[20:23],a[228:231],v[72:75],v[20:23]
v_mul_f32 v177,s47,v177
v_mul_f32 v178,s47,v178
v_mul_f32 v179,s47,v179
m v[36:39],a[228:231],v[88:91],v[36:39]
v_mul_f32 v180,s47,v180
v_mul_f32 v181,s47,v181
v_mul_f32 v182,s47,v182
m v[12:15],a[232:235],v[56:59],v[12:15]
v_mul_f32 v183,s47,v183
cv v176,v176,v177
cv v177,v178,v179
m v[28:31],a[232:235],v[72:75],v[28:31]
cv v178,v180,v181
cv v179,v182,v183
v_mul_f32 v184,s47,v184
m v[44:47],a[232:235],v[88:91],v[44:47]
v_mul_f32 v185,s47,v185
v_mul_f32 v186,s47,v186
v_mul_f32 v187,s47,v187
s_waitcnt lgkmcnt(0)
m v[4:7],a[204:207],v[96:99],v[4:7]
r a[220:223],v194,256
r a[224:227],v194,8448
r a[228:231],v195,256
m v[20:23],a[204:207],v[112:115],v[20:23]
r a[232:235],v195,8448
v_mul_f32 v188,s47,v188
v_mul_f32 v189,s47,v189
v_mul_f32 v190,s47,v190
m v[36:39],a[204:207],v[128:131],v[36:39]
v_mul_f32 v191,s47,v191
cv v184,v184,v185
cv v185,v186,v187
cv v186,v188,v189
m v[12:15],a[208:211],v[96:99],v[12:15]
cv v187,v190,v191
v_permlane32_swap_b32 v144,v146
v_permlane32_swap_b32 v145,v147
v_permlane32_swap_b32 v152,v154
m v[28:31],a[208:211],v[112:115],v[28:31]
v_permlane32_swap_b32 v153,v155
v_permlane32_swap_b32 v160,v162
v_permlane32_swap_b32 v161,v163
m v[44:47],a[208:211],v[128:131],v[44:47]
v_permlane32_swap_b32 v168,v170
v_permlane32_swap_b32 v169,v171
v_permlane32_swap_b32 v176,v178
v_permlane32_swap_b32 v177,v179
m v[4:7],a[212:215],v[104:107],v[4:7]
v_permlane32_swap_b32 v184,v186
v_permlane32_swap_b32 v185,v187
v_permlane16_swap_b32 v144,v146
v_permlane16_swap_b32 v145,v147
m v[20:23],a[212:215],v[120:123],v[20:23]
v_permlane16_swap_b32 v152,v154
v_permlane16_swap_b32 v153,v155
v_permlane16_swap_b32 v160,v162
v_permlane16_swap_b32 v161,v163
m v[36:39],a[212:215],v[136:139],v[36:39]
v_permlane16_swap_b32 v168,v170
v_permlane16_swap_b32 v169,v171
v_permlane16_swap_b32 v176,v178
m v[12:15],a[216:219],v[104:107],v[12:15]
v_permlane16_swap_b32 v177,v179
v_permlane16_swap_b32 v184,v186
v_permlane16_swap_b32 v185,v187
s_mov_b32 s41,s40
m v[28:31],a[216:219],v[120:123],v[28:31]
s_add_u32 s40,s40,0x8000
s_cmp_eq_u32 s40,0x28000
s_cselect_b32 s42,s49,s48
s_cselect_b32 s40,0,s40
m v[44:47],a[216:219],v[136:139],v[44:47]
v_add_u32 v192,s42,v192
v_add_u32 v193,s42,v193
v_add_u32 v194,s42,v194
v_add_u32 v195,s42,v195
s_waitcnt lgkmcnt(0)
s_barrier
m v[4:7],a[220:223],v[144:147],v[4:7]
r a[204:207],v192,0
r a[208:211],v192,8192
m v[20:23],a[220:223],v[160:163],v[20:23]
r a[212:215],v193,0
r a[216:219],v193,8192
m v[36:39],a[220:223],v[176:179],v[36:39]
v_mov_b32 v148,0x3f803f80
v_mov_b32 v149,0x3f803f80
m v[12:15],a[224:227],v[144:147],v[12:15]
v_mov_b32 v150,0x3f803f80
v_mov_b32 v151,0x3f803f80
m v[28:31],a[224:227],v[160:163],v[28:31]
m v[44:47],a[224:227],v[176:179],v[44:47]
m v[4:7],a[228:231],v[152:155],v[4:7]
m v[20:23],a[228:231],v[168:171],v[20:23]
m v[36:39],a[228:231],v[184:187],v[36:39]
m v[12:15],a[232:235],v[152:155],v[12:15]
m v[28:31],a[232:235],v[168:171],v[28:31]
m v[44:47],a[232:235],v[184:187],v[44:47]
s_nop 2
v_max3_f32 v220,v4,v5,v6
v_max3_f32 v221,v20,v21,v22
v_max3_f32 v222,v36,v37,v38
v_max3_f32 v220,v220,v7,v12
v_max3_f32 v221,v221,v23,v28
v_max3_f32 v222,v222,v39,v44
v_max3_f32 v220,v220,v13,v14
v_max3_f32 v221,v221,v29,v30
v_max3_f32 v222,v222,v45,v46
v_max_f32 v220,v220,v15
v_max_f32 v221,v221,v31
v_max_f32 v222,v222,v47
v_mov_b32 v224,v220
v_mov_b32 v225,v221
v_mov_b32 v226,v222
v_permlane32_swap_b32 v220,v224
v_permlane32_swap_b32 v221,v225
v_permlane32_swap_b32 v222,v226
v_max_f32 v220,v220,v224
v_max_f32 v221,v221,v225
v_max_f32 v222,v222,v226
v_mov_b32 v224,v220
v_mov_b32 v225,v221
v_mov_b32 v226,v222
v_permlane16_swap_b32 v220,v224
v_permlane16_swap_b32 v221,v225
v_permlane16_swap_b32 v222,v226
v_max_f32 v220,v220,v224
v_max_f32 v221,v221,v225
v_max_f32 v222,v222,v226
v_add_f32 v220,0x40e00000,v220
v_add_f32 v221,0x40e00000,v221
v_add_f32 v222,0x40e00000,v222
)ASM"
R"ASM(v_sub_f32 v124,0,v220
v_sub_f32 v125,0,v220
v_sub_f32 v126,0,v220
v_sub_f32 v127,0,v220
v_sub_f32 v132,0,v221
v_sub_f32 v133,0,v221
v_sub_f32 v134,0,v221
v_sub_f32 v135,0,v221
v_sub_f32 v140,0,v222
v_sub_f32 v141,0,v222
v_sub_f32 v142,0,v222
v_sub_f32 v143,0,v222
v_sub_f32 v4,v4,v220
v_sub_f32 v5,v5,v220
v_sub_f32 v6,v6,v220
v_sub_f32 v7,v7,v220
v_sub_f32 v12,v12,v220
v_sub_f32 v13,v13,v220
v_sub_f32 v14,v14,v220
v_sub_f32 v15,v15,v220
v_sub_f32 v20,v20,v221
v_sub_f32 v21,v21,v221
v_sub_f32 v22,v22,v221
v_sub_f32 v23,v23,v221
v_sub_f32 v28,v28,v221
v_sub_f32 v29,v29,v221
v_sub_f32 v30,v30,v221
v_sub_f32 v31,v31,v221
v_sub_f32 v36,v36,v222
v_sub_f32 v37,v37,v222
v_sub_f32 v38,v38,v222
v_sub_f32 v39,v39,v222
v_sub_f32 v44,v44,v222
v_sub_f32 v45,v45,v222
v_sub_f32 v46,v46,v222
v_sub_f32 v47,v47,v222
s_waitcnt lgkmcnt(0)
m v[52:55],a[204:207],v[0:3],v[124:127]
r a[220:223],v194,0
r a[224:227],v194,8192
m v[68:71],a[204:207],v[16:19],v[132:135]
r a[228:231],v195,0
r a[232:235],v195,8192
v_add_u32 v196,s42,v196
m v[84:87],a[204:207],v[32:35],v[140:143]
v_add_u32 v197,s42,v197
v_add_u32 v198,s42,v198
v_add_u32 v199,s42,v199
m v[60:63],a[208:211],v[0:3],v[124:127]
v_add_u32 v200,s42,v200
v_add_u32 v201,s42,v201
v_add_u32 v202,s42,v202
m v[76:79],a[208:211],v[16:19],v[132:135]
v_add_u32 v203,s42,v203
v_exp_f32 v220,v4
m v[92:95],a[208:211],v[32:35],v[140:143]
v_exp_f32 v221,v5
m v[52:55],a[212:215],v[8:11],v[52:55]
v_exp_f32 v222,v6
m v[68:71],a[212:215],v[24:27],v[68:71]
v_exp_f32 v223,v7
cv v100,v220,v221
m v[84:87],a[212:215],v[40:43],v[84:87]
v_exp_f32 v224,v12
m v[60:63],a[216:219],v[8:11],v[60:63]
v_exp_f32 v225,v13
cv v101,v222,v223
m v[76:79],a[216:219],v[24:27],v[76:79]
v_exp_f32 v220,v14
m v[92:95],a[216:219],v[40:43],v[92:95]
v_exp_f32 v221,v15
s_waitcnt lgkmcnt(0)
m v[52:55],a[220:223],v[48:51],v[52:55]
r a[204:207],v192,256
r a[208:211],v192,8448
m v[68:71],a[220:223],v[64:67],v[68:71]
r a[212:215],v193,256
r a[216:219],v193,8448
m v[84:87],a[220:223],v[80:83],v[84:87]
cv v102,v224,v225
m v[60:63],a[224:227],v[48:51],v[60:63]
v_exp_f32 v222,v20
m v[76:79],a[224:227],v[64:67],v[76:79]
v_exp_f32 v223,v21
m v[92:95],a[224:227],v[80:83],v[92:95]
cv v103,v220,v221
m v[52:55],a[228:231],v[56:59],v[52:55]
v_exp_f32 v224,v22
m v[68:71],a[228:231],v[72:75],v[68:71]
v_exp_f32 v225,v23
m v[84:87],a[228:231],v[88:91],v[84:87]
cv v108,v222,v223
m v[60:63],a[232:235],v[56:59],v[60:63]
v_exp_f32 v220,v28
m v[76:79],a[232:235],v[72:75],v[76:79]
v_exp_f32 v221,v29
m v[92:95],a[232:235],v[88:91],v[92:95]
cv v109,v224,v225
v_exp_f32 v222,v30
s_waitcnt lgkmcnt(0)
m v[52:55],a[204:207],v[96:99],v[52:55]
r a[220:223],v194,256
r a[224:227],v194,8448
m v[68:71],a[204:207],v[112:115],v[68:71]
r a[228:231],v195,256
r a[232:235],v195,8448
m v[84:87],a[204:207],v[128:131],v[84:87]
v_exp_f32 v223,v31
m v[60:63],a[208:211],v[96:99],v[60:63]
cv v110,v220,v221
m v[76:79],a[208:211],v[112:115],v[76:79]
v_exp_f32 v224,v36
m v[92:95],a[208:211],v[128:131],v[92:95]
v_exp_f32 v225,v37
m v[52:55],a[212:215],v[104:107],v[52:55]
cv v111,v222,v223
m v[68:71],a[212:215],v[120:123],v[68:71]
v_exp_f32 v220,v38
m v[84:87],a[212:215],v[136:139],v[84:87]
v_exp_f32 v221,v39
m v[60:63],a[216:219],v[104:107],v[60:63]
cv v116,v224,v225
m v[76:79],a[216:219],v[120:123],v[76:79]
v_exp_f32 v222,v44
m v[92:95],a[216:219],v[136:139],v[92:95]
v_exp_f32 v223,v45
cv v117,v220,v221
s_waitcnt lgkmcnt(0)
m v[52:55],a[220:223],v[144:147],v[52:55]
t a[204:205],v196,0
t a[206:207],v196,8192
m v[68:71],a[220:223],v[160:163],v[68:71]
t a[208:209],v197,0
t a[210:211],v197,8192
m v[84:87],a[220:223],v[176:179],v[84:87]
t a[212:213],v198,0
t a[214:215],v198,8192
m v[60:63],a[224:227],v[144:147],v[60:63]
t a[216:217],v199,0
t a[218:219],v199,8192
m v[76:79],a[224:227],v[160:163],v[76:79]
v_exp_f32 v224,v46
m v[92:95],a[224:227],v[176:179],v[92:95]
v_exp_f32 v225,v47
m v[52:55],a[228:231],v[152:155],v[52:55]
cv v118,v222,v223
cv v119,v224,v225
m v[68:71],a[228:231],v[168:171],v[68:71]
v_or3_b32 v228,v100,v101,v102
v_or3_b32 v228,v228,v103,v108
m v[84:87],a[228:231],v[184:187],v[84:87]
v_or3_b32 v228,v228,v109,v110
v_or3_b32 v228,v228,v111,v116
m v[60:63],a[232:235],v[152:155],v[60:63]
v_or3_b32 v228,v228,v117,v118
v_or_b32 v228,v228,v119
m v[76:79],a[232:235],v[168:171],v[76:79]
v_and_b32 v228,0x40004000,v228
m v[92:95],a[232:235],v[184:187],v[92:95]
v_cmp_ne_u32_e32 vcc,0,v228
s_nop 0
s_cbranch_vccnz LA_stub1%=
LA_ret1%=:
s_waitcnt lgkmcnt(0)
m a[192:195],v[148:151],v[100:103],0
t a[220:221],v200,0
t a[222:223],v200,8192
m a[196:199],v[148:151],v[108:111],0
t a[224:225],v201,0
t a[226:227],v201,8192
m a[200:203],v[148:151],v[116:119],0
t a[228:229],v202,0
t a[230:231],v202,8192
m a[0:3],a[204:207],v[100:103],0
t a[232:233],v203,0
t a[234:235],v203,8192
m a[64:67],a[204:207],v[108:111],0
v_exp_f32 v156,v52
m a[128:131],a[204:207],v[116:119],0
v_exp_f32 v157,v53
m a[4:7],a[208:211],v[100:103],0
v_exp_f32 v158,v54
m a[68:71],a[208:211],v[108:111],0
v_exp_f32 v159,v55
m a[132:135],a[208:211],v[116:119],0
m a[8:11],a[212:215],v[100:103],0
m a[72:75],a[212:215],v[108:111],0
m a[136:139],a[212:215],v[116:119],0
m a[12:15],a[216:219],v[100:103],0
m a[76:79],a[216:219],v[108:111],0
m a[140:143],a[216:219],v[116:119],0
s_waitcnt lgkmcnt(0)
m a[16:19],a[220:223],v[100:103],0
t a[204:205],v196,256
t a[206:207],v196,8448
m a[80:83],a[220:223],v[108:111],0
t a[208:209],v197,256
t a[210:211],v197,8448
m a[144:147],a[220:223],v[116:119],0
t a[212:213],v198,256
t a[214:215],v198,8448
m a[20:23],a[224:227],v[100:103],0
t a[216:217],v199,256
t a[218:219],v199,8448
m a[84:87],a[224:227],v[108:111],0
s_mov_b32 s41,s40
s_add_u32 s40,s40,0x8000
m a[148:151],a[224:227],v[116:119],0
s_cmp_eq_u32 s40,0x28000
s_cselect_b32 s42,s49,s48
m a[24:27],a[228:231],v[100:103],0
s_cselect_b32 s40,0,s40
v_add_u32 v192,s42,v192
m a[88:91],a[228:231],v[108:111],0
v_add_u32 v193,s42,v193
v_add_u32 v194,s42,v194
m a[152:155],a[228:231],v[116:119],0
v_add_u32 v195,s42,v195
m a[28:31],a[232:235],v[100:103],0
v_exp_f32 v164,v60
m a[92:95],a[232:235],v[108:111],0
v_exp_f32 v165,v61
m a[156:159],a[232:235],v[116:119],0
s_waitcnt lgkmcnt(0)
m a[32:35],a[204:207],v[100:103],0
t a[220:221],v200,256
t a[222:223],v200,8448
m a[96:99],a[204:207],v[108:111],0
t a[224:225],v201,256
t a[226:227],v201,8448
m a[160:163],a[204:207],v[116:119],0
t a[228:229],v202,256
t a[230:231],v202,8448
m a[36:39],a[208:211],v[100:103],0
t a[232:233],v203,256
t a[234:235],v203,8448
m a[100:103],a[208:211],v[108:111],0
v_exp_f32 v166,v62
m a[164:167],a[208:211],v[116:119],0
v_exp_f32 v167,v63
m a[40:43],a[212:215],v[100:103],0
v_exp_f32 v172,v68
m a[104:107],a[212:215],v[108:111],0
v_exp_f32 v173,v69
m a[168:171],a[212:215],v[116:119],0
v_exp_f32 v174,v70
m a[44:47],a[216:219],v[100:103],0
m a[108:111],a[216:219],v[108:111],0
m a[172:175],a[216:219],v[116:119],0
s_waitcnt lgkmcnt(0)
s_waitcnt vmcnt(0)
s_barrier
m a[48:51],a[220:223],v[100:103],0
r a[204:207],v192,0
r a[208:211],v192,8192
m a[112:115],a[220:223],v[108:111],0
r a[212:215],v193,0
r a[216:219],v193,8192
m a[176:179],a[220:223],v[116:119],0
v_exp_f32 v175,v71
m a[52:55],a[224:227],v[100:103],0
v_exp_f32 v180,v76
m a[116:119],a[224:227],v[108:111],0
v_exp_f32 v181,v77
m a[180:183],a[224:227],v[116:119],0
v_exp_f32 v182,v78
m a[56:59],a[228:231],v[100:103],0
v_exp_f32 v183,v79
m a[120:123],a[228:231],v[108:111],0
m a[184:187],a[228:231],v[116:119],0
m a[60:63],a[232:235],v[100:103],0
m a[124:127],a[232:235],v[108:111],0
m a[188:191],a[232:235],v[116:119],0
s_waitcnt lgkmcnt(0)
m v[4:7],a[204:207],v[0:3],v[124:127]
r a[220:223],v194,0
r a[224:227],v194,8192
m v[20:23],a[204:207],v[16:19],v[132:135]
r a[228:231],v195,0
r a[232:235],v195,8192
m v[36:39],a[204:207],v[32:35],v[140:143]
v_add_u32 v196,s42,v196
v_add_u32 v197,s42,v197
m v[12:15],a[208:211],v[0:3],v[124:127]
v_add_u32 v198,s42,v198
v_add_u32 v199,s42,v199
m v[28:31],a[208:211],v[16:19],v[132:135]
v_add_u32 v200,s42,v200
v_add_u32 v201,s42,v201
m v[44:47],a[208:211],v[32:35],v[140:143]
v_add_u32 v202,s42,v202
v_add_u32 v203,s42,v203
m v[4:7],a[212:215],v[8:11],v[4:7]
v_exp_f32 v220,v84
m v[20:23],a[212:215],v[24:27],v[20:23]
v_exp_f32 v221,v85
m v[36:39],a[212:215],v[40:43],v[36:39]
cv v100,v156,v157
cv v101,v158,v159
m v[12:15],a[216:219],v[8:11],v[12:15]
v_exp_f32 v222,v86
m v[28:31],a[216:219],v[24:27],v[28:31]
v_exp_f32 v223,v87
m v[44:47],a[216:219],v[40:43],v[44:47]
s_waitcnt lgkmcnt(0)
m v[4:7],a[220:223],v[48:51],v[4:7]
r a[204:207],v192,256
r a[208:211],v192,8448
m v[20:23],a[220:223],v[64:67],v[20:23]
r a[212:215],v193,256
r a[216:219],v193,8448
m v[36:39],a[220:223],v[80:83],v[36:39]
cv v116,v220,v221
cv v102,v164,v165
m v[12:15],a[224:227],v[48:51],v[12:15]
cv v103,v166,v167
m v[28:31],a[224:227],v[64:67],v[28:31]
v_exp_f32 v224,v92
m v[44:47],a[224:227],v[80:83],v[44:47]
v_exp_f32 v225,v93
m v[4:7],a[228:231],v[56:59],v[4:7]
cv v117,v222,v223
cv v108,v172,v173
m v[20:23],a[228:231],v[72:75],v[20:23]
m v[36:39],a[228:231],v[88:91],v[36:39]
m v[12:15],a[232:235],v[56:59],v[12:15]
m v[28:31],a[232:235],v[72:75],v[28:31]
m v[44:47],a[232:235],v[88:91],v[44:47]
s_waitcnt lgkmcnt(0)
m v[4:7],a[204:207],v[96:99],v[4:7]
r a[220:223],v194,256
r a[224:227],v194,8448
m v[20:23],a[204:207],v[112:115],v[20:23]
r a[228:231],v195,256
r a[232:235],v195,8448
m v[36:39],a[204:207],v[128:131],v[36:39]
cv v109,v174,v175
m v[12:15],a[208:211],v[96:99],v[12:15]
v_exp_f32 v220,v94
m v[28:31],a[208:211],v[112:115],v[28:31]
v_exp_f32 v221,v95
m v[44:47],a[208:211],v[128:131],v[44:47]
cv v118,v224,v225
cv v110,v180,v181
m v[4:7],a[212:215],v[104:107],v[4:7]
cv v111,v182,v183
cv v119,v220,v221
m v[20:23],a[212:215],v[120:123],v[20:23]
m v[36:39],a[212:215],v[136:139],v[36:39]
m v[12:15],a[216:219],v[104:107],v[12:15]
m v[28:31],a[216:219],v[120:123],v[28:31]
m v[44:47],a[216:219],v[136:139],v[44:47]
s_waitcnt lgkmcnt(0)
m v[4:7],a[220:223],v[144:147],v[4:7]
t a[204:205],v196,0
t a[206:207],v196,8192
m v[20:23],a[220:223],v[160:163],v[20:23]
t a[208:209],v197,0
t a[210:211],v197,8192
m v[36:39],a[220:223],v[176:179],v[36:39]
t a[212:213],v198,0
t a[214:215],v198,8192
m v[12:15],a[224:227],v[144:147],v[12:15]
t a[216:217],v199,0
t a[218:219],v199,8192
m v[28:31],a[224:227],v[160:163],v[28:31]
v_or3_b32 v228,v100,v101,v102
v_or3_b32 v228,v228,v103,v108
m v[44:47],a[224:227],v[176:179],v[44:47]
v_or3_b32 v228,v228,v109,v110
v_or3_b32 v228,v228,v111,v116
m v[4:7],a[228:231],v[152:155],v[4:7]
v_or3_b32 v228,v228,v117,v118
v_or_b32 v228,v228,v119
m v[20:23],a[228:231],v[168:171],v[20:23]
v_and_b32 v228,0x40004000,v228
m v[36:39],a[228:231],v[184:187],v[36:39]
m v[12:15],a[232:235],v[152:155],v[12:15]
m v[28:31],a[232:235],v[168:171],v[28:31]
m v[44:47],a[232:235],v[184:187],v[44:47]
v_cmp_ne_u32_e32 vcc,0,v228
s_nop 0
s_cbranch_vccnz LA_stub2%=
LA_ret2%=:
s_waitcnt lgkmcnt(0)
m a[192:195],v[148:151],v[100:103],a[192:195]
t a[220:221],v200,0
t a[222:223],v200,8192
m a[196:199],v[148:151],v[108:111],a[196:199]
t a[224:225],v201,0
t a[226:227],v201,8192
m a[200:203],v[148:151],v[116:119],a[200:203]
t a[228:229],v202,0
t a[230:231],v202,8192
m a[0:3],a[204:207],v[100:103],a[0:3]
t a[232:233],v203,0
t a[234:235],v203,8192
m a[64:67],a[204:207],v[108:111],a[64:67]
v_exp_f32 v156,v4
m a[128:131],a[204:207],v[116:119],a[128:131]
v_exp_f32 v157,v5
m a[4:7],a[208:211],v[100:103],a[4:7]
v_exp_f32 v158,v6
m a[68:71],a[208:211],v[108:111],a[68:71]
v_exp_f32 v159,v7
m a[132:135],a[208:211],v[116:119],a[132:135]
m a[8:11],a[212:215],v[100:103],a[8:11]
m a[72:75],a[212:215],v[108:111],a[72:75]
m a[136:139],a[212:215],v[116:119],a[136:139]
m a[12:15],a[216:219],v[100:103],a[12:15]
m a[76:79],a[216:219],v[108:111],a[76:79]
m a[140:143],a[216:219],v[116:119],a[140:143]
s_waitcnt lgkmcnt(0)
m a[16:19],a[220:223],v[100:103],a[16:19]
t a[204:205],v196,256
t a[206:207],v196,8448
m a[80:83],a[220:223],v[108:111],a[80:83]
t a[208:209],v197,256
t a[210:211],v197,8448
m a[144:147],a[220:223],v[116:119],a[144:147]
t a[212:213],v198,256
t a[214:215],v198,8448
m a[20:23],a[224:227],v[100:103],a[20:23]
t a[216:217],v199,256
t a[218:219],v199,8448
m a[84:87],a[224:227],v[108:111],a[84:87]
s_mov_b32 s41,s40
s_add_u32 s40,s40,0x8000
m a[148:151],a[224:227],v[116:119],a[148:151]
s_cmp_eq_u32 s40,0x28000
s_cselect_b32 s42,s49,s48
m a[24:27],a[228:231],v[100:103],a[24:27]
s_cselect_b32 s40,0,s40
v_add_u32 v192,s42,v192
m a[88:91],a[228:231],v[108:111],a[88:91]
v_add_u32 v193,s42,v193
v_add_u32 v194,s42,v194
m a[152:155],a[228:231],v[116:119],a[152:155]
v_add_u32 v195,s42,v195
m a[28:31],a[232:235],v[100:103],a[28:31]
v_exp_f32 v164,v12
m a[92:95],a[232:235],v[108:111],a[92:95]
v_exp_f32 v165,v13
m a[156:159],a[232:235],v[116:119],a[156:159]
s_waitcnt lgkmcnt(0)
m a[32:35],a[204:207],v[100:103],a[32:35]
t a[220:221],v200,256
t a[222:223],v200,8448
m a[96:99],a[204:207],v[108:111],a[96:99]
t a[224:225],v201,256
t a[226:227],v201,8448
m a[160:163],a[204:207],v[116:119],a[160:163]
t a[228:229],v202,256
t a[230:231],v202,8448
m a[36:39],a[208:211],v[100:103],a[36:39]
t a[232:233],v203,256
t a[234:235],v203,8448
m a[100:103],a[208:211],v[108:111],a[100:103]
v_exp_f32 v166,v14
m a[164:167],a[208:211],v[116:119],a[164:167]
v_exp_f32 v167,v15
m a[40:43],a[212:215],v[100:103],a[40:43]
v_exp_f32 v172,v20
m a[104:107],a[212:215],v[108:111],a[104:107]
v_exp_f32 v173,v21
m a[168:171],a[212:215],v[116:119],a[168:171]
v_exp_f32 v174,v22
m a[44:47],a[216:219],v[100:103],a[44:47]
m a[108:111],a[216:219],v[108:111],a[108:111]
m a[172:175],a[216:219],v[116:119],a[172:175]
s_mov_b32 s44,13
LA_loop%=:
s_waitcnt lgkmcnt(0)
s_barrier
m a[48:51],a[220:223],v[100:103],a[48:51]
r a[204:207],v192,0
r a[208:211],v192,8192
m a[112:115],a[220:223],v[108:111],a[112:115]
r a[212:215],v193,0
r a[216:219],v193,8192
m a[176:179],a[220:223],v[116:119],a[176:179]
v_exp_f32 v175,v23
m a[52:55],a[224:227],v[100:103],a[52:55]
v_exp_f32 v180,v28
m a[116:119],a[224:227],v[108:111],a[116:119]
v_exp_f32 v181,v29
m a[180:183],a[224:227],v[116:119],a[180:183]
v_exp_f32 v182,v30
m a[56:59],a[228:231],v[100:103],a[56:59]
v_exp_f32 v183,v31
m a[120:123],a[228:231],v[108:111],a[120:123]
m a[184:187],a[228:231],v[116:119],a[184:187]
m a[60:63],a[232:235],v[100:103],a[60:63]
m a[124:127],a[232:235],v[108:111],a[124:127]
m a[188:191],a[232:235],v[116:119],a[188:191]
s_waitcnt lgkmcnt(0)
m v[52:55],a[204:207],v[0:3],v[124:127]
r a[220:223],v194,0
r a[224:227],v194,8192
m v[68:71],a[204:207],v[16:19],v[132:135]
r a[228:231],v195,0
r a[232:235],v195,8192
m v[84:87],a[204:207],v[32:35],v[140:143]
v_add_u32 v196,s42,v196
v_add_u32 v197,s42,v197
m v[60:63],a[208:211],v[0:3],v[124:127]
v_add_u32 v198,s42,v198
v_add_u32 v199,s42,v199
m v[76:79],a[208:211],v[16:19],v[132:135]
v_add_u32 v200,s42,v200
v_add_u32 v201,s42,v201
m v[92:95],a[208:211],v[32:35],v[140:143]
v_add_u32 v202,s42,v202
v_add_u32 v203,s42,v203
m v[52:55],a[212:215],v[8:11],v[52:55]
v_exp_f32 v220,v36
m v[68:71],a[212:215],v[24:27],v[68:71]
v_exp_f32 v221,v37
m v[84:87],a[212:215],v[40:43],v[84:87]
cv v100,v156,v157
cv v101,v158,v159
m v[60:63],a[216:219],v[8:11],v[60:63]
v_exp_f32 v222,v38
m v[76:79],a[216:219],v[24:27],v[76:79]
v_exp_f32 v223,v39
m v[92:95],a[216:219],v[40:43],v[92:95]
s_waitcnt lgkmcnt(0)
m v[52:55],a[220:223],v[48:51],v[52:55]
r a[204:207],v192,256
r a[208:211],v192,8448
m v[68:71],a[220:223],v[64:67],v[68:71]
r a[212:215],v193,256
r a[216:219],v193,8448
m v[84:87],a[220:223],v[80:83],v[84:87]
cv v116,v220,v221
cv v102,v164,v165
m v[60:63],a[224:227],v[48:51],v[60:63]
cv v103,v166,v167
m v[76:79],a[224:227],v[64:67],v[76:79]
v_exp_f32 v224,v44
m v[92:95],a[224:227],v[80:83],v[92:95]
v_exp_f32 v225,v45
m v[52:55],a[228:231],v[56:59],v[52:55]
cv v117,v222,v223
cv v108,v172,v173
m v[68:71],a[228:231],v[72:75],v[68:71]
m v[84:87],a[228:231],v[88:91],v[84:87]
m v[60:63],a[232:235],v[56:59],v[60:63]
m v[76:79],a[232:235],v[72:75],v[76:79]
m v[92:95],a[232:235],v[88:91],v[92:95]
s_waitcnt lgkmcnt(0)
m v[52:55],a[204:207],v[96:99],v[52:55]
r a[220:223],v194,256
r a[224:227],v194,8448
m v[68:71],a[204:207],v[112:115],v[68:71]
r a[228:231],v195,256
r a[232:235],v195,8448
m v[84:87],a[204:207],v[128:131],v[84:87]
cv v109,v174,v175
m v[60:63],a[208:211],v[96:99],v[60:63]
v_exp_f32 v220,v46
m v[76:79],a[208:211],v[112:115],v[76:79]
v_exp_f32 v221,v47
m v[92:95],a[208:211],v[128:131],v[92:95]
cv v118,v224,v225
cv v110,v180,v181
m v[52:55],a[212:215],v[104:107],v[52:55]
cv v111,v182,v183
cv v119,v220,v221
m v[68:71],a[212:215],v[120:123],v[68:71]
m v[84:87],a[212:215],v[136:139],v[84:87]
m v[60:63],a[216:219],v[104:107],v[60:63]
m v[76:79],a[216:219],v[120:123],v[76:79]
m v[92:95],a[216:219],v[136:139],v[92:95]
s_waitcnt lgkmcnt(0)
m v[52:55],a[220:223],v[144:147],v[52:55]
t a[204:205],v196,0
t a[206:207],v196,8192
m v[68:71],a[220:223],v[160:163],v[68:71]
t a[208:209],v197,0
t a[210:211],v197,8192
m v[84:87],a[220:223],v[176:179],v[84:87]
t a[212:213],v198,0
t a[214:215],v198,8192
m v[60:63],a[224:227],v[144:147],v[60:63]
t a[216:217],v199,0
t a[218:219],v199,8192
m v[76:79],a[224:227],v[160:163],v[76:79]
v_or3_b32 v228,v100,v101,v102
v_or3_b32 v228,v228,v103,v108
m v[92:95],a[224:227],v[176:179],v[92:95]
v_or3_b32 v228,v228,v109,v110
v_or3_b32 v228,v228,v111,v116
m v[52:55],a[228:231],v[152:155],v[52:55]
v_or3_b32 v228,v228,v117,v118
v_or_b32 v228,v228,v119
m v[68:71],a[228:231],v[168:171],v[68:71]
v_and_b32 v228,0x40004000,v228
m v[84:87],a[228:231],v[184:187],v[84:87]
m v[60:63],a[232:235],v[152:155],v[60:63]
m v[76:79],a[232:235],v[168:171],v[76:79]
m v[92:95],a[232:235],v[184:187],v[92:95]
v_cmp_ne_u32_e32 vcc,0,v228
s_nop 0
s_cbranch_vccnz LA_stub3%=
LA_ret3%=:
s_waitcnt lgkmcnt(0)
m a[192:195],v[148:151],v[100:103],a[192:195]
t a[220:221],v200,0
t a[222:223],v200,8192
m a[196:199],v[148:151],v[108:111],a[196:199]
t a[224:225],v201,0
t a[226:227],v201,8192
m a[200:203],v[148:151],v[116:119],a[200:203]
t a[228:229],v202,0
t a[230:231],v202,8192
m a[0:3],a[204:207],v[100:103],a[0:3]
t a[232:233],v203,0
t a[234:235],v203,8192
m a[64:67],a[204:207],v[108:111],a[64:67]
v_exp_f32 v156,v52
m a[128:131],a[204:207],v[116:119],a[128:131]
v_exp_f32 v157,v53
m a[4:7],a[208:211],v[100:103],a[4:7]
v_exp_f32 v158,v54
m a[68:71],a[208:211],v[108:111],a[68:71]
v_exp_f32 v159,v55
m a[132:135],a[208:211],v[116:119],a[132:135]
m a[8:11],a[212:215],v[100:103],a[8:11]
m a[72:75],a[212:215],v[108:111],a[72:75]
m a[136:139],a[212:215],v[116:119],a[136:139]
m a[12:15],a[216:219],v[100:103],a[12:15]
m a[76:79],a[216:219],v[108:111],a[76:79]
m a[140:143],a[216:219],v[116:119],a[140:143]
s_waitcnt lgkmcnt(0)
m a[16:19],a[220:223],v[100:103],a[16:19]
t a[204:205],v196,256
t a[206:207],v196,8448
m a[80:83],a[220:223],v[108:111],a[80:83]
t a[208:209],v197,256
t a[210:211],v197,8448
m a[144:147],a[220:223],v[116:119],a[144:147]
t a[212:213],v198,256
t a[214:215],v198,8448
m a[20:23],a[224:227],v[100:103],a[20:23]
t a[216:217],v199,256
t a[218:219],v199,8448
m a[84:87],a[224:227],v[108:111],a[84:87]
s_mov_b32 s41,s40
s_add_u32 s40,s40,0x8000
m a[148:151],a[224:227],v[116:119],a[148:151]
s_cmp_eq_u32 s40,0x28000
s_cselect_b32 s42,s49,s48
m a[24:27],a[228:231],v[100:103],a[24:27]
s_cselect_b32 s40,0,s40
v_add_u32 v192,s42,v192
m a[88:91],a[228:231],v[108:111],a[88:91]
v_add_u32 v193,s42,v193
v_add_u32 v194,s42,v194
m a[152:155],a[228:231],v[116:119],a[152:155]
v_add_u32 v195,s42,v195
m a[28:31],a[232:235],v[100:103],a[28:31]
v_exp_f32 v164,v60
m a[92:95],a[232:235],v[108:111],a[92:95]
v_exp_f32 v165,v61
m a[156:159],a[232:235],v[116:119],a[156:159]
s_waitcnt lgkmcnt(0)
m a[32:35],a[204:207],v[100:103],a[32:35]
t a[220:221],v200,256
t a[222:223],v200,8448
m a[96:99],a[204:207],v[108:111],a[96:99]
t a[224:225],v201,256
t a[226:227],v201,8448
m a[160:163],a[204:207],v[116:119],a[160:163]
t a[228:229],v202,256
t a[230:231],v202,8448
m a[36:39],a[208:211],v[100:103],a[36:39]
t a[232:233],v203,256
t a[234:235],v203,8448
m a[100:103],a[208:211],v[108:111],a[100:103]
v_exp_f32 v166,v62
m a[164:167],a[208:211],v[116:119],a[164:167]
v_exp_f32 v167,v63
m a[40:43],a[212:215],v[100:103],a[40:43]
v_exp_f32 v172,v68
m a[104:107],a[212:215],v[108:111],a[104:107]
v_exp_f32 v173,v69
m a[168:171],a[212:215],v[116:119],a[168:171]
v_exp_f32 v174,v70
m a[44:47],a[216:219],v[100:103],a[44:47]
m a[108:111],a[216:219],v[108:111],a[108:111]
m a[172:175],a[216:219],v[116:119],a[172:175]
s_waitcnt lgkmcnt(0)
s_barrier
m a[48:51],a[220:223],v[100:103],a[48:51]
r a[204:207],v192,0
r a[208:211],v192,8192
m a[112:115],a[220:223],v[108:111],a[112:115]
r a[212:215],v193,0
r a[216:219],v193,8192
m a[176:179],a[220:223],v[116:119],a[176:179]
v_exp_f32 v175,v71
m a[52:55],a[224:227],v[100:103],a[52:55]
v_exp_f32 v180,v76
m a[116:119],a[224:227],v[108:111],a[116:119]
v_exp_f32 v181,v77
m a[180:183],a[224:227],v[116:119],a[180:183]
v_exp_f32 v182,v78
m a[56:59],a[228:231],v[100:103],a[56:59]
v_exp_f32 v183,v79
m a[120:123],a[228:231],v[108:111],a[120:123]
m a[184:187],a[228:231],v[116:119],a[184:187]
m a[60:63],a[232:235],v[100:103],a[60:63]
m a[124:127],a[232:235],v[108:111],a[124:127]
m a[188:191],a[232:235],v[116:119],a[188:191]
s_waitcnt lgkmcnt(0)
m v[4:7],a[204:207],v[0:3],v[124:127]
r a[220:223],v194,0
r a[224:227],v194,8192
m v[20:23],a[204:207],v[16:19],v[132:135]
r a[228:231],v195,0
r a[232:235],v195,8192
m v[36:39],a[204:207],v[32:35],v[140:143]
v_add_u32 v196,s42,v196
v_add_u32 v197,s42,v197
m v[12:15],a[208:211],v[0:3],v[124:127]
v_add_u32 v198,s42,v198
v_add_u32 v199,s42,v199
m v[28:31],a[208:211],v[16:19],v[132:135]
v_add_u32 v200,s42,v200
v_add_u32 v201,s42,v201
m v[44:47],a[208:211],v[32:35],v[140:143]
v_add_u32 v202,s42,v202
v_add_u32 v203,s42,v203
m v[4:7],a[212:215],v[8:11],v[4:7]
v_exp_f32 v220,v84
m v[20:23],a[212:215],v[24:27],v[20:23]
v_exp_f32 v221,v85
m v[36:39],a[212:215],v[40:43],v[36:39]
cv v100,v156,v157
cv v101,v158,v159
m v[12:15],a[216:219],v[8:11],v[12:15]
v_exp_f32 v222,v86
m v[28:31],a[216:219],v[24:27],v[28:31]
v_exp_f32 v223,v87
m v[44:47],a[216:219],v[40:43],v[44:47]
s_waitcnt lgkmcnt(0)
m v[4:7],a[220:223],v[48:51],v[4:7]
r a[204:207],v192,256
r a[208:211],v192,8448
m v[20:23],a[220:223],v[64:67],v[20:23]
r a[212:215],v193,256
r a[216:219],v193,8448
m v[36:39],a[220:223],v[80:83],v[36:39]
cv v116,v220,v221
cv v102,v164,v165
m v[12:15],a[224:227],v[48:51],v[12:15]
cv v103,v166,v167
m v[28:31],a[224:227],v[64:67],v[28:31]
v_exp_f32 v224,v92
m v[44:47],a[224:227],v[80:83],v[44:47]
v_exp_f32 v225,v93
m v[4:7],a[228:231],v[56:59],v[4:7]
cv v117,v222,v223
cv v108,v172,v173
m v[20:23],a[228:231],v[72:75],v[20:23]
m v[36:39],a[228:231],v[88:91],v[36:39]
m v[12:15],a[232:235],v[56:59],v[12:15]
m v[28:31],a[232:235],v[72:75],v[28:31]
m v[44:47],a[232:235],v[88:91],v[44:47]
s_waitcnt lgkmcnt(0)
m v[4:7],a[204:207],v[96:99],v[4:7]
r a[220:223],v194,256
r a[224:227],v194,8448
m v[20:23],a[204:207],v[112:115],v[20:23]
r a[228:231],v195,256
r a[232:235],v195,8448
m v[36:39],a[204:207],v[128:131],v[36:39]
cv v109,v174,v175
m v[12:15],a[208:211],v[96:99],v[12:15]
v_exp_f32 v220,v94
m v[28:31],a[208:211],v[112:115],v[28:31]
v_exp_f32 v221,v95
m v[44:47],a[208:211],v[128:131],v[44:47]
cv v118,v224,v225
cv v110,v180,v181
m v[4:7],a[212:215],v[104:107],v[4:7]
cv v111,v182,v183
cv v119,v220,v221
m v[20:23],a[212:215],v[120:123],v[20:23]
m v[36:39],a[212:215],v[136:139],v[36:39]
m v[12:15],a[216:219],v[104:107],v[12:15]
m v[28:31],a[216:219],v[120:123],v[28:31]
m v[44:47],a[216:219],v[136:139],v[44:47]
s_waitcnt lgkmcnt(0)
m v[4:7],a[220:223],v[144:147],v[4:7]
t a[204:205],v196,0
t a[206:207],v196,8192
m v[20:23],a[220:223],v[160:163],v[20:23]
t a[208:209],v197,0
t a[210:211],v197,8192
m v[36:39],a[220:223],v[176:179],v[36:39]
t a[212:213],v198,0
t a[214:215],v198,8192
m v[12:15],a[224:227],v[144:147],v[12:15]
t a[216:217],v199,0
t a[218:219],v199,8192
m v[28:31],a[224:227],v[160:163],v[28:31]
v_or3_b32 v228,v100,v101,v102
v_or3_b32 v228,v228,v103,v108
m v[44:47],a[224:227],v[176:179],v[44:47]
v_or3_b32 v228,v228,v109,v110
v_or3_b32 v228,v228,v111,v116
m v[4:7],a[228:231],v[152:155],v[4:7]
v_or3_b32 v228,v228,v117,v118
v_or_b32 v228,v228,v119
m v[20:23],a[228:231],v[168:171],v[20:23]
v_and_b32 v228,0x40004000,v228
m v[36:39],a[228:231],v[184:187],v[36:39]
m v[12:15],a[232:235],v[152:155],v[12:15]
m v[28:31],a[232:235],v[168:171],v[28:31]
m v[44:47],a[232:235],v[184:187],v[44:47]
v_cmp_ne_u32_e32 vcc,0,v228
s_nop 0
s_cbranch_vccnz LA_stub4%=
LA_ret4%=:
s_waitcnt lgkmcnt(0)
m a[192:195],v[148:151],v[100:103],a[192:195]
t a[220:221],v200,0
t a[222:223],v200,8192
m a[196:199],v[148:151],v[108:111],a[196:199]
t a[224:225],v201,0
t a[226:227],v201,8192
m a[200:203],v[148:151],v[116:119],a[200:203]
t a[228:229],v202,0
t a[230:231],v202,8192
m a[0:3],a[204:207],v[100:103],a[0:3]
t a[232:233],v203,0
t a[234:235],v203,8192
m a[64:67],a[204:207],v[108:111],a[64:67]
v_exp_f32 v156,v4
m a[128:131],a[204:207],v[116:119],a[128:131]
v_exp_f32 v157,v5
m a[4:7],a[208:211],v[100:103],a[4:7]
v_exp_f32 v158,v6
m a[68:71],a[208:211],v[108:111],a[68:71]
v_exp_f32 v159,v7
m a[132:135],a[208:211],v[116:119],a[132:135]
m a[8:11],a[212:215],v[100:103],a[8:11]
m a[72:75],a[212:215],v[108:111],a[72:75]
m a[136:139],a[212:215],v[116:119],a[136:139]
m a[12:15],a[216:219],v[100:103],a[12:15]
m a[76:79],a[216:219],v[108:111],a[76:79]
m a[140:143],a[216:219],v[116:119],a[140:143]
s_waitcnt lgkmcnt(0)
m a[16:19],a[220:223],v[100:103],a[16:19]
t a[204:205],v196,256
t a[206:207],v196,8448
m a[80:83],a[220:223],v[108:111],a[80:83]
t a[208:209],v197,256
t a[210:211],v197,8448
m a[144:147],a[220:223],v[116:119],a[144:147]
t a[212:213],v198,256
t a[214:215],v198,8448
m a[20:23],a[224:227],v[100:103],a[20:23]
t a[216:217],v199,256
t a[218:219],v199,8448
m a[84:87],a[224:227],v[108:111],a[84:87]
s_mov_b32 s41,s40
s_add_u32 s40,s40,0x8000
m a[148:151],a[224:227],v[116:119],a[148:151]
s_cmp_eq_u32 s40,0x28000
s_cselect_b32 s42,s49,s48
m a[24:27],a[228:231],v[100:103],a[24:27]
s_cselect_b32 s40,0,s40
v_add_u32 v192,s42,v192
m a[88:91],a[228:231],v[108:111],a[88:91]
v_add_u32 v193,s42,v193
v_add_u32 v194,s42,v194
m a[152:155],a[228:231],v[116:119],a[152:155]
v_add_u32 v195,s42,v195
m a[28:31],a[232:235],v[100:103],a[28:31]
v_exp_f32 v164,v12
m a[92:95],a[232:235],v[108:111],a[92:95]
v_exp_f32 v165,v13
m a[156:159],a[232:235],v[116:119],a[156:159]
s_waitcnt lgkmcnt(0)
m a[32:35],a[204:207],v[100:103],a[32:35]
t a[220:221],v200,256
t a[222:223],v200,8448
m a[96:99],a[204:207],v[108:111],a[96:99]
t a[224:225],v201,256
t a[226:227],v201,8448
m a[160:163],a[204:207],v[116:119],a[160:163]
t a[228:229],v202,256
t a[230:231],v202,8448
m a[36:39],a[208:211],v[100:103],a[36:39]
t a[232:233],v203,256
t a[234:235],v203,8448
m a[100:103],a[208:211],v[108:111],a[100:103]
v_exp_f32 v166,v14
m a[164:167],a[208:211],v[116:119],a[164:167]
v_exp_f32 v167,v15
m a[40:43],a[212:215],v[100:103],a[40:43]
v_exp_f32 v172,v20
m a[104:107],a[212:215],v[108:111],a[104:107]
v_exp_f32 v173,v21
m a[168:171],a[212:215],v[116:119],a[168:171]
v_exp_f32 v174,v22
m a[44:47],a[216:219],v[100:103],a[44:47]
m a[108:111],a[216:219],v[108:111],a[108:111]
m a[172:175],a[216:219],v[116:119],a[172:175]
s_sub_u32 s44,s44,1
s_lshl_b32 s46,%6,8
s_add_u32 s46,s46,s44
s_cmp_eq_u32 s46,1
s_cbranch_scc0 LA_wot%=
v_lshlrev_b32 v240,7,v214
s_mul_i32 s46,%7,0x7000
buffer_load_dword v255,v240,%4,s46 offen
s_add_u32 s46,s46,0x2000
buffer_load_dword v255,v240,%4,s46 offen
s_add_u32 s46,s46,0x2000
buffer_load_dword v255,v240,%4,s46 offen
s_add_u32 s46,s46,0x2000
v_min_u32 v240,0xf80,v240
buffer_load_dword v255,v240,%4,s46 offen
LA_wot%=:
s_cmp_lg_u32 s44,0
s_cbranch_scc1 LA_loop%=
s_waitcnt lgkmcnt(0)
s_barrier
m a[48:51],a[220:223],v[100:103],a[48:51]
r a[204:207],v192,0
r a[208:211],v192,8192
m a[112:115],a[220:223],v[108:111],a[112:115]
r a[212:215],v193,0
r a[216:219],v193,8192
m a[176:179],a[220:223],v[116:119],a[176:179]
v_exp_f32 v175,v23
m a[52:55],a[224:227],v[100:103],a[52:55]
v_exp_f32 v180,v28
m a[116:119],a[224:227],v[108:111],a[116:119]
v_exp_f32 v181,v29
m a[180:183],a[224:227],v[116:119],a[180:183]
v_exp_f32 v182,v30
m a[56:59],a[228:231],v[100:103],a[56:59]
v_exp_f32 v183,v31
m a[120:123],a[228:231],v[108:111],a[120:123]
m a[184:187],a[228:231],v[116:119],a[184:187]
m a[60:63],a[232:235],v[100:103],a[60:63]
m a[124:127],a[232:235],v[108:111],a[124:127]
m a[188:191],a[232:235],v[116:119],a[188:191]
s_waitcnt lgkmcnt(0)
m v[52:55],a[204:207],v[0:3],v[124:127]
r a[220:223],v194,0
r a[224:227],v194,8192
m v[68:71],a[204:207],v[16:19],v[132:135]
r a[228:231],v195,0
r a[232:235],v195,8192
m v[84:87],a[204:207],v[32:35],v[140:143]
v_add_u32 v196,s42,v196
v_add_u32 v197,s42,v197
m v[60:63],a[208:211],v[0:3],v[124:127]
v_add_u32 v198,s42,v198
v_add_u32 v199,s42,v199
m v[76:79],a[208:211],v[16:19],v[132:135]
v_add_u32 v200,s42,v200
v_add_u32 v201,s42,v201
m v[92:95],a[208:211],v[32:35],v[140:143]
v_add_u32 v202,s42,v202
v_add_u32 v203,s42,v203
m v[52:55],a[212:215],v[8:11],v[52:55]
v_exp_f32 v220,v36
m v[68:71],a[212:215],v[24:27],v[68:71]
v_exp_f32 v221,v37
m v[84:87],a[212:215],v[40:43],v[84:87]
cv v100,v156,v157
cv v101,v158,v159
m v[60:63],a[216:219],v[8:11],v[60:63]
v_exp_f32 v222,v38
m v[76:79],a[216:219],v[24:27],v[76:79]
v_exp_f32 v223,v39
m v[92:95],a[216:219],v[40:43],v[92:95]
s_waitcnt lgkmcnt(0)
m v[52:55],a[220:223],v[48:51],v[52:55]
r a[204:207],v192,256
r a[208:211],v192,8448
m v[68:71],a[220:223],v[64:67],v[68:71]
r a[212:215],v193,256
r a[216:219],v193,8448
m v[84:87],a[220:223],v[80:83],v[84:87]
cv v116,v220,v221
cv v102,v164,v165
m v[60:63],a[224:227],v[48:51],v[60:63]
cv v103,v166,v167
m v[76:79],a[224:227],v[64:67],v[76:79]
v_exp_f32 v224,v44
m v[92:95],a[224:227],v[80:83],v[92:95]
v_exp_f32 v225,v45
m v[52:55],a[228:231],v[56:59],v[52:55]
cv v117,v222,v223
cv v108,v172,v173
m v[68:71],a[228:231],v[72:75],v[68:71]
m v[84:87],a[228:231],v[88:91],v[84:87]
m v[60:63],a[232:235],v[56:59],v[60:63]
m v[76:79],a[232:235],v[72:75],v[76:79]
m v[92:95],a[232:235],v[88:91],v[92:95]
s_waitcnt lgkmcnt(0)
m v[52:55],a[204:207],v[96:99],v[52:55]
r a[220:223],v194,256
r a[224:227],v194,8448
m v[68:71],a[204:207],v[112:115],v[68:71]
r a[228:231],v195,256
r a[232:235],v195,8448
m v[84:87],a[204:207],v[128:131],v[84:87]
cv v109,v174,v175
m v[60:63],a[208:211],v[96:99],v[60:63]
v_exp_f32 v220,v46
m v[76:79],a[208:211],v[112:115],v[76:79]
v_exp_f32 v221,v47
m v[92:95],a[208:211],v[128:131],v[92:95]
cv v118,v224,v225
cv v110,v180,v181
m v[52:55],a[212:215],v[104:107],v[52:55]
cv v111,v182,v183
cv v119,v220,v221
m v[68:71],a[212:215],v[120:123],v[68:71]
m v[84:87],a[212:215],v[136:139],v[84:87]
m v[60:63],a[216:219],v[104:107],v[60:63]
m v[76:79],a[216:219],v[120:123],v[76:79]
m v[92:95],a[216:219],v[136:139],v[92:95]
s_waitcnt lgkmcnt(0)
m v[52:55],a[220:223],v[144:147],v[52:55]
t a[204:205],v196,0
t a[206:207],v196,8192
m v[68:71],a[220:223],v[160:163],v[68:71]
t a[208:209],v197,0
t a[210:211],v197,8192
m v[84:87],a[220:223],v[176:179],v[84:87]
t a[212:213],v198,0
t a[214:215],v198,8192
m v[60:63],a[224:227],v[144:147],v[60:63]
t a[216:217],v199,0
t a[218:219],v199,8192
m v[76:79],a[224:227],v[160:163],v[76:79]
v_or3_b32 v228,v100,v101,v102
v_or3_b32 v228,v228,v103,v108
m v[92:95],a[224:227],v[176:179],v[92:95]
v_or3_b32 v228,v228,v109,v110
v_or3_b32 v228,v228,v111,v116
m v[52:55],a[228:231],v[152:155],v[52:55]
v_or3_b32 v228,v228,v117,v118
v_or_b32 v228,v228,v119
m v[68:71],a[228:231],v[168:171],v[68:71]
v_and_b32 v228,0x40004000,v228
m v[84:87],a[228:231],v[184:187],v[84:87]
m v[60:63],a[232:235],v[152:155],v[60:63]
m v[76:79],a[232:235],v[168:171],v[76:79]
m v[92:95],a[232:235],v[184:187],v[92:95]
v_cmp_ne_u32_e32 vcc,0,v228
s_nop 0
s_cbranch_vccnz LA_stub5%=
LA_ret5%=:
s_waitcnt lgkmcnt(0)
m a[192:195],v[148:151],v[100:103],a[192:195]
t a[220:221],v200,0
t a[222:223],v200,8192
m a[196:199],v[148:151],v[108:111],a[196:199]
t a[224:225],v201,0
t a[226:227],v201,8192
m a[200:203],v[148:151],v[116:119],a[200:203]
t a[228:229],v202,0
t a[230:231],v202,8192
m a[0:3],a[204:207],v[100:103],a[0:3]
t a[232:233],v203,0
t a[234:235],v203,8192
m a[64:67],a[204:207],v[108:111],a[64:67]
v_exp_f32 v156,v52
m a[128:131],a[204:207],v[116:119],a[128:131]
v_exp_f32 v157,v53
m a[4:7],a[208:211],v[100:103],a[4:7]
v_exp_f32 v158,v54
m a[68:71],a[208:211],v[108:111],a[68:71]
v_exp_f32 v159,v55
m a[132:135],a[208:211],v[116:119],a[132:135]
m a[8:11],a[212:215],v[100:103],a[8:11]
m a[72:75],a[212:215],v[108:111],a[72:75]
m a[136:139],a[212:215],v[116:119],a[136:139]
m a[12:15],a[216:219],v[100:103],a[12:15]
m a[76:79],a[216:219],v[108:111],a[76:79]
m a[140:143],a[216:219],v[116:119],a[140:143]
s_waitcnt lgkmcnt(0)
m a[16:19],a[220:223],v[100:103],a[16:19]
t a[204:205],v196,256
t a[206:207],v196,8448
m a[80:83],a[220:223],v[108:111],a[80:83]
t a[208:209],v197,256
t a[210:211],v197,8448
m a[144:147],a[220:223],v[116:119],a[144:147]
t a[212:213],v198,256
t a[214:215],v198,8448
m a[20:23],a[224:227],v[100:103],a[20:23]
t a[216:217],v199,256
t a[218:219],v199,8448
m a[84:87],a[224:227],v[108:111],a[84:87]
s_mov_b32 s41,s40
s_add_u32 s40,s40,0x8000
m a[148:151],a[224:227],v[116:119],a[148:151]
s_cmp_eq_u32 s40,0x28000
s_cselect_b32 s42,s49,s48
m a[24:27],a[228:231],v[100:103],a[24:27]
s_cselect_b32 s40,0,s40
v_add_u32 v192,s42,v192
m a[88:91],a[228:231],v[108:111],a[88:91]
v_add_u32 v193,s42,v193
v_add_u32 v194,s42,v194
m a[152:155],a[228:231],v[116:119],a[152:155]
v_add_u32 v195,s42,v195
m a[28:31],a[232:235],v[100:103],a[28:31]
v_exp_f32 v164,v60
m a[92:95],a[232:235],v[108:111],a[92:95]
v_exp_f32 v165,v61
m a[156:159],a[232:235],v[116:119],a[156:159]
s_waitcnt lgkmcnt(0)
m a[32:35],a[204:207],v[100:103],a[32:35]
t a[220:221],v200,256
t a[222:223],v200,8448
m a[96:99],a[204:207],v[108:111],a[96:99]
t a[224:225],v201,256
t a[226:227],v201,8448
m a[160:163],a[204:207],v[116:119],a[160:163]
t a[228:229],v202,256
t a[230:231],v202,8448
m a[36:39],a[208:211],v[100:103],a[36:39]
t a[232:233],v203,256
t a[234:235],v203,8448
m a[100:103],a[208:211],v[108:111],a[100:103]
v_exp_f32 v166,v62
m a[164:167],a[208:211],v[116:119],a[164:167]
v_exp_f32 v167,v63
m a[40:43],a[212:215],v[100:103],a[40:43]
v_exp_f32 v172,v68
m a[104:107],a[212:215],v[108:111],a[104:107]
v_exp_f32 v173,v69
m a[168:171],a[212:215],v[116:119],a[168:171]
v_exp_f32 v174,v70
m a[44:47],a[216:219],v[100:103],a[44:47]
m a[108:111],a[216:219],v[108:111],a[108:111]
m a[172:175],a[216:219],v[116:119],a[172:175]
s_waitcnt lgkmcnt(0)
s_barrier
m a[48:51],a[220:223],v[100:103],a[48:51]
r a[204:207],v192,0
r a[208:211],v192,8192
m a[112:115],a[220:223],v[108:111],a[112:115]
r a[212:215],v193,0
r a[216:219],v193,8192
m a[176:179],a[220:223],v[116:119],a[176:179]
v_exp_f32 v175,v71
m a[52:55],a[224:227],v[100:103],a[52:55]
v_exp_f32 v180,v76
m a[116:119],a[224:227],v[108:111],a[116:119]
v_exp_f32 v181,v77
m a[180:183],a[224:227],v[116:119],a[180:183]
v_exp_f32 v182,v78
m a[56:59],a[228:231],v[100:103],a[56:59]
v_exp_f32 v183,v79
m a[120:123],a[228:231],v[108:111],a[120:123]
m a[184:187],a[228:231],v[116:119],a[184:187]
m a[60:63],a[232:235],v[100:103],a[60:63]
m a[124:127],a[232:235],v[108:111],a[124:127]
m a[188:191],a[232:235],v[116:119],a[188:191]
s_waitcnt lgkmcnt(0)
m v[4:7],a[204:207],v[0:3],v[124:127]
r a[220:223],v194,0
r a[224:227],v194,8192
m v[20:23],a[204:207],v[16:19],v[132:135]
r a[228:231],v195,0
r a[232:235],v195,8192
m v[36:39],a[204:207],v[32:35],v[140:143]
v_add_u32 v196,s42,v196
v_add_u32 v197,s42,v197
m v[12:15],a[208:211],v[0:3],v[124:127]
v_add_u32 v198,s42,v198
v_add_u32 v199,s42,v199
m v[28:31],a[208:211],v[16:19],v[132:135]
v_add_u32 v200,s42,v200
v_add_u32 v201,s42,v201
m v[44:47],a[208:211],v[32:35],v[140:143]
v_add_u32 v202,s42,v202
v_add_u32 v203,s42,v203
m v[4:7],a[212:215],v[8:11],v[4:7]
v_exp_f32 v220,v84
m v[20:23],a[212:215],v[24:27],v[20:23]
v_exp_f32 v221,v85
m v[36:39],a[212:215],v[40:43],v[36:39]
cv v100,v156,v157
cv v101,v158,v159
m v[12:15],a[216:219],v[8:11],v[12:15]
v_exp_f32 v222,v86
m v[28:31],a[216:219],v[24:27],v[28:31]
v_exp_f32 v223,v87
m v[44:47],a[216:219],v[40:43],v[44:47]
s_waitcnt lgkmcnt(0)
m v[4:7],a[220:223],v[48:51],v[4:7]
r a[204:207],v192,256
r a[208:211],v192,8448
m v[20:23],a[220:223],v[64:67],v[20:23]
r a[212:215],v193,256
r a[216:219],v193,8448
m v[36:39],a[220:223],v[80:83],v[36:39]
cv v116,v220,v221
cv v102,v164,v165
m v[12:15],a[224:227],v[48:51],v[12:15]
cv v103,v166,v167
m v[28:31],a[224:227],v[64:67],v[28:31]
v_exp_f32 v224,v92
m v[44:47],a[224:227],v[80:83],v[44:47]
v_exp_f32 v225,v93
m v[4:7],a[228:231],v[56:59],v[4:7]
cv v117,v222,v223
cv v108,v172,v173
m v[20:23],a[228:231],v[72:75],v[20:23]
m v[36:39],a[228:231],v[88:91],v[36:39]
m v[12:15],a[232:235],v[56:59],v[12:15]
m v[28:31],a[232:235],v[72:75],v[28:31]
m v[44:47],a[232:235],v[88:91],v[44:47]
s_waitcnt lgkmcnt(0)
m v[4:7],a[204:207],v[96:99],v[4:7]
r a[220:223],v194,256
r a[224:227],v194,8448
m v[20:23],a[204:207],v[112:115],v[20:23]
r a[228:231],v195,256
r a[232:235],v195,8448
m v[36:39],a[204:207],v[128:131],v[36:39]
cv v109,v174,v175
m v[12:15],a[208:211],v[96:99],v[12:15]
v_exp_f32 v220,v94
m v[28:31],a[208:211],v[112:115],v[28:31]
v_exp_f32 v221,v95
m v[44:47],a[208:211],v[128:131],v[44:47]
cv v118,v224,v225
cv v110,v180,v181
m v[4:7],a[212:215],v[104:107],v[4:7]
cv v111,v182,v183
cv v119,v220,v221
m v[20:23],a[212:215],v[120:123],v[20:23]
m v[36:39],a[212:215],v[136:139],v[36:39]
m v[12:15],a[216:219],v[104:107],v[12:15]
m v[28:31],a[216:219],v[120:123],v[28:31]
m v[44:47],a[216:219],v[136:139],v[44:47]
s_waitcnt lgkmcnt(0)
m v[4:7],a[220:223],v[144:147],v[4:7]
t a[204:205],v196,0
t a[206:207],v196,8192
m v[20:23],a[220:223],v[160:163],v[20:23]
t a[208:209],v197,0
t a[210:211],v197,8192
m v[36:39],a[220:223],v[176:179],v[36:39]
t a[212:213],v198,0
t a[214:215],v198,8192
m v[12:15],a[224:227],v[144:147],v[12:15]
t a[216:217],v199,0
t a[218:219],v199,8192
m v[28:31],a[224:227],v[160:163],v[28:31]
v_or3_b32 v228,v100,v101,v102
v_or3_b32 v228,v228,v103,v108
m v[44:47],a[224:227],v[176:179],v[44:47]
v_or3_b32 v228,v228,v109,v110
v_or3_b32 v228,v228,v111,v116
m v[4:7],a[228:231],v[152:155],v[4:7]
v_or3_b32 v228,v228,v117,v118
v_or_b32 v228,v228,v119
m v[20:23],a[228:231],v[168:171],v[20:23]
v_and_b32 v228,0x40004000,v228
m v[36:39],a[228:231],v[184:187],v[36:39]
m v[12:15],a[232:235],v[152:155],v[12:15]
m v[28:31],a[232:235],v[168:171],v[28:31]
m v[44:47],a[232:235],v[184:187],v[44:47]
v_cmp_ne_u32_e32 vcc,0,v228
s_nop 0
s_cbranch_vccnz LA_stub6%=
LA_ret6%=:
s_waitcnt lgkmcnt(0)
m a[192:195],v[148:151],v[100:103],a[192:195]
t a[220:221],v200,0
t a[222:223],v200,8192
m a[196:199],v[148:151],v[108:111],a[196:199]
t a[224:225],v201,0
t a[226:227],v201,8192
m a[200:203],v[148:151],v[116:119],a[200:203]
t a[228:229],v202,0
t a[230:231],v202,8192
m a[0:3],a[204:207],v[100:103],a[0:3]
t a[232:233],v203,0
t a[234:235],v203,8192
m a[64:67],a[204:207],v[108:111],a[64:67]
v_exp_f32 v156,v4
m a[128:131],a[204:207],v[116:119],a[128:131]
v_exp_f32 v157,v5
m a[4:7],a[208:211],v[100:103],a[4:7]
v_exp_f32 v158,v6
m a[68:71],a[208:211],v[108:111],a[68:71]
v_exp_f32 v159,v7
m a[132:135],a[208:211],v[116:119],a[132:135]
m a[8:11],a[212:215],v[100:103],a[8:11]
m a[72:75],a[212:215],v[108:111],a[72:75]
m a[136:139],a[212:215],v[116:119],a[136:139]
m a[12:15],a[216:219],v[100:103],a[12:15]
m a[76:79],a[216:219],v[108:111],a[76:79]
m a[140:143],a[216:219],v[116:119],a[140:143]
s_waitcnt lgkmcnt(0)
m a[16:19],a[220:223],v[100:103],a[16:19]
t a[204:205],v196,256
t a[206:207],v196,8448
m a[80:83],a[220:223],v[108:111],a[80:83]
t a[208:209],v197,256
t a[210:211],v197,8448
m a[144:147],a[220:223],v[116:119],a[144:147]
t a[212:213],v198,256
t a[214:215],v198,8448
m a[20:23],a[224:227],v[100:103],a[20:23]
t a[216:217],v199,256
t a[218:219],v199,8448
m a[84:87],a[224:227],v[108:111],a[84:87]
s_mov_b32 s41,s40
s_add_u32 s40,s40,0x8000
m a[148:151],a[224:227],v[116:119],a[148:151]
s_cmp_eq_u32 s40,0x28000
s_cselect_b32 s42,s49,s48
m a[24:27],a[228:231],v[100:103],a[24:27]
s_cselect_b32 s40,0,s40
v_add_u32 v192,s42,v192
m a[88:91],a[228:231],v[108:111],a[88:91]
v_add_u32 v193,s42,v193
v_add_u32 v194,s42,v194
m a[152:155],a[228:231],v[116:119],a[152:155]
v_add_u32 v195,s42,v195
m a[28:31],a[232:235],v[100:103],a[28:31]
v_exp_f32 v164,v12
m a[92:95],a[232:235],v[108:111],a[92:95]
v_exp_f32 v165,v13
m a[156:159],a[232:235],v[116:119],a[156:159]
s_waitcnt lgkmcnt(0)
m a[32:35],a[204:207],v[100:103],a[32:35]
t a[220:221],v200,256
t a[222:223],v200,8448
m a[96:99],a[204:207],v[108:111],a[96:99]
t a[224:225],v201,256
t a[226:227],v201,8448
m a[160:163],a[204:207],v[116:119],a[160:163]
t a[228:229],v202,256
t a[230:231],v202,8448
m a[36:39],a[208:211],v[100:103],a[36:39]
t a[232:233],v203,256
t a[234:235],v203,8448
m a[100:103],a[208:211],v[108:111],a[100:103]
v_exp_f32 v166,v14
m a[164:167],a[208:211],v[116:119],a[164:167]
v_exp_f32 v167,v15
m a[40:43],a[212:215],v[100:103],a[40:43]
v_exp_f32 v172,v20
m a[104:107],a[212:215],v[108:111],a[104:107]
v_exp_f32 v173,v21
m a[168:171],a[212:215],v[116:119],a[168:171]
v_exp_f32 v174,v22
m a[44:47],a[216:219],v[100:103],a[44:47]
m a[108:111],a[216:219],v[108:111],a[108:111]
m a[172:175],a[216:219],v[116:119],a[172:175]
s_waitcnt lgkmcnt(0)
s_barrier
m a[48:51],a[220:223],v[100:103],a[48:51]
r a[204:207],v192,0
r a[208:211],v192,8192
m a[112:115],a[220:223],v[108:111],a[112:115]
r a[212:215],v193,0
r a[216:219],v193,8192
m a[176:179],a[220:223],v[116:119],a[176:179]
v_exp_f32 v175,v23
m a[52:55],a[224:227],v[100:103],a[52:55]
v_exp_f32 v180,v28
m a[116:119],a[224:227],v[108:111],a[116:119]
v_exp_f32 v181,v29
m a[180:183],a[224:227],v[116:119],a[180:183]
v_exp_f32 v182,v30
m a[56:59],a[228:231],v[100:103],a[56:59]
v_exp_f32 v183,v31
m a[120:123],a[228:231],v[108:111],a[120:123]
m a[184:187],a[228:231],v[116:119],a[184:187]
m a[60:63],a[232:235],v[100:103],a[60:63]
m a[124:127],a[232:235],v[108:111],a[124:127]
m a[188:191],a[232:235],v[116:119],a[188:191]
s_waitcnt lgkmcnt(0)
m v[52:55],a[204:207],v[0:3],v[124:127]
r a[220:223],v194,0
r a[224:227],v194,8192
m v[68:71],a[204:207],v[16:19],v[132:135]
r a[228:231],v195,0
r a[232:235],v195,8192
m v[84:87],a[204:207],v[32:35],v[140:143]
v_add_u32 v196,s42,v196
v_add_u32 v197,s42,v197
m v[60:63],a[208:211],v[0:3],v[124:127]
v_add_u32 v198,s42,v198
v_add_u32 v199,s42,v199
m v[76:79],a[208:211],v[16:19],v[132:135]
v_add_u32 v200,s42,v200
v_add_u32 v201,s42,v201
m v[92:95],a[208:211],v[32:35],v[140:143]
v_add_u32 v202,s42,v202
v_add_u32 v203,s42,v203
m v[52:55],a[212:215],v[8:11],v[52:55]
v_exp_f32 v220,v36
m v[68:71],a[212:215],v[24:27],v[68:71]
v_exp_f32 v221,v37
m v[84:87],a[212:215],v[40:43],v[84:87]
cv v100,v156,v157
cv v101,v158,v159
m v[60:63],a[216:219],v[8:11],v[60:63]
v_exp_f32 v222,v38
m v[76:79],a[216:219],v[24:27],v[76:79]
v_exp_f32 v223,v39
m v[92:95],a[216:219],v[40:43],v[92:95]
s_waitcnt lgkmcnt(0)
m v[52:55],a[220:223],v[48:51],v[52:55]
r a[204:207],v192,256
r a[208:211],v192,8448
m v[68:71],a[220:223],v[64:67],v[68:71]
r a[212:215],v193,256
r a[216:219],v193,8448
m v[84:87],a[220:223],v[80:83],v[84:87]
cv v116,v220,v221
cv v102,v164,v165
m v[60:63],a[224:227],v[48:51],v[60:63]
cv v103,v166,v167
m v[76:79],a[224:227],v[64:67],v[76:79]
v_exp_f32 v224,v44
m v[92:95],a[224:227],v[80:83],v[92:95]
v_exp_f32 v225,v45
m v[52:55],a[228:231],v[56:59],v[52:55]
cv v117,v222,v223
cv v108,v172,v173
m v[68:71],a[228:231],v[72:75],v[68:71]
m v[84:87],a[228:231],v[88:91],v[84:87]
m v[60:63],a[232:235],v[56:59],v[60:63]
m v[76:79],a[232:235],v[72:75],v[76:79]
m v[92:95],a[232:235],v[88:91],v[92:95]
s_waitcnt lgkmcnt(0)
m v[52:55],a[204:207],v[96:99],v[52:55]
r a[220:223],v194,256
r a[224:227],v194,8448
m v[68:71],a[204:207],v[112:115],v[68:71]
r a[228:231],v195,256
r a[232:235],v195,8448
m v[84:87],a[204:207],v[128:131],v[84:87]
cv v109,v174,v175
m v[60:63],a[208:211],v[96:99],v[60:63]
v_exp_f32 v220,v46
m v[76:79],a[208:211],v[112:115],v[76:79]
v_exp_f32 v221,v47
m v[92:95],a[208:211],v[128:131],v[92:95]
cv v118,v224,v225
cv v110,v180,v181
m v[52:55],a[212:215],v[104:107],v[52:55]
cv v111,v182,v183
cv v119,v220,v221
m v[68:71],a[212:215],v[120:123],v[68:71]
m v[84:87],a[212:215],v[136:139],v[84:87]
m v[60:63],a[216:219],v[104:107],v[60:63]
m v[76:79],a[216:219],v[120:123],v[76:79]
m v[92:95],a[216:219],v[136:139],v[92:95]
s_waitcnt lgkmcnt(0)
m v[52:55],a[220:223],v[144:147],v[52:55]
t a[204:205],v196,0
t a[206:207],v196,8192
m v[68:71],a[220:223],v[160:163],v[68:71]
t a[208:209],v197,0
t a[210:211],v197,8192
m v[84:87],a[220:223],v[176:179],v[84:87]
t a[212:213],v198,0
t a[214:215],v198,8192
m v[60:63],a[224:227],v[144:147],v[60:63]
t a[216:217],v199,0
t a[218:219],v199,8192
m v[76:79],a[224:227],v[160:163],v[76:79]
v_or3_b32 v228,v100,v101,v102
v_or3_b32 v228,v228,v103,v108
m v[92:95],a[224:227],v[176:179],v[92:95]
v_or3_b32 v228,v228,v109,v110
v_or3_b32 v228,v228,v111,v116
m v[52:55],a[228:231],v[152:155],v[52:55]
v_or3_b32 v228,v228,v117,v118
v_or_b32 v228,v228,v119
m v[68:71],a[228:231],v[168:171],v[68:71]
v_and_b32 v228,0x40004000,v228
m v[84:87],a[228:231],v[184:187],v[84:87]
m v[60:63],a[232:235],v[152:155],v[60:63]
m v[76:79],a[232:235],v[168:171],v[76:79]
m v[92:95],a[232:235],v[184:187],v[92:95]
v_cmp_ne_u32_e32 vcc,0,v228
s_nop 0
s_cbranch_vccnz LA_stub7%=
LA_ret7%=:
s_waitcnt lgkmcnt(0)
m a[192:195],v[148:151],v[100:103],a[192:195]
t a[220:221],v200,0
t a[222:223],v200,8192
m a[196:199],v[148:151],v[108:111],a[196:199]
t a[224:225],v201,0
t a[226:227],v201,8192
m a[200:203],v[148:151],v[116:119],a[200:203]
t a[228:229],v202,0
t a[230:231],v202,8192
m a[0:3],a[204:207],v[100:103],a[0:3]
t a[232:233],v203,0
t a[234:235],v203,8192
m a[64:67],a[204:207],v[108:111],a[64:67]
v_exp_f32 v156,v52
m a[128:131],a[204:207],v[116:119],a[128:131]
v_exp_f32 v157,v53
m a[4:7],a[208:211],v[100:103],a[4:7]
v_exp_f32 v158,v54
m a[68:71],a[208:211],v[108:111],a[68:71]
v_exp_f32 v159,v55
m a[132:135],a[208:211],v[116:119],a[132:135]
m a[8:11],a[212:215],v[100:103],a[8:11]
m a[72:75],a[212:215],v[108:111],a[72:75]
m a[136:139],a[212:215],v[116:119],a[136:139]
m a[12:15],a[216:219],v[100:103],a[12:15]
m a[76:79],a[216:219],v[108:111],a[76:79]
m a[140:143],a[216:219],v[116:119],a[140:143]
s_waitcnt lgkmcnt(0)
m a[16:19],a[220:223],v[100:103],a[16:19]
t a[204:205],v196,256
t a[206:207],v196,8448
m a[80:83],a[220:223],v[108:111],a[80:83]
t a[208:209],v197,256
t a[210:211],v197,8448
m a[144:147],a[220:223],v[116:119],a[144:147]
t a[212:213],v198,256
t a[214:215],v198,8448
m a[20:23],a[224:227],v[100:103],a[20:23]
t a[216:217],v199,256
t a[218:219],v199,8448
m a[84:87],a[224:227],v[108:111],a[84:87]
s_mov_b32 s41,s40
s_add_u32 s40,s40,0x8000
m a[148:151],a[224:227],v[116:119],a[148:151]
s_cmp_eq_u32 s40,0x28000
s_cselect_b32 s42,s49,s48
s_cselect_b32 s40,0,s40
m a[24:27],a[228:231],v[100:103],a[24:27]
v_add_u32 v192,s42,v192
v_add_u32 v193,s42,v193
m a[88:91],a[228:231],v[108:111],a[88:91]
v_add_u32 v194,s42,v194
v_add_u32 v195,s42,v195
m a[152:155],a[228:231],v[116:119],a[152:155]
v_exp_f32 v164,v60
m a[28:31],a[232:235],v[100:103],a[28:31]
v_exp_f32 v165,v61
m a[92:95],a[232:235],v[108:111],a[92:95]
v_exp_f32 v166,v62
m a[156:159],a[232:235],v[116:119],a[156:159]
v_exp_f32 v167,v63
s_waitcnt lgkmcnt(0)
m a[32:35],a[204:207],v[100:103],a[32:35]
t a[220:221],v200,256
t a[222:223],v200,8448
m a[96:99],a[204:207],v[108:111],a[96:99]
t a[224:225],v201,256
t a[226:227],v201,8448
m a[160:163],a[204:207],v[116:119],a[160:163]
t a[228:229],v202,256
t a[230:231],v202,8448
m a[36:39],a[208:211],v[100:103],a[36:39]
t a[232:233],v203,256
t a[234:235],v203,8448
m a[100:103],a[208:211],v[108:111],a[100:103]
v_add_u32 v196,s42,v196
v_add_u32 v197,s42,v197
m a[164:167],a[208:211],v[116:119],a[164:167]
v_add_u32 v198,s42,v198
v_add_u32 v199,s42,v199
m a[40:43],a[212:215],v[100:103],a[40:43]
v_add_u32 v200,s42,v200
v_add_u32 v201,s42,v201
m a[104:107],a[212:215],v[108:111],a[104:107]
v_add_u32 v202,s42,v202
v_add_u32 v203,s42,v203
m a[168:171],a[212:215],v[116:119],a[168:171]
v_exp_f32 v172,v68
m a[44:47],a[216:219],v[100:103],a[44:47]
v_exp_f32 v173,v69
m a[108:111],a[216:219],v[108:111],a[108:111]
m a[172:175],a[216:219],v[116:119],a[172:175]
s_waitcnt lgkmcnt(0)
s_barrier
m a[48:51],a[220:223],v[100:103],a[48:51]
t a[204:205],v196,0
t a[206:207],v196,8192
m a[112:115],a[220:223],v[108:111],a[112:115]
t a[208:209],v197,0
t a[210:211],v197,8192
m a[176:179],a[220:223],v[116:119],a[176:179]
t a[212:213],v198,0
t a[214:215],v198,8192
m a[52:55],a[224:227],v[100:103],a[52:55]
t a[216:217],v199,0
t a[218:219],v199,8192
m a[116:119],a[224:227],v[108:111],a[116:119]
v_exp_f32 v174,v70
m a[180:183],a[224:227],v[116:119],a[180:183]
v_exp_f32 v175,v71
m a[56:59],a[228:231],v[100:103],a[56:59]
v_exp_f32 v180,v76
m a[120:123],a[228:231],v[108:111],a[120:123]
v_exp_f32 v181,v77
m a[184:187],a[228:231],v[116:119],a[184:187]
v_exp_f32 v182,v78
m a[60:63],a[232:235],v[100:103],a[60:63]
v_exp_f32 v183,v79
m a[124:127],a[232:235],v[108:111],a[124:127]
m a[188:191],a[232:235],v[116:119],a[188:191]
v_exp_f32 v220,v84
v_exp_f32 v221,v85
cv v100,v156,v157
cv v101,v158,v159
v_exp_f32 v222,v86
v_exp_f32 v223,v87
cv v116,v220,v221
cv v102,v164,v165
cv v103,v166,v167
v_exp_f32 v224,v92
v_exp_f32 v225,v93
cv v117,v222,v223
cv v108,v172,v173
cv v109,v174,v175
v_exp_f32 v220,v94
v_exp_f32 v221,v95
cv v118,v224,v225
cv v110,v180,v181
cv v111,v182,v183
cv v119,v220,v221
v_or3_b32 v228,v100,v101,v102
v_or3_b32 v228,v228,v103,v108
v_or3_b32 v228,v228,v109,v110
v_or3_b32 v228,v228,v111,v116
v_or3_b32 v228,v228,v117,v118
v_or_b32 v228,v228,v119
v_and_b32 v228,0x40004000,v228
v_cmp_ne_u32_e32 vcc,0,v228
s_nop 0
s_cbranch_vccnz LA_stub8%=
LA_ret8%=:
s_waitcnt lgkmcnt(0)
m a[192:195],v[148:151],v[100:103],a[192:195]
t a[220:221],v200,0
t a[222:223],v200,8192
m a[196:199],v[148:151],v[108:111],a[196:199]
t a[224:225],v201,0
t a[226:227],v201,8192
m a[200:203],v[148:151],v[116:119],a[200:203]
t a[228:229],v202,0
t a[230:231],v202,8192
m a[0:3],a[204:207],v[100:103],a[0:3]
t a[232:233],v203,0
t a[234:235],v203,8192
m a[64:67],a[204:207],v[108:111],a[64:67]
m a[128:131],a[204:207],v[116:119],a[128:131]
m a[4:7],a[208:211],v[100:103],a[4:7]
m a[68:71],a[208:211],v[108:111],a[68:71]
m a[132:135],a[208:211],v[116:119],a[132:135]
m a[8:11],a[212:215],v[100:103],a[8:11]
m a[72:75],a[212:215],v[108:111],a[72:75]
m a[136:139],a[212:215],v[116:119],a[136:139]
m a[12:15],a[216:219],v[100:103],a[12:15]
m a[76:79],a[216:219],v[108:111],a[76:79]
m a[140:143],a[216:219],v[116:119],a[140:143]
s_waitcnt lgkmcnt(0)
m a[16:19],a[220:223],v[100:103],a[16:19]
t a[204:205],v196,256
t a[206:207],v196,8448
t a[208:209],v197,256
m a[80:83],a[220:223],v[108:111],a[80:83]
t a[210:211],v197,8448
t a[212:213],v198,256
t a[214:215],v198,8448
m a[144:147],a[220:223],v[116:119],a[144:147]
t a[216:217],v199,256
t a[218:219],v199,8448
ar v216,a192
ar v217,a196
m a[20:23],a[224:227],v[100:103],a[20:23]
ar v218,a200
v_rcp_f32 v216,v216
m a[84:87],a[224:227],v[108:111],a[84:87]
v_rcp_f32 v217,v217
v_rcp_f32 v218,v218
m a[148:151],a[224:227],v[116:119],a[148:151]
ar v144,a0
ar v145,a1
ar v146,a2
m a[24:27],a[228:231],v[100:103],a[24:27]
ar v147,a3
ar v148,a4
ar v149,a5
m a[88:91],a[228:231],v[108:111],a[88:91]
ar v150,a6
ar v151,a7
cv v0,v144,v145
cv v1,v146,v147
m a[152:155],a[228:231],v[116:119],a[152:155]
cv v2,v148,v149
cv v3,v150,v151
ar v152,a64
m a[28:31],a[232:235],v[100:103],a[28:31]
ar v153,a65
ar v154,a66
ar v155,a67
ar v156,a68
m a[92:95],a[232:235],v[108:111],a[92:95]
ar v157,a69
ar v158,a70
ar v159,a71
m a[156:159],a[232:235],v[116:119],a[156:159]
cv v32,v152,v153
cv v33,v154,v155
cv v34,v156,v157
s_waitcnt lgkmcnt(0)
m a[32:35],a[204:207],v[100:103],a[32:35]
t a[220:221],v200,256
t a[222:223],v200,8448
t a[224:225],v201,256
m a[96:99],a[204:207],v[108:111],a[96:99]
t a[226:227],v201,8448
t a[228:229],v202,256
t a[230:231],v202,8448
m a[160:163],a[204:207],v[116:119],a[160:163]
t a[232:233],v203,256
t a[234:235],v203,8448
cv v35,v158,v159
m a[36:39],a[208:211],v[100:103],a[36:39]
ar v144,a128
ar v145,a129
ar v146,a130
m a[100:103],a[208:211],v[108:111],a[100:103]
ar v147,a131
ar v148,a132
ar v149,a133
m a[164:167],a[208:211],v[116:119],a[164:167]
ar v150,a134
ar v151,a135
cv v64,v144,v145
m a[40:43],a[212:215],v[100:103],a[40:43]
cv v65,v146,v147
cv v66,v148,v149
cv v67,v150,v151
m a[104:107],a[212:215],v[108:111],a[104:107]
ar v152,a8
ar v153,a9
ar v154,a10
m a[168:171],a[212:215],v[116:119],a[168:171]
ar v155,a11
ar v156,a12
ar v157,a13
m a[44:47],a[216:219],v[100:103],a[44:47]
ar v158,a14
ar v159,a15
cv v4,v152,v153
m a[108:111],a[216:219],v[108:111],a[108:111]
cv v5,v154,v155
cv v6,v156,v157
cv v7,v158,v159
m a[172:175],a[216:219],v[116:119],a[172:175]
ar v144,a72
ar v145,a73
ar v146,a74
ar v147,a75
s_waitcnt lgkmcnt(0)
s_barrier
m a[48:51],a[220:223],v[100:103],a[48:51]
t a[204:205],v208,0
t a[206:207],v208,2048
t a[208:209],v209,0
m a[112:115],a[220:223],v[108:111],a[112:115]
t a[210:211],v209,2048
t a[212:213],v210,0
t a[214:215],v210,2048
m a[176:179],a[220:223],v[116:119],a[176:179]
t a[216:217],v211,0
t a[218:219],v211,2048
ar v148,a76
m a[52:55],a[224:227],v[100:103],a[52:55]
ar v149,a77
ar v150,a78
ar v151,a79
m a[116:119],a[224:227],v[108:111],a[116:119]
cv v36,v144,v145
cv v37,v146,v147
cv v38,v148,v149
m a[180:183],a[224:227],v[116:119],a[180:183]
cv v39,v150,v151
ar v152,a136
ar v153,a137
ar v154,a138
m a[56:59],a[228:231],v[100:103],a[56:59]
ar v155,a139
ar v156,a140
ar v157,a141
m a[120:123],a[228:231],v[108:111],a[120:123]
ar v158,a142
ar v159,a143
cv v68,v152,v153
m a[184:187],a[228:231],v[116:119],a[184:187]
cv v69,v154,v155
cv v70,v156,v157
cv v71,v158,v159
m a[60:63],a[232:235],v[100:103],a[60:63]
ar v144,a16
ar v145,a17
ar v146,a18
m a[124:127],a[232:235],v[108:111],a[124:127]
ar v147,a19
ar v148,a20
ar v149,a21
m a[188:191],a[232:235],v[116:119],a[188:191]
ar v150,a22
ar v151,a23
cv v8,v144,v145
cv v9,v146,v147
s_waitcnt lgkmcnt(0)
m v[96:99],a[204:207],v[0:3],0
t a[220:221],v208,4096
t a[222:223],v208,6144
t a[224:225],v209,4096
m v[112:115],a[204:207],v[32:35],0
t a[226:227],v209,6144
t a[228:229],v210,4096
t a[230:231],v210,6144
m v[128:131],a[204:207],v[64:67],0
t a[232:233],v211,4096
t a[234:235],v211,6144
cv v10,v148,v149
m v[100:103],a[208:211],v[0:3],0
cv v11,v150,v151
ar v152,a80
ar v153,a81
m v[116:119],a[208:211],v[32:35],0
ar v154,a82
ar v155,a83
ar v156,a84
m v[132:135],a[208:211],v[64:67],0
ar v157,a85
ar v158,a86
ar v159,a87
m v[104:107],a[212:215],v[0:3],0
cv v40,v152,v153
cv v41,v154,v155
cv v42,v156,v157
m v[120:123],a[212:215],v[32:35],0
cv v43,v158,v159
ar v144,a144
ar v145,a145
m v[136:139],a[212:215],v[64:67],0
ar v146,a146
ar v147,a147
ar v148,a148
m v[108:111],a[216:219],v[0:3],0
ar v149,a149
ar v150,a150
ar v151,a151
m v[124:127],a[216:219],v[32:35],0
cv v72,v144,v145
cv v73,v146,v147
cv v74,v148,v149
m v[140:143],a[216:219],v[64:67],0
cv v75,v150,v151
ar v152,a24
ar v153,a25
ar v154,a26
s_waitcnt lgkmcnt(0)
m v[96:99],a[220:223],v[4:7],v[96:99]
t a[204:205],v208,8192
t a[206:207],v208,10240
t a[208:209],v209,8192
m v[112:115],a[220:223],v[36:39],v[112:115]
t a[210:211],v209,10240
t a[212:213],v210,8192
t a[214:215],v210,10240
m v[128:131],a[220:223],v[68:71],v[128:131]
t a[216:217],v211,8192
t a[218:219],v211,10240
ar v155,a27
m v[100:103],a[224:227],v[4:7],v[100:103]
ar v156,a28
ar v157,a29
ar v158,a30
m v[116:119],a[224:227],v[36:39],v[116:119]
ar v159,a31
cv v12,v152,v153
cv v13,v154,v155
m v[132:135],a[224:227],v[68:71],v[132:135]
cv v14,v156,v157
cv v15,v158,v159
ar v144,a88
m v[104:107],a[228:231],v[4:7],v[104:107]
ar v145,a89
ar v146,a90
ar v147,a91
m v[120:123],a[228:231],v[36:39],v[120:123]
ar v148,a92
ar v149,a93
ar v150,a94
m v[136:139],a[228:231],v[68:71],v[136:139]
ar v151,a95
cv v44,v144,v145
cv v45,v146,v147
m v[108:111],a[232:235],v[4:7],v[108:111]
cv v46,v148,v149
cv v47,v150,v151
ar v152,a152
m v[124:127],a[232:235],v[36:39],v[124:127]
ar v153,a153
ar v154,a154
ar v155,a155
m v[140:143],a[232:235],v[68:71],v[140:143]
ar v156,a156
ar v157,a157
ar v158,a158
ar v159,a159
s_waitcnt lgkmcnt(0)
m v[96:99],a[204:207],v[8:11],v[96:99]
t a[220:221],v208,12288
t a[222:223],v208,14336
t a[224:225],v209,12288
m v[112:115],a[204:207],v[40:43],v[112:115]
t a[226:227],v209,14336
t a[228:229],v210,12288
t a[230:231],v210,14336
m v[128:131],a[204:207],v[72:75],v[128:131]
t a[232:233],v211,12288
t a[234:235],v211,14336
cv v76,v152,v153
m v[100:103],a[208:211],v[8:11],v[100:103]
cv v77,v154,v155
cv v78,v156,v157
cv v79,v158,v159
m v[116:119],a[208:211],v[40:43],v[116:119]
ar v144,a32
ar v145,a33
ar v146,a34
m v[132:135],a[208:211],v[72:75],v[132:135]
ar v147,a35
ar v148,a36
ar v149,a37
ar v150,a38
m v[104:107],a[212:215],v[8:11],v[104:107]
ar v151,a39
cv v16,v144,v145
cv v17,v146,v147
m v[120:123],a[212:215],v[40:43],v[120:123]
cv v18,v148,v149
cv v19,v150,v151
ar v152,a96
m v[136:139],a[212:215],v[72:75],v[136:139]
ar v153,a97
ar v154,a98
ar v155,a99
m v[108:111],a[216:219],v[8:11],v[108:111]
ar v156,a100
ar v157,a101
ar v158,a102
m v[124:127],a[216:219],v[40:43],v[124:127]
ar v159,a103
cv v48,v152,v153
cv v49,v154,v155
m v[140:143],a[216:219],v[72:75],v[140:143]
cv v50,v156,v157
cv v51,v158,v159
ar v144,a160
ar v145,a161
s_waitcnt lgkmcnt(0)
m v[96:99],a[220:223],v[12:15],v[96:99]
t a[204:205],v208,16384
t a[206:207],v208,18432
t a[208:209],v209,16384
m v[112:115],a[220:223],v[44:47],v[112:115]
t a[210:211],v209,18432
t a[212:213],v210,16384
t a[214:215],v210,18432
m v[128:131],a[220:223],v[76:79],v[128:131]
t a[216:217],v211,16384
t a[218:219],v211,18432
ar v146,a162
m v[100:103],a[224:227],v[12:15],v[100:103]
ar v147,a163
ar v148,a164
ar v149,a165
)ASM"
R"ASM(m v[116:119],a[224:227],v[44:47],v[116:119]
ar v150,a166
ar v151,a167
cv v80,v144,v145
m v[132:135],a[224:227],v[76:79],v[132:135]
cv v81,v146,v147
cv v82,v148,v149
cv v83,v150,v151
m v[104:107],a[228:231],v[12:15],v[104:107]
ar v152,a40
ar v153,a41
ar v154,a42
m v[120:123],a[228:231],v[44:47],v[120:123]
ar v155,a43
ar v156,a44
ar v157,a45
m v[136:139],a[228:231],v[76:79],v[136:139]
ar v158,a46
ar v159,a47
cv v20,v152,v153
m v[108:111],a[232:235],v[12:15],v[108:111]
cv v21,v154,v155
cv v22,v156,v157
cv v23,v158,v159
m v[124:127],a[232:235],v[44:47],v[124:127]
ar v144,a104
ar v145,a105
ar v146,a106
m v[140:143],a[232:235],v[76:79],v[140:143]
ar v147,a107
ar v148,a108
ar v149,a109
ar v150,a110
s_waitcnt lgkmcnt(0)
m v[96:99],a[204:207],v[16:19],v[96:99]
t a[220:221],v208,20480
t a[222:223],v208,22528
t a[224:225],v209,20480
m v[112:115],a[204:207],v[48:51],v[112:115]
t a[226:227],v209,22528
t a[228:229],v210,20480
t a[230:231],v210,22528
m v[128:131],a[204:207],v[80:83],v[128:131]
t a[232:233],v211,20480
t a[234:235],v211,22528
ar v151,a111
m v[100:103],a[208:211],v[16:19],v[100:103]
cv v52,v144,v145
cv v53,v146,v147
cv v54,v148,v149
m v[116:119],a[208:211],v[48:51],v[116:119]
cv v55,v150,v151
ar v152,a168
ar v153,a169
m v[132:135],a[208:211],v[80:83],v[132:135]
ar v154,a170
ar v155,a171
ar v156,a172
ar v157,a173
m v[104:107],a[212:215],v[16:19],v[104:107]
ar v158,a174
ar v159,a175
cv v84,v152,v153
m v[120:123],a[212:215],v[48:51],v[120:123]
cv v85,v154,v155
cv v86,v156,v157
cv v87,v158,v159
m v[136:139],a[212:215],v[80:83],v[136:139]
ar v144,a48
ar v145,a49
ar v146,a50
m v[108:111],a[216:219],v[16:19],v[108:111]
ar v147,a51
ar v148,a52
ar v149,a53
m v[124:127],a[216:219],v[48:51],v[124:127]
ar v150,a54
ar v151,a55
cv v24,v144,v145
m v[140:143],a[216:219],v[80:83],v[140:143]
cv v25,v146,v147
cv v26,v148,v149
cv v27,v150,v151
ar v152,a112
s_waitcnt lgkmcnt(0)
m v[96:99],a[220:223],v[20:23],v[96:99]
t a[204:205],v208,24576
t a[206:207],v208,26624
t a[208:209],v209,24576
m v[112:115],a[220:223],v[52:55],v[112:115]
t a[210:211],v209,26624
t a[212:213],v210,24576
t a[214:215],v210,26624
m v[128:131],a[220:223],v[84:87],v[128:131]
t a[216:217],v211,24576
t a[218:219],v211,26624
ar v153,a113
m v[100:103],a[224:227],v[20:23],v[100:103]
ar v154,a114
ar v155,a115
ar v156,a116
m v[116:119],a[224:227],v[52:55],v[116:119]
ar v157,a117
ar v158,a118
ar v159,a119
m v[132:135],a[224:227],v[84:87],v[132:135]
cv v56,v152,v153
cv v57,v154,v155
cv v58,v156,v157
m v[104:107],a[228:231],v[20:23],v[104:107]
cv v59,v158,v159
ar v144,a176
ar v145,a177
m v[120:123],a[228:231],v[52:55],v[120:123]
ar v146,a178
ar v147,a179
ar v148,a180
m v[136:139],a[228:231],v[84:87],v[136:139]
ar v149,a181
ar v150,a182
ar v151,a183
m v[108:111],a[232:235],v[20:23],v[108:111]
cv v88,v144,v145
cv v89,v146,v147
cv v90,v148,v149
m v[124:127],a[232:235],v[52:55],v[124:127]
cv v91,v150,v151
ar v152,a56
ar v153,a57
m v[140:143],a[232:235],v[84:87],v[140:143]
ar v154,a58
ar v155,a59
ar v156,a60
ar v157,a61
s_waitcnt lgkmcnt(0)
m v[96:99],a[204:207],v[24:27],v[96:99]
t a[220:221],v208,28672
t a[222:223],v208,30720
t a[224:225],v209,28672
m v[112:115],a[204:207],v[56:59],v[112:115]
t a[226:227],v209,30720
t a[228:229],v210,28672
t a[230:231],v210,30720
t a[232:233],v211,28672
m v[128:131],a[204:207],v[88:91],v[128:131]
t a[234:235],v211,30720
v_add_u32 v208,s48,v208
v_add_u32 v209,s48,v209
m v[100:103],a[208:211],v[24:27],v[100:103]
v_add_u32 v210,s48,v210
v_add_u32 v211,s48,v211
ar v158,a62
ar v159,a63
m v[116:119],a[208:211],v[56:59],v[116:119]
cv v28,v152,v153
cv v29,v154,v155
cv v30,v156,v157
m v[132:135],a[208:211],v[88:91],v[132:135]
cv v31,v158,v159
ar v144,a120
ar v145,a121
ar v146,a122
m v[104:107],a[212:215],v[24:27],v[104:107]
ar v147,a123
ar v148,a124
ar v149,a125
m v[120:123],a[212:215],v[56:59],v[120:123]
ar v150,a126
ar v151,a127
cv v60,v144,v145
cv v61,v146,v147
m v[136:139],a[212:215],v[88:91],v[136:139]
cv v62,v148,v149
cv v63,v150,v151
ar v152,a184
m v[108:111],a[216:219],v[24:27],v[108:111]
ar v153,a185
ar v154,a186
ar v155,a187
ar v156,a188
m v[124:127],a[216:219],v[56:59],v[124:127]
ar v157,a189
ar v158,a190
ar v159,a191
m v[140:143],a[216:219],v[88:91],v[140:143]
cv v92,v152,v153
cv v93,v154,v155
cv v94,v156,v157
cv v95,v158,v159
s_waitcnt lgkmcnt(0)
s_barrier
m v[96:99],a[220:223],v[28:31],v[96:99]
t a[204:205],v208,0
t a[206:207],v208,2048
m v[112:115],a[220:223],v[60:63],v[112:115]
t a[208:209],v209,0
t a[210:211],v209,2048
m v[128:131],a[220:223],v[92:95],v[128:131]
t a[212:213],v210,0
t a[214:215],v210,2048
m v[100:103],a[224:227],v[28:31],v[100:103]
t a[216:217],v211,0
t a[218:219],v211,2048
m v[116:119],a[224:227],v[60:63],v[116:119]
m v[132:135],a[224:227],v[92:95],v[132:135]
m v[104:107],a[228:231],v[28:31],v[104:107]
m v[120:123],a[228:231],v[60:63],v[120:123]
m v[136:139],a[228:231],v[92:95],v[136:139]
m v[108:111],a[232:235],v[28:31],v[108:111]
m v[124:127],a[232:235],v[60:63],v[124:127]
m v[140:143],a[232:235],v[92:95],v[140:143]
s_waitcnt lgkmcnt(0)
m v[144:147],a[204:207],v[0:3],0
t a[220:221],v208,4096
t a[222:223],v208,6144
m v[160:163],a[204:207],v[32:35],0
t a[224:225],v209,4096
t a[226:227],v209,6144
m v[176:179],a[204:207],v[64:67],0
t a[228:229],v210,4096
t a[230:231],v210,6144
m v[148:151],a[208:211],v[0:3],0
t a[232:233],v211,4096
t a[234:235],v211,6144
m v[164:167],a[208:211],v[32:35],0
v_lshrrev_b32 v235,4,v214
v_bfe_u32 v236,v214,2,2
m v[180:183],a[208:211],v[64:67],0
v_cmp_eq_u32_e32 vcc,v235,v236
v_and_b32 v237,1,v214
m v[152:155],a[212:215],v[0:3],0
m v[168:171],a[212:215],v[32:35],0
m v[184:187],a[212:215],v[64:67],0
m v[156:159],a[216:219],v[0:3],0
m v[172:175],a[216:219],v[32:35],0
m v[188:191],a[216:219],v[64:67],0
s_waitcnt lgkmcnt(0)
m v[144:147],a[220:223],v[4:7],v[144:147]
t a[204:205],v208,8192
t a[206:207],v208,10240
m v[160:163],a[220:223],v[36:39],v[160:163]
t a[208:209],v209,8192
t a[210:211],v209,10240
t a[212:213],v210,8192
m v[176:179],a[220:223],v[68:71],v[176:179]
t a[214:215],v210,10240
t a[216:217],v211,8192
t a[218:219],v211,10240
m v[148:151],a[224:227],v[4:7],v[148:151]
s_add_u32 s55,s54,0
s_add_u32 s59,s55,0x1000
m v[164:167],a[224:227],v[36:39],v[164:167]
v_mul_f32 v96,v96,v216
v_mul_f32 v97,v97,v216
v_mul_f32 v98,v98,v216
m v[180:183],a[224:227],v[68:71],v[180:183]
v_mul_f32 v99,v99,v216
v_mul_f32 v100,v100,v216
v_mul_f32 v101,v101,v216
m v[152:155],a[228:231],v[4:7],v[152:155]
v_mul_f32 v102,v102,v216
v_mul_f32 v103,v103,v216
m v[168:171],a[228:231],v[36:39],v[168:171]
cv v96,v96,v97
cv v97,v98,v99
cv v98,v100,v101
m v[184:187],a[228:231],v[68:71],v[184:187]
cv v99,v102,v103
buffer_store_dwordx4 v[96:99],v215,%5,s55 offen sc1
v_mul_f32 v104,v104,v216
m v[156:159],a[232:235],v[4:7],v[156:159]
v_mul_f32 v105,v105,v216
v_mul_f32 v106,v106,v216
m v[172:175],a[232:235],v[36:39],v[172:175]
v_mul_f32 v107,v107,v216
v_mul_f32 v108,v108,v216
v_lshlrev_b32 v237,4,v237
m v[188:191],a[232:235],v[68:71],v[188:191]
v_mov_b32 v238,0x3f80
v_lshlrev_b32 v238,v237,v238
v_cndmask_b32_e32 v238,0,v238,vcc
s_waitcnt lgkmcnt(0)
m v[144:147],a[204:207],v[8:11],v[144:147]
t a[220:221],v208,12288
t a[222:223],v208,14336
m v[160:163],a[204:207],v[40:43],v[160:163]
t a[224:225],v209,12288
t a[226:227],v209,14336
t a[228:229],v210,12288
m v[176:179],a[204:207],v[72:75],v[176:179]
t a[230:231],v210,14336
t a[232:233],v211,12288
t a[234:235],v211,14336
m v[148:151],a[208:211],v[8:11],v[148:151]
v_mul_f32 v109,v109,v216
v_mul_f32 v110,v110,v216
m v[164:167],a[208:211],v[40:43],v[164:167]
v_mul_f32 v111,v111,v216
cv v104,v104,v105
cv v105,v106,v107
m v[180:183],a[208:211],v[72:75],v[180:183]
cv v106,v108,v109
cv v107,v110,v111
buffer_store_dwordx4 v[104:107],v215,%5,s55 offen offset:1024 sc1
m v[152:155],a[212:215],v[8:11],v[152:155]
v_mul_f32 v112,v112,v217
v_mul_f32 v113,v113,v217
m v[168:171],a[212:215],v[40:43],v[168:171]
v_mul_f32 v114,v114,v217
v_mul_f32 v115,v115,v217
v_mul_f32 v116,v116,v217
m v[184:187],a[212:215],v[72:75],v[184:187]
v_mul_f32 v117,v117,v217
v_mul_f32 v118,v118,v217
v_mul_f32 v119,v119,v217
m v[156:159],a[216:219],v[8:11],v[156:159]
cv v112,v112,v113
cv v113,v114,v115
m v[172:175],a[216:219],v[40:43],v[172:175]
cv v114,v116,v117
cv v115,v118,v119
v_bfe_u32 v237,v214,1,1
m v[188:191],a[216:219],v[72:75],v[188:191]
v_cmp_eq_u32_e32 vcc,0,v237
s_nop 1
v_cndmask_b32_e32 v222,0,v238,vcc
v_sub_u32 v223,v238,v222
s_waitcnt lgkmcnt(0)
m v[144:147],a[220:223],v[12:15],v[144:147]
t a[204:205],v208,16384
t a[206:207],v208,18432
m v[160:163],a[220:223],v[44:47],v[160:163]
t a[208:209],v209,16384
t a[210:211],v209,18432
t a[212:213],v210,16384
m v[176:179],a[220:223],v[76:79],v[176:179]
t a[214:215],v210,18432
t a[216:217],v211,16384
t a[218:219],v211,18432
m v[148:151],a[224:227],v[12:15],v[148:151]
buffer_store_dwordx4 v[112:115],v215,%5,s55 offen offset:2048 sc1
v_mul_f32 v120,v120,v217
v_mul_f32 v121,v121,v217
m v[164:167],a[224:227],v[44:47],v[164:167]
v_mul_f32 v122,v122,v217
v_mul_f32 v123,v123,v217
m v[180:183],a[224:227],v[76:79],v[180:183]
v_mul_f32 v124,v124,v217
v_mul_f32 v125,v125,v217
v_mul_f32 v126,v126,v217
m v[152:155],a[228:231],v[12:15],v[152:155]
v_mul_f32 v127,v127,v217
cv v120,v120,v121
cv v121,v122,v123
m v[168:171],a[228:231],v[44:47],v[168:171]
cv v122,v124,v125
cv v123,v126,v127
buffer_store_dwordx4 v[120:123],v215,%5,s55 offen offset:3072 sc1
m v[184:187],a[228:231],v[76:79],v[184:187]
v_mul_f32 v128,v128,v218
v_mul_f32 v129,v129,v218
m v[156:159],a[232:235],v[12:15],v[156:159]
v_mul_f32 v130,v130,v218
v_mul_f32 v131,v131,v218
v_mul_f32 v132,v132,v218
m v[172:175],a[232:235],v[44:47],v[172:175]
v_mul_f32 v133,v133,v218
v_mov_b32 v220,0
v_mov_b32 v221,0
m v[188:191],a[232:235],v[76:79],v[188:191]
v_mov_b32 v224,0
v_mov_b32 v225,0
s_lshl_b32 s46,%11,3
s_waitcnt lgkmcnt(0)
m v[144:147],a[204:207],v[16:19],v[144:147]
t a[220:221],v208,20480
t a[222:223],v208,22528
m v[160:163],a[204:207],v[48:51],v[160:163]
t a[224:225],v209,20480
t a[226:227],v209,22528
t a[228:229],v210,20480
m v[176:179],a[204:207],v[80:83],v[176:179]
t a[230:231],v210,22528
t a[232:233],v211,20480
t a[234:235],v211,22528
m v[148:151],a[208:211],v[16:19],v[148:151]
v_mul_f32 v134,v134,v218
v_mul_f32 v135,v135,v218
m v[164:167],a[208:211],v[48:51],v[164:167]
cv v128,v128,v129
cv v129,v130,v131
cv v130,v132,v133
m v[180:183],a[208:211],v[80:83],v[180:183]
cv v131,v134,v135
buffer_store_dwordx4 v[128:131],v215,%5,s59 offen sc1
v_mul_f32 v136,v136,v218
m v[152:155],a[212:215],v[16:19],v[152:155]
v_mul_f32 v137,v137,v218
v_mul_f32 v138,v138,v218
m v[168:171],a[212:215],v[48:51],v[168:171]
v_mul_f32 v139,v139,v218
v_mul_f32 v140,v140,v218
v_mul_f32 v141,v141,v218
m v[184:187],a[212:215],v[80:83],v[184:187]
v_mul_f32 v142,v142,v218
v_mul_f32 v143,v143,v218
cv v136,v136,v137
m v[156:159],a[216:219],v[16:19],v[156:159]
cv v137,v138,v139
cv v138,v140,v141
m v[172:175],a[216:219],v[48:51],v[172:175]
cv v139,v142,v143
buffer_store_dwordx4 v[136:139],v215,%5,s59 offen offset:1024 sc1
v_lshlrev_b32 v234,7,v214
m v[188:191],a[216:219],v[80:83],v[188:191]
v_add_u32 v234,s46,v234
v_add_u32 v234,4,v234
v_cmp_gt_u32_e32 vcc,16,v214
s_waitcnt lgkmcnt(0)
m v[144:147],a[220:223],v[20:23],v[144:147]
t a[204:205],v208,24576
t a[206:207],v208,26624
m v[160:163],a[220:223],v[52:55],v[160:163]
t a[208:209],v209,24576
t a[210:211],v209,26624
m v[176:179],a[220:223],v[84:87],v[176:179]
t a[212:213],v210,24576
t a[214:215],v210,26624
m v[148:151],a[224:227],v[20:23],v[148:151]
t a[216:217],v211,24576
t a[218:219],v211,26624
m v[164:167],a[224:227],v[52:55],v[164:167]
v_mov_b32 v235,0x7fffff00
v_cndmask_b32_e32 v230,v235,v234,vcc
m v[180:183],a[224:227],v[84:87],v[180:183]
s_lshl_b32 s46,%11,7
v_and_b32 v234,15,v214
m v[152:155],a[228:231],v[20:23],v[152:155]
m v[168:171],a[228:231],v[52:55],v[168:171]
m v[184:187],a[228:231],v[84:87],v[184:187]
m v[156:159],a[232:235],v[20:23],v[156:159]
m v[172:175],a[232:235],v[52:55],v[172:175]
m v[188:191],a[232:235],v[84:87],v[188:191]
s_waitcnt lgkmcnt(0)
m v[144:147],a[204:207],v[24:27],v[144:147]
t a[220:221],v208,28672
t a[222:223],v208,30720
m v[160:163],a[204:207],v[56:59],v[160:163]
t a[224:225],v209,28672
t a[226:227],v209,30720
m v[176:179],a[204:207],v[88:91],v[176:179]
t a[228:229],v210,28672
t a[230:231],v210,30720
m v[148:151],a[208:211],v[24:27],v[148:151]
t a[232:233],v211,28672
t a[234:235],v211,30720
m v[164:167],a[208:211],v[56:59],v[164:167]
v_add_u32 v208,s48,v208
v_add_u32 v209,s48,v209
m v[180:183],a[208:211],v[88:91],v[180:183]
v_add_u32 v210,s48,v210
v_add_u32 v211,s48,v211
m v[152:155],a[212:215],v[24:27],v[152:155]
v_lshlrev_b32 v234,3,v234
v_add_u32 v234,4,v234
m v[168:171],a[212:215],v[56:59],v[168:171]
v_add_u32 v231,s46,v234
s_mul_i32 s46,%11,0x50
m v[184:187],a[212:215],v[88:91],v[184:187]
m v[156:159],a[216:219],v[24:27],v[156:159]
m v[172:175],a[216:219],v[56:59],v[172:175]
m v[188:191],a[216:219],v[88:91],v[188:191]
s_waitcnt lgkmcnt(0)
s_barrier
m v[144:147],a[220:223],v[28:31],v[144:147]
t a[204:205],v208,0
t a[206:207],v208,2048
m v[160:163],a[220:223],v[60:63],v[160:163]
t a[208:209],v209,0
t a[210:211],v209,2048
m v[176:179],a[220:223],v[92:95],v[176:179]
t a[212:213],v210,0
t a[214:215],v210,2048
m v[148:151],a[224:227],v[28:31],v[148:151]
t a[216:217],v211,0
t a[218:219],v211,2048
m v[164:167],a[224:227],v[60:63],v[164:167]
v_add_u32 v234,s46,v214
v_lshlrev_b32 v226,4,v234
m v[180:183],a[224:227],v[92:95],v[180:183]
v_lshrrev_b32 v227,7,v234
v_lshlrev_b32 v227,4,v227
m v[152:155],a[228:231],v[28:31],v[152:155]
v_and_b32 v235,15,v234
m v[168:171],a[228:231],v[60:63],v[168:171]
m v[184:187],a[228:231],v[92:95],v[184:187]
m v[156:159],a[232:235],v[28:31],v[156:159]
m v[172:175],a[232:235],v[60:63],v[172:175]
m v[188:191],a[232:235],v[92:95],v[188:191]
s_waitcnt lgkmcnt(0)
m v[96:99],a[204:207],v[0:3],0
t a[220:221],v208,4096
t a[222:223],v208,6144
m v[112:115],a[204:207],v[32:35],0
t a[224:225],v209,4096
t a[226:227],v209,6144
m v[128:131],a[204:207],v[64:67],0
t a[228:229],v210,4096
t a[230:231],v210,6144
m v[100:103],a[208:211],v[0:3],0
t a[232:233],v211,4096
t a[234:235],v211,6144
m v[116:119],a[208:211],v[32:35],0
v_add_u32 v227,v227,v235
v_mul_u32_u24 v227,0x700,v227
m v[132:135],a[208:211],v[64:67],0
v_bfe_u32 v235,v234,6,1
v_lshl_add_u32 v227,v235,6,v227
m v[104:107],a[212:215],v[0:3],0
m v[120:123],a[212:215],v[32:35],0
m v[136:139],a[212:215],v[64:67],0
m v[108:111],a[216:219],v[0:3],0
m v[124:127],a[216:219],v[32:35],0
m v[140:143],a[216:219],v[64:67],0
s_waitcnt lgkmcnt(0)
m v[96:99],a[220:223],v[4:7],v[96:99]
t a[204:205],v208,8192
t a[206:207],v208,10240
m v[112:115],a[220:223],v[36:39],v[112:115]
t a[208:209],v209,8192
t a[210:211],v209,10240
t a[212:213],v210,8192
m v[128:131],a[220:223],v[68:71],v[128:131]
t a[214:215],v210,10240
t a[216:217],v211,8192
t a[218:219],v211,10240
m v[100:103],a[224:227],v[4:7],v[100:103]
s_add_u32 s55,s54,0x5000
s_add_u32 s59,s55,0x1000
m v[116:119],a[224:227],v[36:39],v[116:119]
v_mul_f32 v144,v144,v216
v_mul_f32 v145,v145,v216
v_mul_f32 v146,v146,v216
m v[132:135],a[224:227],v[68:71],v[132:135]
v_mul_f32 v147,v147,v216
v_mul_f32 v148,v148,v216
v_mul_f32 v149,v149,v216
m v[104:107],a[228:231],v[4:7],v[104:107]
v_mul_f32 v150,v150,v216
v_mul_f32 v151,v151,v216
m v[120:123],a[228:231],v[36:39],v[120:123]
cv v144,v144,v145
cv v145,v146,v147
cv v146,v148,v149
m v[136:139],a[228:231],v[68:71],v[136:139]
cv v147,v150,v151
buffer_store_dwordx4 v[144:147],v215,%5,s55 offen sc1
v_mul_f32 v152,v152,v216
m v[108:111],a[232:235],v[4:7],v[108:111]
v_mul_f32 v153,v153,v216
v_mul_f32 v154,v154,v216
m v[124:127],a[232:235],v[36:39],v[124:127]
v_mul_f32 v155,v155,v216
v_mul_f32 v156,v156,v216
v_bfe_u32 v235,v234,4,2
m v[140:143],a[232:235],v[68:71],v[140:143]
v_lshl_add_u32 v227,v235,3,v227
v_and_b32 v234,15,v214
v_add_u32 v234,s46,v234
s_waitcnt lgkmcnt(0)
m v[96:99],a[204:207],v[8:11],v[96:99]
t a[220:221],v208,12288
t a[222:223],v208,14336
m v[112:115],a[204:207],v[40:43],v[112:115]
t a[224:225],v209,12288
t a[226:227],v209,14336
t a[228:229],v210,12288
m v[128:131],a[204:207],v[72:75],v[128:131]
t a[230:231],v210,14336
t a[232:233],v211,12288
t a[234:235],v211,14336
m v[100:103],a[208:211],v[8:11],v[100:103]
v_mul_f32 v157,v157,v216
v_mul_f32 v158,v158,v216
m v[116:119],a[208:211],v[40:43],v[116:119]
v_mul_f32 v159,v159,v216
cv v152,v152,v153
cv v153,v154,v155
m v[132:135],a[208:211],v[72:75],v[132:135]
cv v154,v156,v157
cv v155,v158,v159
buffer_store_dwordx4 v[152:155],v215,%5,s55 offen offset:1024 sc1
m v[104:107],a[212:215],v[8:11],v[104:107]
v_mul_f32 v160,v160,v217
v_mul_f32 v161,v161,v217
m v[120:123],a[212:215],v[40:43],v[120:123]
v_mul_f32 v162,v162,v217
v_mul_f32 v163,v163,v217
v_mul_f32 v164,v164,v217
m v[136:139],a[212:215],v[72:75],v[136:139]
v_mul_f32 v165,v165,v217
v_mul_f32 v166,v166,v217
v_mul_f32 v167,v167,v217
m v[108:111],a[216:219],v[8:11],v[108:111]
cv v160,v160,v161
cv v161,v162,v163
m v[124:127],a[216:219],v[40:43],v[124:127]
cv v162,v164,v165
cv v163,v166,v167
v_add_u32 v234,64,v234
m v[140:143],a[216:219],v[72:75],v[140:143]
v_lshrrev_b32 v229,7,v234
v_lshlrev_b32 v229,4,v229
v_and_b32 v235,15,v234
s_waitcnt lgkmcnt(0)
m v[96:99],a[220:223],v[12:15],v[96:99]
t a[204:205],v208,16384
t a[206:207],v208,18432
m v[112:115],a[220:223],v[44:47],v[112:115]
t a[208:209],v209,16384
t a[210:211],v209,18432
t a[212:213],v210,16384
m v[128:131],a[220:223],v[76:79],v[128:131]
t a[214:215],v210,18432
t a[216:217],v211,16384
t a[218:219],v211,18432
m v[100:103],a[224:227],v[12:15],v[100:103]
buffer_store_dwordx4 v[160:163],v215,%5,s55 offen offset:2048 sc1
v_mul_f32 v168,v168,v217
v_mul_f32 v169,v169,v217
m v[116:119],a[224:227],v[44:47],v[116:119]
v_mul_f32 v170,v170,v217
v_mul_f32 v171,v171,v217
m v[132:135],a[224:227],v[76:79],v[132:135]
v_mul_f32 v172,v172,v217
v_mul_f32 v173,v173,v217
v_mul_f32 v174,v174,v217
m v[104:107],a[228:231],v[12:15],v[104:107]
v_mul_f32 v175,v175,v217
cv v168,v168,v169
cv v169,v170,v171
m v[120:123],a[228:231],v[44:47],v[120:123]
cv v170,v172,v173
cv v171,v174,v175
buffer_store_dwordx4 v[168:171],v215,%5,s55 offen offset:3072 sc1
m v[136:139],a[228:231],v[76:79],v[136:139]
v_mul_f32 v176,v176,v218
v_mul_f32 v177,v177,v218
m v[108:111],a[232:235],v[12:15],v[108:111]
v_mul_f32 v178,v178,v218
v_mul_f32 v179,v179,v218
v_mul_f32 v180,v180,v218
m v[124:127],a[232:235],v[44:47],v[124:127]
v_mul_f32 v181,v181,v218
v_add_u32 v229,v229,v235
v_mul_u32_u24 v229,0x700,v229
m v[140:143],a[232:235],v[76:79],v[140:143]
v_bfe_u32 v235,v234,6,1
v_lshl_add_u32 v229,v235,6,v229
v_bfe_u32 v235,v234,4,2
s_waitcnt lgkmcnt(0)
m v[96:99],a[204:207],v[16:19],v[96:99]
t a[220:221],v208,20480
t a[222:223],v208,22528
m v[112:115],a[204:207],v[48:51],v[112:115]
t a[224:225],v209,20480
t a[226:227],v209,22528
t a[228:229],v210,20480
m v[128:131],a[204:207],v[80:83],v[128:131]
t a[230:231],v210,22528
t a[232:233],v211,20480
t a[234:235],v211,22528
m v[100:103],a[208:211],v[16:19],v[100:103]
v_mul_f32 v182,v182,v218
v_mul_f32 v183,v183,v218
m v[116:119],a[208:211],v[48:51],v[116:119]
cv v176,v176,v177
cv v177,v178,v179
cv v178,v180,v181
m v[132:135],a[208:211],v[80:83],v[132:135]
cv v179,v182,v183
buffer_store_dwordx4 v[176:179],v215,%5,s59 offen sc1
v_mul_f32 v184,v184,v218
m v[104:107],a[212:215],v[16:19],v[104:107]
v_mul_f32 v185,v185,v218
v_mul_f32 v186,v186,v218
m v[120:123],a[212:215],v[48:51],v[120:123]
v_mul_f32 v187,v187,v218
v_mul_f32 v188,v188,v218
v_mul_f32 v189,v189,v218
m v[136:139],a[212:215],v[80:83],v[136:139]
v_mul_f32 v190,v190,v218
v_mul_f32 v191,v191,v218
cv v184,v184,v185
m v[108:111],a[216:219],v[16:19],v[108:111]
cv v185,v186,v187
cv v186,v188,v189
m v[124:127],a[216:219],v[48:51],v[124:127]
cv v187,v190,v191
buffer_store_dwordx4 v[184:187],v215,%5,s59 offen offset:1024 sc1
v_lshl_add_u32 v229,v235,3,v229
m v[140:143],a[216:219],v[80:83],v[140:143]
v_lshrrev_b32 v236,4,v214
v_lshl_add_u32 v229,v236,7,v229
v_mul_u32_u24 v237,0x5000,v236
s_waitcnt lgkmcnt(0)
m v[96:99],a[220:223],v[20:23],v[96:99]
t a[204:205],v208,24576
t a[206:207],v208,26624
m v[112:115],a[220:223],v[52:55],v[112:115]
t a[208:209],v209,24576
t a[210:211],v209,26624
m v[128:131],a[220:223],v[84:87],v[128:131]
t a[212:213],v210,24576
t a[214:215],v210,26624
m v[100:103],a[224:227],v[20:23],v[100:103]
t a[216:217],v211,24576
t a[218:219],v211,26624
m v[116:119],a[224:227],v[52:55],v[116:119]
v_lshl_add_u32 v228,v234,4,v237
v_add_u32 v234,0xf000,v226
m v[132:135],a[224:227],v[84:87],v[132:135]
v_add_u32 v235,0x180,v227
s_cmp_eq_u32 %6,0
m v[104:107],a[228:231],v[20:23],v[104:107]
m v[120:123],a[228:231],v[52:55],v[120:123]
m v[136:139],a[228:231],v[84:87],v[136:139]
m v[108:111],a[232:235],v[20:23],v[108:111]
m v[124:127],a[232:235],v[52:55],v[124:127]
m v[140:143],a[232:235],v[84:87],v[140:143]
s_waitcnt lgkmcnt(0)
m v[96:99],a[204:207],v[24:27],v[96:99]
t a[220:221],v208,28672
t a[222:223],v208,30720
m v[112:115],a[204:207],v[56:59],v[112:115]
t a[224:225],v209,28672
t a[226:227],v209,30720
m v[128:131],a[204:207],v[88:91],v[128:131]
t a[228:229],v210,28672
t a[230:231],v210,30720
m v[100:103],a[208:211],v[24:27],v[100:103]
t a[232:233],v211,28672
t a[234:235],v211,30720
m v[116:119],a[208:211],v[56:59],v[116:119]
v_add_u32 v208,s49,v208
v_add_u32 v209,s49,v209
m v[132:135],a[208:211],v[88:91],v[132:135]
v_add_u32 v210,s49,v210
v_add_u32 v211,s49,v211
m v[104:107],a[212:215],v[24:27],v[104:107]
s_cselect_b64 vcc,-1,0
v_cndmask_b32_e32 v228,v228,v234,vcc
m v[120:123],a[212:215],v[56:59],v[120:123]
v_cndmask_b32_e32 v229,v229,v235,vcc
s_mul_i32 s46,%6,0x5000
m v[136:139],a[212:215],v[88:91],v[136:139]
m v[108:111],a[216:219],v[24:27],v[108:111]
m v[124:127],a[216:219],v[56:59],v[124:127]
m v[140:143],a[216:219],v[88:91],v[140:143]
s_waitcnt lgkmcnt(0)
s_waitcnt vmcnt(6)
s_barrier
m v[96:99],a[220:223],v[28:31],v[96:99]
t a[204:205],v208,0
t a[206:207],v208,2048
m v[112:115],a[220:223],v[60:63],v[112:115]
t a[208:209],v209,0
t a[210:211],v209,2048
m v[128:131],a[220:223],v[92:95],v[128:131]
t a[212:213],v210,0
t a[214:215],v210,2048
m v[100:103],a[224:227],v[28:31],v[100:103]
t a[216:217],v211,0
t a[218:219],v211,2048
m v[116:119],a[224:227],v[60:63],v[116:119]
s_add_u32 s46,s46,0x14000
v_add_u32 v226,s46,v226
m v[132:135],a[224:227],v[92:95],v[132:135]
s_lshl_b32 s46,%6,7
s_add_u32 s46,s46,0x200
m v[104:107],a[228:231],v[28:31],v[104:107]
v_add_u32 v227,s46,v227
m v[120:123],a[228:231],v[60:63],v[120:123]
m v[136:139],a[228:231],v[92:95],v[136:139]
m v[108:111],a[232:235],v[28:31],v[108:111]
m v[124:127],a[232:235],v[60:63],v[124:127]
m v[140:143],a[232:235],v[92:95],v[140:143]
s_waitcnt lgkmcnt(0)
m v[144:147],a[204:207],v[0:3],0
t a[220:221],v208,4096
t a[222:223],v208,6144
m v[160:163],a[204:207],v[32:35],0
t a[224:225],v209,4096
t a[226:227],v209,6144
m v[176:179],a[204:207],v[64:67],0
t a[228:229],v210,4096
t a[230:231],v210,6144
m v[148:151],a[208:211],v[0:3],0
t a[232:233],v211,4096
t a[234:235],v211,6144
m v[164:167],a[208:211],v[32:35],0
m v[180:183],a[208:211],v[64:67],0
m v[152:155],a[212:215],v[0:3],0
m v[168:171],a[212:215],v[32:35],0
m v[184:187],a[212:215],v[64:67],0
m v[156:159],a[216:219],v[0:3],0
m v[172:175],a[216:219],v[32:35],0
m v[188:191],a[216:219],v[64:67],0
s_waitcnt lgkmcnt(0)
m v[144:147],a[220:223],v[4:7],v[144:147]
t a[204:205],v208,8192
t a[206:207],v208,10240
m v[160:163],a[220:223],v[36:39],v[160:163]
t a[208:209],v209,8192
t a[210:211],v209,10240
m v[176:179],a[220:223],v[68:71],v[176:179]
t a[212:213],v210,8192
t a[214:215],v210,10240
t a[216:217],v211,8192
m v[148:151],a[224:227],v[4:7],v[148:151]
t a[218:219],v211,10240
s_add_u32 s55,s54,0xa000
m v[164:167],a[224:227],v[36:39],v[164:167]
s_add_u32 s59,s55,0x1000
v_mul_f32 v96,v96,v216
m v[180:183],a[224:227],v[68:71],v[180:183]
v_mul_f32 v97,v97,v216
v_mul_f32 v98,v98,v216
v_mul_f32 v99,v99,v216
m v[152:155],a[228:231],v[4:7],v[152:155]
v_mul_f32 v100,v100,v216
v_mul_f32 v101,v101,v216
m v[168:171],a[228:231],v[36:39],v[168:171]
v_mul_f32 v102,v102,v216
v_mul_f32 v103,v103,v216
m v[184:187],a[228:231],v[68:71],v[184:187]
cv v96,v96,v97
cv v97,v98,v99
cv v98,v100,v101
m v[156:159],a[232:235],v[4:7],v[156:159]
cv v99,v102,v103
buffer_store_dwordx4 v[96:99],v215,%5,s55 offen sc1
m v[172:175],a[232:235],v[36:39],v[172:175]
v_mul_f32 v104,v104,v216
v_mul_f32 v105,v105,v216
m v[188:191],a[232:235],v[68:71],v[188:191]
v_mul_f32 v106,v106,v216
v_mul_f32 v107,v107,v216
v_mul_f32 v108,v108,v216
s_waitcnt lgkmcnt(0)
m v[144:147],a[204:207],v[8:11],v[144:147]
t a[220:221],v208,12288
t a[222:223],v208,14336
m v[160:163],a[204:207],v[40:43],v[160:163]
t a[224:225],v209,12288
t a[226:227],v209,14336
m v[176:179],a[204:207],v[72:75],v[176:179]
t a[228:229],v210,12288
t a[230:231],v210,14336
t a[232:233],v211,12288
m v[148:151],a[208:211],v[8:11],v[148:151]
t a[234:235],v211,14336
v_mul_f32 v109,v109,v216
m v[164:167],a[208:211],v[40:43],v[164:167]
v_mul_f32 v110,v110,v216
v_mul_f32 v111,v111,v216
m v[180:183],a[208:211],v[72:75],v[180:183]
cv v104,v104,v105
cv v105,v106,v107
cv v106,v108,v109
m v[152:155],a[212:215],v[8:11],v[152:155]
cv v107,v110,v111
buffer_store_dwordx4 v[104:107],v215,%5,s55 offen offset:1024 sc1
m v[168:171],a[212:215],v[40:43],v[168:171]
v_mul_f32 v112,v112,v217
v_mul_f32 v113,v113,v217
m v[184:187],a[212:215],v[72:75],v[184:187]
v_mul_f32 v114,v114,v217
v_mul_f32 v115,v115,v217
v_mul_f32 v116,v116,v217
m v[156:159],a[216:219],v[8:11],v[156:159]
v_mul_f32 v117,v117,v217
v_mul_f32 v118,v118,v217
m v[172:175],a[216:219],v[40:43],v[172:175]
v_mul_f32 v119,v119,v217
cv v112,v112,v113
m v[188:191],a[216:219],v[72:75],v[188:191]
cv v113,v114,v115
cv v114,v116,v117
cv v115,v118,v119
s_waitcnt lgkmcnt(0)
m v[144:147],a[220:223],v[12:15],v[144:147]
t a[204:205],v208,16384
t a[206:207],v208,18432
m v[160:163],a[220:223],v[44:47],v[160:163]
t a[208:209],v209,16384
t a[210:211],v209,18432
m v[176:179],a[220:223],v[76:79],v[176:179]
t a[212:213],v210,16384
t a[214:215],v210,18432
t a[216:217],v211,16384
m v[148:151],a[224:227],v[12:15],v[148:151]
t a[218:219],v211,18432
buffer_store_dwordx4 v[112:115],v215,%5,s55 offen offset:2048 sc1
m v[164:167],a[224:227],v[44:47],v[164:167]
v_mul_f32 v120,v120,v217
v_mul_f32 v121,v121,v217
m v[180:183],a[224:227],v[76:79],v[180:183]
v_mul_f32 v122,v122,v217
v_mul_f32 v123,v123,v217
v_mul_f32 v124,v124,v217
m v[152:155],a[228:231],v[12:15],v[152:155]
v_mul_f32 v125,v125,v217
v_mul_f32 v126,v126,v217
m v[168:171],a[228:231],v[44:47],v[168:171]
v_mul_f32 v127,v127,v217
cv v120,v120,v121
m v[184:187],a[228:231],v[76:79],v[184:187]
cv v121,v122,v123
cv v122,v124,v125
cv v123,v126,v127
m v[156:159],a[232:235],v[12:15],v[156:159]
buffer_store_dwordx4 v[120:123],v215,%5,s55 offen offset:3072 sc1
v_mul_f32 v128,v128,v218
m v[172:175],a[232:235],v[44:47],v[172:175]
v_mul_f32 v129,v129,v218
v_mul_f32 v130,v130,v218
m v[188:191],a[232:235],v[76:79],v[188:191]
v_mul_f32 v131,v131,v218
v_mul_f32 v132,v132,v218
v_mul_f32 v133,v133,v218
s_waitcnt lgkmcnt(0)
m v[144:147],a[204:207],v[16:19],v[144:147]
t a[220:221],v208,20480
t a[222:223],v208,22528
m v[160:163],a[204:207],v[48:51],v[160:163]
t a[224:225],v209,20480
t a[226:227],v209,22528
m v[176:179],a[204:207],v[80:83],v[176:179]
t a[228:229],v210,20480
t a[230:231],v210,22528
t a[232:233],v211,20480
m v[148:151],a[208:211],v[16:19],v[148:151]
t a[234:235],v211,22528
v_mul_f32 v134,v134,v218
m v[164:167],a[208:211],v[48:51],v[164:167]
v_mul_f32 v135,v135,v218
cv v128,v128,v129
m v[180:183],a[208:211],v[80:83],v[180:183]
cv v129,v130,v131
cv v130,v132,v133
cv v131,v134,v135
m v[152:155],a[212:215],v[16:19],v[152:155]
buffer_store_dwordx4 v[128:131],v215,%5,s59 offen sc1
v_mul_f32 v136,v136,v218
m v[168:171],a[212:215],v[48:51],v[168:171]
v_mul_f32 v137,v137,v218
v_mul_f32 v138,v138,v218
m v[184:187],a[212:215],v[80:83],v[184:187]
v_mul_f32 v139,v139,v218
v_mul_f32 v140,v140,v218
v_mul_f32 v141,v141,v218
m v[156:159],a[216:219],v[16:19],v[156:159]
v_mul_f32 v142,v142,v218
v_mul_f32 v143,v143,v218
m v[172:175],a[216:219],v[48:51],v[172:175]
cv v136,v136,v137
cv v137,v138,v139
m v[188:191],a[216:219],v[80:83],v[188:191]
cv v138,v140,v141
cv v139,v142,v143
buffer_store_dwordx4 v[136:139],v215,%5,s59 offen offset:1024 sc1
s_waitcnt lgkmcnt(0)
m v[144:147],a[220:223],v[20:23],v[144:147]
t a[204:205],v208,24576
t a[206:207],v208,26624
m v[160:163],a[220:223],v[52:55],v[160:163]
t a[208:209],v209,24576
t a[210:211],v209,26624
m v[176:179],a[220:223],v[84:87],v[176:179]
t a[212:213],v210,24576
t a[214:215],v210,26624
m v[148:151],a[224:227],v[20:23],v[148:151]
t a[216:217],v211,24576
t a[218:219],v211,26624
m v[164:167],a[224:227],v[52:55],v[164:167]
m v[180:183],a[224:227],v[84:87],v[180:183]
m v[152:155],a[228:231],v[20:23],v[152:155]
m v[168:171],a[228:231],v[52:55],v[168:171]
m v[184:187],a[228:231],v[84:87],v[184:187]
m v[156:159],a[232:235],v[20:23],v[156:159]
m v[172:175],a[232:235],v[52:55],v[172:175]
m v[188:191],a[232:235],v[84:87],v[188:191]
s_waitcnt lgkmcnt(0)
m v[144:147],a[204:207],v[24:27],v[144:147]
t a[220:221],v208,28672
t a[222:223],v208,30720
m v[160:163],a[204:207],v[56:59],v[160:163]
t a[224:225],v209,28672
t a[226:227],v209,30720
m v[176:179],a[204:207],v[88:91],v[176:179]
t a[228:229],v210,28672
t a[230:231],v210,30720
m v[148:151],a[208:211],v[24:27],v[148:151]
t a[232:233],v211,28672
t a[234:235],v211,30720
m v[164:167],a[208:211],v[56:59],v[164:167]
v_add_u32 v208,s48,v208
v_add_u32 v209,s48,v209
m v[180:183],a[208:211],v[88:91],v[180:183]
v_add_u32 v210,s48,v210
v_add_u32 v211,s48,v211
m v[152:155],a[212:215],v[24:27],v[152:155]
m v[168:171],a[212:215],v[56:59],v[168:171]
m v[184:187],a[212:215],v[88:91],v[184:187]
m v[156:159],a[216:219],v[24:27],v[156:159]
m v[172:175],a[216:219],v[56:59],v[172:175]
m v[188:191],a[216:219],v[88:91],v[188:191]
s_waitcnt lgkmcnt(0)
s_waitcnt vmcnt(6)
s_barrier
m v[144:147],a[220:223],v[28:31],v[144:147]
t a[204:205],v208,0
t a[206:207],v208,2048
m v[160:163],a[220:223],v[60:63],v[160:163]
t a[208:209],v209,0
t a[210:211],v209,2048
m v[176:179],a[220:223],v[92:95],v[176:179]
t a[212:213],v210,0
t a[214:215],v210,2048
m v[148:151],a[224:227],v[28:31],v[148:151]
t a[216:217],v211,0
t a[218:219],v211,2048
m v[164:167],a[224:227],v[60:63],v[164:167]
m v[180:183],a[224:227],v[92:95],v[180:183]
m v[152:155],a[228:231],v[28:31],v[152:155]
m v[168:171],a[228:231],v[60:63],v[168:171]
m v[184:187],a[228:231],v[92:95],v[184:187]
m v[156:159],a[232:235],v[28:31],v[156:159]
m v[172:175],a[232:235],v[60:63],v[172:175]
m v[188:191],a[232:235],v[92:95],v[188:191]
s_waitcnt lgkmcnt(0)
m v[96:99],a[204:207],v[0:3],0
t a[220:221],v208,4096
t a[222:223],v208,6144
m v[112:115],a[204:207],v[32:35],0
t a[224:225],v209,4096
t a[226:227],v209,6144
m v[128:131],a[204:207],v[64:67],0
t a[228:229],v210,4096
t a[230:231],v210,6144
m v[100:103],a[208:211],v[0:3],0
t a[232:233],v211,4096
t a[234:235],v211,6144
m v[116:119],a[208:211],v[32:35],0
m v[132:135],a[208:211],v[64:67],0
m v[104:107],a[212:215],v[0:3],0
m v[120:123],a[212:215],v[32:35],0
m v[136:139],a[212:215],v[64:67],0
m v[108:111],a[216:219],v[0:3],0
m v[124:127],a[216:219],v[32:35],0
m v[140:143],a[216:219],v[64:67],0
s_waitcnt lgkmcnt(0)
m v[96:99],a[220:223],v[4:7],v[96:99]
t a[204:205],v208,8192
t a[206:207],v208,10240
m v[112:115],a[220:223],v[36:39],v[112:115]
t a[208:209],v209,8192
t a[210:211],v209,10240
m v[128:131],a[220:223],v[68:71],v[128:131]
t a[212:213],v210,8192
t a[214:215],v210,10240
t a[216:217],v211,8192
m v[100:103],a[224:227],v[4:7],v[100:103]
t a[218:219],v211,10240
s_add_u32 s55,s54,0xf000
m v[116:119],a[224:227],v[36:39],v[116:119]
s_add_u32 s59,s55,0x1000
v_mul_f32 v144,v144,v216
m v[132:135],a[224:227],v[68:71],v[132:135]
v_mul_f32 v145,v145,v216
v_mul_f32 v146,v146,v216
v_mul_f32 v147,v147,v216
m v[104:107],a[228:231],v[4:7],v[104:107]
v_mul_f32 v148,v148,v216
v_mul_f32 v149,v149,v216
m v[120:123],a[228:231],v[36:39],v[120:123]
v_mul_f32 v150,v150,v216
v_mul_f32 v151,v151,v216
m v[136:139],a[228:231],v[68:71],v[136:139]
cv v144,v144,v145
cv v145,v146,v147
cv v146,v148,v149
m v[108:111],a[232:235],v[4:7],v[108:111]
cv v147,v150,v151
buffer_store_dwordx4 v[144:147],v215,%5,s55 offen sc1
m v[124:127],a[232:235],v[36:39],v[124:127]
v_mul_f32 v152,v152,v216
v_mul_f32 v153,v153,v216
m v[140:143],a[232:235],v[68:71],v[140:143]
v_mul_f32 v154,v154,v216
v_mul_f32 v155,v155,v216
v_mul_f32 v156,v156,v216
s_waitcnt lgkmcnt(0)
m v[96:99],a[204:207],v[8:11],v[96:99]
t a[220:221],v208,12288
t a[222:223],v208,14336
m v[112:115],a[204:207],v[40:43],v[112:115]
t a[224:225],v209,12288
t a[226:227],v209,14336
m v[128:131],a[204:207],v[72:75],v[128:131]
t a[228:229],v210,12288
t a[230:231],v210,14336
t a[232:233],v211,12288
m v[100:103],a[208:211],v[8:11],v[100:103]
t a[234:235],v211,14336
v_mul_f32 v157,v157,v216
m v[116:119],a[208:211],v[40:43],v[116:119]
v_mul_f32 v158,v158,v216
v_mul_f32 v159,v159,v216
m v[132:135],a[208:211],v[72:75],v[132:135]
cv v152,v152,v153
cv v153,v154,v155
cv v154,v156,v157
m v[104:107],a[212:215],v[8:11],v[104:107]
cv v155,v158,v159
buffer_store_dwordx4 v[152:155],v215,%5,s55 offen offset:1024 sc1
m v[120:123],a[212:215],v[40:43],v[120:123]
v_mul_f32 v160,v160,v217
v_mul_f32 v161,v161,v217
m v[136:139],a[212:215],v[72:75],v[136:139]
v_mul_f32 v162,v162,v217
v_mul_f32 v163,v163,v217
v_mul_f32 v164,v164,v217
m v[108:111],a[216:219],v[8:11],v[108:111]
v_mul_f32 v165,v165,v217
v_mul_f32 v166,v166,v217
m v[124:127],a[216:219],v[40:43],v[124:127]
v_mul_f32 v167,v167,v217
cv v160,v160,v161
m v[140:143],a[216:219],v[72:75],v[140:143]
cv v161,v162,v163
cv v162,v164,v165
cv v163,v166,v167
s_waitcnt lgkmcnt(0)
m v[96:99],a[220:223],v[12:15],v[96:99]
t a[204:205],v208,16384
t a[206:207],v208,18432
m v[112:115],a[220:223],v[44:47],v[112:115]
t a[208:209],v209,16384
t a[210:211],v209,18432
m v[128:131],a[220:223],v[76:79],v[128:131]
t a[212:213],v210,16384
t a[214:215],v210,18432
t a[216:217],v211,16384
m v[100:103],a[224:227],v[12:15],v[100:103]
t a[218:219],v211,18432
buffer_store_dwordx4 v[160:163],v215,%5,s55 offen offset:2048 sc1
m v[116:119],a[224:227],v[44:47],v[116:119]
v_mul_f32 v168,v168,v217
v_mul_f32 v169,v169,v217
m v[132:135],a[224:227],v[76:79],v[132:135]
v_mul_f32 v170,v170,v217
v_mul_f32 v171,v171,v217
v_mul_f32 v172,v172,v217
m v[104:107],a[228:231],v[12:15],v[104:107]
v_mul_f32 v173,v173,v217
v_mul_f32 v174,v174,v217
m v[120:123],a[228:231],v[44:47],v[120:123]
v_mul_f32 v175,v175,v217
cv v168,v168,v169
m v[136:139],a[228:231],v[76:79],v[136:139]
cv v169,v170,v171
cv v170,v172,v173
cv v171,v174,v175
m v[108:111],a[232:235],v[12:15],v[108:111]
buffer_store_dwordx4 v[168:171],v215,%5,s55 offen offset:3072 sc1
v_mul_f32 v176,v176,v218
m v[124:127],a[232:235],v[44:47],v[124:127]
v_mul_f32 v177,v177,v218
v_mul_f32 v178,v178,v218
m v[140:143],a[232:235],v[76:79],v[140:143]
v_mul_f32 v179,v179,v218
v_mul_f32 v180,v180,v218
v_mul_f32 v181,v181,v218
s_waitcnt lgkmcnt(0)
m v[96:99],a[204:207],v[16:19],v[96:99]
t a[220:221],v208,20480
t a[222:223],v208,22528
m v[112:115],a[204:207],v[48:51],v[112:115]
t a[224:225],v209,20480
t a[226:227],v209,22528
m v[128:131],a[204:207],v[80:83],v[128:131]
t a[228:229],v210,20480
t a[230:231],v210,22528
t a[232:233],v211,20480
m v[100:103],a[208:211],v[16:19],v[100:103]
t a[234:235],v211,22528
v_mul_f32 v182,v182,v218
m v[116:119],a[208:211],v[48:51],v[116:119]
v_mul_f32 v183,v183,v218
cv v176,v176,v177
m v[132:135],a[208:211],v[80:83],v[132:135]
cv v177,v178,v179
cv v178,v180,v181
cv v179,v182,v183
m v[104:107],a[212:215],v[16:19],v[104:107]
buffer_store_dwordx4 v[176:179],v215,%5,s59 offen sc1
v_mul_f32 v184,v184,v218
m v[120:123],a[212:215],v[48:51],v[120:123]
v_mul_f32 v185,v185,v218
v_mul_f32 v186,v186,v218
m v[136:139],a[212:215],v[80:83],v[136:139]
v_mul_f32 v187,v187,v218
v_mul_f32 v188,v188,v218
v_mul_f32 v189,v189,v218
m v[108:111],a[216:219],v[16:19],v[108:111]
v_mul_f32 v190,v190,v218
v_mul_f32 v191,v191,v218
m v[124:127],a[216:219],v[48:51],v[124:127]
cv v184,v184,v185
cv v185,v186,v187
m v[140:143],a[216:219],v[80:83],v[140:143]
cv v186,v188,v189
cv v187,v190,v191
buffer_store_dwordx4 v[184:187],v215,%5,s59 offen offset:1024 sc1
s_waitcnt lgkmcnt(0)
m v[96:99],a[220:223],v[20:23],v[96:99]
t a[204:205],v208,24576
t a[206:207],v208,26624
m v[112:115],a[220:223],v[52:55],v[112:115]
t a[208:209],v209,24576
t a[210:211],v209,26624
m v[128:131],a[220:223],v[84:87],v[128:131]
t a[212:213],v210,24576
t a[214:215],v210,26624
m v[100:103],a[224:227],v[20:23],v[100:103]
t a[216:217],v211,24576
t a[218:219],v211,26624
m v[116:119],a[224:227],v[52:55],v[116:119]
m v[132:135],a[224:227],v[84:87],v[132:135]
m v[104:107],a[228:231],v[20:23],v[104:107]
m v[120:123],a[228:231],v[52:55],v[120:123]
m v[136:139],a[228:231],v[84:87],v[136:139]
m v[108:111],a[232:235],v[20:23],v[108:111]
m v[124:127],a[232:235],v[52:55],v[124:127]
m v[140:143],a[232:235],v[84:87],v[140:143]
s_waitcnt lgkmcnt(0)
m v[96:99],a[204:207],v[24:27],v[96:99]
t a[220:221],v208,28672
t a[222:223],v208,30720
m v[112:115],a[204:207],v[56:59],v[112:115]
t a[224:225],v209,28672
t a[226:227],v209,30720
m v[128:131],a[204:207],v[88:91],v[128:131]
t a[228:229],v210,28672
t a[230:231],v210,30720
m v[100:103],a[208:211],v[24:27],v[100:103]
t a[232:233],v211,28672
t a[234:235],v211,30720
m v[116:119],a[208:211],v[56:59],v[116:119]
v_add_u32 v208,s48,v208
v_add_u32 v209,s48,v209
m v[132:135],a[208:211],v[88:91],v[132:135]
v_add_u32 v210,s48,v210
v_add_u32 v211,s48,v211
m v[104:107],a[212:215],v[24:27],v[104:107]
m v[120:123],a[212:215],v[56:59],v[120:123]
m v[136:139],a[212:215],v[88:91],v[136:139]
m v[108:111],a[216:219],v[24:27],v[108:111]
m v[124:127],a[216:219],v[56:59],v[124:127]
m v[140:143],a[216:219],v[88:91],v[140:143]
s_waitcnt lgkmcnt(0)
s_waitcnt vmcnt(6)
s_barrier
m v[96:99],a[220:223],v[28:31],v[96:99]
t a[204:205],v208,0
t a[206:207],v208,2048
m v[112:115],a[220:223],v[60:63],v[112:115]
t a[208:209],v209,0
t a[210:211],v209,2048
m v[128:131],a[220:223],v[92:95],v[128:131]
t a[212:213],v210,0
t a[214:215],v210,2048
m v[100:103],a[224:227],v[28:31],v[100:103]
t a[216:217],v211,0
t a[218:219],v211,2048
m v[116:119],a[224:227],v[60:63],v[116:119]
m v[132:135],a[224:227],v[92:95],v[132:135]
m v[104:107],a[228:231],v[28:31],v[104:107]
m v[120:123],a[228:231],v[60:63],v[120:123]
m v[136:139],a[228:231],v[92:95],v[136:139]
m v[108:111],a[232:235],v[28:31],v[108:111]
m v[124:127],a[232:235],v[60:63],v[124:127]
m v[140:143],a[232:235],v[92:95],v[140:143]
s_waitcnt lgkmcnt(0)
m v[144:147],a[204:207],v[0:3],0
t a[220:221],v208,4096
t a[222:223],v208,6144
m v[160:163],a[204:207],v[32:35],0
t a[224:225],v209,4096
t a[226:227],v209,6144
m v[176:179],a[204:207],v[64:67],0
t a[228:229],v210,4096
t a[230:231],v210,6144
m v[148:151],a[208:211],v[0:3],0
t a[232:233],v211,4096
t a[234:235],v211,6144
m v[164:167],a[208:211],v[32:35],0
m v[180:183],a[208:211],v[64:67],0
m v[152:155],a[212:215],v[0:3],0
m v[168:171],a[212:215],v[32:35],0
m v[184:187],a[212:215],v[64:67],0
m v[156:159],a[216:219],v[0:3],0
m v[172:175],a[216:219],v[32:35],0
m v[188:191],a[216:219],v[64:67],0
s_waitcnt lgkmcnt(0)
m v[144:147],a[220:223],v[4:7],v[144:147]
t a[204:205],v208,8192
t a[206:207],v208,10240
m v[160:163],a[220:223],v[36:39],v[160:163]
t a[208:209],v209,8192
t a[210:211],v209,10240
m v[176:179],a[220:223],v[68:71],v[176:179]
t a[212:213],v210,8192
t a[214:215],v210,10240
t a[216:217],v211,8192
m v[148:151],a[224:227],v[4:7],v[148:151]
t a[218:219],v211,10240
s_add_u32 s55,s54,0x14000
m v[164:167],a[224:227],v[36:39],v[164:167]
s_add_u32 s59,s55,0x1000
v_mul_f32 v96,v96,v216
m v[180:183],a[224:227],v[68:71],v[180:183]
v_mul_f32 v97,v97,v216
v_mul_f32 v98,v98,v216
v_mul_f32 v99,v99,v216
m v[152:155],a[228:231],v[4:7],v[152:155]
v_mul_f32 v100,v100,v216
v_mul_f32 v101,v101,v216
m v[168:171],a[228:231],v[36:39],v[168:171]
v_mul_f32 v102,v102,v216
v_mul_f32 v103,v103,v216
m v[184:187],a[228:231],v[68:71],v[184:187]
cv v96,v96,v97
cv v97,v98,v99
cv v98,v100,v101
m v[156:159],a[232:235],v[4:7],v[156:159]
cv v99,v102,v103
buffer_store_dwordx4 v[96:99],v215,%5,s55 offen sc1
m v[172:175],a[232:235],v[36:39],v[172:175]
v_mul_f32 v104,v104,v216
v_mul_f32 v105,v105,v216
m v[188:191],a[232:235],v[68:71],v[188:191]
v_mul_f32 v106,v106,v216
v_mul_f32 v107,v107,v216
v_mul_f32 v108,v108,v216
s_waitcnt lgkmcnt(0)
m v[144:147],a[204:207],v[8:11],v[144:147]
t a[220:221],v208,12288
t a[222:223],v208,14336
m v[160:163],a[204:207],v[40:43],v[160:163]
t a[224:225],v209,12288
t a[226:227],v209,14336
m v[176:179],a[204:207],v[72:75],v[176:179]
t a[228:229],v210,12288
t a[230:231],v210,14336
t a[232:233],v211,12288
m v[148:151],a[208:211],v[8:11],v[148:151]
t a[234:235],v211,14336
v_mul_f32 v109,v109,v216
m v[164:167],a[208:211],v[40:43],v[164:167]
v_mul_f32 v110,v110,v216
v_mul_f32 v111,v111,v216
m v[180:183],a[208:211],v[72:75],v[180:183]
cv v104,v104,v105
cv v105,v106,v107
cv v106,v108,v109
m v[152:155],a[212:215],v[8:11],v[152:155]
cv v107,v110,v111
buffer_store_dwordx4 v[104:107],v215,%5,s55 offen offset:1024 sc1
m v[168:171],a[212:215],v[40:43],v[168:171]
v_mul_f32 v112,v112,v217
v_mul_f32 v113,v113,v217
m v[184:187],a[212:215],v[72:75],v[184:187]
v_mul_f32 v114,v114,v217
v_mul_f32 v115,v115,v217
v_mul_f32 v116,v116,v217
m v[156:159],a[216:219],v[8:11],v[156:159]
v_mul_f32 v117,v117,v217
v_mul_f32 v118,v118,v217
m v[172:175],a[216:219],v[40:43],v[172:175]
v_mul_f32 v119,v119,v217
cv v112,v112,v113
m v[188:191],a[216:219],v[72:75],v[188:191]
cv v113,v114,v115
cv v114,v116,v117
cv v115,v118,v119
s_waitcnt lgkmcnt(0)
m v[144:147],a[220:223],v[12:15],v[144:147]
t a[204:205],v208,16384
t a[206:207],v208,18432
m v[160:163],a[220:223],v[44:47],v[160:163]
t a[208:209],v209,16384
t a[210:211],v209,18432
m v[176:179],a[220:223],v[76:79],v[176:179]
t a[212:213],v210,16384
t a[214:215],v210,18432
t a[216:217],v211,16384
m v[148:151],a[224:227],v[12:15],v[148:151]
t a[218:219],v211,18432
buffer_store_dwordx4 v[112:115],v215,%5,s55 offen offset:2048 sc1
m v[164:167],a[224:227],v[44:47],v[164:167]
v_mul_f32 v120,v120,v217
v_mul_f32 v121,v121,v217
m v[180:183],a[224:227],v[76:79],v[180:183]
v_mul_f32 v122,v122,v217
v_mul_f32 v123,v123,v217
v_mul_f32 v124,v124,v217
m v[152:155],a[228:231],v[12:15],v[152:155]
v_mul_f32 v125,v125,v217
v_mul_f32 v126,v126,v217
m v[168:171],a[228:231],v[44:47],v[168:171]
v_mul_f32 v127,v127,v217
cv v120,v120,v121
m v[184:187],a[228:231],v[76:79],v[184:187]
cv v121,v122,v123
cv v122,v124,v125
cv v123,v126,v127
m v[156:159],a[232:235],v[12:15],v[156:159]
buffer_store_dwordx4 v[120:123],v215,%5,s55 offen offset:3072 sc1
v_mul_f32 v128,v128,v218
m v[172:175],a[232:235],v[44:47],v[172:175]
v_mul_f32 v129,v129,v218
v_mul_f32 v130,v130,v218
m v[188:191],a[232:235],v[76:79],v[188:191]
v_mul_f32 v131,v131,v218
v_mul_f32 v132,v132,v218
v_mul_f32 v133,v133,v218
s_waitcnt lgkmcnt(0)
m v[144:147],a[204:207],v[16:19],v[144:147]
t a[220:221],v208,20480
t a[222:223],v208,22528
m v[160:163],a[204:207],v[48:51],v[160:163]
t a[224:225],v209,20480
t a[226:227],v209,22528
m v[176:179],a[204:207],v[80:83],v[176:179]
t a[228:229],v210,20480
t a[230:231],v210,22528
t a[232:233],v211,20480
m v[148:151],a[208:211],v[16:19],v[148:151]
t a[234:235],v211,22528
v_mul_f32 v134,v134,v218
m v[164:167],a[208:211],v[48:51],v[164:167]
v_mul_f32 v135,v135,v218
cv v128,v128,v129
m v[180:183],a[208:211],v[80:83],v[180:183]
cv v129,v130,v131
cv v130,v132,v133
cv v131,v134,v135
m v[152:155],a[212:215],v[16:19],v[152:155]
buffer_store_dwordx4 v[128:131],v215,%5,s59 offen sc1
v_mul_f32 v136,v136,v218
m v[168:171],a[212:215],v[48:51],v[168:171]
v_mul_f32 v137,v137,v218
v_mul_f32 v138,v138,v218
m v[184:187],a[212:215],v[80:83],v[184:187]
v_mul_f32 v139,v139,v218
v_mul_f32 v140,v140,v218
v_mul_f32 v141,v141,v218
m v[156:159],a[216:219],v[16:19],v[156:159]
v_mul_f32 v142,v142,v218
v_mul_f32 v143,v143,v218
m v[172:175],a[216:219],v[48:51],v[172:175]
cv v136,v136,v137
cv v137,v138,v139
m v[188:191],a[216:219],v[80:83],v[188:191]
cv v138,v140,v141
cv v139,v142,v143
buffer_store_dwordx4 v[136:139],v215,%5,s59 offen offset:1024 sc1
s_waitcnt lgkmcnt(0)
m v[144:147],a[220:223],v[20:23],v[144:147]
t a[204:205],v208,24576
t a[206:207],v208,26624
m v[160:163],a[220:223],v[52:55],v[160:163]
t a[208:209],v209,24576
t a[210:211],v209,26624
m v[176:179],a[220:223],v[84:87],v[176:179]
t a[212:213],v210,24576
t a[214:215],v210,26624
m v[148:151],a[224:227],v[20:23],v[148:151]
t a[216:217],v211,24576
t a[218:219],v211,26624
m v[164:167],a[224:227],v[52:55],v[164:167]
m v[180:183],a[224:227],v[84:87],v[180:183]
m v[152:155],a[228:231],v[20:23],v[152:155]
m v[168:171],a[228:231],v[52:55],v[168:171]
m v[184:187],a[228:231],v[84:87],v[184:187]
m v[156:159],a[232:235],v[20:23],v[156:159]
m v[172:175],a[232:235],v[52:55],v[172:175]
m v[188:191],a[232:235],v[84:87],v[188:191]
s_waitcnt lgkmcnt(0)
m v[144:147],a[204:207],v[24:27],v[144:147]
t a[220:221],v208,28672
t a[222:223],v208,30720
m v[160:163],a[204:207],v[56:59],v[160:163]
t a[224:225],v209,28672
t a[226:227],v209,30720
m v[176:179],a[204:207],v[88:91],v[176:179]
t a[228:229],v210,28672
t a[230:231],v210,30720
m v[148:151],a[208:211],v[24:27],v[148:151]
t a[232:233],v211,28672
t a[234:235],v211,30720
m v[164:167],a[208:211],v[56:59],v[164:167]
v_add_u32 v208,s48,v208
v_add_u32 v209,s48,v209
m v[180:183],a[208:211],v[88:91],v[180:183]
v_add_u32 v210,s48,v210
v_add_u32 v211,s48,v211
m v[152:155],a[212:215],v[24:27],v[152:155]
m v[168:171],a[212:215],v[56:59],v[168:171]
m v[184:187],a[212:215],v[88:91],v[184:187]
m v[156:159],a[216:219],v[24:27],v[156:159]
m v[172:175],a[216:219],v[56:59],v[172:175]
m v[188:191],a[216:219],v[88:91],v[188:191]
s_waitcnt lgkmcnt(0)
s_waitcnt vmcnt(6)
s_barrier
m v[144:147],a[220:223],v[28:31],v[144:147]
t a[204:205],v208,0
t a[206:207],v208,2048
m v[160:163],a[220:223],v[60:63],v[160:163]
t a[208:209],v209,0
t a[210:211],v209,2048
m v[176:179],a[220:223],v[92:95],v[176:179]
t a[212:213],v210,0
t a[214:215],v210,2048
m v[148:151],a[224:227],v[28:31],v[148:151]
t a[216:217],v211,0
t a[218:219],v211,2048
m v[164:167],a[224:227],v[60:63],v[164:167]
m v[180:183],a[224:227],v[92:95],v[180:183]
m v[152:155],a[228:231],v[28:31],v[152:155]
m v[168:171],a[228:231],v[60:63],v[168:171]
m v[184:187],a[228:231],v[92:95],v[184:187]
m v[156:159],a[232:235],v[28:31],v[156:159]
m v[172:175],a[232:235],v[60:63],v[172:175]
m v[188:191],a[232:235],v[92:95],v[188:191]
s_waitcnt lgkmcnt(0)
m v[96:99],a[204:207],v[0:3],0
t a[220:221],v208,4096
t a[222:223],v208,6144
m v[112:115],a[204:207],v[32:35],0
t a[224:225],v209,4096
t a[226:227],v209,6144
m v[128:131],a[204:207],v[64:67],0
t a[228:229],v210,4096
t a[230:231],v210,6144
m v[100:103],a[208:211],v[0:3],0
t a[232:233],v211,4096
t a[234:235],v211,6144
m v[116:119],a[208:211],v[32:35],0
m v[132:135],a[208:211],v[64:67],0
m v[104:107],a[212:215],v[0:3],0
m v[120:123],a[212:215],v[32:35],0
m v[136:139],a[212:215],v[64:67],0
m v[108:111],a[216:219],v[0:3],0
m v[124:127],a[216:219],v[32:35],0
m v[140:143],a[216:219],v[64:67],0
s_waitcnt lgkmcnt(0)
m v[96:99],a[220:223],v[4:7],v[96:99]
t a[204:205],v208,8192
t a[206:207],v208,10240
m v[112:115],a[220:223],v[36:39],v[112:115]
t a[208:209],v209,8192
t a[210:211],v209,10240
m v[128:131],a[220:223],v[68:71],v[128:131]
t a[212:213],v210,8192
t a[214:215],v210,10240
t a[216:217],v211,8192
m v[100:103],a[224:227],v[4:7],v[100:103]
t a[218:219],v211,10240
s_add_u32 s55,s54,0x19000
m v[116:119],a[224:227],v[36:39],v[116:119]
s_add_u32 s59,s55,0x1000
v_mul_f32 v144,v144,v216
m v[132:135],a[224:227],v[68:71],v[132:135]
v_mul_f32 v145,v145,v216
v_mul_f32 v146,v146,v216
v_mul_f32 v147,v147,v216
m v[104:107],a[228:231],v[4:7],v[104:107]
v_mul_f32 v148,v148,v216
v_mul_f32 v149,v149,v216
m v[120:123],a[228:231],v[36:39],v[120:123]
v_mul_f32 v150,v150,v216
v_mul_f32 v151,v151,v216
m v[136:139],a[228:231],v[68:71],v[136:139]
cv v144,v144,v145
cv v145,v146,v147
cv v146,v148,v149
m v[108:111],a[232:235],v[4:7],v[108:111]
cv v147,v150,v151
buffer_store_dwordx4 v[144:147],v215,%5,s55 offen sc1
m v[124:127],a[232:235],v[36:39],v[124:127]
v_mul_f32 v152,v152,v216
v_mul_f32 v153,v153,v216
m v[140:143],a[232:235],v[68:71],v[140:143]
v_mul_f32 v154,v154,v216
v_mul_f32 v155,v155,v216
v_mul_f32 v156,v156,v216
s_waitcnt lgkmcnt(0)
m v[96:99],a[204:207],v[8:11],v[96:99]
t a[220:221],v208,12288
t a[222:223],v208,14336
m v[112:115],a[204:207],v[40:43],v[112:115]
t a[224:225],v209,12288
t a[226:227],v209,14336
m v[128:131],a[204:207],v[72:75],v[128:131]
t a[228:229],v210,12288
t a[230:231],v210,14336
t a[232:233],v211,12288
m v[100:103],a[208:211],v[8:11],v[100:103]
t a[234:235],v211,14336
v_mul_f32 v157,v157,v216
m v[116:119],a[208:211],v[40:43],v[116:119]
v_mul_f32 v158,v158,v216
v_mul_f32 v159,v159,v216
m v[132:135],a[208:211],v[72:75],v[132:135]
cv v152,v152,v153
cv v153,v154,v155
cv v154,v156,v157
m v[104:107],a[212:215],v[8:11],v[104:107]
cv v155,v158,v159
buffer_store_dwordx4 v[152:155],v215,%5,s55 offen offset:1024 sc1
m v[120:123],a[212:215],v[40:43],v[120:123]
v_mul_f32 v160,v160,v217
v_mul_f32 v161,v161,v217
m v[136:139],a[212:215],v[72:75],v[136:139]
v_mul_f32 v162,v162,v217
v_mul_f32 v163,v163,v217
v_mul_f32 v164,v164,v217
m v[108:111],a[216:219],v[8:11],v[108:111]
v_mul_f32 v165,v165,v217
v_mul_f32 v166,v166,v217
m v[124:127],a[216:219],v[40:43],v[124:127]
v_mul_f32 v167,v167,v217
cv v160,v160,v161
m v[140:143],a[216:219],v[72:75],v[140:143]
cv v161,v162,v163
cv v162,v164,v165
cv v163,v166,v167
s_waitcnt lgkmcnt(0)
m v[96:99],a[220:223],v[12:15],v[96:99]
t a[204:205],v208,16384
t a[206:207],v208,18432
m v[112:115],a[220:223],v[44:47],v[112:115]
t a[208:209],v209,16384
t a[210:211],v209,18432
m v[128:131],a[220:223],v[76:79],v[128:131]
t a[212:213],v210,16384
t a[214:215],v210,18432
t a[216:217],v211,16384
m v[100:103],a[224:227],v[12:15],v[100:103]
t a[218:219],v211,18432
buffer_store_dwordx4 v[160:163],v215,%5,s55 offen offset:2048 sc1
m v[116:119],a[224:227],v[44:47],v[116:119]
v_mul_f32 v168,v168,v217
v_mul_f32 v169,v169,v217
m v[132:135],a[224:227],v[76:79],v[132:135]
v_mul_f32 v170,v170,v217
v_mul_f32 v171,v171,v217
v_mul_f32 v172,v172,v217
m v[104:107],a[228:231],v[12:15],v[104:107]
v_mul_f32 v173,v173,v217
v_mul_f32 v174,v174,v217
m v[120:123],a[228:231],v[44:47],v[120:123]
v_mul_f32 v175,v175,v217
cv v168,v168,v169
m v[136:139],a[228:231],v[76:79],v[136:139]
cv v169,v170,v171
cv v170,v172,v173
cv v171,v174,v175
m v[108:111],a[232:235],v[12:15],v[108:111]
buffer_store_dwordx4 v[168:171],v215,%5,s55 offen offset:3072 sc1
v_mul_f32 v176,v176,v218
m v[124:127],a[232:235],v[44:47],v[124:127]
v_mul_f32 v177,v177,v218
v_mul_f32 v178,v178,v218
m v[140:143],a[232:235],v[76:79],v[140:143]
v_mul_f32 v179,v179,v218
v_mul_f32 v180,v180,v218
v_mul_f32 v181,v181,v218
s_waitcnt lgkmcnt(0)
m v[96:99],a[204:207],v[16:19],v[96:99]
t a[220:221],v208,20480
t a[222:223],v208,22528
m v[112:115],a[204:207],v[48:51],v[112:115]
t a[224:225],v209,20480
t a[226:227],v209,22528
m v[128:131],a[204:207],v[80:83],v[128:131]
t a[228:229],v210,20480
t a[230:231],v210,22528
t a[232:233],v211,20480
m v[100:103],a[208:211],v[16:19],v[100:103]
t a[234:235],v211,22528
v_mul_f32 v182,v182,v218
m v[116:119],a[208:211],v[48:51],v[116:119]
v_mul_f32 v183,v183,v218
cv v176,v176,v177
m v[132:135],a[208:211],v[80:83],v[132:135]
cv v177,v178,v179
cv v178,v180,v181
cv v179,v182,v183
m v[104:107],a[212:215],v[16:19],v[104:107]
buffer_store_dwordx4 v[176:179],v215,%5,s59 offen sc1
v_mul_f32 v184,v184,v218
m v[120:123],a[212:215],v[48:51],v[120:123]
v_mul_f32 v185,v185,v218
v_mul_f32 v186,v186,v218
m v[136:139],a[212:215],v[80:83],v[136:139]
v_mul_f32 v187,v187,v218
v_mul_f32 v188,v188,v218
v_mul_f32 v189,v189,v218
m v[108:111],a[216:219],v[16:19],v[108:111]
v_mul_f32 v190,v190,v218
v_mul_f32 v191,v191,v218
m v[124:127],a[216:219],v[48:51],v[124:127]
cv v184,v184,v185
cv v185,v186,v187
m v[140:143],a[216:219],v[80:83],v[140:143]
cv v186,v188,v189
cv v187,v190,v191
buffer_store_dwordx4 v[184:187],v215,%5,s59 offen offset:1024 sc1
s_waitcnt lgkmcnt(0)
m v[96:99],a[220:223],v[20:23],v[96:99]
t a[204:205],v208,24576
t a[206:207],v208,26624
m v[112:115],a[220:223],v[52:55],v[112:115]
t a[208:209],v209,24576
t a[210:211],v209,26624
m v[128:131],a[220:223],v[84:87],v[128:131]
t a[212:213],v210,24576
t a[214:215],v210,26624
m v[100:103],a[224:227],v[20:23],v[100:103]
t a[216:217],v211,24576
t a[218:219],v211,26624
m v[116:119],a[224:227],v[52:55],v[116:119]
buffer_load_dword v233,v231,%5,%12 offen sc1
m v[132:135],a[224:227],v[84:87],v[132:135]
m v[104:107],a[228:231],v[20:23],v[104:107]
m v[120:123],a[228:231],v[52:55],v[120:123]
m v[136:139],a[228:231],v[84:87],v[136:139]
m v[108:111],a[232:235],v[20:23],v[108:111]
m v[124:127],a[232:235],v[52:55],v[124:127]
m v[140:143],a[232:235],v[84:87],v[140:143]
s_waitcnt lgkmcnt(0)
m v[96:99],a[204:207],v[24:27],v[96:99]
t a[220:221],v208,28672
t a[222:223],v208,30720
m v[112:115],a[204:207],v[56:59],v[112:115]
t a[224:225],v209,28672
t a[226:227],v209,30720
m v[128:131],a[204:207],v[88:91],v[128:131]
t a[228:229],v210,28672
t a[230:231],v210,30720
m v[100:103],a[208:211],v[24:27],v[100:103]
t a[232:233],v211,28672
t a[234:235],v211,30720
m v[116:119],a[208:211],v[56:59],v[116:119]
m v[132:135],a[208:211],v[88:91],v[132:135]
m v[104:107],a[212:215],v[24:27],v[104:107]
m v[120:123],a[212:215],v[56:59],v[120:123]
m v[136:139],a[212:215],v[88:91],v[136:139]
m v[108:111],a[216:219],v[24:27],v[108:111]
m v[124:127],a[216:219],v[56:59],v[124:127]
m v[140:143],a[216:219],v[88:91],v[140:143]
s_waitcnt lgkmcnt(0)
m v[96:99],a[220:223],v[28:31],v[96:99]
m v[112:115],a[220:223],v[60:63],v[112:115]
m v[128:131],a[220:223],v[92:95],v[128:131]
m v[100:103],a[224:227],v[28:31],v[100:103]
m v[116:119],a[224:227],v[60:63],v[116:119]
m v[132:135],a[224:227],v[92:95],v[132:135]
m v[104:107],a[228:231],v[28:31],v[104:107]
m v[120:123],a[228:231],v[60:63],v[120:123]
m v[136:139],a[228:231],v[92:95],v[136:139]
m v[108:111],a[232:235],v[28:31],v[108:111]
m v[124:127],a[232:235],v[60:63],v[124:127]
m v[140:143],a[232:235],v[92:95],v[140:143]
s_add_u32 s55,s54,0x1e000
s_add_u32 s59,s55,0x1000
v_mul_f32 v96,v96,v216
v_mul_f32 v97,v97,v216
v_mul_f32 v98,v98,v216
v_mul_f32 v99,v99,v216
v_mul_f32 v100,v100,v216
v_mul_f32 v101,v101,v216
v_mul_f32 v102,v102,v216
v_mul_f32 v103,v103,v216
cv v96,v96,v97
cv v97,v98,v99
cv v98,v100,v101
cv v99,v102,v103
buffer_store_dwordx4 v[96:99],v215,%5,s55 offen sc1
v_mul_f32 v104,v104,v216
v_mul_f32 v105,v105,v216
v_mul_f32 v106,v106,v216
v_mul_f32 v107,v107,v216
v_mul_f32 v108,v108,v216
v_mul_f32 v109,v109,v216
v_mul_f32 v110,v110,v216
v_mul_f32 v111,v111,v216
cv v104,v104,v105
cv v105,v106,v107
cv v106,v108,v109
cv v107,v110,v111
buffer_store_dwordx4 v[104:107],v215,%5,s55 offen offset:1024 sc1
v_mul_f32 v112,v112,v217
v_mul_f32 v113,v113,v217
v_mul_f32 v114,v114,v217
v_mul_f32 v115,v115,v217
v_mul_f32 v116,v116,v217
v_mul_f32 v117,v117,v217
v_mul_f32 v118,v118,v217
v_mul_f32 v119,v119,v217
cv v112,v112,v113
cv v113,v114,v115
cv v114,v116,v117
cv v115,v118,v119
buffer_store_dwordx4 v[112:115],v215,%5,s55 offen offset:2048 sc1
v_mul_f32 v120,v120,v217
v_mul_f32 v121,v121,v217
v_mul_f32 v122,v122,v217
v_mul_f32 v123,v123,v217
v_mul_f32 v124,v124,v217
v_mul_f32 v125,v125,v217
v_mul_f32 v126,v126,v217
v_mul_f32 v127,v127,v217
cv v120,v120,v121
cv v121,v122,v123
cv v122,v124,v125
cv v123,v126,v127
buffer_store_dwordx4 v[120:123],v215,%5,s55 offen offset:3072 sc1
v_mul_f32 v128,v128,v218
v_mul_f32 v129,v129,v218
v_mul_f32 v130,v130,v218
v_mul_f32 v131,v131,v218
v_mul_f32 v132,v132,v218
v_mul_f32 v133,v133,v218
v_mul_f32 v134,v134,v218
v_mul_f32 v135,v135,v218
cv v128,v128,v129
cv v129,v130,v131
cv v130,v132,v133
cv v131,v134,v135
buffer_store_dwordx4 v[128:131],v215,%5,s59 offen sc1
v_mul_f32 v136,v136,v218
v_mul_f32 v137,v137,v218
v_mul_f32 v138,v138,v218
v_mul_f32 v139,v139,v218
v_mul_f32 v140,v140,v218
v_mul_f32 v141,v141,v218
v_mul_f32 v142,v142,v218
v_mul_f32 v143,v143,v218
cv v136,v136,v137
cv v137,v138,v139
cv v138,v140,v141
cv v139,v142,v143
buffer_store_dwordx4 v[136:139],v215,%5,s59 offen offset:1024 sc1
s_waitcnt vmcnt(6)
v_sub_u32 v234,v233,v219
v_add_u32 v234,-4,v234
v_cmp_lt_u32_e32 vcc,3,v234
s_nop 0
s_cbranch_vccnz LA_ps9%=
LA_pr9%=:
s_mov_b32 s45,0
buffer_load_dwordx4 a[0:3],v228,%5,s45 offen sc1
s_mov_b32 s45,0x118000
buffer_load_dwordx4 a[4:7],v228,%5,s45 offen sc1
s_mov_b32 s45,0x230000
buffer_load_dwordx4 a[8:11],v228,%5,s45 offen sc1
s_mov_b32 s45,0x348000
buffer_load_dwordx4 a[12:15],v228,%5,s45 offen sc1
s_mov_b32 s45,0x460000
buffer_load_dwordx4 a[16:19],v228,%5,s45 offen sc1
s_mov_b32 s45,0x578000
buffer_load_dwordx4 a[20:23],v228,%5,s45 offen sc1
s_mov_b32 s45,0x690000
buffer_load_dwordx4 a[24:27],v228,%5,s45 offen sc1
s_mov_b32 s45,0x7a8000
buffer_load_dwordx4 a[28:31],v228,%5,s45 offen sc1
s_mov_b32 s45,0x8c0000
buffer_load_dwordx4 a[32:35],v228,%5,s45 offen sc1
s_mov_b32 s45,0x9d8000
buffer_load_dwordx4 a[36:39],v228,%5,s45 offen sc1
s_mov_b32 s45,0xaf0000
buffer_load_dwordx4 a[40:43],v228,%5,s45 offen sc1
s_mov_b32 s45,0xc08000
buffer_load_dwordx4 a[44:47],v228,%5,s45 offen sc1
s_mov_b32 s45,0xd20000
buffer_load_dwordx4 a[48:51],v228,%5,s45 offen sc1
s_mov_b32 s45,0xe38000
buffer_load_dwordx4 a[52:55],v228,%5,s45 offen sc1
s_mov_b32 s45,0xf50000
buffer_load_dwordx4 a[56:59],v228,%5,s45 offen sc1
s_mov_b32 s45,0x1068000
buffer_load_dwordx4 a[60:63],v228,%5,s45 offen sc1
s_waitcnt vmcnt(16)
s_barrier
s_waitcnt vmcnt(15)
m v[240:243],v[222:225],a[0:3],0
m v[244:247],v[220:223],a[0:3],0
s_waitcnt vmcnt(14)
m v[240:243],v[222:225],a[4:7],v[240:243]
m v[244:247],v[220:223],a[4:7],v[244:247]
s_waitcnt vmcnt(13)
m v[240:243],v[222:225],a[8:11],v[240:243]
m v[244:247],v[220:223],a[8:11],v[244:247]
s_waitcnt vmcnt(12)
m v[240:243],v[222:225],a[12:15],v[240:243]
m v[244:247],v[220:223],a[12:15],v[244:247]
s_waitcnt vmcnt(11)
m v[240:243],v[222:225],a[16:19],v[240:243]
m v[244:247],v[220:223],a[16:19],v[244:247]
s_waitcnt vmcnt(10)
m v[240:243],v[222:225],a[20:23],v[240:243]
m v[244:247],v[220:223],a[20:23],v[244:247]
s_waitcnt vmcnt(9)
m v[240:243],v[222:225],a[24:27],v[240:243]
m v[244:247],v[220:223],a[24:27],v[244:247]
s_waitcnt vmcnt(8)
m v[240:243],v[222:225],a[28:31],v[240:243]
m v[244:247],v[220:223],a[28:31],v[244:247]
s_waitcnt vmcnt(7)
m v[240:243],v[222:225],a[32:35],v[240:243]
m v[244:247],v[220:223],a[32:35],v[244:247]
s_waitcnt vmcnt(6)
m v[240:243],v[222:225],a[36:39],v[240:243]
m v[244:247],v[220:223],a[36:39],v[244:247]
s_waitcnt vmcnt(5)
m v[240:243],v[222:225],a[40:43],v[240:243]
m v[244:247],v[220:223],a[40:43],v[244:247]
s_waitcnt vmcnt(4)
m v[240:243],v[222:225],a[44:47],v[240:243]
m v[244:247],v[220:223],a[44:47],v[244:247]
s_waitcnt vmcnt(3)
m v[240:243],v[222:225],a[48:51],v[240:243]
m v[244:247],v[220:223],a[48:51],v[244:247]
s_waitcnt vmcnt(2)
m v[240:243],v[222:225],a[52:55],v[240:243]
m v[244:247],v[220:223],a[52:55],v[244:247]
s_waitcnt vmcnt(1)
m v[240:243],v[222:225],a[56:59],v[240:243]
m v[244:247],v[220:223],a[56:59],v[244:247]
s_waitcnt vmcnt(0)
m v[240:243],v[222:225],a[60:63],v[240:243]
m v[244:247],v[220:223],a[60:63],v[244:247]
s_nop 6
cv v248,v240,v241
cv v249,v242,v243
cv v250,v244,v245
cv v251,v246,v247
s_mov_b32 s45,0
buffer_store_dwordx2 v[248:249],v229,%10,s45 offen
buffer_store_dwordx2 v[250:251],v229,%10,s45 offen offset:32
s_mov_b32 s44,0x186a0
LA_pl11%=:
buffer_load_dword v233,v231,%5,%12 offen sc1
s_waitcnt vmcnt(0)
v_sub_u32 v234,v233,v219
v_add_u32 v234,-7,v234
v_cmp_lt_u32_e32 vcc,0,v234
s_nop 0
s_cbranch_vccz LA_pd11%=
s_sleep 2
s_sub_u32 s44,s44,1
s_cmp_lg_u32 s44,0
s_cbranch_scc1 LA_pl11%=
LA_pd11%=:
s_mov_b32 s45,0
buffer_load_dwordx4 a[64:67],v226,%5,s45 offen sc1
s_mov_b32 s45,0x118000
buffer_load_dwordx4 a[68:71],v226,%5,s45 offen sc1
s_mov_b32 s45,0x230000
buffer_load_dwordx4 a[72:75],v226,%5,s45 offen sc1
s_mov_b32 s45,0x348000
buffer_load_dwordx4 a[76:79],v226,%5,s45 offen sc1
s_mov_b32 s45,0x460000
buffer_load_dwordx4 a[80:83],v226,%5,s45 offen sc1
s_mov_b32 s45,0x578000
buffer_load_dwordx4 a[84:87],v226,%5,s45 offen sc1
s_mov_b32 s45,0x690000
buffer_load_dwordx4 a[88:91],v226,%5,s45 offen sc1
s_mov_b32 s45,0x7a8000
buffer_load_dwordx4 a[92:95],v226,%5,s45 offen sc1
s_mov_b32 s45,0x8c0000
buffer_load_dwordx4 a[96:99],v226,%5,s45 offen sc1
s_mov_b32 s45,0x9d8000
)ASM"
R"ASM(buffer_load_dwordx4 a[100:103],v226,%5,s45 offen sc1
s_mov_b32 s45,0xaf0000
buffer_load_dwordx4 a[104:107],v226,%5,s45 offen sc1
s_mov_b32 s45,0xc08000
buffer_load_dwordx4 a[108:111],v226,%5,s45 offen sc1
s_mov_b32 s45,0xd20000
buffer_load_dwordx4 a[112:115],v226,%5,s45 offen sc1
s_mov_b32 s45,0xe38000
buffer_load_dwordx4 a[116:119],v226,%5,s45 offen sc1
s_mov_b32 s45,0xf50000
buffer_load_dwordx4 a[120:123],v226,%5,s45 offen sc1
s_mov_b32 s45,0x1068000
buffer_load_dwordx4 a[124:127],v226,%5,s45 offen sc1
s_waitcnt vmcnt(15)
m v[240:243],v[222:225],a[64:67],0
m v[244:247],v[220:223],a[64:67],0
s_waitcnt vmcnt(14)
m v[240:243],v[222:225],a[68:71],v[240:243]
m v[244:247],v[220:223],a[68:71],v[244:247]
s_waitcnt vmcnt(13)
m v[240:243],v[222:225],a[72:75],v[240:243]
m v[244:247],v[220:223],a[72:75],v[244:247]
s_waitcnt vmcnt(12)
m v[240:243],v[222:225],a[76:79],v[240:243]
m v[244:247],v[220:223],a[76:79],v[244:247]
s_waitcnt vmcnt(11)
m v[240:243],v[222:225],a[80:83],v[240:243]
m v[244:247],v[220:223],a[80:83],v[244:247]
s_waitcnt vmcnt(10)
m v[240:243],v[222:225],a[84:87],v[240:243]
m v[244:247],v[220:223],a[84:87],v[244:247]
s_waitcnt vmcnt(9)
m v[240:243],v[222:225],a[88:91],v[240:243]
m v[244:247],v[220:223],a[88:91],v[244:247]
s_waitcnt vmcnt(8)
m v[240:243],v[222:225],a[92:95],v[240:243]
m v[244:247],v[220:223],a[92:95],v[244:247]
s_waitcnt vmcnt(7)
m v[240:243],v[222:225],a[96:99],v[240:243]
m v[244:247],v[220:223],a[96:99],v[244:247]
s_waitcnt vmcnt(6)
m v[240:243],v[222:225],a[100:103],v[240:243]
m v[244:247],v[220:223],a[100:103],v[244:247]
s_waitcnt vmcnt(5)
m v[240:243],v[222:225],a[104:107],v[240:243]
m v[244:247],v[220:223],a[104:107],v[244:247]
s_waitcnt vmcnt(4)
m v[240:243],v[222:225],a[108:111],v[240:243]
m v[244:247],v[220:223],a[108:111],v[244:247]
s_waitcnt vmcnt(3)
m v[240:243],v[222:225],a[112:115],v[240:243]
m v[244:247],v[220:223],a[112:115],v[244:247]
s_waitcnt vmcnt(2)
m v[240:243],v[222:225],a[116:119],v[240:243]
m v[244:247],v[220:223],a[116:119],v[244:247]
s_waitcnt vmcnt(1)
m v[240:243],v[222:225],a[120:123],v[240:243]
m v[244:247],v[220:223],a[120:123],v[244:247]
s_waitcnt vmcnt(0)
m v[240:243],v[222:225],a[124:127],v[240:243]
m v[244:247],v[220:223],a[124:127],v[244:247]
s_nop 6
cv v248,v240,v241
cv v249,v242,v243
cv v250,v244,v245
cv v251,v246,v247
s_mov_b32 s45,0
buffer_store_dwordx2 v[248:249],v227,%10,s45 offen
buffer_store_dwordx2 v[250:251],v227,%10,s45 offen offset:32
s_waitcnt vmcnt(0)
s_endpgm
LA_stub1%=:
s_call_b64 s[50:51],LA_rare0%=
s_branch LA_ret1%=
LA_stub2%=:
s_call_b64 s[50:51],LA_rare1%=
s_branch LA_ret2%=
LA_stub3%=:
s_call_b64 s[50:51],LA_rare0%=
s_branch LA_ret3%=
LA_stub4%=:
s_call_b64 s[50:51],LA_rare1%=
s_branch LA_ret4%=
LA_stub5%=:
s_call_b64 s[50:51],LA_rare0%=
s_branch LA_ret5%=
LA_stub6%=:
s_call_b64 s[50:51],LA_rare1%=
s_branch LA_ret6%=
LA_stub7%=:
s_call_b64 s[50:51],LA_rare0%=
s_branch LA_ret7%=
LA_stub8%=:
s_call_b64 s[50:51],LA_rare1%=
s_branch LA_ret8%=
LA_ps9%=:
s_mov_b32 s44,0x186a0
LA_pl10%=:
buffer_load_dword v233,v231,%5,%12 offen sc1
s_waitcnt vmcnt(0)
v_sub_u32 v234,v233,v219
v_add_u32 v234,-4,v234
v_cmp_lt_u32_e32 vcc,3,v234
s_nop 0
s_cbranch_vccz LA_pr9%=
s_sleep 2
s_sub_u32 s44,s44,1
s_cmp_lg_u32 s44,0
s_cbranch_scc1 LA_pl10%=
s_branch LA_pr9%=
LA_rare0%=:
s_nop 7
s_nop 1
v_max3_f32 v220,v4,v5,v6
v_max3_f32 v220,v220,v7,v12
v_max3_f32 v220,v220,v13,v14
v_max_f32 v220,v220,v15
v_mov_b32 v225,v220
s_nop 1
v_permlane32_swap_b32 v220,v225
v_max_f32 v220,v220,v225
v_mov_b32 v225,v220
s_nop 1
v_permlane16_swap_b32 v220,v225
v_max_f32 v220,v220,v225
v_add_f32 v220,0x40e00000,v220
v_max_f32 v220,0,v220
v_sub_f32 v224,0,v220
v_exp_f32 v224,v224
v_sub_f32 v4,v4,v220
v_sub_f32 v5,v5,v220
v_sub_f32 v6,v6,v220
v_sub_f32 v7,v7,v220
v_sub_f32 v12,v12,v220
v_sub_f32 v13,v13,v220
v_sub_f32 v14,v14,v220
v_sub_f32 v15,v15,v220
v_sub_f32 v52,v52,v220
v_sub_f32 v53,v53,v220
v_sub_f32 v54,v54,v220
v_sub_f32 v55,v55,v220
v_sub_f32 v60,v60,v220
v_sub_f32 v61,v61,v220
v_sub_f32 v62,v62,v220
v_sub_f32 v63,v63,v220
v_sub_f32 v124,v124,v220
v_sub_f32 v125,v125,v220
v_sub_f32 v126,v126,v220
v_sub_f32 v127,v127,v220
ar v230,a0
ar v231,a1
ar v232,a2
ar v233,a3
v_mul_f32 v230,v230,v224
v_mul_f32 v231,v231,v224
v_mul_f32 v232,v232,v224
v_mul_f32 v233,v233,v224
v_accvgpr_write_b32 a0,v230
v_accvgpr_write_b32 a1,v231
v_accvgpr_write_b32 a2,v232
v_accvgpr_write_b32 a3,v233
ar v230,a4
ar v231,a5
ar v232,a6
ar v233,a7
v_mul_f32 v230,v230,v224
v_mul_f32 v231,v231,v224
v_mul_f32 v232,v232,v224
v_mul_f32 v233,v233,v224
v_accvgpr_write_b32 a4,v230
v_accvgpr_write_b32 a5,v231
v_accvgpr_write_b32 a6,v232
v_accvgpr_write_b32 a7,v233
ar v230,a8
ar v231,a9
ar v232,a10
ar v233,a11
v_mul_f32 v230,v230,v224
v_mul_f32 v231,v231,v224
v_mul_f32 v232,v232,v224
v_mul_f32 v233,v233,v224
v_accvgpr_write_b32 a8,v230
v_accvgpr_write_b32 a9,v231
v_accvgpr_write_b32 a10,v232
v_accvgpr_write_b32 a11,v233
ar v230,a12
ar v231,a13
ar v232,a14
ar v233,a15
v_mul_f32 v230,v230,v224
v_mul_f32 v231,v231,v224
v_mul_f32 v232,v232,v224
v_mul_f32 v233,v233,v224
v_accvgpr_write_b32 a12,v230
v_accvgpr_write_b32 a13,v231
v_accvgpr_write_b32 a14,v232
v_accvgpr_write_b32 a15,v233
ar v230,a16
ar v231,a17
ar v232,a18
ar v233,a19
v_mul_f32 v230,v230,v224
v_mul_f32 v231,v231,v224
v_mul_f32 v232,v232,v224
v_mul_f32 v233,v233,v224
v_accvgpr_write_b32 a16,v230
v_accvgpr_write_b32 a17,v231
v_accvgpr_write_b32 a18,v232
v_accvgpr_write_b32 a19,v233
ar v230,a20
ar v231,a21
ar v232,a22
ar v233,a23
v_mul_f32 v230,v230,v224
v_mul_f32 v231,v231,v224
v_mul_f32 v232,v232,v224
v_mul_f32 v233,v233,v224
v_accvgpr_write_b32 a20,v230
v_accvgpr_write_b32 a21,v231
v_accvgpr_write_b32 a22,v232
v_accvgpr_write_b32 a23,v233
ar v230,a24
ar v231,a25
ar v232,a26
ar v233,a27
v_mul_f32 v230,v230,v224
v_mul_f32 v231,v231,v224
v_mul_f32 v232,v232,v224
v_mul_f32 v233,v233,v224
v_accvgpr_write_b32 a24,v230
v_accvgpr_write_b32 a25,v231
v_accvgpr_write_b32 a26,v232
v_accvgpr_write_b32 a27,v233
ar v230,a28
ar v231,a29
ar v232,a30
ar v233,a31
v_mul_f32 v230,v230,v224
v_mul_f32 v231,v231,v224
v_mul_f32 v232,v232,v224
v_mul_f32 v233,v233,v224
v_accvgpr_write_b32 a28,v230
v_accvgpr_write_b32 a29,v231
v_accvgpr_write_b32 a30,v232
v_accvgpr_write_b32 a31,v233
ar v230,a32
ar v231,a33
ar v232,a34
ar v233,a35
v_mul_f32 v230,v230,v224
v_mul_f32 v231,v231,v224
v_mul_f32 v232,v232,v224
v_mul_f32 v233,v233,v224
v_accvgpr_write_b32 a32,v230
v_accvgpr_write_b32 a33,v231
v_accvgpr_write_b32 a34,v232
v_accvgpr_write_b32 a35,v233
ar v230,a36
ar v231,a37
ar v232,a38
ar v233,a39
v_mul_f32 v230,v230,v224
v_mul_f32 v231,v231,v224
v_mul_f32 v232,v232,v224
v_mul_f32 v233,v233,v224
v_accvgpr_write_b32 a36,v230
v_accvgpr_write_b32 a37,v231
v_accvgpr_write_b32 a38,v232
v_accvgpr_write_b32 a39,v233
ar v230,a40
ar v231,a41
ar v232,a42
ar v233,a43
v_mul_f32 v230,v230,v224
v_mul_f32 v231,v231,v224
v_mul_f32 v232,v232,v224
v_mul_f32 v233,v233,v224
v_accvgpr_write_b32 a40,v230
v_accvgpr_write_b32 a41,v231
v_accvgpr_write_b32 a42,v232
v_accvgpr_write_b32 a43,v233
ar v230,a44
ar v231,a45
ar v232,a46
ar v233,a47
v_mul_f32 v230,v230,v224
v_mul_f32 v231,v231,v224
v_mul_f32 v232,v232,v224
v_mul_f32 v233,v233,v224
v_accvgpr_write_b32 a44,v230
v_accvgpr_write_b32 a45,v231
v_accvgpr_write_b32 a46,v232
v_accvgpr_write_b32 a47,v233
ar v230,a48
ar v231,a49
ar v232,a50
ar v233,a51
v_mul_f32 v230,v230,v224
v_mul_f32 v231,v231,v224
v_mul_f32 v232,v232,v224
v_mul_f32 v233,v233,v224
v_accvgpr_write_b32 a48,v230
v_accvgpr_write_b32 a49,v231
v_accvgpr_write_b32 a50,v232
v_accvgpr_write_b32 a51,v233
ar v230,a52
ar v231,a53
ar v232,a54
ar v233,a55
v_mul_f32 v230,v230,v224
v_mul_f32 v231,v231,v224
v_mul_f32 v232,v232,v224
v_mul_f32 v233,v233,v224
v_accvgpr_write_b32 a52,v230
v_accvgpr_write_b32 a53,v231
v_accvgpr_write_b32 a54,v232
v_accvgpr_write_b32 a55,v233
ar v230,a56
ar v231,a57
ar v232,a58
ar v233,a59
v_mul_f32 v230,v230,v224
v_mul_f32 v231,v231,v224
v_mul_f32 v232,v232,v224
v_mul_f32 v233,v233,v224
v_accvgpr_write_b32 a56,v230
v_accvgpr_write_b32 a57,v231
v_accvgpr_write_b32 a58,v232
v_accvgpr_write_b32 a59,v233
ar v230,a60
ar v231,a61
ar v232,a62
ar v233,a63
v_mul_f32 v230,v230,v224
v_mul_f32 v231,v231,v224
v_mul_f32 v232,v232,v224
v_mul_f32 v233,v233,v224
v_accvgpr_write_b32 a60,v230
v_accvgpr_write_b32 a61,v231
v_accvgpr_write_b32 a62,v232
v_accvgpr_write_b32 a63,v233
ar v230,a192
ar v231,a193
ar v232,a194
ar v233,a195
v_mul_f32 v230,v230,v224
v_mul_f32 v231,v231,v224
v_mul_f32 v232,v232,v224
v_mul_f32 v233,v233,v224
v_accvgpr_write_b32 a192,v230
v_accvgpr_write_b32 a193,v231
v_accvgpr_write_b32 a194,v232
v_accvgpr_write_b32 a195,v233
v_max3_f32 v221,v20,v21,v22
v_max3_f32 v221,v221,v23,v28
v_max3_f32 v221,v221,v29,v30
v_max_f32 v221,v221,v31
v_mov_b32 v225,v221
s_nop 1
v_permlane32_swap_b32 v221,v225
v_max_f32 v221,v221,v225
v_mov_b32 v225,v221
s_nop 1
v_permlane16_swap_b32 v221,v225
v_max_f32 v221,v221,v225
v_add_f32 v221,0x40e00000,v221
v_max_f32 v221,0,v221
v_sub_f32 v224,0,v221
v_exp_f32 v224,v224
v_sub_f32 v20,v20,v221
v_sub_f32 v21,v21,v221
v_sub_f32 v22,v22,v221
v_sub_f32 v23,v23,v221
v_sub_f32 v28,v28,v221
v_sub_f32 v29,v29,v221
v_sub_f32 v30,v30,v221
v_sub_f32 v31,v31,v221
v_sub_f32 v68,v68,v221
v_sub_f32 v69,v69,v221
v_sub_f32 v70,v70,v221
v_sub_f32 v71,v71,v221
v_sub_f32 v76,v76,v221
v_sub_f32 v77,v77,v221
v_sub_f32 v78,v78,v221
v_sub_f32 v79,v79,v221
v_sub_f32 v132,v132,v221
v_sub_f32 v133,v133,v221
v_sub_f32 v134,v134,v221
v_sub_f32 v135,v135,v221
ar v230,a64
ar v231,a65
ar v232,a66
ar v233,a67
v_mul_f32 v230,v230,v224
v_mul_f32 v231,v231,v224
v_mul_f32 v232,v232,v224
v_mul_f32 v233,v233,v224
v_accvgpr_write_b32 a64,v230
v_accvgpr_write_b32 a65,v231
v_accvgpr_write_b32 a66,v232
v_accvgpr_write_b32 a67,v233
ar v230,a68
ar v231,a69
ar v232,a70
ar v233,a71
v_mul_f32 v230,v230,v224
v_mul_f32 v231,v231,v224
v_mul_f32 v232,v232,v224
v_mul_f32 v233,v233,v224
v_accvgpr_write_b32 a68,v230
v_accvgpr_write_b32 a69,v231
v_accvgpr_write_b32 a70,v232
v_accvgpr_write_b32 a71,v233
ar v230,a72
ar v231,a73
ar v232,a74
ar v233,a75
v_mul_f32 v230,v230,v224
v_mul_f32 v231,v231,v224
v_mul_f32 v232,v232,v224
v_mul_f32 v233,v233,v224
v_accvgpr_write_b32 a72,v230
v_accvgpr_write_b32 a73,v231
v_accvgpr_write_b32 a74,v232
v_accvgpr_write_b32 a75,v233
ar v230,a76
ar v231,a77
ar v232,a78
ar v233,a79
v_mul_f32 v230,v230,v224
v_mul_f32 v231,v231,v224
v_mul_f32 v232,v232,v224
v_mul_f32 v233,v233,v224
v_accvgpr_write_b32 a76,v230
v_accvgpr_write_b32 a77,v231
v_accvgpr_write_b32 a78,v232
v_accvgpr_write_b32 a79,v233
ar v230,a80
ar v231,a81
ar v232,a82
ar v233,a83
v_mul_f32 v230,v230,v224
v_mul_f32 v231,v231,v224
v_mul_f32 v232,v232,v224
v_mul_f32 v233,v233,v224
v_accvgpr_write_b32 a80,v230
v_accvgpr_write_b32 a81,v231
v_accvgpr_write_b32 a82,v232
v_accvgpr_write_b32 a83,v233
ar v230,a84
ar v231,a85
ar v232,a86
ar v233,a87
v_mul_f32 v230,v230,v224
v_mul_f32 v231,v231,v224
v_mul_f32 v232,v232,v224
v_mul_f32 v233,v233,v224
v_accvgpr_write_b32 a84,v230
v_accvgpr_write_b32 a85,v231
v_accvgpr_write_b32 a86,v232
v_accvgpr_write_b32 a87,v233
ar v230,a88
ar v231,a89
ar v232,a90
ar v233,a91
v_mul_f32 v230,v230,v224
v_mul_f32 v231,v231,v224
v_mul_f32 v232,v232,v224
v_mul_f32 v233,v233,v224
v_accvgpr_write_b32 a88,v230
v_accvgpr_write_b32 a89,v231
v_accvgpr_write_b32 a90,v232
v_accvgpr_write_b32 a91,v233
ar v230,a92
ar v231,a93
ar v232,a94
ar v233,a95
v_mul_f32 v230,v230,v224
v_mul_f32 v231,v231,v224
v_mul_f32 v232,v232,v224
v_mul_f32 v233,v233,v224
v_accvgpr_write_b32 a92,v230
v_accvgpr_write_b32 a93,v231
v_accvgpr_write_b32 a94,v232
v_accvgpr_write_b32 a95,v233
ar v230,a96
ar v231,a97
ar v232,a98
ar v233,a99
v_mul_f32 v230,v230,v224
v_mul_f32 v231,v231,v224
v_mul_f32 v232,v232,v224
v_mul_f32 v233,v233,v224
v_accvgpr_write_b32 a96,v230
v_accvgpr_write_b32 a97,v231
v_accvgpr_write_b32 a98,v232
v_accvgpr_write_b32 a99,v233
ar v230,a100
ar v231,a101
ar v232,a102
ar v233,a103
v_mul_f32 v230,v230,v224
v_mul_f32 v231,v231,v224
v_mul_f32 v232,v232,v224
v_mul_f32 v233,v233,v224
v_accvgpr_write_b32 a100,v230
v_accvgpr_write_b32 a101,v231
v_accvgpr_write_b32 a102,v232
v_accvgpr_write_b32 a103,v233
ar v230,a104
ar v231,a105
ar v232,a106
ar v233,a107
v_mul_f32 v230,v230,v224
v_mul_f32 v231,v231,v224
v_mul_f32 v232,v232,v224
v_mul_f32 v233,v233,v224
v_accvgpr_write_b32 a104,v230
v_accvgpr_write_b32 a105,v231
v_accvgpr_write_b32 a106,v232
v_accvgpr_write_b32 a107,v233
ar v230,a108
ar v231,a109
ar v232,a110
ar v233,a111
v_mul_f32 v230,v230,v224
v_mul_f32 v231,v231,v224
v_mul_f32 v232,v232,v224
v_mul_f32 v233,v233,v224
v_accvgpr_write_b32 a108,v230
v_accvgpr_write_b32 a109,v231
v_accvgpr_write_b32 a110,v232
v_accvgpr_write_b32 a111,v233
ar v230,a112
ar v231,a113
ar v232,a114
ar v233,a115
v_mul_f32 v230,v230,v224
v_mul_f32 v231,v231,v224
v_mul_f32 v232,v232,v224
v_mul_f32 v233,v233,v224
v_accvgpr_write_b32 a112,v230
v_accvgpr_write_b32 a113,v231
v_accvgpr_write_b32 a114,v232
v_accvgpr_write_b32 a115,v233
ar v230,a116
ar v231,a117
ar v232,a118
ar v233,a119
v_mul_f32 v230,v230,v224
v_mul_f32 v231,v231,v224
v_mul_f32 v232,v232,v224
v_mul_f32 v233,v233,v224
v_accvgpr_write_b32 a116,v230
v_accvgpr_write_b32 a117,v231
v_accvgpr_write_b32 a118,v232
v_accvgpr_write_b32 a119,v233
ar v230,a120
ar v231,a121
ar v232,a122
ar v233,a123
v_mul_f32 v230,v230,v224
v_mul_f32 v231,v231,v224
v_mul_f32 v232,v232,v224
v_mul_f32 v233,v233,v224
v_accvgpr_write_b32 a120,v230
v_accvgpr_write_b32 a121,v231
v_accvgpr_write_b32 a122,v232
v_accvgpr_write_b32 a123,v233
ar v230,a124
ar v231,a125
ar v232,a126
ar v233,a127
v_mul_f32 v230,v230,v224
v_mul_f32 v231,v231,v224
v_mul_f32 v232,v232,v224
v_mul_f32 v233,v233,v224
v_accvgpr_write_b32 a124,v230
v_accvgpr_write_b32 a125,v231
v_accvgpr_write_b32 a126,v232
v_accvgpr_write_b32 a127,v233
ar v230,a196
ar v231,a197
ar v232,a198
ar v233,a199
v_mul_f32 v230,v230,v224
v_mul_f32 v231,v231,v224
v_mul_f32 v232,v232,v224
v_mul_f32 v233,v233,v224
v_accvgpr_write_b32 a196,v230
v_accvgpr_write_b32 a197,v231
v_accvgpr_write_b32 a198,v232
v_accvgpr_write_b32 a199,v233
v_max3_f32 v222,v36,v37,v38
v_max3_f32 v222,v222,v39,v44
v_max3_f32 v222,v222,v45,v46
v_max_f32 v222,v222,v47
v_mov_b32 v225,v222
s_nop 1
v_permlane32_swap_b32 v222,v225
v_max_f32 v222,v222,v225
v_mov_b32 v225,v222
s_nop 1
v_permlane16_swap_b32 v222,v225
v_max_f32 v222,v222,v225
v_add_f32 v222,0x40e00000,v222
v_max_f32 v222,0,v222
v_sub_f32 v224,0,v222
v_exp_f32 v224,v224
v_sub_f32 v36,v36,v222
v_sub_f32 v37,v37,v222
v_sub_f32 v38,v38,v222
v_sub_f32 v39,v39,v222
v_sub_f32 v44,v44,v222
v_sub_f32 v45,v45,v222
v_sub_f32 v46,v46,v222
v_sub_f32 v47,v47,v222
v_sub_f32 v84,v84,v222
v_sub_f32 v85,v85,v222
v_sub_f32 v86,v86,v222
v_sub_f32 v87,v87,v222
v_sub_f32 v92,v92,v222
v_sub_f32 v93,v93,v222
v_sub_f32 v94,v94,v222
v_sub_f32 v95,v95,v222
v_sub_f32 v140,v140,v222
v_sub_f32 v141,v141,v222
v_sub_f32 v142,v142,v222
v_sub_f32 v143,v143,v222
ar v230,a128
ar v231,a129
ar v232,a130
ar v233,a131
v_mul_f32 v230,v230,v224
v_mul_f32 v231,v231,v224
v_mul_f32 v232,v232,v224
v_mul_f32 v233,v233,v224
v_accvgpr_write_b32 a128,v230
v_accvgpr_write_b32 a129,v231
v_accvgpr_write_b32 a130,v232
v_accvgpr_write_b32 a131,v233
ar v230,a132
ar v231,a133
ar v232,a134
ar v233,a135
v_mul_f32 v230,v230,v224
v_mul_f32 v231,v231,v224
v_mul_f32 v232,v232,v224
v_mul_f32 v233,v233,v224
v_accvgpr_write_b32 a132,v230
v_accvgpr_write_b32 a133,v231
v_accvgpr_write_b32 a134,v232
v_accvgpr_write_b32 a135,v233
ar v230,a136
ar v231,a137
ar v232,a138
ar v233,a139
v_mul_f32 v230,v230,v224
v_mul_f32 v231,v231,v224
v_mul_f32 v232,v232,v224
v_mul_f32 v233,v233,v224
v_accvgpr_write_b32 a136,v230
v_accvgpr_write_b32 a137,v231
v_accvgpr_write_b32 a138,v232
v_accvgpr_write_b32 a139,v233
ar v230,a140
ar v231,a141
ar v232,a142
ar v233,a143
v_mul_f32 v230,v230,v224
v_mul_f32 v231,v231,v224
v_mul_f32 v232,v232,v224
v_mul_f32 v233,v233,v224
v_accvgpr_write_b32 a140,v230
v_accvgpr_write_b32 a141,v231
v_accvgpr_write_b32 a142,v232
v_accvgpr_write_b32 a143,v233
ar v230,a144
ar v231,a145
ar v232,a146
ar v233,a147
v_mul_f32 v230,v230,v224
v_mul_f32 v231,v231,v224
v_mul_f32 v232,v232,v224
v_mul_f32 v233,v233,v224
v_accvgpr_write_b32 a144,v230
v_accvgpr_write_b32 a145,v231
v_accvgpr_write_b32 a146,v232
v_accvgpr_write_b32 a147,v233
ar v230,a148
ar v231,a149
ar v232,a150
ar v233,a151
v_mul_f32 v230,v230,v224
v_mul_f32 v231,v231,v224
v_mul_f32 v232,v232,v224
v_mul_f32 v233,v233,v224
v_accvgpr_write_b32 a148,v230
v_accvgpr_write_b32 a149,v231
v_accvgpr_write_b32 a150,v232
v_accvgpr_write_b32 a151,v233
ar v230,a152
ar v231,a153
ar v232,a154
ar v233,a155
v_mul_f32 v230,v230,v224
v_mul_f32 v231,v231,v224
v_mul_f32 v232,v232,v224
v_mul_f32 v233,v233,v224
v_accvgpr_write_b32 a152,v230
v_accvgpr_write_b32 a153,v231
v_accvgpr_write_b32 a154,v232
v_accvgpr_write_b32 a155,v233
ar v230,a156
ar v231,a157
ar v232,a158
ar v233,a159
v_mul_f32 v230,v230,v224
v_mul_f32 v231,v231,v224
v_mul_f32 v232,v232,v224
v_mul_f32 v233,v233,v224
v_accvgpr_write_b32 a156,v230
v_accvgpr_write_b32 a157,v231
v_accvgpr_write_b32 a158,v232
v_accvgpr_write_b32 a159,v233
ar v230,a160
ar v231,a161
ar v232,a162
ar v233,a163
v_mul_f32 v230,v230,v224
v_mul_f32 v231,v231,v224
v_mul_f32 v232,v232,v224
v_mul_f32 v233,v233,v224
v_accvgpr_write_b32 a160,v230
v_accvgpr_write_b32 a161,v231
v_accvgpr_write_b32 a162,v232
v_accvgpr_write_b32 a163,v233
ar v230,a164
ar v231,a165
ar v232,a166
ar v233,a167
v_mul_f32 v230,v230,v224
v_mul_f32 v231,v231,v224
v_mul_f32 v232,v232,v224
v_mul_f32 v233,v233,v224
v_accvgpr_write_b32 a164,v230
v_accvgpr_write_b32 a165,v231
v_accvgpr_write_b32 a166,v232
v_accvgpr_write_b32 a167,v233
ar v230,a168
ar v231,a169
ar v232,a170
ar v233,a171
v_mul_f32 v230,v230,v224
v_mul_f32 v231,v231,v224
v_mul_f32 v232,v232,v224
v_mul_f32 v233,v233,v224
v_accvgpr_write_b32 a168,v230
v_accvgpr_write_b32 a169,v231
v_accvgpr_write_b32 a170,v232
v_accvgpr_write_b32 a171,v233
ar v230,a172
ar v231,a173
ar v232,a174
ar v233,a175
v_mul_f32 v230,v230,v224
v_mul_f32 v231,v231,v224
v_mul_f32 v232,v232,v224
v_mul_f32 v233,v233,v224
v_accvgpr_write_b32 a172,v230
v_accvgpr_write_b32 a173,v231
v_accvgpr_write_b32 a174,v232
v_accvgpr_write_b32 a175,v233
ar v230,a176
ar v231,a177
ar v232,a178
ar v233,a179
v_mul_f32 v230,v230,v224
v_mul_f32 v231,v231,v224
v_mul_f32 v232,v232,v224
v_mul_f32 v233,v233,v224
v_accvgpr_write_b32 a176,v230
v_accvgpr_write_b32 a177,v231
v_accvgpr_write_b32 a178,v232
v_accvgpr_write_b32 a179,v233
ar v230,a180
ar v231,a181
ar v232,a182
ar v233,a183
v_mul_f32 v230,v230,v224
v_mul_f32 v231,v231,v224
v_mul_f32 v232,v232,v224
v_mul_f32 v233,v233,v224
v_accvgpr_write_b32 a180,v230
v_accvgpr_write_b32 a181,v231
v_accvgpr_write_b32 a182,v232
v_accvgpr_write_b32 a183,v233
ar v230,a184
ar v231,a185
ar v232,a186
ar v233,a187
v_mul_f32 v230,v230,v224
v_mul_f32 v231,v231,v224
v_mul_f32 v232,v232,v224
v_mul_f32 v233,v233,v224
v_accvgpr_write_b32 a184,v230
v_accvgpr_write_b32 a185,v231
v_accvgpr_write_b32 a186,v232
v_accvgpr_write_b32 a187,v233
ar v230,a188
ar v231,a189
ar v232,a190
ar v233,a191
v_mul_f32 v230,v230,v224
v_mul_f32 v231,v231,v224
v_mul_f32 v232,v232,v224
v_mul_f32 v233,v233,v224
v_accvgpr_write_b32 a188,v230
v_accvgpr_write_b32 a189,v231
v_accvgpr_write_b32 a190,v232
v_accvgpr_write_b32 a191,v233
ar v230,a200
ar v231,a201
ar v232,a202
ar v233,a203
v_mul_f32 v230,v230,v224
v_mul_f32 v231,v231,v224
v_mul_f32 v232,v232,v224
v_mul_f32 v233,v233,v224
v_accvgpr_write_b32 a200,v230
v_accvgpr_write_b32 a201,v231
v_accvgpr_write_b32 a202,v232
v_accvgpr_write_b32 a203,v233
v_exp_f32 v220,v4
v_exp_f32 v221,v5
v_exp_f32 v222,v6
v_exp_f32 v223,v7
cv v100,v220,v221
v_exp_f32 v224,v12
v_exp_f32 v225,v13
cv v101,v222,v223
v_exp_f32 v220,v14
v_exp_f32 v221,v15
cv v102,v224,v225
v_exp_f32 v222,v20
v_exp_f32 v223,v21
cv v103,v220,v221
v_exp_f32 v224,v22
v_exp_f32 v225,v23
cv v108,v222,v223
v_exp_f32 v220,v28
v_exp_f32 v221,v29
cv v109,v224,v225
v_exp_f32 v222,v30
v_exp_f32 v223,v31
cv v110,v220,v221
v_exp_f32 v224,v36
v_exp_f32 v225,v37
cv v111,v222,v223
v_exp_f32 v220,v38
v_exp_f32 v221,v39
cv v116,v224,v225
v_exp_f32 v222,v44
v_exp_f32 v223,v45
cv v117,v220,v221
v_exp_f32 v224,v46
v_exp_f32 v225,v47
cv v118,v222,v223
cv v119,v224,v225
v_or3_b32 v228,v100,v101,v102
v_or3_b32 v228,v228,v103,v108
v_or3_b32 v228,v228,v109,v110
v_or3_b32 v228,v228,v111,v116
v_or3_b32 v228,v228,v117,v118
v_or_b32 v228,v228,v119
v_and_b32 v228,0x40004000,v228
s_nop 7
s_setpc_b64 s[50:51]
LA_rare1%=:
s_nop 7
s_nop 1
v_max3_f32 v220,v52,v53,v54
v_max3_f32 v220,v220,v55,v60
v_max3_f32 v220,v220,v61,v62
v_max_f32 v220,v220,v63
v_mov_b32 v225,v220
s_nop 1
v_permlane32_swap_b32 v220,v225
v_max_f32 v220,v220,v225
v_mov_b32 v225,v220
s_nop 1
v_permlane16_swap_b32 v220,v225
v_max_f32 v220,v220,v225
v_add_f32 v220,0x40e00000,v220
v_max_f32 v220,0,v220
v_sub_f32 v224,0,v220
v_exp_f32 v224,v224
v_sub_f32 v4,v4,v220
v_sub_f32 v5,v5,v220
v_sub_f32 v6,v6,v220
v_sub_f32 v7,v7,v220
v_sub_f32 v12,v12,v220
v_sub_f32 v13,v13,v220
v_sub_f32 v14,v14,v220
v_sub_f32 v15,v15,v220
v_sub_f32 v52,v52,v220
v_sub_f32 v53,v53,v220
v_sub_f32 v54,v54,v220
v_sub_f32 v55,v55,v220
v_sub_f32 v60,v60,v220
v_sub_f32 v61,v61,v220
v_sub_f32 v62,v62,v220
v_sub_f32 v63,v63,v220
v_sub_f32 v124,v124,v220
v_sub_f32 v125,v125,v220
v_sub_f32 v126,v126,v220
v_sub_f32 v127,v127,v220
ar v230,a0
ar v231,a1
ar v232,a2
ar v233,a3
v_mul_f32 v230,v230,v224
v_mul_f32 v231,v231,v224
v_mul_f32 v232,v232,v224
v_mul_f32 v233,v233,v224
v_accvgpr_write_b32 a0,v230
v_accvgpr_write_b32 a1,v231
v_accvgpr_write_b32 a2,v232
v_accvgpr_write_b32 a3,v233
ar v230,a4
ar v231,a5
ar v232,a6
ar v233,a7
v_mul_f32 v230,v230,v224
v_mul_f32 v231,v231,v224
v_mul_f32 v232,v232,v224
v_mul_f32 v233,v233,v224
v_accvgpr_write_b32 a4,v230
v_accvgpr_write_b32 a5,v231
v_accvgpr_write_b32 a6,v232
v_accvgpr_write_b32 a7,v233
ar v230,a8
ar v231,a9
ar v232,a10
ar v233,a11
v_mul_f32 v230,v230,v224
v_mul_f32 v231,v231,v224
v_mul_f32 v232,v232,v224
v_mul_f32 v233,v233,v224
v_accvgpr_write_b32 a8,v230
v_accvgpr_write_b32 a9,v231
v_accvgpr_write_b32 a10,v232
v_accvgpr_write_b32 a11,v233
ar v230,a12
ar v231,a13
ar v232,a14
ar v233,a15
v_mul_f32 v230,v230,v224
v_mul_f32 v231,v231,v224
v_mul_f32 v232,v232,v224
v_mul_f32 v233,v233,v224
v_accvgpr_write_b32 a12,v230
v_accvgpr_write_b32 a13,v231
v_accvgpr_write_b32 a14,v232
v_accvgpr_write_b32 a15,v233
ar v230,a16
ar v231,a17
ar v232,a18
ar v233,a19
v_mul_f32 v230,v230,v224
v_mul_f32 v231,v231,v224
v_mul_f32 v232,v232,v224
v_mul_f32 v233,v233,v224
v_accvgpr_write_b32 a16,v230
v_accvgpr_write_b32 a17,v231
v_accvgpr_write_b32 a18,v232
v_accvgpr_write_b32 a19,v233
ar v230,a20
ar v231,a21
ar v232,a22
ar v233,a23
v_mul_f32 v230,v230,v224
v_mul_f32 v231,v231,v224
v_mul_f32 v232,v232,v224
v_mul_f32 v233,v233,v224
v_accvgpr_write_b32 a20,v230
v_accvgpr_write_b32 a21,v231
v_accvgpr_write_b32 a22,v232
v_accvgpr_write_b32 a23,v233
ar v230,a24
ar v231,a25
ar v232,a26
ar v233,a27
v_mul_f32 v230,v230,v224
v_mul_f32 v231,v231,v224
v_mul_f32 v232,v232,v224
v_mul_f32 v233,v233,v224
v_accvgpr_write_b32 a24,v230
v_accvgpr_write_b32 a25,v231
v_accvgpr_write_b32 a26,v232
v_accvgpr_write_b32 a27,v233
ar v230,a28
ar v231,a29
ar v232,a30
ar v233,a31
v_mul_f32 v230,v230,v224
v_mul_f32 v231,v231,v224
v_mul_f32 v232,v232,v224
v_mul_f32 v233,v233,v224
v_accvgpr_write_b32 a28,v230
v_accvgpr_write_b32 a29,v231
v_accvgpr_write_b32 a30,v232
v_accvgpr_write_b32 a31,v233
ar v230,a32
ar v231,a33
ar v232,a34
ar v233,a35
v_mul_f32 v230,v230,v224
v_mul_f32 v231,v231,v224
v_mul_f32 v232,v232,v224
v_mul_f32 v233,v233,v224
v_accvgpr_write_b32 a32,v230
v_accvgpr_write_b32 a33,v231
v_accvgpr_write_b32 a34,v232
v_accvgpr_write_b32 a35,v233
ar v230,a36
ar v231,a37
ar v232,a38
ar v233,a39
v_mul_f32 v230,v230,v224
v_mul_f32 v231,v231,v224
v_mul_f32 v232,v232,v224
v_mul_f32 v233,v233,v224
v_accvgpr_write_b32 a36,v230
v_accvgpr_write_b32 a37,v231
v_accvgpr_write_b32 a38,v232
v_accvgpr_write_b32 a39,v233
ar v230,a40
ar v231,a41
ar v232,a42
ar v233,a43
v_mul_f32 v230,v230,v224
v_mul_f32 v231,v231,v224
v_mul_f32 v232,v232,v224
v_mul_f32 v233,v233,v224
v_accvgpr_write_b32 a40,v230
v_accvgpr_write_b32 a41,v231
v_accvgpr_write_b32 a42,v232
v_accvgpr_write_b32 a43,v233
ar v230,a44
ar v231,a45
ar v232,a46
ar v233,a47
v_mul_f32 v230,v230,v224
v_mul_f32 v231,v231,v224
v_mul_f32 v232,v232,v224
v_mul_f32 v233,v233,v224
v_accvgpr_write_b32 a44,v230
v_accvgpr_write_b32 a45,v231
v_accvgpr_write_b32 a46,v232
v_accvgpr_write_b32 a47,v233
ar v230,a48
ar v231,a49
ar v232,a50
ar v233,a51
v_mul_f32 v230,v230,v224
v_mul_f32 v231,v231,v224
v_mul_f32 v232,v232,v224
v_mul_f32 v233,v233,v224
v_accvgpr_write_b32 a48,v230
v_accvgpr_write_b32 a49,v231
v_accvgpr_write_b32 a50,v232
v_accvgpr_write_b32 a51,v233
ar v230,a52
ar v231,a53
ar v232,a54
ar v233,a55
v_mul_f32 v230,v230,v224
v_mul_f32 v231,v231,v224
v_mul_f32 v232,v232,v224
v_mul_f32 v233,v233,v224
v_accvgpr_write_b32 a52,v230
v_accvgpr_write_b32 a53,v231
v_accvgpr_write_b32 a54,v232
v_accvgpr_write_b32 a55,v233
ar v230,a56
ar v231,a57
ar v232,a58
ar v233,a59
v_mul_f32 v230,v230,v224
v_mul_f32 v231,v231,v224
v_mul_f32 v232,v232,v224
v_mul_f32 v233,v233,v224
v_accvgpr_write_b32 a56,v230
v_accvgpr_write_b32 a57,v231
v_accvgpr_write_b32 a58,v232
v_accvgpr_write_b32 a59,v233
ar v230,a60
ar v231,a61
ar v232,a62
ar v233,a63
v_mul_f32 v230,v230,v224
v_mul_f32 v231,v231,v224
v_mul_f32 v232,v232,v224
v_mul_f32 v233,v233,v224
v_accvgpr_write_b32 a60,v230
v_accvgpr_write_b32 a61,v231
v_accvgpr_write_b32 a62,v232
v_accvgpr_write_b32 a63,v233
ar v230,a192
ar v231,a193
ar v232,a194
ar v233,a195
v_mul_f32 v230,v230,v224
v_mul_f32 v231,v231,v224
v_mul_f32 v232,v232,v224
v_mul_f32 v233,v233,v224
v_accvgpr_write_b32 a192,v230
v_accvgpr_write_b32 a193,v231
v_accvgpr_write_b32 a194,v232
v_accvgpr_write_b32 a195,v233
v_max3_f32 v221,v68,v69,v70
v_max3_f32 v221,v221,v71,v76
v_max3_f32 v221,v221,v77,v78
v_max_f32 v221,v221,v79
v_mov_b32 v225,v221
s_nop 1
v_permlane32_swap_b32 v221,v225
v_max_f32 v221,v221,v225
v_mov_b32 v225,v221
s_nop 1
v_permlane16_swap_b32 v221,v225
v_max_f32 v221,v221,v225
v_add_f32 v221,0x40e00000,v221
v_max_f32 v221,0,v221
v_sub_f32 v224,0,v221
v_exp_f32 v224,v224
v_sub_f32 v20,v20,v221
v_sub_f32 v21,v21,v221
v_sub_f32 v22,v22,v221
v_sub_f32 v23,v23,v221
v_sub_f32 v28,v28,v221
v_sub_f32 v29,v29,v221
v_sub_f32 v30,v30,v221
v_sub_f32 v31,v31,v221
v_sub_f32 v68,v68,v221
v_sub_f32 v69,v69,v221
v_sub_f32 v70,v70,v221
v_sub_f32 v71,v71,v221
v_sub_f32 v76,v76,v221
v_sub_f32 v77,v77,v221
v_sub_f32 v78,v78,v221
v_sub_f32 v79,v79,v221
v_sub_f32 v132,v132,v221
v_sub_f32 v133,v133,v221
v_sub_f32 v134,v134,v221
v_sub_f32 v135,v135,v221
ar v230,a64
ar v231,a65
ar v232,a66
ar v233,a67
v_mul_f32 v230,v230,v224
v_mul_f32 v231,v231,v224
v_mul_f32 v232,v232,v224
v_mul_f32 v233,v233,v224
v_accvgpr_write_b32 a64,v230
v_accvgpr_write_b32 a65,v231
v_accvgpr_write_b32 a66,v232
v_accvgpr_write_b32 a67,v233
ar v230,a68
ar v231,a69
ar v232,a70
ar v233,a71
v_mul_f32 v230,v230,v224
v_mul_f32 v231,v231,v224
v_mul_f32 v232,v232,v224
v_mul_f32 v233,v233,v224
v_accvgpr_write_b32 a68,v230
v_accvgpr_write_b32 a69,v231
v_accvgpr_write_b32 a70,v232
v_accvgpr_write_b32 a71,v233
ar v230,a72
ar v231,a73
ar v232,a74
ar v233,a75
v_mul_f32 v230,v230,v224
v_mul_f32 v231,v231,v224
v_mul_f32 v232,v232,v224
v_mul_f32 v233,v233,v224
v_accvgpr_write_b32 a72,v230
v_accvgpr_write_b32 a73,v231
v_accvgpr_write_b32 a74,v232
v_accvgpr_write_b32 a75,v233
ar v230,a76
ar v231,a77
ar v232,a78
ar v233,a79
v_mul_f32 v230,v230,v224
v_mul_f32 v231,v231,v224
v_mul_f32 v232,v232,v224
v_mul_f32 v233,v233,v224
v_accvgpr_write_b32 a76,v230
v_accvgpr_write_b32 a77,v231
v_accvgpr_write_b32 a78,v232
v_accvgpr_write_b32 a79,v233
ar v230,a80
ar v231,a81
ar v232,a82
ar v233,a83
v_mul_f32 v230,v230,v224
v_mul_f32 v231,v231,v224
v_mul_f32 v232,v232,v224
v_mul_f32 v233,v233,v224
v_accvgpr_write_b32 a80,v230
v_accvgpr_write_b32 a81,v231
v_accvgpr_write_b32 a82,v232
v_accvgpr_write_b32 a83,v233
ar v230,a84
ar v231,a85
ar v232,a86
ar v233,a87
v_mul_f32 v230,v230,v224
v_mul_f32 v231,v231,v224
v_mul_f32 v232,v232,v224
v_mul_f32 v233,v233,v224
v_accvgpr_write_b32 a84,v230
v_accvgpr_write_b32 a85,v231
v_accvgpr_write_b32 a86,v232
v_accvgpr_write_b32 a87,v233
ar v230,a88
ar v231,a89
ar v232,a90
ar v233,a91
v_mul_f32 v230,v230,v224
v_mul_f32 v231,v231,v224
v_mul_f32 v232,v232,v224
v_mul_f32 v233,v233,v224
v_accvgpr_write_b32 a88,v230
v_accvgpr_write_b32 a89,v231
v_accvgpr_write_b32 a90,v232
v_accvgpr_write_b32 a91,v233
ar v230,a92
ar v231,a93
ar v232,a94
ar v233,a95
v_mul_f32 v230,v230,v224
v_mul_f32 v231,v231,v224
v_mul_f32 v232,v232,v224
v_mul_f32 v233,v233,v224
v_accvgpr_write_b32 a92,v230
v_accvgpr_write_b32 a93,v231
v_accvgpr_write_b32 a94,v232
v_accvgpr_write_b32 a95,v233
ar v230,a96
ar v231,a97
ar v232,a98
ar v233,a99
v_mul_f32 v230,v230,v224
v_mul_f32 v231,v231,v224
v_mul_f32 v232,v232,v224
v_mul_f32 v233,v233,v224
v_accvgpr_write_b32 a96,v230
v_accvgpr_write_b32 a97,v231
v_accvgpr_write_b32 a98,v232
v_accvgpr_write_b32 a99,v233
ar v230,a100
ar v231,a101
ar v232,a102
ar v233,a103
v_mul_f32 v230,v230,v224
v_mul_f32 v231,v231,v224
v_mul_f32 v232,v232,v224
v_mul_f32 v233,v233,v224
v_accvgpr_write_b32 a100,v230
v_accvgpr_write_b32 a101,v231
v_accvgpr_write_b32 a102,v232
v_accvgpr_write_b32 a103,v233
ar v230,a104
ar v231,a105
ar v232,a106
ar v233,a107
v_mul_f32 v230,v230,v224
v_mul_f32 v231,v231,v224
v_mul_f32 v232,v232,v224
v_mul_f32 v233,v233,v224
v_accvgpr_write_b32 a104,v230
v_accvgpr_write_b32 a105,v231
v_accvgpr_write_b32 a106,v232
v_accvgpr_write_b32 a107,v233
ar v230,a108
ar v231,a109
ar v232,a110
ar v233,a111
v_mul_f32 v230,v230,v224
v_mul_f32 v231,v231,v224
v_mul_f32 v232,v232,v224
v_mul_f32 v233,v233,v224
v_accvgpr_write_b32 a108,v230
v_accvgpr_write_b32 a109,v231
v_accvgpr_write_b32 a110,v232
v_accvgpr_write_b32 a111,v233
ar v230,a112
ar v231,a113
ar v232,a114
ar v233,a115
v_mul_f32 v230,v230,v224
v_mul_f32 v231,v231,v224
v_mul_f32 v232,v232,v224
v_mul_f32 v233,v233,v224
v_accvgpr_write_b32 a112,v230
v_accvgpr_write_b32 a113,v231
v_accvgpr_write_b32 a114,v232
v_accvgpr_write_b32 a115,v233
ar v230,a116
ar v231,a117
ar v232,a118
ar v233,a119
v_mul_f32 v230,v230,v224
v_mul_f32 v231,v231,v224
v_mul_f32 v232,v232,v224
v_mul_f32 v233,v233,v224
v_accvgpr_write_b32 a116,v230
v_accvgpr_write_b32 a117,v231
v_accvgpr_write_b32 a118,v232
v_accvgpr_write_b32 a119,v233
ar v230,a120
ar v231,a121
ar v232,a122
ar v233,a123
v_mul_f32 v230,v230,v224
v_mul_f32 v231,v231,v224
v_mul_f32 v232,v232,v224
v_mul_f32 v233,v233,v224
v_accvgpr_write_b32 a120,v230
v_accvgpr_write_b32 a121,v231
v_accvgpr_write_b32 a122,v232
v_accvgpr_write_b32 a123,v233
ar v230,a124
ar v231,a125
ar v232,a126
ar v233,a127
v_mul_f32 v230,v230,v224
v_mul_f32 v231,v231,v224
v_mul_f32 v232,v232,v224
v_mul_f32 v233,v233,v224
v_accvgpr_write_b32 a124,v230
v_accvgpr_write_b32 a125,v231
v_accvgpr_write_b32 a126,v232
v_accvgpr_write_b32 a127,v233
ar v230,a196
ar v231,a197
ar v232,a198
ar v233,a199
v_mul_f32 v230,v230,v224
v_mul_f32 v231,v231,v224
v_mul_f32 v232,v232,v224
v_mul_f32 v233,v233,v224
v_accvgpr_write_b32 a196,v230
v_accvgpr_write_b32 a197,v231
v_accvgpr_write_b32 a198,v232
v_accvgpr_write_b32 a199,v233
v_max3_f32 v222,v84,v85,v86
v_max3_f32 v222,v222,v87,v92
v_max3_f32 v222,v222,v93,v94
v_max_f32 v222,v222,v95
v_mov_b32 v225,v222
s_nop 1
v_permlane32_swap_b32 v222,v225
v_max_f32 v222,v222,v225
v_mov_b32 v225,v222
s_nop 1
v_permlane16_swap_b32 v222,v225
v_max_f32 v222,v222,v225
v_add_f32 v222,0x40e00000,v222
v_max_f32 v222,0,v222
v_sub_f32 v224,0,v222
v_exp_f32 v224,v224
v_sub_f32 v36,v36,v222
v_sub_f32 v37,v37,v222
v_sub_f32 v38,v38,v222
v_sub_f32 v39,v39,v222
v_sub_f32 v44,v44,v222
v_sub_f32 v45,v45,v222
v_sub_f32 v46,v46,v222
v_sub_f32 v47,v47,v222
v_sub_f32 v84,v84,v222
v_sub_f32 v85,v85,v222
v_sub_f32 v86,v86,v222
v_sub_f32 v87,v87,v222
v_sub_f32 v92,v92,v222
v_sub_f32 v93,v93,v222
v_sub_f32 v94,v94,v222
v_sub_f32 v95,v95,v222
v_sub_f32 v140,v140,v222
v_sub_f32 v141,v141,v222
v_sub_f32 v142,v142,v222
v_sub_f32 v143,v143,v222
ar v230,a128
ar v231,a129
ar v232,a130
ar v233,a131
v_mul_f32 v230,v230,v224
v_mul_f32 v231,v231,v224
v_mul_f32 v232,v232,v224
v_mul_f32 v233,v233,v224
v_accvgpr_write_b32 a128,v230
v_accvgpr_write_b32 a129,v231
v_accvgpr_write_b32 a130,v232
v_accvgpr_write_b32 a131,v233
ar v230,a132
ar v231,a133
ar v232,a134
ar v233,a135
v_mul_f32 v230,v230,v224
v_mul_f32 v231,v231,v224
v_mul_f32 v232,v232,v224
v_mul_f32 v233,v233,v224
v_accvgpr_write_b32 a132,v230
v_accvgpr_write_b32 a133,v231
v_accvgpr_write_b32 a134,v232
v_accvgpr_write_b32 a135,v233
ar v230,a136
ar v231,a137
ar v232,a138
ar v233,a139
v_mul_f32 v230,v230,v224
v_mul_f32 v231,v231,v224
v_mul_f32 v232,v232,v224
v_mul_f32 v233,v233,v224
v_accvgpr_write_b32 a136,v230
v_accvgpr_write_b32 a137,v231
v_accvgpr_write_b32 a138,v232
v_accvgpr_write_b32 a139,v233
ar v230,a140
ar v231,a141
ar v232,a142
ar v233,a143
v_mul_f32 v230,v230,v224
v_mul_f32 v231,v231,v224
v_mul_f32 v232,v232,v224
v_mul_f32 v233,v233,v224
v_accvgpr_write_b32 a140,v230
v_accvgpr_write_b32 a141,v231
v_accvgpr_write_b32 a142,v232
v_accvgpr_write_b32 a143,v233
ar v230,a144
ar v231,a145
ar v232,a146
ar v233,a147
v_mul_f32 v230,v230,v224
v_mul_f32 v231,v231,v224
v_mul_f32 v232,v232,v224
v_mul_f32 v233,v233,v224
v_accvgpr_write_b32 a144,v230
v_accvgpr_write_b32 a145,v231
v_accvgpr_write_b32 a146,v232
v_accvgpr_write_b32 a147,v233
ar v230,a148
ar v231,a149
ar v232,a150
ar v233,a151
v_mul_f32 v230,v230,v224
v_mul_f32 v231,v231,v224
v_mul_f32 v232,v232,v224
v_mul_f32 v233,v233,v224
v_accvgpr_write_b32 a148,v230
v_accvgpr_write_b32 a149,v231
v_accvgpr_write_b32 a150,v232
v_accvgpr_write_b32 a151,v233
ar v230,a152
ar v231,a153
ar v232,a154
ar v233,a155
v_mul_f32 v230,v230,v224
v_mul_f32 v231,v231,v224
v_mul_f32 v232,v232,v224
v_mul_f32 v233,v233,v224
v_accvgpr_write_b32 a152,v230
v_accvgpr_write_b32 a153,v231
v_accvgpr_write_b32 a154,v232
v_accvgpr_write_b32 a155,v233
ar v230,a156
ar v231,a157
ar v232,a158
ar v233,a159
v_mul_f32 v230,v230,v224
v_mul_f32 v231,v231,v224
v_mul_f32 v232,v232,v224
v_mul_f32 v233,v233,v224
v_accvgpr_write_b32 a156,v230
v_accvgpr_write_b32 a157,v231
v_accvgpr_write_b32 a158,v232
v_accvgpr_write_b32 a159,v233
ar v230,a160
ar v231,a161
ar v232,a162
ar v233,a163
v_mul_f32 v230,v230,v224
v_mul_f32 v231,v231,v224
v_mul_f32 v232,v232,v224
v_mul_f32 v233,v233,v224
v_accvgpr_write_b32 a160,v230
v_accvgpr_write_b32 a161,v231
v_accvgpr_write_b32 a162,v232
v_accvgpr_write_b32 a163,v233
ar v230,a164
ar v231,a165
ar v232,a166
ar v233,a167
v_mul_f32 v230,v230,v224
v_mul_f32 v231,v231,v224
v_mul_f32 v232,v232,v224
v_mul_f32 v233,v233,v224
v_accvgpr_write_b32 a164,v230
v_accvgpr_write_b32 a165,v231
v_accvgpr_write_b32 a166,v232
v_accvgpr_write_b32 a167,v233
ar v230,a168
ar v231,a169
ar v232,a170
ar v233,a171
v_mul_f32 v230,v230,v224
v_mul_f32 v231,v231,v224
v_mul_f32 v232,v232,v224
v_mul_f32 v233,v233,v224
v_accvgpr_write_b32 a168,v230
v_accvgpr_write_b32 a169,v231
v_accvgpr_write_b32 a170,v232
v_accvgpr_write_b32 a171,v233
ar v230,a172
ar v231,a173
ar v232,a174
ar v233,a175
v_mul_f32 v230,v230,v224
v_mul_f32 v231,v231,v224
v_mul_f32 v232,v232,v224
v_mul_f32 v233,v233,v224
v_accvgpr_write_b32 a172,v230
v_accvgpr_write_b32 a173,v231
v_accvgpr_write_b32 a174,v232
v_accvgpr_write_b32 a175,v233
ar v230,a176
ar v231,a177
ar v232,a178
ar v233,a179
v_mul_f32 v230,v230,v224
v_mul_f32 v231,v231,v224
v_mul_f32 v232,v232,v224
v_mul_f32 v233,v233,v224
v_accvgpr_write_b32 a176,v230
v_accvgpr_write_b32 a177,v231
v_accvgpr_write_b32 a178,v232
v_accvgpr_write_b32 a179,v233
ar v230,a180
ar v231,a181
ar v232,a182
ar v233,a183
v_mul_f32 v230,v230,v224
v_mul_f32 v231,v231,v224
v_mul_f32 v232,v232,v224
v_mul_f32 v233,v233,v224
v_accvgpr_write_b32 a180,v230
v_accvgpr_write_b32 a181,v231
v_accvgpr_write_b32 a182,v232
v_accvgpr_write_b32 a183,v233
ar v230,a184
ar v231,a185
ar v232,a186
ar v233,a187
v_mul_f32 v230,v230,v224
v_mul_f32 v231,v231,v224
v_mul_f32 v232,v232,v224
v_mul_f32 v233,v233,v224
v_accvgpr_write_b32 a184,v230
v_accvgpr_write_b32 a185,v231
v_accvgpr_write_b32 a186,v232
v_accvgpr_write_b32 a187,v233
ar v230,a188
ar v231,a189
ar v232,a190
ar v233,a191
v_mul_f32 v230,v230,v224
v_mul_f32 v231,v231,v224
v_mul_f32 v232,v232,v224
v_mul_f32 v233,v233,v224
v_accvgpr_write_b32 a188,v230
v_accvgpr_write_b32 a189,v231
v_accvgpr_write_b32 a190,v232
v_accvgpr_write_b32 a191,v233
ar v230,a200
ar v231,a201
ar v232,a202
ar v233,a203
v_mul_f32 v230,v230,v224
v_mul_f32 v231,v231,v224
v_mul_f32 v232,v232,v224
v_mul_f32 v233,v233,v224
v_accvgpr_write_b32 a200,v230
v_accvgpr_write_b32 a201,v231
v_accvgpr_write_b32 a202,v232
v_accvgpr_write_b32 a203,v233
v_exp_f32 v220,v52
v_exp_f32 v221,v53
v_exp_f32 v222,v54
v_exp_f32 v223,v55
cv v100,v220,v221
v_exp_f32 v224,v60
v_exp_f32 v225,v61
cv v101,v222,v223
v_exp_f32 v220,v62
v_exp_f32 v221,v63
cv v102,v224,v225
v_exp_f32 v222,v68
v_exp_f32 v223,v69
cv v103,v220,v221
v_exp_f32 v224,v70
v_exp_f32 v225,v71
cv v108,v222,v223
v_exp_f32 v220,v76
v_exp_f32 v221,v77
cv v109,v224,v225
v_exp_f32 v222,v78
v_exp_f32 v223,v79
cv v110,v220,v221
v_exp_f32 v224,v84
v_exp_f32 v225,v85
cv v111,v222,v223
v_exp_f32 v220,v86
v_exp_f32 v221,v87
cv v116,v224,v225
v_exp_f32 v222,v92
v_exp_f32 v223,v93
cv v117,v220,v221
v_exp_f32 v224,v94
v_exp_f32 v225,v95
cv v118,v222,v223
cv v119,v224,v225
v_or3_b32 v228,v100,v101,v102
v_or3_b32 v228,v228,v103,v108
v_or3_b32 v228,v228,v109,v110
v_or3_b32 v228,v228,v111,v116
v_or3_b32 v228,v228,v117,v118
v_or_b32 v228,v228,v119
v_and_b32 v228,0x40004000,v228
s_nop 7
s_setpc_b64 s[50:51]
LB_start%=:
s_mov_b32 s43,0x14000
s_lshl_b32 s57,%9,14
s_add_u32 s43,s43,s57
s_and_b32 s43,s43,0x7ffff
s_waitcnt lgkmcnt(0)
s_waitcnt vmcnt(9)
s_barrier
r a[0:3],v212,40960
r a[56:59],v212,43008
t a[204:205],v204,16384
t a[206:207],v204,16896
t a[208:209],v205,16384
t a[210:211],v205,16896
t a[212:213],v206,16384
t a[214:215],v206,16896
t a[216:217],v207,16384
t a[218:219],v207,16896
s_mov_b32 m0,0x18000
s_add_u32 s45,s87,0
dn v244,%1,s45,0
dn v245,%1,s45,1024
dn v246,%1,s45,2048
dn v247,%1,s45,3072
s_mov_b32 m0,0x19000
s_add_u32 s45,s87,0x80
dn v244,%1,s45,0
dn v245,%1,s45,1024
dn v246,%1,s45,2048
dn v247,%1,s45,3072
s_mov_b32 m0,0x1a000
s_add_u32 s45,s87,0x40000
dn v244,%1,s45,0
dn v245,%1,s45,1024
dn v246,%1,s45,2048
dn v247,%1,s45,3072
s_mov_b32 m0,0x1b000
s_add_u32 s45,s87,0x40080
dn v244,%1,s45,0
dn v245,%1,s45,1024
dn v246,%1,s45,2048
dn v247,%1,s45,3072
s_waitcnt lgkmcnt(0)
m v[0:3],a[204:207],a[0:3],0
t a[220:221],v204,20480
t a[222:223],v204,20992
m v[16:19],a[204:207],a[56:59],0
t a[224:225],v205,20480
t a[226:227],v205,20992
m v[4:7],a[208:211],a[0:3],0
t a[228:229],v206,20480
t a[230:231],v206,20992
t a[232:233],v207,20480
m v[20:23],a[208:211],a[56:59],0
t a[234:235],v207,20992
v_lshrrev_b32 v236,5,v214
m v[8:11],a[212:215],a[0:3],0
v_and_b32 v237,31,v214
v_xor_b32 v237,v237,v236
m v[24:27],a[212:215],a[56:59],0
v_lshlrev_b32 v237,4,v237
v_lshlrev_b32 v236,9,v236
v_add_u32 v128,v236,v237
m v[12:15],a[216:219],a[0:3],0
v_xor_b32 v129,32,v128
v_xor_b32 v130,64,v128
m v[28:31],a[216:219],a[56:59],0
v_xor_b32 v131,0x60,v128
v_xor_b32 v132,0x80,v128
v_xor_b32 v133,0xa0,v128
s_waitcnt lgkmcnt(0)
m v[32:35],a[220:223],a[0:3],0
r a[4:7],v213,40960
r a[60:63],v213,43008
m v[48:51],a[220:223],a[56:59],0
t a[204:205],v204,24576
t a[206:207],v204,25088
t a[208:209],v205,24576
m v[36:39],a[224:227],a[0:3],0
t a[210:211],v205,25088
t a[212:213],v206,24576
m v[52:55],a[224:227],a[56:59],0
t a[214:215],v206,25088
t a[216:217],v207,24576
t a[218:219],v207,25088
m v[40:43],a[228:231],a[0:3],0
v_xor_b32 v134,0xc0,v128
v_xor_b32 v135,0xe0,v128
v_lshrrev_b32 v236,5,v214
m v[56:59],a[228:231],a[56:59],0
v_and_b32 v237,31,v214
v_lshlrev_b32 v238,1,v236
m v[44:47],a[232:235],a[0:3],0
v_xor_b32 v237,v237,v238
v_lshlrev_b32 v237,4,v237
v_lshlrev_b32 v236,9,v236
m v[60:63],a[232:235],a[56:59],0
v_add_u32 v136,v236,v237
v_xor_b32 v137,64,v136
v_xor_b32 v138,0x80,v136
s_waitcnt lgkmcnt(0)
m v[0:3],a[204:207],a[4:7],v[0:3]
t a[220:221],v204,28672
t a[222:223],v204,29184
m v[16:19],a[204:207],a[60:63],v[16:19]
t a[224:225],v205,28672
t a[226:227],v205,29184
m v[4:7],a[208:211],a[4:7],v[4:7]
t a[228:229],v206,28672
t a[230:231],v206,29184
t a[232:233],v207,28672
m v[20:23],a[208:211],a[60:63],v[20:23]
t a[234:235],v207,29184
v_xor_b32 v139,0xc0,v136
m v[8:11],a[212:215],a[4:7],v[8:11]
v_lshrrev_b32 v236,3,v214
v_and_b32 v237,6,v236
m v[24:27],a[212:215],a[60:63],v[24:27]
v_and_b32 v238,7,v214
v_xor_b32 v237,v237,v238
v_lshlrev_b32 v237,4,v237
m v[12:15],a[216:219],a[4:7],v[12:15]
v_mul_u32_u24 v236,0x380,v236
v_add_u32 v144,v236,v237
m v[28:31],a[216:219],a[60:63],v[28:31]
v_add_u32 v145,0x1800,v144
v_add_u32 v146,0x3000,v144
v_add_u32 v147,0x4800,v144
s_waitcnt lgkmcnt(0)
s_waitcnt vmcnt(16)
s_barrier
m v[32:35],a[220:223],a[4:7],v[32:35]
r a[8:11],v212,0
r a[64:67],v212,2048
t a[204:205],v204,49152
t a[206:207],v204,49664
t a[208:209],v205,49152
t a[210:211],v205,49664
t a[212:213],v206,49152
t a[214:215],v206,49664
t a[216:217],v207,49152
t a[218:219],v207,49664
m v[48:51],a[220:223],a[60:63],v[48:51]
s_mov_b32 m0,0x1c000
s_add_u32 s45,s88,0
dn v244,%1,s45,0
dn v245,%1,s45,1024
m v[36:39],a[224:227],a[4:7],v[36:39]
dn v246,%1,s45,2048
dn v247,%1,s45,3072
s_mov_b32 m0,0x1d000
s_add_u32 s45,s88,0x80
m v[52:55],a[224:227],a[60:63],v[52:55]
dn v244,%1,s45,0
dn v245,%1,s45,1024
m v[40:43],a[228:231],a[4:7],v[40:43]
dn v246,%1,s45,2048
dn v247,%1,s45,3072
s_mov_b32 m0,0x1e000
s_add_u32 s45,s88,0x40000
m v[56:59],a[228:231],a[60:63],v[56:59]
dn v244,%1,s45,0
dn v245,%1,s45,1024
m v[44:47],a[232:235],a[4:7],v[44:47]
dn v246,%1,s45,2048
dn v247,%1,s45,3072
s_mov_b32 m0,0x1f000
s_add_u32 s45,s88,0x40080
m v[60:63],a[232:235],a[60:63],v[60:63]
dn v244,%1,s45,0
dn v245,%1,s45,1024
dn v246,%1,s45,2048
dn v247,%1,s45,3072
s_mov_b32 m0,0x20000
s_add_u32 s45,s57,0
s_and_b32 s45,s45,0x7ffff
dn v128,%2,s45,0
dn v129,%2,s45,1024
dn v130,%2,s45,2048
dn v131,%2,s45,3072
s_waitcnt lgkmcnt(0)
m v[0:3],a[204:207],a[8:11],v[0:3]
t a[220:221],v204,53248
t a[222:223],v204,53760
m v[16:19],a[204:207],a[64:67],v[16:19]
t a[224:225],v205,53248
t a[226:227],v205,53760
m v[4:7],a[208:211],a[8:11],v[4:7]
t a[228:229],v206,53248
t a[230:231],v206,53760
m v[20:23],a[208:211],a[64:67],v[20:23]
t a[232:233],v207,53248
t a[234:235],v207,53760
m v[8:11],a[212:215],a[8:11],v[8:11]
m v[24:27],a[212:215],a[64:67],v[24:27]
m v[12:15],a[216:219],a[8:11],v[12:15]
m v[28:31],a[216:219],a[64:67],v[28:31]
s_waitcnt lgkmcnt(0)
m v[32:35],a[220:223],a[8:11],v[32:35]
r a[12:15],v213,0
r a[68:71],v213,2048
m v[48:51],a[220:223],a[64:67],v[48:51]
t a[204:205],v204,57344
t a[206:207],v204,57856
m v[36:39],a[224:227],a[8:11],v[36:39]
t a[208:209],v205,57344
t a[210:211],v205,57856
m v[52:55],a[224:227],a[64:67],v[52:55]
t a[212:213],v206,57344
t a[214:215],v206,57856
m v[40:43],a[228:231],a[8:11],v[40:43]
t a[216:217],v207,57344
t a[218:219],v207,57856
m v[56:59],a[228:231],a[64:67],v[56:59]
m v[44:47],a[232:235],a[8:11],v[44:47]
m v[60:63],a[232:235],a[64:67],v[60:63]
s_waitcnt lgkmcnt(0)
m v[0:3],a[204:207],a[12:15],v[0:3]
t a[220:221],v204,61440
t a[222:223],v204,61952
m v[16:19],a[204:207],a[68:71],v[16:19]
t a[224:225],v205,61440
t a[226:227],v205,61952
m v[4:7],a[208:211],a[12:15],v[4:7]
t a[228:229],v206,61440
t a[230:231],v206,61952
m v[20:23],a[208:211],a[68:71],v[20:23]
t a[232:233],v207,61440
t a[234:235],v207,61952
m v[8:11],a[212:215],a[12:15],v[8:11]
m v[24:27],a[212:215],a[68:71],v[24:27]
m v[12:15],a[216:219],a[12:15],v[12:15]
m v[28:31],a[216:219],a[68:71],v[28:31]
s_waitcnt lgkmcnt(0)
s_waitcnt vmcnt(20)
s_barrier
m v[32:35],a[220:223],a[12:15],v[32:35]
r a[16:19],v212,20480
r a[72:75],v212,22528
t a[204:205],v204,0
t a[206:207],v204,512
t a[208:209],v205,0
t a[210:211],v205,512
t a[212:213],v206,0
t a[214:215],v206,512
t a[216:217],v207,0
t a[218:219],v207,512
m v[48:51],a[220:223],a[68:71],v[48:51]
s_mov_b32 m0,0x24000
s_add_u32 s45,s89,0
dn v244,%1,s45,0
dn v245,%1,s45,1024
m v[36:39],a[224:227],a[12:15],v[36:39]
dn v246,%1,s45,2048
dn v247,%1,s45,3072
s_mov_b32 m0,0x25000
s_add_u32 s45,s89,0x80
m v[52:55],a[224:227],a[68:71],v[52:55]
dn v244,%1,s45,0
dn v245,%1,s45,1024
m v[40:43],a[228:231],a[12:15],v[40:43]
dn v246,%1,s45,2048
dn v247,%1,s45,3072
s_mov_b32 m0,0x26000
s_add_u32 s45,s89,0x40000
m v[56:59],a[228:231],a[68:71],v[56:59]
dn v244,%1,s45,0
dn v245,%1,s45,1024
m v[44:47],a[232:235],a[12:15],v[44:47]
dn v246,%1,s45,2048
dn v247,%1,s45,3072
s_mov_b32 m0,0x27000
s_add_u32 s45,s89,0x40080
m v[60:63],a[232:235],a[68:71],v[60:63]
dn v244,%1,s45,0
dn v245,%1,s45,1024
dn v246,%1,s45,2048
dn v247,%1,s45,3072
s_mov_b32 m0,0x21000
s_add_u32 s45,s57,0x1000
s_and_b32 s45,s45,0x7ffff
dn v132,%2,s45,0
dn v133,%2,s45,1024
dn v134,%2,s45,2048
dn v135,%2,s45,3072
s_waitcnt lgkmcnt(0)
m v[0:3],a[204:207],a[16:19],v[0:3]
t a[220:221],v204,4096
t a[222:223],v204,4608
m v[16:19],a[204:207],a[72:75],v[16:19]
t a[224:225],v205,4096
t a[226:227],v205,4608
m v[4:7],a[208:211],a[16:19],v[4:7]
t a[228:229],v206,4096
t a[230:231],v206,4608
m v[20:23],a[208:211],a[72:75],v[20:23]
t a[232:233],v207,4096
t a[234:235],v207,4608
m v[8:11],a[212:215],a[16:19],v[8:11]
m v[24:27],a[212:215],a[72:75],v[24:27]
m v[12:15],a[216:219],a[16:19],v[12:15]
m v[28:31],a[216:219],a[72:75],v[28:31]
s_waitcnt lgkmcnt(0)
m v[32:35],a[220:223],a[16:19],v[32:35]
r a[20:23],v213,20480
r a[76:79],v213,22528
m v[48:51],a[220:223],a[72:75],v[48:51]
t a[204:205],v204,8192
t a[206:207],v204,8704
m v[36:39],a[224:227],a[16:19],v[36:39]
t a[208:209],v205,8192
t a[210:211],v205,8704
m v[52:55],a[224:227],a[72:75],v[52:55]
t a[212:213],v206,8192
t a[214:215],v206,8704
m v[40:43],a[228:231],a[16:19],v[40:43]
t a[216:217],v207,8192
t a[218:219],v207,8704
m v[56:59],a[228:231],a[72:75],v[56:59]
m v[44:47],a[232:235],a[16:19],v[44:47]
m v[60:63],a[232:235],a[72:75],v[60:63]
s_waitcnt lgkmcnt(0)
m v[0:3],a[204:207],a[20:23],v[0:3]
t a[220:221],v204,12288
t a[222:223],v204,12800
m v[16:19],a[204:207],a[76:79],v[16:19]
t a[224:225],v205,12288
t a[226:227],v205,12800
m v[4:7],a[208:211],a[20:23],v[4:7]
t a[228:229],v206,12288
t a[230:231],v206,12800
m v[20:23],a[208:211],a[76:79],v[20:23]
t a[232:233],v207,12288
t a[234:235],v207,12800
m v[8:11],a[212:215],a[20:23],v[8:11]
m v[24:27],a[212:215],a[76:79],v[24:27]
m v[12:15],a[216:219],a[20:23],v[12:15]
m v[28:31],a[216:219],a[76:79],v[28:31]
s_waitcnt lgkmcnt(0)
s_waitcnt vmcnt(24)
s_barrier
m v[32:35],a[220:223],a[20:23],v[32:35]
r a[24:27],v212,40960
r a[80:83],v212,43008
t a[204:205],v204,16384
t a[206:207],v204,16896
t a[208:209],v205,16384
t a[210:211],v205,16896
t a[212:213],v206,16384
t a[214:215],v206,16896
t a[216:217],v207,16384
t a[218:219],v207,16896
m v[48:51],a[220:223],a[76:79],v[48:51]
s_mov_b32 m0,0x18000
)ASM"
R"ASM(s_add_u32 s45,s90,0
dn v244,%1,s45,0
dn v245,%1,s45,1024
m v[36:39],a[224:227],a[20:23],v[36:39]
dn v246,%1,s45,2048
dn v247,%1,s45,3072
s_mov_b32 m0,0x19000
s_add_u32 s45,s90,0x80
m v[52:55],a[224:227],a[76:79],v[52:55]
dn v244,%1,s45,0
dn v245,%1,s45,1024
m v[40:43],a[228:231],a[20:23],v[40:43]
dn v246,%1,s45,2048
dn v247,%1,s45,3072
s_mov_b32 m0,0x1a000
s_add_u32 s45,s90,0x40000
m v[56:59],a[228:231],a[76:79],v[56:59]
dn v244,%1,s45,0
dn v245,%1,s45,1024
m v[44:47],a[232:235],a[20:23],v[44:47]
dn v246,%1,s45,2048
dn v247,%1,s45,3072
s_mov_b32 m0,0x1b000
s_add_u32 s45,s90,0x40080
m v[60:63],a[232:235],a[76:79],v[60:63]
dn v244,%1,s45,0
dn v245,%1,s45,1024
dn v246,%1,s45,2048
dn v247,%1,s45,3072
s_mov_b32 m0,0x22000
s_add_u32 s45,s57,0x2000
s_and_b32 s45,s45,0x7ffff
dn v128,%2,s45,0
dn v129,%2,s45,1024
dn v130,%2,s45,2048
dn v131,%2,s45,3072
s_waitcnt lgkmcnt(0)
m v[0:3],a[204:207],a[24:27],v[0:3]
t a[220:221],v204,20480
t a[222:223],v204,20992
m v[16:19],a[204:207],a[80:83],v[16:19]
t a[224:225],v205,20480
t a[226:227],v205,20992
m v[4:7],a[208:211],a[24:27],v[4:7]
t a[228:229],v206,20480
t a[230:231],v206,20992
m v[20:23],a[208:211],a[80:83],v[20:23]
t a[232:233],v207,20480
t a[234:235],v207,20992
m v[8:11],a[212:215],a[24:27],v[8:11]
m v[24:27],a[212:215],a[80:83],v[24:27]
m v[12:15],a[216:219],a[24:27],v[12:15]
m v[28:31],a[216:219],a[80:83],v[28:31]
s_waitcnt lgkmcnt(0)
m v[32:35],a[220:223],a[24:27],v[32:35]
r a[28:31],v213,40960
r a[84:87],v213,43008
m v[48:51],a[220:223],a[80:83],v[48:51]
t a[204:205],v204,24576
t a[206:207],v204,25088
m v[36:39],a[224:227],a[24:27],v[36:39]
t a[208:209],v205,24576
t a[210:211],v205,25088
m v[52:55],a[224:227],a[80:83],v[52:55]
t a[212:213],v206,24576
t a[214:215],v206,25088
m v[40:43],a[228:231],a[24:27],v[40:43]
t a[216:217],v207,24576
t a[218:219],v207,25088
m v[56:59],a[228:231],a[80:83],v[56:59]
m v[44:47],a[232:235],a[24:27],v[44:47]
m v[60:63],a[232:235],a[80:83],v[60:63]
s_waitcnt lgkmcnt(0)
m v[0:3],a[204:207],a[28:31],v[0:3]
t a[220:221],v204,28672
t a[222:223],v204,29184
m v[16:19],a[204:207],a[84:87],v[16:19]
t a[224:225],v205,28672
t a[226:227],v205,29184
m v[4:7],a[208:211],a[28:31],v[4:7]
t a[228:229],v206,28672
t a[230:231],v206,29184
m v[20:23],a[208:211],a[84:87],v[20:23]
t a[232:233],v207,28672
t a[234:235],v207,29184
m v[8:11],a[212:215],a[28:31],v[8:11]
m v[24:27],a[212:215],a[84:87],v[24:27]
m v[12:15],a[216:219],a[28:31],v[12:15]
m v[28:31],a[216:219],a[84:87],v[28:31]
s_waitcnt lgkmcnt(0)
s_waitcnt vmcnt(24)
s_barrier
m v[32:35],a[220:223],a[28:31],v[32:35]
r a[32:35],v212,0
r a[88:91],v212,2048
t a[204:205],v204,49152
t a[206:207],v204,49664
t a[208:209],v205,49152
t a[210:211],v205,49664
t a[212:213],v206,49152
t a[214:215],v206,49664
t a[216:217],v207,49152
t a[218:219],v207,49664
m v[48:51],a[220:223],a[84:87],v[48:51]
s_mov_b32 m0,0x1c000
s_add_u32 s45,s91,0
dn v244,%1,s45,0
dn v245,%1,s45,1024
m v[36:39],a[224:227],a[28:31],v[36:39]
dn v246,%1,s45,2048
dn v247,%1,s45,3072
s_mov_b32 m0,0x1d000
s_add_u32 s45,s91,0x80
m v[52:55],a[224:227],a[84:87],v[52:55]
dn v244,%1,s45,0
dn v245,%1,s45,1024
m v[40:43],a[228:231],a[28:31],v[40:43]
dn v246,%1,s45,2048
dn v247,%1,s45,3072
s_mov_b32 m0,0x1e000
s_add_u32 s45,s91,0x40000
m v[56:59],a[228:231],a[84:87],v[56:59]
dn v244,%1,s45,0
dn v245,%1,s45,1024
m v[44:47],a[232:235],a[28:31],v[44:47]
dn v246,%1,s45,2048
dn v247,%1,s45,3072
s_mov_b32 m0,0x1f000
s_add_u32 s45,s91,0x40080
m v[60:63],a[232:235],a[84:87],v[60:63]
dn v244,%1,s45,0
dn v245,%1,s45,1024
dn v246,%1,s45,2048
dn v247,%1,s45,3072
s_mov_b32 m0,0x23000
s_add_u32 s45,s57,0x3000
s_and_b32 s45,s45,0x7ffff
dn v132,%2,s45,0
dn v133,%2,s45,1024
dn v134,%2,s45,2048
dn v135,%2,s45,3072
s_waitcnt lgkmcnt(0)
m v[0:3],a[204:207],a[32:35],v[0:3]
t a[220:221],v204,53248
t a[222:223],v204,53760
m v[16:19],a[204:207],a[88:91],v[16:19]
t a[224:225],v205,53248
t a[226:227],v205,53760
m v[4:7],a[208:211],a[32:35],v[4:7]
t a[228:229],v206,53248
t a[230:231],v206,53760
m v[20:23],a[208:211],a[88:91],v[20:23]
t a[232:233],v207,53248
t a[234:235],v207,53760
m v[8:11],a[212:215],a[32:35],v[8:11]
m v[24:27],a[212:215],a[88:91],v[24:27]
m v[12:15],a[216:219],a[32:35],v[12:15]
m v[28:31],a[216:219],a[88:91],v[28:31]
s_waitcnt lgkmcnt(0)
m v[32:35],a[220:223],a[32:35],v[32:35]
r a[36:39],v213,0
r a[92:95],v213,2048
m v[48:51],a[220:223],a[88:91],v[48:51]
t a[204:205],v204,57344
t a[206:207],v204,57856
m v[36:39],a[224:227],a[32:35],v[36:39]
t a[208:209],v205,57344
t a[210:211],v205,57856
m v[52:55],a[224:227],a[88:91],v[52:55]
t a[212:213],v206,57344
t a[214:215],v206,57856
m v[40:43],a[228:231],a[32:35],v[40:43]
t a[216:217],v207,57344
t a[218:219],v207,57856
m v[56:59],a[228:231],a[88:91],v[56:59]
v_lshlrev_b32 v251,2,v214
v_add_u32 v251,0x17000,v251
m v[44:47],a[232:235],a[32:35],v[44:47]
ds_read_b32 v219,v251
m v[60:63],a[232:235],a[88:91],v[60:63]
s_waitcnt lgkmcnt(0)
m v[0:3],a[204:207],a[36:39],v[0:3]
t a[220:221],v204,61440
t a[222:223],v204,61952
m v[16:19],a[204:207],a[92:95],v[16:19]
t a[224:225],v205,61440
t a[226:227],v205,61952
m v[4:7],a[208:211],a[36:39],v[4:7]
t a[228:229],v206,61440
t a[230:231],v206,61952
m v[20:23],a[208:211],a[92:95],v[20:23]
t a[232:233],v207,61440
t a[234:235],v207,61952
m v[8:11],a[212:215],a[36:39],v[8:11]
m v[24:27],a[212:215],a[92:95],v[24:27]
m v[12:15],a[216:219],a[36:39],v[12:15]
m v[28:31],a[216:219],a[92:95],v[28:31]
s_waitcnt lgkmcnt(0)
s_waitcnt vmcnt(24)
s_barrier
m v[32:35],a[220:223],a[36:39],v[32:35]
r a[40:43],v212,20480
r a[96:99],v212,22528
t a[204:205],v204,0
t a[206:207],v204,512
t a[208:209],v205,0
t a[210:211],v205,512
t a[212:213],v206,0
t a[214:215],v206,512
t a[216:217],v207,0
t a[218:219],v207,512
m v[48:51],a[220:223],a[92:95],v[48:51]
s_mov_b32 m0,0x24000
s_add_u32 s45,s85,0x100
dn v244,%1,s45,0
dn v245,%1,s45,1024
m v[36:39],a[224:227],a[36:39],v[36:39]
dn v246,%1,s45,2048
dn v247,%1,s45,3072
s_mov_b32 m0,0x25000
s_add_u32 s45,s85,0x40100
m v[52:55],a[224:227],a[92:95],v[52:55]
dn v244,%1,s45,0
dn v245,%1,s45,1024
m v[40:43],a[228:231],a[36:39],v[40:43]
dn v246,%1,s45,2048
dn v247,%1,s45,3072
s_mov_b32 m0,0x26000
s_add_u32 s45,s86,0x100
m v[56:59],a[228:231],a[92:95],v[56:59]
dn v244,%1,s45,0
dn v245,%1,s45,1024
m v[44:47],a[232:235],a[36:39],v[44:47]
dn v246,%1,s45,2048
dn v247,%1,s45,3072
s_mov_b32 m0,0x27000
s_add_u32 s45,s86,0x40100
m v[60:63],a[232:235],a[92:95],v[60:63]
dn v244,%1,s45,0
dn v245,%1,s45,1024
dn v246,%1,s45,2048
dn v247,%1,s45,3072
s_waitcnt lgkmcnt(0)
m v[0:3],a[204:207],a[40:43],v[0:3]
t a[220:221],v204,4096
t a[222:223],v204,4608
m v[16:19],a[204:207],a[96:99],v[16:19]
t a[224:225],v205,4096
t a[226:227],v205,4608
m v[4:7],a[208:211],a[40:43],v[4:7]
t a[228:229],v206,4096
t a[230:231],v206,4608
m v[20:23],a[208:211],a[96:99],v[20:23]
t a[232:233],v207,4096
t a[234:235],v207,4608
m v[8:11],a[212:215],a[40:43],v[8:11]
m v[24:27],a[212:215],a[96:99],v[24:27]
m v[12:15],a[216:219],a[40:43],v[12:15]
m v[28:31],a[216:219],a[96:99],v[28:31]
s_waitcnt lgkmcnt(0)
m v[32:35],a[220:223],a[40:43],v[32:35]
r a[44:47],v213,20480
r a[100:103],v213,22528
m v[48:51],a[220:223],a[96:99],v[48:51]
t a[204:205],v204,8192
t a[206:207],v204,8704
m v[36:39],a[224:227],a[40:43],v[36:39]
t a[208:209],v205,8192
t a[210:211],v205,8704
m v[52:55],a[224:227],a[96:99],v[52:55]
t a[212:213],v206,8192
t a[214:215],v206,8704
m v[40:43],a[228:231],a[40:43],v[40:43]
t a[216:217],v207,8192
t a[218:219],v207,8704
m v[56:59],a[228:231],a[96:99],v[56:59]
m v[44:47],a[232:235],a[40:43],v[44:47]
m v[60:63],a[232:235],a[96:99],v[60:63]
s_waitcnt lgkmcnt(0)
m v[0:3],a[204:207],a[44:47],v[0:3]
t a[220:221],v204,12288
t a[222:223],v204,12800
m v[16:19],a[204:207],a[100:103],v[16:19]
t a[224:225],v205,12288
t a[226:227],v205,12800
m v[4:7],a[208:211],a[44:47],v[4:7]
t a[228:229],v206,12288
t a[230:231],v206,12800
m v[20:23],a[208:211],a[100:103],v[20:23]
t a[232:233],v207,12288
t a[234:235],v207,12800
m v[8:11],a[212:215],a[44:47],v[8:11]
m v[24:27],a[212:215],a[100:103],v[24:27]
m v[12:15],a[216:219],a[44:47],v[12:15]
m v[28:31],a[216:219],a[100:103],v[28:31]
s_waitcnt lgkmcnt(0)
s_waitcnt vmcnt(20)
s_barrier
m v[32:35],a[220:223],a[44:47],v[32:35]
r a[48:51],v212,40960
r a[104:107],v212,43008
t a[204:205],v204,16384
t a[206:207],v204,16896
t a[208:209],v205,16384
t a[210:211],v205,16896
t a[212:213],v206,16384
t a[214:215],v206,16896
t a[216:217],v207,16384
t a[218:219],v207,16896
m v[48:51],a[220:223],a[100:103],v[48:51]
s_mov_b32 m0,0x18000
s_add_u32 s45,s87,0x100
dn v244,%1,s45,0
dn v245,%1,s45,1024
m v[36:39],a[224:227],a[44:47],v[36:39]
dn v246,%1,s45,2048
dn v247,%1,s45,3072
s_mov_b32 m0,0x19000
s_add_u32 s45,s87,0x40100
m v[52:55],a[224:227],a[100:103],v[52:55]
dn v244,%1,s45,0
dn v245,%1,s45,1024
m v[40:43],a[228:231],a[44:47],v[40:43]
dn v246,%1,s45,2048
dn v247,%1,s45,3072
s_mov_b32 m0,0x1a000
s_add_u32 s45,s88,0x100
m v[56:59],a[228:231],a[100:103],v[56:59]
dn v244,%1,s45,0
dn v245,%1,s45,1024
m v[44:47],a[232:235],a[44:47],v[44:47]
dn v246,%1,s45,2048
dn v247,%1,s45,3072
s_mov_b32 m0,0x1b000
s_add_u32 s45,s88,0x40100
m v[60:63],a[232:235],a[100:103],v[60:63]
dn v244,%1,s45,0
dn v245,%1,s45,1024
dn v246,%1,s45,2048
dn v247,%1,s45,3072
s_waitcnt lgkmcnt(0)
m v[0:3],a[204:207],a[48:51],v[0:3]
t a[220:221],v204,20480
t a[222:223],v204,20992
m v[16:19],a[204:207],a[104:107],v[16:19]
t a[224:225],v205,20480
t a[226:227],v205,20992
m v[4:7],a[208:211],a[48:51],v[4:7]
t a[228:229],v206,20480
t a[230:231],v206,20992
m v[20:23],a[208:211],a[104:107],v[20:23]
t a[232:233],v207,20480
t a[234:235],v207,20992
m v[8:11],a[212:215],a[48:51],v[8:11]
m v[24:27],a[212:215],a[104:107],v[24:27]
m v[12:15],a[216:219],a[48:51],v[12:15]
m v[28:31],a[216:219],a[104:107],v[28:31]
s_waitcnt lgkmcnt(0)
m v[32:35],a[220:223],a[48:51],v[32:35]
r a[52:55],v213,40960
r a[108:111],v213,43008
m v[48:51],a[220:223],a[104:107],v[48:51]
t a[204:205],v204,24576
t a[206:207],v204,25088
m v[36:39],a[224:227],a[48:51],v[36:39]
t a[208:209],v205,24576
t a[210:211],v205,25088
m v[52:55],a[224:227],a[104:107],v[52:55]
t a[212:213],v206,24576
t a[214:215],v206,25088
m v[40:43],a[228:231],a[48:51],v[40:43]
t a[216:217],v207,24576
t a[218:219],v207,25088
m v[56:59],a[228:231],a[104:107],v[56:59]
m v[44:47],a[232:235],a[48:51],v[44:47]
m v[60:63],a[232:235],a[104:107],v[60:63]
s_waitcnt lgkmcnt(0)
m v[0:3],a[204:207],a[52:55],v[0:3]
t a[220:221],v204,28672
t a[222:223],v204,29184
m v[16:19],a[204:207],a[108:111],v[16:19]
t a[224:225],v205,28672
t a[226:227],v205,29184
m v[4:7],a[208:211],a[52:55],v[4:7]
t a[228:229],v206,28672
t a[230:231],v206,29184
m v[20:23],a[208:211],a[108:111],v[20:23]
t a[232:233],v207,28672
t a[234:235],v207,29184
m v[8:11],a[212:215],a[52:55],v[8:11]
m v[24:27],a[212:215],a[108:111],v[24:27]
m v[12:15],a[216:219],a[52:55],v[12:15]
m v[28:31],a[216:219],a[108:111],v[28:31]
s_waitcnt lgkmcnt(0)
s_waitcnt vmcnt(16)
s_barrier
m v[32:35],a[220:223],a[52:55],v[32:35]
t a[204:205],v204,49152
t a[206:207],v204,49664
t a[208:209],v205,49152
t a[210:211],v205,49664
t a[212:213],v206,49152
t a[214:215],v206,49664
t a[216:217],v207,49152
t a[218:219],v207,49664
s_mov_b32 m0,0x1c000
s_add_u32 s45,s89,0x100
m v[48:51],a[220:223],a[108:111],v[48:51]
dn v244,%1,s45,0
dn v245,%1,s45,1024
m v[36:39],a[224:227],a[52:55],v[36:39]
dn v246,%1,s45,2048
dn v247,%1,s45,3072
s_mov_b32 m0,0x1d000
s_add_u32 s45,s89,0x40100
m v[52:55],a[224:227],a[108:111],v[52:55]
dn v244,%1,s45,0
dn v245,%1,s45,1024
m v[40:43],a[228:231],a[52:55],v[40:43]
dn v246,%1,s45,2048
dn v247,%1,s45,3072
s_mov_b32 m0,0x1e000
s_add_u32 s45,s90,0x100
m v[56:59],a[228:231],a[108:111],v[56:59]
dn v244,%1,s45,0
dn v245,%1,s45,1024
m v[44:47],a[232:235],a[52:55],v[44:47]
dn v246,%1,s45,2048
dn v247,%1,s45,3072
s_mov_b32 m0,0x1f000
s_add_u32 s45,s90,0x40100
m v[60:63],a[232:235],a[108:111],v[60:63]
dn v244,%1,s45,0
dn v245,%1,s45,1024
dn v246,%1,s45,2048
dn v247,%1,s45,3072
s_waitcnt lgkmcnt(0)
m v[64:67],a[204:207],a[0:3],0
t a[220:221],v204,53248
t a[222:223],v204,53760
m v[80:83],a[204:207],a[56:59],0
t a[224:225],v205,53248
t a[226:227],v205,53760
m v[68:71],a[208:211],a[0:3],0
t a[228:229],v206,53248
t a[230:231],v206,53760
m v[84:87],a[208:211],a[56:59],0
t a[232:233],v207,53248
t a[234:235],v207,53760
m v[72:75],a[212:215],a[0:3],0
m v[88:91],a[212:215],a[56:59],0
m v[76:79],a[216:219],a[0:3],0
m v[92:95],a[216:219],a[56:59],0
s_waitcnt lgkmcnt(0)
m v[64:67],a[220:223],a[4:7],v[64:67]
t a[204:205],v204,57344
t a[206:207],v204,57856
m v[80:83],a[220:223],a[60:63],v[80:83]
t a[208:209],v205,57344
t a[210:211],v205,57856
m v[68:71],a[224:227],a[4:7],v[68:71]
t a[212:213],v206,57344
t a[214:215],v206,57856
m v[84:87],a[224:227],a[60:63],v[84:87]
t a[216:217],v207,57344
t a[218:219],v207,57856
m v[72:75],a[228:231],a[4:7],v[72:75]
v_mul_f32 v0,s47,v0
v_mul_f32 v1,s47,v1
m v[88:91],a[228:231],a[60:63],v[88:91]
v_mul_f32 v2,s47,v2
v_mul_f32 v3,s47,v3
m v[76:79],a[232:235],a[4:7],v[76:79]
v_mul_f32 v4,s47,v4
v_mul_f32 v5,s47,v5
m v[92:95],a[232:235],a[60:63],v[92:95]
v_mul_f32 v6,s47,v6
v_mul_f32 v7,s47,v7
cv v0,v0,v1
s_waitcnt lgkmcnt(0)
m v[64:67],a[204:207],a[8:11],v[64:67]
t a[220:221],v204,61440
t a[222:223],v204,61952
m v[80:83],a[204:207],a[64:67],v[80:83]
t a[224:225],v205,61440
t a[226:227],v205,61952
m v[68:71],a[208:211],a[8:11],v[68:71]
t a[228:229],v206,61440
t a[230:231],v206,61952
m v[84:87],a[208:211],a[64:67],v[84:87]
t a[232:233],v207,61440
t a[234:235],v207,61952
cv v1,v2,v3
m v[72:75],a[212:215],a[8:11],v[72:75]
cv v2,v4,v5
cv v3,v6,v7
m v[88:91],a[212:215],a[64:67],v[88:91]
v_mul_f32 v8,s47,v8
v_mul_f32 v9,s47,v9
m v[76:79],a[216:219],a[8:11],v[76:79]
v_mul_f32 v10,s47,v10
v_mul_f32 v11,s47,v11
m v[92:95],a[216:219],a[64:67],v[92:95]
v_mul_f32 v12,s47,v12
v_mul_f32 v13,s47,v13
v_mul_f32 v14,s47,v14
s_waitcnt lgkmcnt(0)
s_waitcnt vmcnt(16)
s_barrier
m v[64:67],a[220:223],a[12:15],v[64:67]
t a[204:205],v204,0
t a[206:207],v204,512
t a[208:209],v205,0
t a[210:211],v205,512
t a[212:213],v206,0
t a[214:215],v206,512
t a[216:217],v207,0
t a[218:219],v207,512
s_mov_b32 m0,0x24000
s_add_u32 s45,s91,0x100
m v[80:83],a[220:223],a[68:71],v[80:83]
dn v244,%1,s45,0
dn v245,%1,s45,1024
m v[68:71],a[224:227],a[12:15],v[68:71]
dn v246,%1,s45,2048
dn v247,%1,s45,3072
s_mov_b32 m0,0x25000
s_add_u32 s45,s91,0x40100
m v[84:87],a[224:227],a[68:71],v[84:87]
dn v244,%1,s45,0
dn v245,%1,s45,1024
m v[72:75],a[228:231],a[12:15],v[72:75]
dn v246,%1,s45,2048
dn v247,%1,s45,3072
s_mov_b32 m0,0x26000
s_add_u32 s45,s85,0x180
m v[88:91],a[228:231],a[68:71],v[88:91]
dn v244,%1,s45,0
dn v245,%1,s45,1024
m v[76:79],a[232:235],a[12:15],v[76:79]
dn v246,%1,s45,2048
dn v247,%1,s45,3072
s_mov_b32 m0,0x27000
s_add_u32 s45,s85,0x40180
m v[92:95],a[232:235],a[68:71],v[92:95]
dn v244,%1,s45,0
dn v245,%1,s45,1024
dn v246,%1,s45,2048
dn v247,%1,s45,3072
v_mul_f32 v15,s47,v15
cv v8,v8,v9
cv v9,v10,v11
cv v10,v12,v13
cv v11,v14,v15
v_mul_f32 v16,s47,v16
v_mul_f32 v17,s47,v17
v_mul_f32 v18,s47,v18
v_mul_f32 v19,s47,v19
v_mul_f32 v20,s47,v20
s_waitcnt lgkmcnt(0)
m v[64:67],a[204:207],a[16:19],v[64:67]
t a[220:221],v204,4096
t a[222:223],v204,4608
m v[80:83],a[204:207],a[72:75],v[80:83]
t a[224:225],v205,4096
t a[226:227],v205,4608
m v[68:71],a[208:211],a[16:19],v[68:71]
t a[228:229],v206,4096
t a[230:231],v206,4608
m v[84:87],a[208:211],a[72:75],v[84:87]
t a[232:233],v207,4096
t a[234:235],v207,4608
v_mul_f32 v21,s47,v21
m v[72:75],a[212:215],a[16:19],v[72:75]
v_mul_f32 v22,s47,v22
v_mul_f32 v23,s47,v23
m v[88:91],a[212:215],a[72:75],v[88:91]
cv v16,v16,v17
cv v17,v18,v19
m v[76:79],a[216:219],a[16:19],v[76:79]
cv v18,v20,v21
cv v19,v22,v23
m v[92:95],a[216:219],a[72:75],v[92:95]
v_mul_f32 v24,s47,v24
v_mul_f32 v25,s47,v25
v_mul_f32 v26,s47,v26
s_waitcnt lgkmcnt(0)
m v[64:67],a[220:223],a[20:23],v[64:67]
t a[204:205],v204,8192
t a[206:207],v204,8704
m v[80:83],a[220:223],a[76:79],v[80:83]
t a[208:209],v205,8192
t a[210:211],v205,8704
m v[68:71],a[224:227],a[20:23],v[68:71]
t a[212:213],v206,8192
t a[214:215],v206,8704
m v[84:87],a[224:227],a[76:79],v[84:87]
t a[216:217],v207,8192
t a[218:219],v207,8704
v_mul_f32 v27,s47,v27
m v[72:75],a[228:231],a[20:23],v[72:75]
v_mul_f32 v28,s47,v28
v_mul_f32 v29,s47,v29
m v[88:91],a[228:231],a[76:79],v[88:91]
v_mul_f32 v30,s47,v30
v_mul_f32 v31,s47,v31
m v[76:79],a[232:235],a[20:23],v[76:79]
cv v24,v24,v25
cv v25,v26,v27
m v[92:95],a[232:235],a[76:79],v[92:95]
cv v26,v28,v29
cv v27,v30,v31
v_permlane32_swap_b32 v0,v2
s_waitcnt lgkmcnt(0)
m v[64:67],a[204:207],a[24:27],v[64:67]
t a[220:221],v204,12288
t a[222:223],v204,12800
m v[80:83],a[204:207],a[80:83],v[80:83]
t a[224:225],v205,12288
t a[226:227],v205,12800
m v[68:71],a[208:211],a[24:27],v[68:71]
t a[228:229],v206,12288
t a[230:231],v206,12800
m v[84:87],a[208:211],a[80:83],v[84:87]
t a[232:233],v207,12288
t a[234:235],v207,12800
v_permlane32_swap_b32 v1,v3
m v[72:75],a[212:215],a[24:27],v[72:75]
v_permlane32_swap_b32 v8,v10
v_permlane32_swap_b32 v9,v11
m v[88:91],a[212:215],a[80:83],v[88:91]
v_permlane32_swap_b32 v16,v18
v_permlane32_swap_b32 v17,v19
m v[76:79],a[216:219],a[24:27],v[76:79]
v_permlane32_swap_b32 v24,v26
v_permlane32_swap_b32 v25,v27
m v[92:95],a[216:219],a[80:83],v[92:95]
v_permlane16_swap_b32 v0,v2
v_permlane16_swap_b32 v1,v3
v_permlane16_swap_b32 v8,v10
s_waitcnt lgkmcnt(0)
s_waitcnt vmcnt(16)
s_barrier
m v[64:67],a[220:223],a[28:31],v[64:67]
t a[204:205],v204,16384
t a[206:207],v204,16896
t a[208:209],v205,16384
t a[210:211],v205,16896
t a[212:213],v206,16384
t a[214:215],v206,16896
t a[216:217],v207,16384
t a[218:219],v207,16896
s_mov_b32 m0,0x18000
s_add_u32 s45,s86,0x180
m v[80:83],a[220:223],a[84:87],v[80:83]
dn v244,%1,s45,0
dn v245,%1,s45,1024
m v[68:71],a[224:227],a[28:31],v[68:71]
dn v246,%1,s45,2048
dn v247,%1,s45,3072
s_mov_b32 m0,0x19000
s_add_u32 s45,s86,0x40180
m v[84:87],a[224:227],a[84:87],v[84:87]
dn v244,%1,s45,0
dn v245,%1,s45,1024
m v[72:75],a[228:231],a[28:31],v[72:75]
dn v246,%1,s45,2048
dn v247,%1,s45,3072
s_mov_b32 m0,0x1a000
s_add_u32 s45,s87,0x180
m v[88:91],a[228:231],a[84:87],v[88:91]
dn v244,%1,s45,0
dn v245,%1,s45,1024
m v[76:79],a[232:235],a[28:31],v[76:79]
dn v246,%1,s45,2048
dn v247,%1,s45,3072
s_mov_b32 m0,0x1b000
s_add_u32 s45,s87,0x40180
m v[92:95],a[232:235],a[84:87],v[92:95]
dn v244,%1,s45,0
dn v245,%1,s45,1024
dn v246,%1,s45,2048
dn v247,%1,s45,3072
v_permlane16_swap_b32 v9,v11
v_permlane16_swap_b32 v16,v18
v_permlane16_swap_b32 v17,v19
v_permlane16_swap_b32 v24,v26
v_permlane16_swap_b32 v25,v27
v_mul_f32 v32,s47,v32
v_mul_f32 v33,s47,v33
v_mul_f32 v34,s47,v34
v_mul_f32 v35,s47,v35
s_waitcnt lgkmcnt(0)
m v[64:67],a[204:207],a[32:35],v[64:67]
t a[220:221],v204,20480
t a[222:223],v204,20992
m v[80:83],a[204:207],a[88:91],v[80:83]
t a[224:225],v205,20480
t a[226:227],v205,20992
m v[68:71],a[208:211],a[32:35],v[68:71]
t a[228:229],v206,20480
t a[230:231],v206,20992
m v[84:87],a[208:211],a[88:91],v[84:87]
t a[232:233],v207,20480
t a[234:235],v207,20992
v_mul_f32 v36,s47,v36
m v[72:75],a[212:215],a[32:35],v[72:75]
v_mul_f32 v37,s47,v37
v_mul_f32 v38,s47,v38
m v[88:91],a[212:215],a[88:91],v[88:91]
v_mul_f32 v39,s47,v39
cv v32,v32,v33
m v[76:79],a[216:219],a[32:35],v[76:79]
cv v33,v34,v35
cv v34,v36,v37
m v[92:95],a[216:219],a[88:91],v[92:95]
cv v35,v38,v39
v_mul_f32 v40,s47,v40
v_mul_f32 v41,s47,v41
s_waitcnt lgkmcnt(0)
m v[64:67],a[220:223],a[36:39],v[64:67]
t a[204:205],v204,24576
t a[206:207],v204,25088
m v[80:83],a[220:223],a[92:95],v[80:83]
t a[208:209],v205,24576
t a[210:211],v205,25088
m v[68:71],a[224:227],a[36:39],v[68:71]
t a[212:213],v206,24576
t a[214:215],v206,25088
m v[84:87],a[224:227],a[92:95],v[84:87]
t a[216:217],v207,24576
t a[218:219],v207,25088
v_mul_f32 v42,s47,v42
m v[72:75],a[228:231],a[36:39],v[72:75]
v_mul_f32 v43,s47,v43
v_mul_f32 v44,s47,v44
m v[88:91],a[228:231],a[92:95],v[88:91]
v_mul_f32 v45,s47,v45
v_mul_f32 v46,s47,v46
m v[76:79],a[232:235],a[36:39],v[76:79]
v_mul_f32 v47,s47,v47
cv v40,v40,v41
m v[92:95],a[232:235],a[92:95],v[92:95]
cv v41,v42,v43
cv v42,v44,v45
cv v43,v46,v47
s_waitcnt lgkmcnt(0)
m v[64:67],a[204:207],a[40:43],v[64:67]
t a[220:221],v204,28672
t a[222:223],v204,29184
m v[80:83],a[204:207],a[96:99],v[80:83]
t a[224:225],v205,28672
t a[226:227],v205,29184
m v[68:71],a[208:211],a[40:43],v[68:71]
t a[228:229],v206,28672
t a[230:231],v206,29184
m v[84:87],a[208:211],a[96:99],v[84:87]
t a[232:233],v207,28672
t a[234:235],v207,29184
v_mul_f32 v48,s47,v48
m v[72:75],a[212:215],a[40:43],v[72:75]
v_mul_f32 v49,s47,v49
v_mul_f32 v50,s47,v50
m v[88:91],a[212:215],a[96:99],v[88:91]
v_mul_f32 v51,s47,v51
v_mul_f32 v52,s47,v52
m v[76:79],a[216:219],a[40:43],v[76:79]
v_mul_f32 v53,s47,v53
v_mul_f32 v54,s47,v54
m v[92:95],a[216:219],a[96:99],v[92:95]
v_mul_f32 v55,s47,v55
cv v48,v48,v49
cv v49,v50,v51
s_waitcnt lgkmcnt(0)
s_waitcnt vmcnt(16)
s_barrier
m v[64:67],a[220:223],a[44:47],v[64:67]
t a[204:205],v204,49152
t a[206:207],v204,49664
t a[208:209],v205,49152
t a[210:211],v205,49664
t a[212:213],v206,49152
t a[214:215],v206,49664
t a[216:217],v207,49152
t a[218:219],v207,49664
s_mov_b32 m0,0x1c000
s_add_u32 s45,s88,0x180
m v[80:83],a[220:223],a[100:103],v[80:83]
dn v244,%1,s45,0
dn v245,%1,s45,1024
m v[68:71],a[224:227],a[44:47],v[68:71]
dn v246,%1,s45,2048
dn v247,%1,s45,3072
s_mov_b32 m0,0x1d000
s_add_u32 s45,s88,0x40180
m v[84:87],a[224:227],a[100:103],v[84:87]
dn v244,%1,s45,0
dn v245,%1,s45,1024
m v[72:75],a[228:231],a[44:47],v[72:75]
dn v246,%1,s45,2048
dn v247,%1,s45,3072
s_mov_b32 m0,0x1e000
s_add_u32 s45,s89,0x180
m v[88:91],a[228:231],a[100:103],v[88:91]
dn v244,%1,s45,0
dn v245,%1,s45,1024
m v[76:79],a[232:235],a[44:47],v[76:79]
dn v246,%1,s45,2048
dn v247,%1,s45,3072
s_mov_b32 m0,0x1f000
s_add_u32 s45,s89,0x40180
m v[92:95],a[232:235],a[100:103],v[92:95]
dn v244,%1,s45,0
dn v245,%1,s45,1024
dn v246,%1,s45,2048
dn v247,%1,s45,3072
cv v50,v52,v53
cv v51,v54,v55
v_mul_f32 v56,s47,v56
v_mul_f32 v57,s47,v57
v_mul_f32 v58,s47,v58
v_mul_f32 v59,s47,v59
v_mul_f32 v60,s47,v60
v_mul_f32 v61,s47,v61
v_mul_f32 v62,s47,v62
v_mul_f32 v63,s47,v63
s_waitcnt lgkmcnt(0)
m v[64:67],a[204:207],a[48:51],v[64:67]
t a[220:221],v204,53248
t a[222:223],v204,53760
m v[80:83],a[204:207],a[104:107],v[80:83]
t a[224:225],v205,53248
t a[226:227],v205,53760
m v[68:71],a[208:211],a[48:51],v[68:71]
t a[228:229],v206,53248
t a[230:231],v206,53760
m v[84:87],a[208:211],a[104:107],v[84:87]
t a[232:233],v207,53248
t a[234:235],v207,53760
cv v56,v56,v57
m v[72:75],a[212:215],a[48:51],v[72:75]
cv v57,v58,v59
cv v58,v60,v61
m v[88:91],a[212:215],a[104:107],v[88:91]
cv v59,v62,v63
v_permlane32_swap_b32 v32,v34
m v[76:79],a[216:219],a[48:51],v[76:79]
v_permlane32_swap_b32 v33,v35
v_permlane32_swap_b32 v40,v42
m v[92:95],a[216:219],a[104:107],v[92:95]
v_permlane32_swap_b32 v41,v43
v_permlane32_swap_b32 v48,v50
v_permlane32_swap_b32 v49,v51
s_waitcnt lgkmcnt(0)
m v[64:67],a[220:223],a[52:55],v[64:67]
t a[204:205],v204,57344
t a[206:207],v204,57856
m v[80:83],a[220:223],a[108:111],v[80:83]
t a[208:209],v205,57344
t a[210:211],v205,57856
m v[68:71],a[224:227],a[52:55],v[68:71]
t a[212:213],v206,57344
t a[214:215],v206,57856
m v[84:87],a[224:227],a[108:111],v[84:87]
t a[216:217],v207,57344
t a[218:219],v207,57856
v_permlane32_swap_b32 v56,v58
m v[72:75],a[228:231],a[52:55],v[72:75]
v_permlane32_swap_b32 v57,v59
v_permlane16_swap_b32 v32,v34
m v[88:91],a[228:231],a[108:111],v[88:91]
v_permlane16_swap_b32 v33,v35
v_permlane16_swap_b32 v40,v42
m v[76:79],a[232:235],a[52:55],v[76:79]
v_permlane16_swap_b32 v41,v43
v_permlane16_swap_b32 v48,v50
m v[92:95],a[232:235],a[108:111],v[92:95]
v_permlane16_swap_b32 v49,v51
v_permlane16_swap_b32 v56,v58
v_permlane16_swap_b32 v57,v59
s_waitcnt lgkmcnt(0)
m v[96:99],a[204:207],a[0:3],0
t a[220:221],v204,61440
t a[222:223],v204,61952
m v[112:115],a[204:207],a[56:59],0
t a[224:225],v205,61440
t a[226:227],v205,61952
m v[100:103],a[208:211],a[0:3],0
t a[228:229],v206,61440
t a[230:231],v206,61952
m v[116:119],a[208:211],a[56:59],0
t a[232:233],v207,61440
t a[234:235],v207,61952
m v[104:107],a[212:215],a[0:3],0
m v[120:123],a[212:215],a[56:59],0
m v[108:111],a[216:219],a[0:3],0
m v[124:127],a[216:219],a[56:59],0
s_waitcnt lgkmcnt(0)
s_waitcnt vmcnt(16)
s_barrier
m v[96:99],a[220:223],a[4:7],v[96:99]
t a[204:205],v204,0
t a[206:207],v204,512
t a[208:209],v205,0
t a[210:211],v205,512
t a[212:213],v206,0
t a[214:215],v206,512
t a[216:217],v207,0
t a[218:219],v207,512
s_mov_b32 m0,0x24000
s_add_u32 s45,s90,0x180
m v[112:115],a[220:223],a[60:63],v[112:115]
dn v244,%1,s45,0
dn v245,%1,s45,1024
m v[100:103],a[224:227],a[4:7],v[100:103]
dn v246,%1,s45,2048
dn v247,%1,s45,3072
s_mov_b32 m0,0x25000
s_add_u32 s45,s90,0x40180
m v[116:119],a[224:227],a[60:63],v[116:119]
dn v244,%1,s45,0
dn v245,%1,s45,1024
m v[104:107],a[228:231],a[4:7],v[104:107]
dn v246,%1,s45,2048
dn v247,%1,s45,3072
s_mov_b32 m0,0x26000
s_add_u32 s45,s91,0x180
m v[120:123],a[228:231],a[60:63],v[120:123]
dn v244,%1,s45,0
dn v245,%1,s45,1024
m v[108:111],a[232:235],a[4:7],v[108:111]
dn v246,%1,s45,2048
dn v247,%1,s45,3072
s_mov_b32 m0,0x27000
s_add_u32 s45,s91,0x40180
m v[124:127],a[232:235],a[60:63],v[124:127]
dn v244,%1,s45,0
dn v245,%1,s45,1024
dn v246,%1,s45,2048
dn v247,%1,s45,3072
v_mul_f32 v64,s47,v64
v_mul_f32 v65,s47,v65
v_mul_f32 v66,s47,v66
v_mul_f32 v67,s47,v67
s_waitcnt lgkmcnt(0)
m v[96:99],a[204:207],a[8:11],v[96:99]
t a[220:221],v204,4096
t a[222:223],v204,4608
m v[112:115],a[204:207],a[64:67],v[112:115]
t a[224:225],v205,4096
t a[226:227],v205,4608
m v[100:103],a[208:211],a[8:11],v[100:103]
t a[228:229],v206,4096
t a[230:231],v206,4608
m v[116:119],a[208:211],a[64:67],v[116:119]
t a[232:233],v207,4096
t a[234:235],v207,4608
m v[104:107],a[212:215],a[8:11],v[104:107]
v_mul_f32 v68,s47,v68
v_mul_f32 v69,s47,v69
m v[120:123],a[212:215],a[64:67],v[120:123]
v_mul_f32 v70,s47,v70
v_mul_f32 v71,s47,v71
m v[108:111],a[216:219],a[8:11],v[108:111]
cv v64,v64,v65
m v[124:127],a[216:219],a[64:67],v[124:127]
s_waitcnt lgkmcnt(0)
m v[96:99],a[220:223],a[12:15],v[96:99]
t a[204:205],v204,8192
t a[206:207],v204,8704
m v[112:115],a[220:223],a[68:71],v[112:115]
t a[208:209],v205,8192
t a[210:211],v205,8704
m v[100:103],a[224:227],a[12:15],v[100:103]
t a[212:213],v206,8192
t a[214:215],v206,8704
m v[116:119],a[224:227],a[68:71],v[116:119]
t a[216:217],v207,8192
t a[218:219],v207,8704
m v[104:107],a[228:231],a[12:15],v[104:107]
cv v65,v66,v67
cv v66,v68,v69
m v[120:123],a[228:231],a[68:71],v[120:123]
cv v67,v70,v71
v_mul_f32 v72,s47,v72
m v[108:111],a[232:235],a[12:15],v[108:111]
v_mul_f32 v73,s47,v73
m v[124:127],a[232:235],a[68:71],v[124:127]
s_waitcnt lgkmcnt(0)
m v[96:99],a[204:207],a[16:19],v[96:99]
t a[220:221],v204,12288
t a[222:223],v204,12800
m v[112:115],a[204:207],a[72:75],v[112:115]
t a[224:225],v205,12288
t a[226:227],v205,12800
m v[100:103],a[208:211],a[16:19],v[100:103]
t a[228:229],v206,12288
t a[230:231],v206,12800
m v[116:119],a[208:211],a[72:75],v[116:119]
t a[232:233],v207,12288
t a[234:235],v207,12800
m v[104:107],a[212:215],a[16:19],v[104:107]
v_mul_f32 v74,s47,v74
v_mul_f32 v75,s47,v75
m v[120:123],a[212:215],a[72:75],v[120:123]
v_mul_f32 v76,s47,v76
v_mul_f32 v77,s47,v77
m v[108:111],a[216:219],a[16:19],v[108:111]
v_mul_f32 v78,s47,v78
m v[124:127],a[216:219],a[72:75],v[124:127]
s_waitcnt lgkmcnt(0)
s_waitcnt vmcnt(16)
s_barrier
m v[96:99],a[220:223],a[20:23],v[96:99]
t a[204:205],v204,16384
t a[206:207],v204,16896
t a[208:209],v205,16384
t a[210:211],v205,16896
t a[212:213],v206,16384
t a[214:215],v206,16896
t a[216:217],v207,16384
t a[218:219],v207,16896
s_mov_b32 m0,0x18000
s_add_u32 s45,s57,0x10000
m v[112:115],a[220:223],a[76:79],v[112:115]
s_and_b32 s45,s45,0x7ffff
dn v128,%2,s45,0
dn v129,%2,s45,1024
m v[100:103],a[224:227],a[20:23],v[100:103]
dn v130,%2,s45,2048
dn v131,%2,s45,3072
s_mov_b32 m0,0x19000
s_add_u32 s45,s57,0x11000
m v[116:119],a[224:227],a[76:79],v[116:119]
s_and_b32 s45,s45,0x7ffff
dn v132,%2,s45,0
dn v133,%2,s45,1024
m v[104:107],a[228:231],a[20:23],v[104:107]
dn v134,%2,s45,2048
dn v135,%2,s45,3072
s_mov_b32 m0,0x1a000
s_add_u32 s45,s57,0x12000
m v[120:123],a[228:231],a[76:79],v[120:123]
s_and_b32 s45,s45,0x7ffff
dn v128,%2,s45,0
dn v129,%2,s45,1024
m v[108:111],a[232:235],a[20:23],v[108:111]
dn v130,%2,s45,2048
dn v131,%2,s45,3072
s_mov_b32 m0,0x1b000
s_add_u32 s45,s57,0x13000
m v[124:127],a[232:235],a[76:79],v[124:127]
s_and_b32 s45,s45,0x7ffff
dn v132,%2,s45,0
dn v133,%2,s45,1024
dn v134,%2,s45,2048
dn v135,%2,s45,3072
v_mul_f32 v79,s47,v79
cv v72,v72,v73
cv v73,v74,v75
cv v74,v76,v77
cv v75,v78,v79
s_waitcnt lgkmcnt(0)
m v[96:99],a[204:207],a[24:27],v[96:99]
t a[220:221],v204,20480
t a[222:223],v204,20992
m v[112:115],a[204:207],a[80:83],v[112:115]
t a[224:225],v205,20480
t a[226:227],v205,20992
m v[100:103],a[208:211],a[24:27],v[100:103]
t a[228:229],v206,20480
t a[230:231],v206,20992
m v[116:119],a[208:211],a[80:83],v[116:119]
t a[232:233],v207,20480
t a[234:235],v207,20992
m v[104:107],a[212:215],a[24:27],v[104:107]
v_mul_f32 v80,s47,v80
v_mul_f32 v81,s47,v81
m v[120:123],a[212:215],a[80:83],v[120:123]
v_mul_f32 v82,s47,v82
v_mul_f32 v83,s47,v83
m v[108:111],a[216:219],a[24:27],v[108:111]
v_mul_f32 v84,s47,v84
m v[124:127],a[216:219],a[80:83],v[124:127]
s_waitcnt lgkmcnt(0)
m v[96:99],a[220:223],a[28:31],v[96:99]
t a[204:205],v204,24576
t a[206:207],v204,25088
m v[112:115],a[220:223],a[84:87],v[112:115]
t a[208:209],v205,24576
t a[210:211],v205,25088
m v[100:103],a[224:227],a[28:31],v[100:103]
t a[212:213],v206,24576
t a[214:215],v206,25088
m v[116:119],a[224:227],a[84:87],v[116:119]
t a[216:217],v207,24576
t a[218:219],v207,25088
m v[104:107],a[228:231],a[28:31],v[104:107]
v_mul_f32 v85,s47,v85
v_mul_f32 v86,s47,v86
m v[120:123],a[228:231],a[84:87],v[120:123]
v_mul_f32 v87,s47,v87
cv v80,v80,v81
m v[108:111],a[232:235],a[28:31],v[108:111]
cv v81,v82,v83
m v[124:127],a[232:235],a[84:87],v[124:127]
s_waitcnt lgkmcnt(0)
m v[96:99],a[204:207],a[32:35],v[96:99]
t a[220:221],v204,28672
t a[222:223],v204,29184
m v[112:115],a[204:207],a[88:91],v[112:115]
t a[224:225],v205,28672
t a[226:227],v205,29184
m v[100:103],a[208:211],a[32:35],v[100:103]
t a[228:229],v206,28672
t a[230:231],v206,29184
m v[116:119],a[208:211],a[88:91],v[116:119]
t a[232:233],v207,28672
t a[234:235],v207,29184
m v[104:107],a[212:215],a[32:35],v[104:107]
cv v82,v84,v85
cv v83,v86,v87
m v[120:123],a[212:215],a[88:91],v[120:123]
v_mul_f32 v88,s47,v88
v_mul_f32 v89,s47,v89
m v[108:111],a[216:219],a[32:35],v[108:111]
v_mul_f32 v90,s47,v90
m v[124:127],a[216:219],a[88:91],v[124:127]
s_waitcnt lgkmcnt(0)
s_waitcnt vmcnt(16)
s_barrier
m v[96:99],a[220:223],a[36:39],v[96:99]
t a[204:205],v204,49152
t a[206:207],v204,49664
m v[112:115],a[220:223],a[92:95],v[112:115]
t a[208:209],v205,49152
t a[210:211],v205,49664
m v[100:103],a[224:227],a[36:39],v[100:103]
t a[212:213],v206,49152
t a[214:215],v206,49664
m v[116:119],a[224:227],a[92:95],v[116:119]
t a[216:217],v207,49152
t a[218:219],v207,49664
m v[104:107],a[228:231],a[36:39],v[104:107]
v_mul_f32 v91,s47,v91
v_mul_f32 v92,s47,v92
m v[120:123],a[228:231],a[92:95],v[120:123]
v_mul_f32 v93,s47,v93
v_mul_f32 v94,s47,v94
m v[108:111],a[232:235],a[36:39],v[108:111]
v_mul_f32 v95,s47,v95
m v[124:127],a[232:235],a[92:95],v[124:127]
s_waitcnt lgkmcnt(0)
m v[96:99],a[204:207],a[40:43],v[96:99]
t a[220:221],v204,53248
t a[222:223],v204,53760
m v[112:115],a[204:207],a[96:99],v[112:115]
t a[224:225],v205,53248
t a[226:227],v205,53760
m v[100:103],a[208:211],a[40:43],v[100:103]
t a[228:229],v206,53248
t a[230:231],v206,53760
m v[116:119],a[208:211],a[96:99],v[116:119]
t a[232:233],v207,53248
t a[234:235],v207,53760
m v[104:107],a[212:215],a[40:43],v[104:107]
cv v88,v88,v89
cv v89,v90,v91
m v[120:123],a[212:215],a[96:99],v[120:123]
cv v90,v92,v93
cv v91,v94,v95
m v[108:111],a[216:219],a[40:43],v[108:111]
v_permlane32_swap_b32 v64,v66
m v[124:127],a[216:219],a[96:99],v[124:127]
s_waitcnt lgkmcnt(0)
m v[96:99],a[220:223],a[44:47],v[96:99]
t a[204:205],v204,57344
t a[206:207],v204,57856
m v[112:115],a[220:223],a[100:103],v[112:115]
t a[208:209],v205,57344
t a[210:211],v205,57856
m v[100:103],a[224:227],a[44:47],v[100:103]
t a[212:213],v206,57344
t a[214:215],v206,57856
m v[116:119],a[224:227],a[100:103],v[116:119]
t a[216:217],v207,57344
t a[218:219],v207,57856
m v[104:107],a[228:231],a[44:47],v[104:107]
v_permlane32_swap_b32 v65,v67
v_permlane32_swap_b32 v72,v74
m v[120:123],a[228:231],a[100:103],v[120:123]
v_permlane32_swap_b32 v73,v75
v_permlane32_swap_b32 v80,v82
m v[108:111],a[232:235],a[44:47],v[108:111]
v_permlane32_swap_b32 v81,v83
m v[124:127],a[232:235],a[100:103],v[124:127]
s_waitcnt lgkmcnt(0)
m v[96:99],a[204:207],a[48:51],v[96:99]
t a[220:221],v204,61440
t a[222:223],v204,61952
m v[112:115],a[204:207],a[104:107],v[112:115]
t a[224:225],v205,61440
t a[226:227],v205,61952
m v[100:103],a[208:211],a[48:51],v[100:103]
t a[228:229],v206,61440
t a[230:231],v206,61952
m v[116:119],a[208:211],a[104:107],v[116:119]
t a[232:233],v207,61440
t a[234:235],v207,61952
m v[104:107],a[212:215],a[48:51],v[104:107]
v_permlane32_swap_b32 v88,v90
v_permlane32_swap_b32 v89,v91
m v[120:123],a[212:215],a[104:107],v[120:123]
v_permlane16_swap_b32 v64,v66
v_permlane16_swap_b32 v65,v67
m v[108:111],a[216:219],a[48:51],v[108:111]
v_permlane16_swap_b32 v72,v74
m v[124:127],a[216:219],a[104:107],v[124:127]
s_waitcnt lgkmcnt(0)
s_barrier
m v[96:99],a[220:223],a[52:55],v[96:99]
r a[204:207],v192,0
r a[208:211],v192,8192
m v[112:115],a[220:223],a[108:111],v[112:115]
r a[212:215],v193,0
r a[216:219],v193,8192
m v[100:103],a[224:227],a[52:55],v[100:103]
v_permlane16_swap_b32 v73,v75
v_permlane16_swap_b32 v80,v82
m v[116:119],a[224:227],a[108:111],v[116:119]
v_permlane16_swap_b32 v81,v83
v_permlane16_swap_b32 v88,v90
m v[104:107],a[228:231],a[52:55],v[104:107]
v_permlane16_swap_b32 v89,v91
m v[120:123],a[228:231],a[108:111],v[120:123]
m v[108:111],a[232:235],a[52:55],v[108:111]
m v[124:127],a[232:235],a[108:111],v[124:127]
s_waitcnt lgkmcnt(0)
m v[4:7],a[204:207],v[0:3],0
r a[220:223],v194,0
r a[224:227],v194,8192
r a[228:231],v195,0
m v[20:23],a[204:207],v[16:19],0
r a[232:235],v195,8192
v_mul_f32 v96,s47,v96
v_mul_f32 v97,s47,v97
m v[12:15],a[208:211],v[0:3],0
v_mul_f32 v98,s47,v98
v_mul_f32 v99,s47,v99
v_mul_f32 v100,s47,v100
m v[28:31],a[208:211],v[16:19],0
v_mul_f32 v101,s47,v101
v_mul_f32 v102,s47,v102
v_mul_f32 v103,s47,v103
m v[4:7],a[212:215],v[8:11],v[4:7]
cv v96,v96,v97
cv v97,v98,v99
cv v98,v100,v101
m v[20:23],a[212:215],v[24:27],v[20:23]
cv v99,v102,v103
v_mul_f32 v104,s47,v104
v_mul_f32 v105,s47,v105
m v[12:15],a[216:219],v[8:11],v[12:15]
v_mul_f32 v106,s47,v106
v_mul_f32 v107,s47,v107
v_mul_f32 v108,s47,v108
m v[28:31],a[216:219],v[24:27],v[28:31]
v_mul_f32 v109,s47,v109
v_mul_f32 v110,s47,v110
v_mul_f32 v111,s47,v111
cv v104,v104,v105
s_waitcnt lgkmcnt(0)
m v[4:7],a[220:223],v[32:35],v[4:7]
r a[204:207],v192,256
r a[208:211],v192,8448
r a[212:215],v193,256
m v[20:23],a[220:223],v[48:51],v[20:23]
r a[216:219],v193,8448
cv v105,v106,v107
cv v106,v108,v109
m v[12:15],a[224:227],v[32:35],v[12:15]
cv v107,v110,v111
v_mul_f32 v112,s47,v112
v_mul_f32 v113,s47,v113
m v[28:31],a[224:227],v[48:51],v[28:31]
v_mul_f32 v114,s47,v114
v_mul_f32 v115,s47,v115
v_mul_f32 v116,s47,v116
m v[4:7],a[228:231],v[40:43],v[4:7]
v_mul_f32 v117,s47,v117
v_mul_f32 v118,s47,v118
v_mul_f32 v119,s47,v119
m v[20:23],a[228:231],v[56:59],v[20:23]
cv v112,v112,v113
cv v113,v114,v115
cv v114,v116,v117
m v[12:15],a[232:235],v[40:43],v[12:15]
cv v115,v118,v119
v_mul_f32 v120,s47,v120
v_mul_f32 v121,s47,v121
m v[28:31],a[232:235],v[56:59],v[28:31]
v_mul_f32 v122,s47,v122
v_mul_f32 v123,s47,v123
v_mul_f32 v124,s47,v124
v_mul_f32 v125,s47,v125
s_waitcnt lgkmcnt(0)
m v[4:7],a[204:207],v[64:67],v[4:7]
r a[220:223],v194,256
r a[224:227],v194,8448
r a[228:231],v195,256
r a[232:235],v195,8448
m v[20:23],a[204:207],v[80:83],v[20:23]
v_mul_f32 v126,s47,v126
v_mul_f32 v127,s47,v127
cv v120,v120,v121
cv v121,v122,v123
m v[12:15],a[208:211],v[64:67],v[12:15]
cv v122,v124,v125
cv v123,v126,v127
v_permlane32_swap_b32 v96,v98
v_permlane32_swap_b32 v97,v99
v_permlane32_swap_b32 v104,v106
m v[28:31],a[208:211],v[80:83],v[28:31]
v_permlane32_swap_b32 v105,v107
v_permlane32_swap_b32 v112,v114
v_permlane32_swap_b32 v113,v115
v_permlane32_swap_b32 v120,v122
m v[4:7],a[212:215],v[72:75],v[4:7]
v_permlane32_swap_b32 v121,v123
v_permlane16_swap_b32 v96,v98
v_permlane16_swap_b32 v97,v99
v_permlane16_swap_b32 v104,v106
m v[20:23],a[212:215],v[88:91],v[20:23]
v_permlane16_swap_b32 v105,v107
v_permlane16_swap_b32 v112,v114
v_permlane16_swap_b32 v113,v115
v_permlane16_swap_b32 v120,v122
v_permlane16_swap_b32 v121,v123
m v[12:15],a[216:219],v[72:75],v[12:15]
s_mov_b32 s41,s40
s_add_u32 s40,s40,0x8000
s_cmp_eq_u32 s40,0x28000
s_cselect_b32 s42,s49,s48
m v[28:31],a[216:219],v[88:91],v[28:31]
s_cselect_b32 s40,0,s40
v_add_u32 v192,s42,v192
v_add_u32 v193,s42,v193
v_add_u32 v194,s42,v194
v_add_u32 v195,s42,v195
s_waitcnt lgkmcnt(0)
s_barrier
r a[204:207],v192,0
r a[208:211],v192,8192
r a[212:215],v193,0
r a[216:219],v193,8192
s_add_u32 m0,s41,0
s_add_u32 s45,s43,0
dn v128,%2,s45,0
dn v129,%2,s45,1024
dn v130,%2,s45,2048
dn v131,%2,s45,3072
s_add_u32 m0,s41,0x1000
s_add_u32 s45,s43,0x1000
dn v132,%2,s45,0
dn v133,%2,s45,1024
dn v134,%2,s45,2048
dn v135,%2,s45,3072
s_add_u32 m0,s41,0x2000
s_add_u32 s45,s43,0x2000
dn v128,%2,s45,0
dn v129,%2,s45,1024
dn v130,%2,s45,2048
dn v131,%2,s45,3072
s_add_u32 m0,s41,0x3000
s_add_u32 s45,s43,0x3000
dn v132,%2,s45,0
dn v133,%2,s45,1024
dn v134,%2,s45,2048
dn v135,%2,s45,3072
s_add_u32 s43,s43,0x4000
s_and_b32 s43,s43,0x7ffff
m v[4:7],a[220:223],v[96:99],v[4:7]
v_mov_b32 v100,0x3f803f80
v_mov_b32 v101,0x3f803f80
m v[20:23],a[220:223],v[112:115],v[20:23]
v_mov_b32 v102,0x3f803f80
v_mov_b32 v103,0x3f803f80
m v[12:15],a[224:227],v[96:99],v[12:15]
m v[28:31],a[224:227],v[112:115],v[28:31]
m v[4:7],a[228:231],v[104:107],v[4:7]
m v[20:23],a[228:231],v[120:123],v[20:23]
m v[12:15],a[232:235],v[104:107],v[12:15]
m v[28:31],a[232:235],v[120:123],v[28:31]
s_nop 4
v_max3_f32 v220,v4,v5,v6
v_max3_f32 v221,v20,v21,v22
v_max3_f32 v220,v220,v7,v12
v_max3_f32 v221,v221,v23,v28
v_max3_f32 v220,v220,v13,v14
v_max3_f32 v221,v221,v29,v30
v_max_f32 v220,v220,v15
v_max_f32 v221,v221,v31
v_mov_b32 v224,v220
v_mov_b32 v225,v221
s_nop 0
v_permlane32_swap_b32 v220,v224
v_permlane32_swap_b32 v221,v225
v_max_f32 v220,v220,v224
v_max_f32 v221,v221,v225
v_mov_b32 v224,v220
v_mov_b32 v225,v221
s_nop 0
v_permlane16_swap_b32 v220,v224
v_permlane16_swap_b32 v221,v225
v_max_f32 v220,v220,v224
v_max_f32 v221,v221,v225
v_add_f32 v220,0x40e00000,v220
v_add_f32 v221,0x40e00000,v221
v_sub_f32 v84,0,v220
v_sub_f32 v85,0,v220
v_sub_f32 v86,0,v220
v_sub_f32 v87,0,v220
v_sub_f32 v92,0,v221
v_sub_f32 v93,0,v221
v_sub_f32 v94,0,v221
v_sub_f32 v95,0,v221
v_sub_f32 v4,v4,v220
v_sub_f32 v5,v5,v220
v_sub_f32 v6,v6,v220
v_sub_f32 v7,v7,v220
v_sub_f32 v12,v12,v220
v_sub_f32 v13,v13,v220
v_sub_f32 v14,v14,v220
v_sub_f32 v15,v15,v220
v_sub_f32 v20,v20,v221
v_sub_f32 v21,v21,v221
v_sub_f32 v22,v22,v221
v_sub_f32 v23,v23,v221
v_sub_f32 v28,v28,v221
v_sub_f32 v29,v29,v221
v_sub_f32 v30,v30,v221
v_sub_f32 v31,v31,v221
s_waitcnt lgkmcnt(0)
m v[36:39],a[204:207],v[0:3],v[84:87]
r a[220:223],v194,0
r a[224:227],v194,8192
r a[228:231],v195,0
m v[52:55],a[204:207],v[16:19],v[92:95]
r a[232:235],v195,8192
v_add_u32 v196,s42,v196
v_add_u32 v197,s42,v197
m v[44:47],a[208:211],v[0:3],v[84:87]
v_add_u32 v198,s42,v198
v_add_u32 v199,s42,v199
v_add_u32 v200,s42,v200
v_add_u32 v201,s42,v201
m v[60:63],a[208:211],v[16:19],v[92:95]
v_add_u32 v202,s42,v202
v_add_u32 v203,s42,v203
m v[36:39],a[212:215],v[8:11],v[36:39]
v_exp_f32 v220,v4
m v[52:55],a[212:215],v[24:27],v[52:55]
v_exp_f32 v221,v5
v_exp_f32 v222,v6
m v[44:47],a[216:219],v[8:11],v[44:47]
v_exp_f32 v223,v7
cv v68,v220,v221
m v[60:63],a[216:219],v[24:27],v[60:63]
v_exp_f32 v224,v12
v_exp_f32 v225,v13
s_waitcnt lgkmcnt(0)
m v[36:39],a[220:223],v[32:35],v[36:39]
r a[204:207],v192,256
r a[208:211],v192,8448
m v[52:55],a[220:223],v[48:51],v[52:55]
r a[212:215],v193,256
r a[216:219],v193,8448
m v[44:47],a[224:227],v[32:35],v[44:47]
cv v69,v222,v223
m v[60:63],a[224:227],v[48:51],v[60:63]
v_exp_f32 v220,v14
m v[36:39],a[228:231],v[40:43],v[36:39]
v_exp_f32 v221,v15
m v[52:55],a[228:231],v[56:59],v[52:55]
cv v70,v224,v225
m v[44:47],a[232:235],v[40:43],v[44:47]
v_exp_f32 v222,v20
m v[60:63],a[232:235],v[56:59],v[60:63]
v_exp_f32 v223,v21
cv v71,v220,v221
s_waitcnt lgkmcnt(0)
m v[36:39],a[204:207],v[64:67],v[36:39]
r a[220:223],v194,256
r a[224:227],v194,8448
m v[52:55],a[204:207],v[80:83],v[52:55]
r a[228:231],v195,256
r a[232:235],v195,8448
m v[44:47],a[208:211],v[64:67],v[44:47]
v_exp_f32 v224,v22
m v[60:63],a[208:211],v[80:83],v[60:63]
v_exp_f32 v225,v23
m v[36:39],a[212:215],v[72:75],v[36:39]
cv v76,v222,v223
m v[52:55],a[212:215],v[88:91],v[52:55]
v_exp_f32 v220,v28
m v[44:47],a[216:219],v[72:75],v[44:47]
v_exp_f32 v221,v29
m v[60:63],a[216:219],v[88:91],v[60:63]
cv v77,v224,v225
v_exp_f32 v222,v30
s_waitcnt lgkmcnt(0)
m v[36:39],a[220:223],v[96:99],v[36:39]
t a[204:205],v196,0
t a[206:207],v196,8192
m v[52:55],a[220:223],v[112:115],v[52:55]
t a[208:209],v197,0
t a[210:211],v197,8192
m v[44:47],a[224:227],v[96:99],v[44:47]
t a[212:213],v198,0
t a[214:215],v198,8192
m v[60:63],a[224:227],v[112:115],v[60:63]
t a[216:217],v199,0
t a[218:219],v199,8192
m v[36:39],a[228:231],v[104:107],v[36:39]
v_exp_f32 v223,v31
m v[52:55],a[228:231],v[120:123],v[52:55]
cv v78,v220,v221
cv v79,v222,v223
m v[44:47],a[232:235],v[104:107],v[44:47]
v_or3_b32 v228,v68,v69,v70
v_or3_b32 v228,v228,v71,v76
m v[60:63],a[232:235],v[120:123],v[60:63]
v_or3_b32 v228,v228,v77,v78
v_or_b32 v228,v228,v79
v_and_b32 v228,0x40004000,v228
v_cmp_ne_u32_e32 vcc,0,v228
s_nop 0
s_cbranch_vccnz LB_stub1%=
LB_ret1%=:
s_waitcnt lgkmcnt(0)
m a[192:195],v[100:103],v[68:71],0
t a[220:221],v200,0
t a[222:223],v200,8192
m a[196:199],v[100:103],v[76:79],0
t a[224:225],v201,0
t a[226:227],v201,8192
m a[0:3],a[204:207],v[68:71],0
t a[228:229],v202,0
t a[230:231],v202,8192
m a[64:67],a[204:207],v[76:79],0
t a[232:233],v203,0
t a[234:235],v203,8192
m a[4:7],a[208:211],v[68:71],0
m a[68:71],a[208:211],v[76:79],0
m a[8:11],a[212:215],v[68:71],0
m a[72:75],a[212:215],v[76:79],0
m a[12:15],a[216:219],v[68:71],0
m a[76:79],a[216:219],v[76:79],0
s_waitcnt lgkmcnt(0)
m a[16:19],a[220:223],v[68:71],0
t a[204:205],v196,256
t a[206:207],v196,8448
m a[80:83],a[220:223],v[76:79],0
t a[208:209],v197,256
t a[210:211],v197,8448
m a[20:23],a[224:227],v[68:71],0
t a[212:213],v198,256
t a[214:215],v198,8448
m a[84:87],a[224:227],v[76:79],0
t a[216:217],v199,256
t a[218:219],v199,8448
m a[24:27],a[228:231],v[68:71],0
s_mov_b32 s41,s40
s_add_u32 s40,s40,0x8000
m a[88:91],a[228:231],v[76:79],0
s_cmp_eq_u32 s40,0x28000
s_cselect_b32 s42,s49,s48
m a[28:31],a[232:235],v[68:71],0
s_cselect_b32 s40,0,s40
v_add_u32 v192,s42,v192
m a[92:95],a[232:235],v[76:79],0
v_add_u32 v193,s42,v193
v_add_u32 v194,s42,v194
v_add_u32 v195,s42,v195
s_waitcnt lgkmcnt(0)
m a[32:35],a[204:207],v[68:71],0
t a[220:221],v200,256
t a[222:223],v200,8448
m a[96:99],a[204:207],v[76:79],0
t a[224:225],v201,256
t a[226:227],v201,8448
m a[36:39],a[208:211],v[68:71],0
t a[228:229],v202,256
t a[230:231],v202,8448
m a[100:103],a[208:211],v[76:79],0
t a[232:233],v203,256
t a[234:235],v203,8448
m a[40:43],a[212:215],v[68:71],0
m a[104:107],a[212:215],v[76:79],0
m a[44:47],a[216:219],v[68:71],0
m a[108:111],a[216:219],v[76:79],0
s_waitcnt lgkmcnt(0)
s_barrier
r a[204:207],v192,0
r a[208:211],v192,8192
r a[212:215],v193,0
r a[216:219],v193,8192
s_add_u32 m0,s41,0
s_add_u32 s45,s43,0
dn v128,%2,s45,0
dn v129,%2,s45,1024
dn v130,%2,s45,2048
dn v131,%2,s45,3072
s_add_u32 m0,s41,0x1000
s_add_u32 s45,s43,0x1000
dn v132,%2,s45,0
dn v133,%2,s45,1024
dn v134,%2,s45,2048
dn v135,%2,s45,3072
s_add_u32 m0,s41,0x2000
s_add_u32 s45,s43,0x2000
dn v128,%2,s45,0
dn v129,%2,s45,1024
dn v130,%2,s45,2048
dn v131,%2,s45,3072
s_add_u32 m0,s41,0x3000
s_add_u32 s45,s43,0x3000
dn v132,%2,s45,0
dn v133,%2,s45,1024
dn v134,%2,s45,2048
dn v135,%2,s45,3072
s_add_u32 s43,s43,0x4000
s_and_b32 s43,s43,0x7ffff
m a[48:51],a[220:223],v[68:71],0
m a[112:115],a[220:223],v[76:79],0
m a[52:55],a[224:227],v[68:71],0
m a[116:119],a[224:227],v[76:79],0
m a[56:59],a[228:231],v[68:71],0
m a[120:123],a[228:231],v[76:79],0
m a[60:63],a[232:235],v[68:71],0
m a[124:127],a[232:235],v[76:79],0
s_waitcnt lgkmcnt(0)
m v[4:7],a[204:207],v[0:3],v[84:87]
r a[220:223],v194,0
r a[224:227],v194,8192
r a[228:231],v195,0
m v[20:23],a[204:207],v[16:19],v[92:95]
r a[232:235],v195,8192
v_add_u32 v196,s42,v196
v_add_u32 v197,s42,v197
m v[12:15],a[208:211],v[0:3],v[84:87]
v_add_u32 v198,s42,v198
v_add_u32 v199,s42,v199
v_add_u32 v200,s42,v200
v_add_u32 v201,s42,v201
m v[28:31],a[208:211],v[16:19],v[92:95]
v_add_u32 v202,s42,v202
v_add_u32 v203,s42,v203
m v[4:7],a[212:215],v[8:11],v[4:7]
v_exp_f32 v220,v36
m v[20:23],a[212:215],v[24:27],v[20:23]
v_exp_f32 v221,v37
v_exp_f32 v222,v38
m v[12:15],a[216:219],v[8:11],v[12:15]
v_exp_f32 v223,v39
cv v68,v220,v221
m v[28:31],a[216:219],v[24:27],v[28:31]
v_exp_f32 v224,v44
v_exp_f32 v225,v45
s_waitcnt lgkmcnt(0)
m v[4:7],a[220:223],v[32:35],v[4:7]
r a[204:207],v192,256
r a[208:211],v192,8448
m v[20:23],a[220:223],v[48:51],v[20:23]
r a[212:215],v193,256
r a[216:219],v193,8448
m v[12:15],a[224:227],v[32:35],v[12:15]
cv v69,v222,v223
m v[28:31],a[224:227],v[48:51],v[28:31]
v_exp_f32 v220,v46
m v[4:7],a[228:231],v[40:43],v[4:7]
v_exp_f32 v221,v47
m v[20:23],a[228:231],v[56:59],v[20:23]
cv v70,v224,v225
m v[12:15],a[232:235],v[40:43],v[12:15]
v_exp_f32 v222,v52
m v[28:31],a[232:235],v[56:59],v[28:31]
v_exp_f32 v223,v53
cv v71,v220,v221
s_waitcnt lgkmcnt(0)
m v[4:7],a[204:207],v[64:67],v[4:7]
r a[220:223],v194,256
r a[224:227],v194,8448
m v[20:23],a[204:207],v[80:83],v[20:23]
r a[228:231],v195,256
r a[232:235],v195,8448
m v[12:15],a[208:211],v[64:67],v[12:15]
v_exp_f32 v224,v54
m v[28:31],a[208:211],v[80:83],v[28:31]
v_exp_f32 v225,v55
m v[4:7],a[212:215],v[72:75],v[4:7]
cv v76,v222,v223
m v[20:23],a[212:215],v[88:91],v[20:23]
v_exp_f32 v220,v60
m v[12:15],a[216:219],v[72:75],v[12:15]
v_exp_f32 v221,v61
m v[28:31],a[216:219],v[88:91],v[28:31]
cv v77,v224,v225
v_exp_f32 v222,v62
s_waitcnt lgkmcnt(0)
m v[4:7],a[220:223],v[96:99],v[4:7]
t a[204:205],v196,0
t a[206:207],v196,8192
m v[20:23],a[220:223],v[112:115],v[20:23]
t a[208:209],v197,0
t a[210:211],v197,8192
m v[12:15],a[224:227],v[96:99],v[12:15]
t a[212:213],v198,0
t a[214:215],v198,8192
m v[28:31],a[224:227],v[112:115],v[28:31]
t a[216:217],v199,0
t a[218:219],v199,8192
m v[4:7],a[228:231],v[104:107],v[4:7]
v_exp_f32 v223,v63
m v[20:23],a[228:231],v[120:123],v[20:23]
cv v78,v220,v221
cv v79,v222,v223
m v[12:15],a[232:235],v[104:107],v[12:15]
v_or3_b32 v228,v68,v69,v70
v_or3_b32 v228,v228,v71,v76
m v[28:31],a[232:235],v[120:123],v[28:31]
v_or3_b32 v228,v228,v77,v78
v_or_b32 v228,v228,v79
v_and_b32 v228,0x40004000,v228
v_cmp_ne_u32_e32 vcc,0,v228
s_nop 0
s_cbranch_vccnz LB_stub2%=
LB_ret2%=:
s_waitcnt lgkmcnt(0)
m a[192:195],v[100:103],v[68:71],a[192:195]
t a[220:221],v200,0
t a[222:223],v200,8192
m a[196:199],v[100:103],v[76:79],a[196:199]
t a[224:225],v201,0
t a[226:227],v201,8192
m a[0:3],a[204:207],v[68:71],a[0:3]
t a[228:229],v202,0
t a[230:231],v202,8192
m a[64:67],a[204:207],v[76:79],a[64:67]
t a[232:233],v203,0
t a[234:235],v203,8192
m a[4:7],a[208:211],v[68:71],a[4:7]
m a[68:71],a[208:211],v[76:79],a[68:71]
m a[8:11],a[212:215],v[68:71],a[8:11]
m a[72:75],a[212:215],v[76:79],a[72:75]
m a[12:15],a[216:219],v[68:71],a[12:15]
m a[76:79],a[216:219],v[76:79],a[76:79]
s_waitcnt lgkmcnt(0)
m a[16:19],a[220:223],v[68:71],a[16:19]
t a[204:205],v196,256
t a[206:207],v196,8448
m a[80:83],a[220:223],v[76:79],a[80:83]
t a[208:209],v197,256
t a[210:211],v197,8448
m a[20:23],a[224:227],v[68:71],a[20:23]
t a[212:213],v198,256
t a[214:215],v198,8448
m a[84:87],a[224:227],v[76:79],a[84:87]
t a[216:217],v199,256
t a[218:219],v199,8448
m a[24:27],a[228:231],v[68:71],a[24:27]
s_mov_b32 s41,s40
s_add_u32 s40,s40,0x8000
m a[88:91],a[228:231],v[76:79],a[88:91]
s_cmp_eq_u32 s40,0x28000
s_cselect_b32 s42,s49,s48
m a[28:31],a[232:235],v[68:71],a[28:31]
s_cselect_b32 s40,0,s40
v_add_u32 v192,s42,v192
m a[92:95],a[232:235],v[76:79],a[92:95]
v_add_u32 v193,s42,v193
v_add_u32 v194,s42,v194
v_add_u32 v195,s42,v195
s_waitcnt lgkmcnt(0)
m a[32:35],a[204:207],v[68:71],a[32:35]
t a[220:221],v200,256
t a[222:223],v200,8448
m a[96:99],a[204:207],v[76:79],a[96:99]
t a[224:225],v201,256
t a[226:227],v201,8448
m a[36:39],a[208:211],v[68:71],a[36:39]
t a[228:229],v202,256
t a[230:231],v202,8448
m a[100:103],a[208:211],v[76:79],a[100:103]
t a[232:233],v203,256
t a[234:235],v203,8448
m a[40:43],a[212:215],v[68:71],a[40:43]
m a[104:107],a[212:215],v[76:79],a[104:107]
m a[44:47],a[216:219],v[68:71],a[44:47]
m a[108:111],a[216:219],v[76:79],a[108:111]
s_mov_b32 s44,13
LB_loop%=:
s_waitcnt lgkmcnt(0)
s_waitcnt vmcnt(48)
s_barrier
r a[204:207],v192,0
r a[208:211],v192,8192
r a[212:215],v193,0
r a[216:219],v193,8192
s_add_u32 m0,s41,0
s_add_u32 s45,s43,0
dn v128,%2,s45,0
dn v129,%2,s45,1024
dn v130,%2,s45,2048
dn v131,%2,s45,3072
s_add_u32 m0,s41,0x1000
s_add_u32 s45,s43,0x1000
dn v132,%2,s45,0
dn v133,%2,s45,1024
dn v134,%2,s45,2048
dn v135,%2,s45,3072
s_add_u32 m0,s41,0x2000
s_add_u32 s45,s43,0x2000
dn v128,%2,s45,0
dn v129,%2,s45,1024
dn v130,%2,s45,2048
dn v131,%2,s45,3072
s_add_u32 m0,s41,0x3000
s_add_u32 s45,s43,0x3000
dn v132,%2,s45,0
dn v133,%2,s45,1024
dn v134,%2,s45,2048
dn v135,%2,s45,3072
s_add_u32 s43,s43,0x4000
s_and_b32 s43,s43,0x7ffff
m a[48:51],a[220:223],v[68:71],a[48:51]
m a[112:115],a[220:223],v[76:79],a[112:115]
m a[52:55],a[224:227],v[68:71],a[52:55]
m a[116:119],a[224:227],v[76:79],a[116:119]
m a[56:59],a[228:231],v[68:71],a[56:59]
m a[120:123],a[228:231],v[76:79],a[120:123]
m a[60:63],a[232:235],v[68:71],a[60:63]
m a[124:127],a[232:235],v[76:79],a[124:127]
s_waitcnt lgkmcnt(0)
m v[36:39],a[204:207],v[0:3],v[84:87]
r a[220:223],v194,0
r a[224:227],v194,8192
r a[228:231],v195,0
m v[52:55],a[204:207],v[16:19],v[92:95]
r a[232:235],v195,8192
v_add_u32 v196,s42,v196
v_add_u32 v197,s42,v197
m v[44:47],a[208:211],v[0:3],v[84:87]
v_add_u32 v198,s42,v198
v_add_u32 v199,s42,v199
v_add_u32 v200,s42,v200
v_add_u32 v201,s42,v201
m v[60:63],a[208:211],v[16:19],v[92:95]
v_add_u32 v202,s42,v202
v_add_u32 v203,s42,v203
m v[36:39],a[212:215],v[8:11],v[36:39]
v_exp_f32 v220,v4
m v[52:55],a[212:215],v[24:27],v[52:55]
v_exp_f32 v221,v5
v_exp_f32 v222,v6
m v[44:47],a[216:219],v[8:11],v[44:47]
v_exp_f32 v223,v7
cv v68,v220,v221
m v[60:63],a[216:219],v[24:27],v[60:63]
v_exp_f32 v224,v12
v_exp_f32 v225,v13
s_waitcnt lgkmcnt(0)
m v[36:39],a[220:223],v[32:35],v[36:39]
r a[204:207],v192,256
r a[208:211],v192,8448
m v[52:55],a[220:223],v[48:51],v[52:55]
r a[212:215],v193,256
r a[216:219],v193,8448
m v[44:47],a[224:227],v[32:35],v[44:47]
cv v69,v222,v223
m v[60:63],a[224:227],v[48:51],v[60:63]
v_exp_f32 v220,v14
m v[36:39],a[228:231],v[40:43],v[36:39]
v_exp_f32 v221,v15
m v[52:55],a[228:231],v[56:59],v[52:55]
cv v70,v224,v225
m v[44:47],a[232:235],v[40:43],v[44:47]
v_exp_f32 v222,v20
m v[60:63],a[232:235],v[56:59],v[60:63]
v_exp_f32 v223,v21
cv v71,v220,v221
s_waitcnt lgkmcnt(0)
m v[36:39],a[204:207],v[64:67],v[36:39]
r a[220:223],v194,256
r a[224:227],v194,8448
m v[52:55],a[204:207],v[80:83],v[52:55]
r a[228:231],v195,256
r a[232:235],v195,8448
m v[44:47],a[208:211],v[64:67],v[44:47]
v_exp_f32 v224,v22
m v[60:63],a[208:211],v[80:83],v[60:63]
v_exp_f32 v225,v23
m v[36:39],a[212:215],v[72:75],v[36:39]
cv v76,v222,v223
m v[52:55],a[212:215],v[88:91],v[52:55]
v_exp_f32 v220,v28
m v[44:47],a[216:219],v[72:75],v[44:47]
v_exp_f32 v221,v29
m v[60:63],a[216:219],v[88:91],v[60:63]
cv v77,v224,v225
v_exp_f32 v222,v30
s_waitcnt lgkmcnt(0)
m v[36:39],a[220:223],v[96:99],v[36:39]
t a[204:205],v196,0
t a[206:207],v196,8192
m v[52:55],a[220:223],v[112:115],v[52:55]
t a[208:209],v197,0
t a[210:211],v197,8192
m v[44:47],a[224:227],v[96:99],v[44:47]
t a[212:213],v198,0
t a[214:215],v198,8192
m v[60:63],a[224:227],v[112:115],v[60:63]
t a[216:217],v199,0
t a[218:219],v199,8192
m v[36:39],a[228:231],v[104:107],v[36:39]
v_exp_f32 v223,v31
m v[52:55],a[228:231],v[120:123],v[52:55]
cv v78,v220,v221
cv v79,v222,v223
m v[44:47],a[232:235],v[104:107],v[44:47]
v_or3_b32 v228,v68,v69,v70
v_or3_b32 v228,v228,v71,v76
m v[60:63],a[232:235],v[120:123],v[60:63]
v_or3_b32 v228,v228,v77,v78
v_or_b32 v228,v228,v79
v_and_b32 v228,0x40004000,v228
v_cmp_ne_u32_e32 vcc,0,v228
s_nop 0
s_cbranch_vccnz LB_stub3%=
LB_ret3%=:
s_waitcnt lgkmcnt(0)
m a[192:195],v[100:103],v[68:71],a[192:195]
t a[220:221],v200,0
t a[222:223],v200,8192
m a[196:199],v[100:103],v[76:79],a[196:199]
t a[224:225],v201,0
t a[226:227],v201,8192
m a[0:3],a[204:207],v[68:71],a[0:3]
t a[228:229],v202,0
t a[230:231],v202,8192
m a[64:67],a[204:207],v[76:79],a[64:67]
t a[232:233],v203,0
t a[234:235],v203,8192
m a[4:7],a[208:211],v[68:71],a[4:7]
m a[68:71],a[208:211],v[76:79],a[68:71]
m a[8:11],a[212:215],v[68:71],a[8:11]
m a[72:75],a[212:215],v[76:79],a[72:75]
m a[12:15],a[216:219],v[68:71],a[12:15]
m a[76:79],a[216:219],v[76:79],a[76:79]
s_waitcnt lgkmcnt(0)
m a[16:19],a[220:223],v[68:71],a[16:19]
t a[204:205],v196,256
t a[206:207],v196,8448
m a[80:83],a[220:223],v[76:79],a[80:83]
t a[208:209],v197,256
t a[210:211],v197,8448
m a[20:23],a[224:227],v[68:71],a[20:23]
t a[212:213],v198,256
t a[214:215],v198,8448
m a[84:87],a[224:227],v[76:79],a[84:87]
t a[216:217],v199,256
t a[218:219],v199,8448
m a[24:27],a[228:231],v[68:71],a[24:27]
s_mov_b32 s41,s40
s_add_u32 s40,s40,0x8000
m a[88:91],a[228:231],v[76:79],a[88:91]
s_cmp_eq_u32 s40,0x28000
s_cselect_b32 s42,s49,s48
m a[28:31],a[232:235],v[68:71],a[28:31]
s_cselect_b32 s40,0,s40
v_add_u32 v192,s42,v192
m a[92:95],a[232:235],v[76:79],a[92:95]
v_add_u32 v193,s42,v193
v_add_u32 v194,s42,v194
v_add_u32 v195,s42,v195
s_waitcnt lgkmcnt(0)
m a[32:35],a[204:207],v[68:71],a[32:35]
t a[220:221],v200,256
t a[222:223],v200,8448
m a[96:99],a[204:207],v[76:79],a[96:99]
t a[224:225],v201,256
t a[226:227],v201,8448
m a[36:39],a[208:211],v[68:71],a[36:39]
t a[228:229],v202,256
t a[230:231],v202,8448
m a[100:103],a[208:211],v[76:79],a[100:103]
t a[232:233],v203,256
t a[234:235],v203,8448
m a[40:43],a[212:215],v[68:71],a[40:43]
m a[104:107],a[212:215],v[76:79],a[104:107]
)ASM"
R"ASM(m a[44:47],a[216:219],v[68:71],a[44:47]
m a[108:111],a[216:219],v[76:79],a[108:111]
s_waitcnt lgkmcnt(0)
s_waitcnt vmcnt(48)
s_barrier
s_cmp_eq_u32 s44,1
s_cselect_b32 s43,0x80000,s43
r a[204:207],v192,0
r a[208:211],v192,8192
r a[212:215],v193,0
r a[216:219],v193,8192
s_add_u32 m0,s41,0
s_add_u32 s45,s43,0
dn v128,%2,s45,0
dn v129,%2,s45,1024
dn v130,%2,s45,2048
dn v131,%2,s45,3072
s_add_u32 m0,s41,0x1000
s_add_u32 s45,s43,0x1000
dn v132,%2,s45,0
dn v133,%2,s45,1024
dn v134,%2,s45,2048
dn v135,%2,s45,3072
s_add_u32 m0,s41,0x2000
s_add_u32 s45,s43,0x2000
dn v128,%2,s45,0
dn v129,%2,s45,1024
dn v130,%2,s45,2048
dn v131,%2,s45,3072
s_add_u32 m0,s41,0x3000
s_add_u32 s45,s43,0x3000
dn v132,%2,s45,0
dn v133,%2,s45,1024
dn v134,%2,s45,2048
dn v135,%2,s45,3072
s_add_u32 s43,s43,0x4000
s_and_b32 s43,s43,0x7ffff
m a[48:51],a[220:223],v[68:71],a[48:51]
m a[112:115],a[220:223],v[76:79],a[112:115]
m a[52:55],a[224:227],v[68:71],a[52:55]
m a[116:119],a[224:227],v[76:79],a[116:119]
m a[56:59],a[228:231],v[68:71],a[56:59]
m a[120:123],a[228:231],v[76:79],a[120:123]
m a[60:63],a[232:235],v[68:71],a[60:63]
m a[124:127],a[232:235],v[76:79],a[124:127]
s_waitcnt lgkmcnt(0)
m v[4:7],a[204:207],v[0:3],v[84:87]
r a[220:223],v194,0
r a[224:227],v194,8192
r a[228:231],v195,0
m v[20:23],a[204:207],v[16:19],v[92:95]
r a[232:235],v195,8192
v_add_u32 v196,s42,v196
v_add_u32 v197,s42,v197
m v[12:15],a[208:211],v[0:3],v[84:87]
v_add_u32 v198,s42,v198
v_add_u32 v199,s42,v199
v_add_u32 v200,s42,v200
v_add_u32 v201,s42,v201
m v[28:31],a[208:211],v[16:19],v[92:95]
v_add_u32 v202,s42,v202
v_add_u32 v203,s42,v203
m v[4:7],a[212:215],v[8:11],v[4:7]
v_exp_f32 v220,v36
m v[20:23],a[212:215],v[24:27],v[20:23]
v_exp_f32 v221,v37
v_exp_f32 v222,v38
m v[12:15],a[216:219],v[8:11],v[12:15]
v_exp_f32 v223,v39
cv v68,v220,v221
m v[28:31],a[216:219],v[24:27],v[28:31]
v_exp_f32 v224,v44
v_exp_f32 v225,v45
s_waitcnt lgkmcnt(0)
m v[4:7],a[220:223],v[32:35],v[4:7]
r a[204:207],v192,256
r a[208:211],v192,8448
m v[20:23],a[220:223],v[48:51],v[20:23]
r a[212:215],v193,256
r a[216:219],v193,8448
m v[12:15],a[224:227],v[32:35],v[12:15]
cv v69,v222,v223
m v[28:31],a[224:227],v[48:51],v[28:31]
v_exp_f32 v220,v46
m v[4:7],a[228:231],v[40:43],v[4:7]
v_exp_f32 v221,v47
m v[20:23],a[228:231],v[56:59],v[20:23]
cv v70,v224,v225
m v[12:15],a[232:235],v[40:43],v[12:15]
v_exp_f32 v222,v52
m v[28:31],a[232:235],v[56:59],v[28:31]
v_exp_f32 v223,v53
cv v71,v220,v221
s_waitcnt lgkmcnt(0)
m v[4:7],a[204:207],v[64:67],v[4:7]
r a[220:223],v194,256
r a[224:227],v194,8448
m v[20:23],a[204:207],v[80:83],v[20:23]
r a[228:231],v195,256
r a[232:235],v195,8448
m v[12:15],a[208:211],v[64:67],v[12:15]
v_exp_f32 v224,v54
m v[28:31],a[208:211],v[80:83],v[28:31]
v_exp_f32 v225,v55
m v[4:7],a[212:215],v[72:75],v[4:7]
cv v76,v222,v223
m v[20:23],a[212:215],v[88:91],v[20:23]
v_exp_f32 v220,v60
m v[12:15],a[216:219],v[72:75],v[12:15]
v_exp_f32 v221,v61
m v[28:31],a[216:219],v[88:91],v[28:31]
cv v77,v224,v225
v_exp_f32 v222,v62
s_waitcnt lgkmcnt(0)
m v[4:7],a[220:223],v[96:99],v[4:7]
t a[204:205],v196,0
t a[206:207],v196,8192
m v[20:23],a[220:223],v[112:115],v[20:23]
t a[208:209],v197,0
t a[210:211],v197,8192
m v[12:15],a[224:227],v[96:99],v[12:15]
t a[212:213],v198,0
t a[214:215],v198,8192
m v[28:31],a[224:227],v[112:115],v[28:31]
t a[216:217],v199,0
t a[218:219],v199,8192
m v[4:7],a[228:231],v[104:107],v[4:7]
v_exp_f32 v223,v63
m v[20:23],a[228:231],v[120:123],v[20:23]
cv v78,v220,v221
cv v79,v222,v223
m v[12:15],a[232:235],v[104:107],v[12:15]
v_or3_b32 v228,v68,v69,v70
v_or3_b32 v228,v228,v71,v76
m v[28:31],a[232:235],v[120:123],v[28:31]
v_or3_b32 v228,v228,v77,v78
v_or_b32 v228,v228,v79
v_and_b32 v228,0x40004000,v228
v_cmp_ne_u32_e32 vcc,0,v228
s_nop 0
s_cbranch_vccnz LB_stub4%=
LB_ret4%=:
s_waitcnt lgkmcnt(0)
m a[192:195],v[100:103],v[68:71],a[192:195]
t a[220:221],v200,0
t a[222:223],v200,8192
m a[196:199],v[100:103],v[76:79],a[196:199]
t a[224:225],v201,0
t a[226:227],v201,8192
m a[0:3],a[204:207],v[68:71],a[0:3]
t a[228:229],v202,0
t a[230:231],v202,8192
m a[64:67],a[204:207],v[76:79],a[64:67]
t a[232:233],v203,0
t a[234:235],v203,8192
m a[4:7],a[208:211],v[68:71],a[4:7]
m a[68:71],a[208:211],v[76:79],a[68:71]
m a[8:11],a[212:215],v[68:71],a[8:11]
m a[72:75],a[212:215],v[76:79],a[72:75]
m a[12:15],a[216:219],v[68:71],a[12:15]
m a[76:79],a[216:219],v[76:79],a[76:79]
s_waitcnt lgkmcnt(0)
m a[16:19],a[220:223],v[68:71],a[16:19]
t a[204:205],v196,256
t a[206:207],v196,8448
m a[80:83],a[220:223],v[76:79],a[80:83]
t a[208:209],v197,256
t a[210:211],v197,8448
m a[20:23],a[224:227],v[68:71],a[20:23]
t a[212:213],v198,256
t a[214:215],v198,8448
m a[84:87],a[224:227],v[76:79],a[84:87]
t a[216:217],v199,256
t a[218:219],v199,8448
m a[24:27],a[228:231],v[68:71],a[24:27]
s_mov_b32 s41,s40
s_add_u32 s40,s40,0x8000
m a[88:91],a[228:231],v[76:79],a[88:91]
s_cmp_eq_u32 s40,0x28000
s_cselect_b32 s42,s49,s48
m a[28:31],a[232:235],v[68:71],a[28:31]
s_cselect_b32 s40,0,s40
v_add_u32 v192,s42,v192
m a[92:95],a[232:235],v[76:79],a[92:95]
v_add_u32 v193,s42,v193
v_add_u32 v194,s42,v194
v_add_u32 v195,s42,v195
s_waitcnt lgkmcnt(0)
m a[32:35],a[204:207],v[68:71],a[32:35]
t a[220:221],v200,256
t a[222:223],v200,8448
m a[96:99],a[204:207],v[76:79],a[96:99]
t a[224:225],v201,256
t a[226:227],v201,8448
m a[36:39],a[208:211],v[68:71],a[36:39]
t a[228:229],v202,256
t a[230:231],v202,8448
m a[100:103],a[208:211],v[76:79],a[100:103]
t a[232:233],v203,256
t a[234:235],v203,8448
m a[40:43],a[212:215],v[68:71],a[40:43]
m a[104:107],a[212:215],v[76:79],a[104:107]
m a[44:47],a[216:219],v[68:71],a[44:47]
m a[108:111],a[216:219],v[76:79],a[108:111]
s_sub_u32 s44,s44,1
s_cmp_lg_u32 s44,0
s_cbranch_scc1 LB_loop%=
s_waitcnt lgkmcnt(0)
s_waitcnt vmcnt(48)
s_barrier
r a[204:207],v192,0
r a[208:211],v192,8192
r a[212:215],v193,0
r a[216:219],v193,8192
s_mov_b32 m0,0x10000
s_mov_b32 s45,0
dn v144,%4,s45,0
dn v145,%4,s45,1024
dn v146,%4,s45,2048
dn v147,%4,s45,3072
s_mov_b32 m0,0x11000
s_mov_b32 s45,0x7000
dn v144,%4,s45,0
dn v145,%4,s45,1024
dn v146,%4,s45,2048
dn v147,%4,s45,3072
s_mov_b32 m0,0x12000
s_mov_b32 s45,0xe000
dn v144,%4,s45,0
dn v145,%4,s45,1024
dn v146,%4,s45,2048
dn v147,%4,s45,3072
s_mov_b32 m0,0x13000
s_mov_b32 s45,0x15000
dn v144,%4,s45,0
dn v145,%4,s45,1024
dn v146,%4,s45,2048
dn v147,%4,s45,3072
m a[48:51],a[220:223],v[68:71],a[48:51]
m a[112:115],a[220:223],v[76:79],a[112:115]
m a[52:55],a[224:227],v[68:71],a[52:55]
m a[116:119],a[224:227],v[76:79],a[116:119]
m a[56:59],a[228:231],v[68:71],a[56:59]
m a[120:123],a[228:231],v[76:79],a[120:123]
m a[60:63],a[232:235],v[68:71],a[60:63]
m a[124:127],a[232:235],v[76:79],a[124:127]
s_waitcnt lgkmcnt(0)
m v[36:39],a[204:207],v[0:3],v[84:87]
r a[220:223],v194,0
r a[224:227],v194,8192
r a[228:231],v195,0
m v[52:55],a[204:207],v[16:19],v[92:95]
r a[232:235],v195,8192
v_add_u32 v196,s42,v196
v_add_u32 v197,s42,v197
m v[44:47],a[208:211],v[0:3],v[84:87]
v_add_u32 v198,s42,v198
v_add_u32 v199,s42,v199
v_add_u32 v200,s42,v200
v_add_u32 v201,s42,v201
m v[60:63],a[208:211],v[16:19],v[92:95]
v_add_u32 v202,s42,v202
v_add_u32 v203,s42,v203
m v[36:39],a[212:215],v[8:11],v[36:39]
v_exp_f32 v220,v4
m v[52:55],a[212:215],v[24:27],v[52:55]
v_exp_f32 v221,v5
v_exp_f32 v222,v6
m v[44:47],a[216:219],v[8:11],v[44:47]
v_exp_f32 v223,v7
cv v68,v220,v221
m v[60:63],a[216:219],v[24:27],v[60:63]
v_exp_f32 v224,v12
v_exp_f32 v225,v13
s_waitcnt lgkmcnt(0)
m v[36:39],a[220:223],v[32:35],v[36:39]
r a[204:207],v192,256
r a[208:211],v192,8448
m v[52:55],a[220:223],v[48:51],v[52:55]
r a[212:215],v193,256
r a[216:219],v193,8448
m v[44:47],a[224:227],v[32:35],v[44:47]
cv v69,v222,v223
m v[60:63],a[224:227],v[48:51],v[60:63]
v_exp_f32 v220,v14
m v[36:39],a[228:231],v[40:43],v[36:39]
v_exp_f32 v221,v15
m v[52:55],a[228:231],v[56:59],v[52:55]
cv v70,v224,v225
m v[44:47],a[232:235],v[40:43],v[44:47]
v_exp_f32 v222,v20
m v[60:63],a[232:235],v[56:59],v[60:63]
v_exp_f32 v223,v21
cv v71,v220,v221
s_waitcnt lgkmcnt(0)
m v[36:39],a[204:207],v[64:67],v[36:39]
r a[220:223],v194,256
r a[224:227],v194,8448
m v[52:55],a[204:207],v[80:83],v[52:55]
r a[228:231],v195,256
r a[232:235],v195,8448
m v[44:47],a[208:211],v[64:67],v[44:47]
v_exp_f32 v224,v22
m v[60:63],a[208:211],v[80:83],v[60:63]
v_exp_f32 v225,v23
m v[36:39],a[212:215],v[72:75],v[36:39]
cv v76,v222,v223
m v[52:55],a[212:215],v[88:91],v[52:55]
v_exp_f32 v220,v28
m v[44:47],a[216:219],v[72:75],v[44:47]
v_exp_f32 v221,v29
m v[60:63],a[216:219],v[88:91],v[60:63]
cv v77,v224,v225
v_exp_f32 v222,v30
s_waitcnt lgkmcnt(0)
m v[36:39],a[220:223],v[96:99],v[36:39]
t a[204:205],v196,0
t a[206:207],v196,8192
m v[52:55],a[220:223],v[112:115],v[52:55]
t a[208:209],v197,0
t a[210:211],v197,8192
m v[44:47],a[224:227],v[96:99],v[44:47]
t a[212:213],v198,0
t a[214:215],v198,8192
m v[60:63],a[224:227],v[112:115],v[60:63]
t a[216:217],v199,0
t a[218:219],v199,8192
m v[36:39],a[228:231],v[104:107],v[36:39]
v_exp_f32 v223,v31
m v[52:55],a[228:231],v[120:123],v[52:55]
cv v78,v220,v221
cv v79,v222,v223
m v[44:47],a[232:235],v[104:107],v[44:47]
v_or3_b32 v228,v68,v69,v70
v_or3_b32 v228,v228,v71,v76
m v[60:63],a[232:235],v[120:123],v[60:63]
v_or3_b32 v228,v228,v77,v78
v_or_b32 v228,v228,v79
v_and_b32 v228,0x40004000,v228
v_cmp_ne_u32_e32 vcc,0,v228
s_nop 0
s_cbranch_vccnz LB_stub5%=
LB_ret5%=:
s_waitcnt lgkmcnt(0)
m a[192:195],v[100:103],v[68:71],a[192:195]
t a[220:221],v200,0
t a[222:223],v200,8192
m a[196:199],v[100:103],v[76:79],a[196:199]
t a[224:225],v201,0
t a[226:227],v201,8192
m a[0:3],a[204:207],v[68:71],a[0:3]
t a[228:229],v202,0
t a[230:231],v202,8192
m a[64:67],a[204:207],v[76:79],a[64:67]
t a[232:233],v203,0
t a[234:235],v203,8192
m a[4:7],a[208:211],v[68:71],a[4:7]
m a[68:71],a[208:211],v[76:79],a[68:71]
m a[8:11],a[212:215],v[68:71],a[8:11]
m a[72:75],a[212:215],v[76:79],a[72:75]
m a[12:15],a[216:219],v[68:71],a[12:15]
m a[76:79],a[216:219],v[76:79],a[76:79]
s_waitcnt lgkmcnt(0)
m a[16:19],a[220:223],v[68:71],a[16:19]
t a[204:205],v196,256
t a[206:207],v196,8448
m a[80:83],a[220:223],v[76:79],a[80:83]
t a[208:209],v197,256
t a[210:211],v197,8448
m a[20:23],a[224:227],v[68:71],a[20:23]
t a[212:213],v198,256
t a[214:215],v198,8448
m a[84:87],a[224:227],v[76:79],a[84:87]
t a[216:217],v199,256
t a[218:219],v199,8448
m a[24:27],a[228:231],v[68:71],a[24:27]
s_mov_b32 s41,s40
s_add_u32 s40,s40,0x8000
m a[88:91],a[228:231],v[76:79],a[88:91]
s_cmp_eq_u32 s40,0x28000
s_cselect_b32 s42,s49,s48
m a[28:31],a[232:235],v[68:71],a[28:31]
s_cselect_b32 s40,0,s40
v_add_u32 v192,s42,v192
m a[92:95],a[232:235],v[76:79],a[92:95]
v_add_u32 v193,s42,v193
v_add_u32 v194,s42,v194
v_add_u32 v195,s42,v195
s_waitcnt lgkmcnt(0)
m a[32:35],a[204:207],v[68:71],a[32:35]
t a[220:221],v200,256
t a[222:223],v200,8448
m a[96:99],a[204:207],v[76:79],a[96:99]
t a[224:225],v201,256
t a[226:227],v201,8448
m a[36:39],a[208:211],v[68:71],a[36:39]
t a[228:229],v202,256
t a[230:231],v202,8448
m a[100:103],a[208:211],v[76:79],a[100:103]
t a[232:233],v203,256
t a[234:235],v203,8448
m a[40:43],a[212:215],v[68:71],a[40:43]
m a[104:107],a[212:215],v[76:79],a[104:107]
m a[44:47],a[216:219],v[68:71],a[44:47]
m a[108:111],a[216:219],v[76:79],a[108:111]
s_waitcnt lgkmcnt(0)
s_waitcnt vmcnt(48)
s_barrier
r a[204:207],v192,0
r a[208:211],v192,8192
r a[212:215],v193,0
r a[216:219],v193,8192
s_mov_b32 m0,0x18000
s_mov_b32 s45,0x80
dn v144,%4,s45,0
dn v145,%4,s45,1024
dn v146,%4,s45,2048
dn v147,%4,s45,3072
s_mov_b32 m0,0x19000
s_mov_b32 s45,0x7080
dn v144,%4,s45,0
dn v145,%4,s45,1024
dn v146,%4,s45,2048
dn v147,%4,s45,3072
s_mov_b32 m0,0x1a000
s_mov_b32 s45,0xe080
dn v144,%4,s45,0
dn v145,%4,s45,1024
dn v146,%4,s45,2048
dn v147,%4,s45,3072
s_mov_b32 m0,0x1b000
s_mov_b32 s45,0x15080
dn v144,%4,s45,0
dn v145,%4,s45,1024
dn v146,%4,s45,2048
dn v147,%4,s45,3072
m a[48:51],a[220:223],v[68:71],a[48:51]
m a[112:115],a[220:223],v[76:79],a[112:115]
m a[52:55],a[224:227],v[68:71],a[52:55]
m a[116:119],a[224:227],v[76:79],a[116:119]
m a[56:59],a[228:231],v[68:71],a[56:59]
m a[120:123],a[228:231],v[76:79],a[120:123]
m a[60:63],a[232:235],v[68:71],a[60:63]
m a[124:127],a[232:235],v[76:79],a[124:127]
s_waitcnt lgkmcnt(0)
m v[4:7],a[204:207],v[0:3],v[84:87]
r a[220:223],v194,0
r a[224:227],v194,8192
r a[228:231],v195,0
m v[20:23],a[204:207],v[16:19],v[92:95]
r a[232:235],v195,8192
v_add_u32 v196,s42,v196
v_add_u32 v197,s42,v197
m v[12:15],a[208:211],v[0:3],v[84:87]
v_add_u32 v198,s42,v198
v_add_u32 v199,s42,v199
v_add_u32 v200,s42,v200
v_add_u32 v201,s42,v201
m v[28:31],a[208:211],v[16:19],v[92:95]
v_add_u32 v202,s42,v202
v_add_u32 v203,s42,v203
m v[4:7],a[212:215],v[8:11],v[4:7]
v_exp_f32 v220,v36
m v[20:23],a[212:215],v[24:27],v[20:23]
v_exp_f32 v221,v37
v_exp_f32 v222,v38
m v[12:15],a[216:219],v[8:11],v[12:15]
v_exp_f32 v223,v39
cv v68,v220,v221
m v[28:31],a[216:219],v[24:27],v[28:31]
v_exp_f32 v224,v44
v_exp_f32 v225,v45
s_waitcnt lgkmcnt(0)
m v[4:7],a[220:223],v[32:35],v[4:7]
r a[204:207],v192,256
r a[208:211],v192,8448
m v[20:23],a[220:223],v[48:51],v[20:23]
r a[212:215],v193,256
r a[216:219],v193,8448
m v[12:15],a[224:227],v[32:35],v[12:15]
cv v69,v222,v223
m v[28:31],a[224:227],v[48:51],v[28:31]
v_exp_f32 v220,v46
m v[4:7],a[228:231],v[40:43],v[4:7]
v_exp_f32 v221,v47
m v[20:23],a[228:231],v[56:59],v[20:23]
cv v70,v224,v225
m v[12:15],a[232:235],v[40:43],v[12:15]
v_exp_f32 v222,v52
m v[28:31],a[232:235],v[56:59],v[28:31]
v_exp_f32 v223,v53
cv v71,v220,v221
s_waitcnt lgkmcnt(0)
m v[4:7],a[204:207],v[64:67],v[4:7]
r a[220:223],v194,256
r a[224:227],v194,8448
m v[20:23],a[204:207],v[80:83],v[20:23]
r a[228:231],v195,256
r a[232:235],v195,8448
m v[12:15],a[208:211],v[64:67],v[12:15]
v_exp_f32 v224,v54
m v[28:31],a[208:211],v[80:83],v[28:31]
v_exp_f32 v225,v55
m v[4:7],a[212:215],v[72:75],v[4:7]
cv v76,v222,v223
m v[20:23],a[212:215],v[88:91],v[20:23]
v_exp_f32 v220,v60
m v[12:15],a[216:219],v[72:75],v[12:15]
v_exp_f32 v221,v61
m v[28:31],a[216:219],v[88:91],v[28:31]
cv v77,v224,v225
v_exp_f32 v222,v62
s_waitcnt lgkmcnt(0)
m v[4:7],a[220:223],v[96:99],v[4:7]
t a[204:205],v196,0
t a[206:207],v196,8192
m v[20:23],a[220:223],v[112:115],v[20:23]
t a[208:209],v197,0
t a[210:211],v197,8192
m v[12:15],a[224:227],v[96:99],v[12:15]
t a[212:213],v198,0
t a[214:215],v198,8192
m v[28:31],a[224:227],v[112:115],v[28:31]
t a[216:217],v199,0
t a[218:219],v199,8192
m v[4:7],a[228:231],v[104:107],v[4:7]
v_exp_f32 v223,v63
m v[20:23],a[228:231],v[120:123],v[20:23]
cv v78,v220,v221
cv v79,v222,v223
m v[12:15],a[232:235],v[104:107],v[12:15]
v_or3_b32 v228,v68,v69,v70
v_or3_b32 v228,v228,v71,v76
m v[28:31],a[232:235],v[120:123],v[28:31]
v_or3_b32 v228,v228,v77,v78
v_or_b32 v228,v228,v79
v_and_b32 v228,0x40004000,v228
v_cmp_ne_u32_e32 vcc,0,v228
s_nop 0
s_cbranch_vccnz LB_stub6%=
LB_ret6%=:
s_waitcnt lgkmcnt(0)
m a[192:195],v[100:103],v[68:71],a[192:195]
t a[220:221],v200,0
t a[222:223],v200,8192
m a[196:199],v[100:103],v[76:79],a[196:199]
t a[224:225],v201,0
t a[226:227],v201,8192
m a[0:3],a[204:207],v[68:71],a[0:3]
t a[228:229],v202,0
t a[230:231],v202,8192
m a[64:67],a[204:207],v[76:79],a[64:67]
t a[232:233],v203,0
t a[234:235],v203,8192
m a[4:7],a[208:211],v[68:71],a[4:7]
m a[68:71],a[208:211],v[76:79],a[68:71]
m a[8:11],a[212:215],v[68:71],a[8:11]
m a[72:75],a[212:215],v[76:79],a[72:75]
m a[12:15],a[216:219],v[68:71],a[12:15]
m a[76:79],a[216:219],v[76:79],a[76:79]
s_waitcnt lgkmcnt(0)
m a[16:19],a[220:223],v[68:71],a[16:19]
t a[204:205],v196,256
t a[206:207],v196,8448
m a[80:83],a[220:223],v[76:79],a[80:83]
t a[208:209],v197,256
t a[210:211],v197,8448
m a[20:23],a[224:227],v[68:71],a[20:23]
t a[212:213],v198,256
t a[214:215],v198,8448
m a[84:87],a[224:227],v[76:79],a[84:87]
t a[216:217],v199,256
t a[218:219],v199,8448
m a[24:27],a[228:231],v[68:71],a[24:27]
s_mov_b32 s41,s40
s_add_u32 s40,s40,0x8000
m a[88:91],a[228:231],v[76:79],a[88:91]
s_cmp_eq_u32 s40,0x28000
s_cselect_b32 s42,s49,s48
m a[28:31],a[232:235],v[68:71],a[28:31]
s_cselect_b32 s40,0,s40
v_add_u32 v192,s42,v192
m a[92:95],a[232:235],v[76:79],a[92:95]
v_add_u32 v193,s42,v193
v_add_u32 v194,s42,v194
v_add_u32 v195,s42,v195
s_waitcnt lgkmcnt(0)
m a[32:35],a[204:207],v[68:71],a[32:35]
t a[220:221],v200,256
t a[222:223],v200,8448
m a[96:99],a[204:207],v[76:79],a[96:99]
t a[224:225],v201,256
t a[226:227],v201,8448
m a[36:39],a[208:211],v[68:71],a[36:39]
t a[228:229],v202,256
t a[230:231],v202,8448
m a[100:103],a[208:211],v[76:79],a[100:103]
t a[232:233],v203,256
t a[234:235],v203,8448
m a[40:43],a[212:215],v[68:71],a[40:43]
m a[104:107],a[212:215],v[76:79],a[104:107]
m a[44:47],a[216:219],v[68:71],a[44:47]
m a[108:111],a[216:219],v[76:79],a[108:111]
s_waitcnt lgkmcnt(0)
s_waitcnt vmcnt(48)
s_barrier
r a[204:207],v192,0
r a[208:211],v192,8192
r a[212:215],v193,0
r a[216:219],v193,8192
s_mov_b32 m0,0x20000
s_mov_b32 s45,0x100
dn v144,%4,s45,0
dn v145,%4,s45,1024
dn v146,%4,s45,2048
dn v147,%4,s45,3072
s_mov_b32 m0,0x21000
s_mov_b32 s45,0x7100
dn v144,%4,s45,0
dn v145,%4,s45,1024
dn v146,%4,s45,2048
dn v147,%4,s45,3072
s_mov_b32 m0,0x22000
s_mov_b32 s45,0xe100
dn v144,%4,s45,0
dn v145,%4,s45,1024
dn v146,%4,s45,2048
dn v147,%4,s45,3072
s_mov_b32 m0,0x23000
s_mov_b32 s45,0x15100
dn v144,%4,s45,0
dn v145,%4,s45,1024
dn v146,%4,s45,2048
dn v147,%4,s45,3072
m a[48:51],a[220:223],v[68:71],a[48:51]
m a[112:115],a[220:223],v[76:79],a[112:115]
m a[52:55],a[224:227],v[68:71],a[52:55]
m a[116:119],a[224:227],v[76:79],a[116:119]
m a[56:59],a[228:231],v[68:71],a[56:59]
m a[120:123],a[228:231],v[76:79],a[120:123]
m a[60:63],a[232:235],v[68:71],a[60:63]
m a[124:127],a[232:235],v[76:79],a[124:127]
s_waitcnt lgkmcnt(0)
m v[36:39],a[204:207],v[0:3],v[84:87]
r a[220:223],v194,0
r a[224:227],v194,8192
r a[228:231],v195,0
m v[52:55],a[204:207],v[16:19],v[92:95]
r a[232:235],v195,8192
v_add_u32 v196,s42,v196
v_add_u32 v197,s42,v197
m v[44:47],a[208:211],v[0:3],v[84:87]
v_add_u32 v198,s42,v198
v_add_u32 v199,s42,v199
v_add_u32 v200,s42,v200
v_add_u32 v201,s42,v201
m v[60:63],a[208:211],v[16:19],v[92:95]
v_add_u32 v202,s42,v202
v_add_u32 v203,s42,v203
m v[36:39],a[212:215],v[8:11],v[36:39]
v_exp_f32 v220,v4
m v[52:55],a[212:215],v[24:27],v[52:55]
v_exp_f32 v221,v5
v_exp_f32 v222,v6
m v[44:47],a[216:219],v[8:11],v[44:47]
v_exp_f32 v223,v7
cv v68,v220,v221
m v[60:63],a[216:219],v[24:27],v[60:63]
v_exp_f32 v224,v12
v_exp_f32 v225,v13
s_waitcnt lgkmcnt(0)
m v[36:39],a[220:223],v[32:35],v[36:39]
r a[204:207],v192,256
r a[208:211],v192,8448
m v[52:55],a[220:223],v[48:51],v[52:55]
r a[212:215],v193,256
r a[216:219],v193,8448
m v[44:47],a[224:227],v[32:35],v[44:47]
cv v69,v222,v223
m v[60:63],a[224:227],v[48:51],v[60:63]
v_exp_f32 v220,v14
m v[36:39],a[228:231],v[40:43],v[36:39]
v_exp_f32 v221,v15
m v[52:55],a[228:231],v[56:59],v[52:55]
cv v70,v224,v225
m v[44:47],a[232:235],v[40:43],v[44:47]
v_exp_f32 v222,v20
m v[60:63],a[232:235],v[56:59],v[60:63]
v_exp_f32 v223,v21
cv v71,v220,v221
s_waitcnt lgkmcnt(0)
m v[36:39],a[204:207],v[64:67],v[36:39]
r a[220:223],v194,256
r a[224:227],v194,8448
m v[52:55],a[204:207],v[80:83],v[52:55]
r a[228:231],v195,256
r a[232:235],v195,8448
m v[44:47],a[208:211],v[64:67],v[44:47]
v_exp_f32 v224,v22
m v[60:63],a[208:211],v[80:83],v[60:63]
v_exp_f32 v225,v23
m v[36:39],a[212:215],v[72:75],v[36:39]
cv v76,v222,v223
m v[52:55],a[212:215],v[88:91],v[52:55]
v_exp_f32 v220,v28
m v[44:47],a[216:219],v[72:75],v[44:47]
v_exp_f32 v221,v29
m v[60:63],a[216:219],v[88:91],v[60:63]
cv v77,v224,v225
v_exp_f32 v222,v30
s_waitcnt lgkmcnt(0)
m v[36:39],a[220:223],v[96:99],v[36:39]
t a[204:205],v196,0
t a[206:207],v196,8192
m v[52:55],a[220:223],v[112:115],v[52:55]
t a[208:209],v197,0
t a[210:211],v197,8192
m v[44:47],a[224:227],v[96:99],v[44:47]
t a[212:213],v198,0
t a[214:215],v198,8192
m v[60:63],a[224:227],v[112:115],v[60:63]
t a[216:217],v199,0
t a[218:219],v199,8192
m v[36:39],a[228:231],v[104:107],v[36:39]
v_exp_f32 v223,v31
m v[52:55],a[228:231],v[120:123],v[52:55]
cv v78,v220,v221
cv v79,v222,v223
m v[44:47],a[232:235],v[104:107],v[44:47]
v_or3_b32 v228,v68,v69,v70
v_or3_b32 v228,v228,v71,v76
m v[60:63],a[232:235],v[120:123],v[60:63]
v_or3_b32 v228,v228,v77,v78
v_or_b32 v228,v228,v79
v_and_b32 v228,0x40004000,v228
v_cmp_ne_u32_e32 vcc,0,v228
s_nop 0
s_cbranch_vccnz LB_stub7%=
LB_ret7%=:
s_waitcnt lgkmcnt(0)
m a[192:195],v[100:103],v[68:71],a[192:195]
t a[220:221],v200,0
t a[222:223],v200,8192
m a[196:199],v[100:103],v[76:79],a[196:199]
t a[224:225],v201,0
t a[226:227],v201,8192
m a[0:3],a[204:207],v[68:71],a[0:3]
t a[228:229],v202,0
t a[230:231],v202,8192
m a[64:67],a[204:207],v[76:79],a[64:67]
t a[232:233],v203,0
t a[234:235],v203,8192
m a[4:7],a[208:211],v[68:71],a[4:7]
m a[68:71],a[208:211],v[76:79],a[68:71]
m a[8:11],a[212:215],v[68:71],a[8:11]
m a[72:75],a[212:215],v[76:79],a[72:75]
m a[12:15],a[216:219],v[68:71],a[12:15]
m a[76:79],a[216:219],v[76:79],a[76:79]
s_waitcnt lgkmcnt(0)
m a[16:19],a[220:223],v[68:71],a[16:19]
t a[204:205],v196,256
t a[206:207],v196,8448
m a[80:83],a[220:223],v[76:79],a[80:83]
t a[208:209],v197,256
t a[210:211],v197,8448
m a[20:23],a[224:227],v[68:71],a[20:23]
t a[212:213],v198,256
t a[214:215],v198,8448
m a[84:87],a[224:227],v[76:79],a[84:87]
t a[216:217],v199,256
t a[218:219],v199,8448
m a[24:27],a[228:231],v[68:71],a[24:27]
s_mov_b32 s41,s40
s_add_u32 s40,s40,0x8000
m a[88:91],a[228:231],v[76:79],a[88:91]
s_cmp_eq_u32 s40,0x28000
s_cselect_b32 s42,s49,s48
m a[28:31],a[232:235],v[68:71],a[28:31]
s_cselect_b32 s40,0,s40
v_add_u32 v192,s42,v192
m a[92:95],a[232:235],v[76:79],a[92:95]
v_add_u32 v193,s42,v193
v_add_u32 v194,s42,v194
v_add_u32 v195,s42,v195
s_waitcnt lgkmcnt(0)
m a[32:35],a[204:207],v[68:71],a[32:35]
t a[220:221],v200,256
t a[222:223],v200,8448
m a[96:99],a[204:207],v[76:79],a[96:99]
t a[224:225],v201,256
t a[226:227],v201,8448
m a[36:39],a[208:211],v[68:71],a[36:39]
t a[228:229],v202,256
t a[230:231],v202,8448
m a[100:103],a[208:211],v[76:79],a[100:103]
t a[232:233],v203,256
t a[234:235],v203,8448
m a[40:43],a[212:215],v[68:71],a[40:43]
v_add_u32 v196,s42,v196
v_add_u32 v197,s42,v197
m a[104:107],a[212:215],v[76:79],a[104:107]
v_add_u32 v198,s42,v198
v_add_u32 v199,s42,v199
m a[44:47],a[216:219],v[68:71],a[44:47]
v_add_u32 v200,s42,v200
v_add_u32 v201,s42,v201
m a[108:111],a[216:219],v[76:79],a[108:111]
v_add_u32 v202,s42,v202
v_add_u32 v203,s42,v203
s_waitcnt lgkmcnt(0)
s_waitcnt vmcnt(48)
s_barrier
t a[204:205],v196,0
t a[206:207],v196,8192
t a[208:209],v197,0
t a[210:211],v197,8192
t a[212:213],v198,0
t a[214:215],v198,8192
t a[216:217],v199,0
t a[218:219],v199,8192
s_mov_b32 m0,0
s_mov_b32 s45,0x180
dn v144,%4,s45,0
dn v145,%4,s45,1024
dn v146,%4,s45,2048
dn v147,%4,s45,3072
s_mov_b32 m0,0x1000
s_mov_b32 s45,0x7180
dn v144,%4,s45,0
dn v145,%4,s45,1024
dn v146,%4,s45,2048
dn v147,%4,s45,3072
s_mov_b32 m0,0x2000
s_mov_b32 s45,0xe180
dn v144,%4,s45,0
dn v145,%4,s45,1024
dn v146,%4,s45,2048
dn v147,%4,s45,3072
s_mov_b32 m0,0x3000
s_mov_b32 s45,0x15180
dn v144,%4,s45,0
dn v145,%4,s45,1024
dn v146,%4,s45,2048
dn v147,%4,s45,3072
m a[48:51],a[220:223],v[68:71],a[48:51]
m a[112:115],a[220:223],v[76:79],a[112:115]
m a[52:55],a[224:227],v[68:71],a[52:55]
m a[116:119],a[224:227],v[76:79],a[116:119]
m a[56:59],a[228:231],v[68:71],a[56:59]
m a[120:123],a[228:231],v[76:79],a[120:123]
m a[60:63],a[232:235],v[68:71],a[60:63]
m a[124:127],a[232:235],v[76:79],a[124:127]
v_exp_f32 v220,v36
v_exp_f32 v221,v37
v_exp_f32 v222,v38
v_exp_f32 v223,v39
cv v68,v220,v221
v_exp_f32 v224,v44
v_exp_f32 v225,v45
cv v69,v222,v223
v_exp_f32 v220,v46
v_exp_f32 v221,v47
cv v70,v224,v225
v_exp_f32 v222,v52
v_exp_f32 v223,v53
cv v71,v220,v221
v_exp_f32 v224,v54
v_exp_f32 v225,v55
cv v76,v222,v223
v_exp_f32 v220,v60
v_exp_f32 v221,v61
cv v77,v224,v225
v_exp_f32 v222,v62
v_exp_f32 v223,v63
cv v78,v220,v221
cv v79,v222,v223
v_or3_b32 v228,v68,v69,v70
v_or3_b32 v228,v228,v71,v76
v_or3_b32 v228,v228,v77,v78
v_or_b32 v228,v228,v79
v_and_b32 v228,0x40004000,v228
v_cmp_ne_u32_e32 vcc,0,v228
s_nop 0
s_cbranch_vccnz LB_stub8%=
LB_ret8%=:
s_waitcnt lgkmcnt(0)
m a[192:195],v[100:103],v[68:71],a[192:195]
t a[220:221],v200,0
t a[222:223],v200,8192
m a[196:199],v[100:103],v[76:79],a[196:199]
t a[224:225],v201,0
t a[226:227],v201,8192
m a[0:3],a[204:207],v[68:71],a[0:3]
t a[228:229],v202,0
t a[230:231],v202,8192
m a[64:67],a[204:207],v[76:79],a[64:67]
t a[232:233],v203,0
t a[234:235],v203,8192
m a[4:7],a[208:211],v[68:71],a[4:7]
m a[68:71],a[208:211],v[76:79],a[68:71]
m a[8:11],a[212:215],v[68:71],a[8:11]
m a[72:75],a[212:215],v[76:79],a[72:75]
m a[12:15],a[216:219],v[68:71],a[12:15]
m a[76:79],a[216:219],v[76:79],a[76:79]
s_waitcnt lgkmcnt(0)
m a[16:19],a[220:223],v[68:71],a[16:19]
t a[204:205],v196,256
t a[206:207],v196,8448
t a[208:209],v197,256
m a[80:83],a[220:223],v[76:79],a[80:83]
t a[210:211],v197,8448
t a[212:213],v198,256
t a[214:215],v198,8448
t a[216:217],v199,256
m a[20:23],a[224:227],v[68:71],a[20:23]
t a[218:219],v199,8448
ar v216,a192
ar v217,a196
m a[84:87],a[224:227],v[76:79],a[84:87]
v_rcp_f32 v216,v216
v_rcp_f32 v217,v217
m a[24:27],a[228:231],v[68:71],a[24:27]
ar v96,a0
ar v97,a1
ar v98,a2
ar v99,a3
m a[88:91],a[228:231],v[76:79],a[88:91]
ar v100,a4
ar v101,a5
ar v102,a6
m a[28:31],a[232:235],v[68:71],a[28:31]
ar v103,a7
cv v0,v96,v97
cv v1,v98,v99
cv v2,v100,v101
m a[92:95],a[232:235],v[76:79],a[92:95]
cv v3,v102,v103
ar v104,a64
ar v105,a65
ar v106,a66
s_waitcnt lgkmcnt(0)
m a[32:35],a[204:207],v[68:71],a[32:35]
t a[220:221],v200,256
t a[222:223],v200,8448
t a[224:225],v201,256
m a[96:99],a[204:207],v[76:79],a[96:99]
t a[226:227],v201,8448
t a[228:229],v202,256
t a[230:231],v202,8448
m a[36:39],a[208:211],v[68:71],a[36:39]
t a[232:233],v203,256
t a[234:235],v203,8448
ar v107,a67
ar v108,a68
m a[100:103],a[208:211],v[76:79],a[100:103]
ar v109,a69
ar v110,a70
ar v111,a71
m a[40:43],a[212:215],v[68:71],a[40:43]
cv v32,v104,v105
cv v33,v106,v107
cv v34,v108,v109
m a[104:107],a[212:215],v[76:79],a[104:107]
cv v35,v110,v111
ar v96,a8
ar v97,a9
ar v98,a10
m a[44:47],a[216:219],v[68:71],a[44:47]
ar v99,a11
ar v100,a12
ar v101,a13
m a[108:111],a[216:219],v[76:79],a[108:111]
ar v102,a14
ar v103,a15
cv v4,v96,v97
cv v5,v98,v99
s_waitcnt lgkmcnt(0)
s_waitcnt vmcnt(48)
s_barrier
t a[204:205],v208,0
t a[206:207],v208,2048
t a[208:209],v209,0
t a[210:211],v209,2048
t a[212:213],v210,0
t a[214:215],v210,2048
t a[216:217],v211,0
t a[218:219],v211,2048
s_mov_b32 m0,0x8000
s_mov_b32 s45,0x200
dn v144,%4,s45,0
dn v145,%4,s45,1024
dn v146,%4,s45,2048
dn v147,%4,s45,3072
s_mov_b32 m0,0x9000
s_mov_b32 s45,0x7200
dn v144,%4,s45,0
dn v145,%4,s45,1024
dn v146,%4,s45,2048
dn v147,%4,s45,3072
s_mov_b32 m0,0xa000
s_mov_b32 s45,0xe200
dn v144,%4,s45,0
dn v145,%4,s45,1024
dn v146,%4,s45,2048
dn v147,%4,s45,3072
s_mov_b32 m0,0xb000
s_mov_b32 s45,0x15200
dn v144,%4,s45,0
dn v145,%4,s45,1024
dn v146,%4,s45,2048
dn v147,%4,s45,3072
m a[48:51],a[220:223],v[68:71],a[48:51]
cv v6,v100,v101
cv v7,v102,v103
m a[112:115],a[220:223],v[76:79],a[112:115]
ar v104,a72
ar v105,a73
ar v106,a74
m a[52:55],a[224:227],v[68:71],a[52:55]
ar v107,a75
ar v108,a76
m a[116:119],a[224:227],v[76:79],a[116:119]
ar v109,a77
ar v110,a78
ar v111,a79
m a[56:59],a[228:231],v[68:71],a[56:59]
cv v36,v104,v105
cv v37,v106,v107
m a[120:123],a[228:231],v[76:79],a[120:123]
cv v38,v108,v109
cv v39,v110,v111
ar v96,a16
m a[60:63],a[232:235],v[68:71],a[60:63]
ar v97,a17
ar v98,a18
m a[124:127],a[232:235],v[76:79],a[124:127]
ar v99,a19
ar v100,a20
ar v101,a21
s_waitcnt lgkmcnt(0)
m v[64:67],a[204:207],v[0:3],0
t a[220:221],v208,4096
t a[222:223],v208,6144
t a[224:225],v209,4096
m v[80:83],a[204:207],v[32:35],0
t a[226:227],v209,6144
t a[228:229],v210,4096
t a[230:231],v210,6144
m v[68:71],a[208:211],v[0:3],0
t a[232:233],v211,4096
t a[234:235],v211,6144
ar v102,a22
ar v103,a23
m v[84:87],a[208:211],v[32:35],0
cv v8,v96,v97
cv v9,v98,v99
cv v10,v100,v101
m v[72:75],a[212:215],v[0:3],0
cv v11,v102,v103
ar v104,a80
ar v105,a81
m v[88:91],a[212:215],v[32:35],0
ar v106,a82
ar v107,a83
ar v108,a84
ar v109,a85
m v[76:79],a[216:219],v[0:3],0
ar v110,a86
ar v111,a87
cv v40,v104,v105
m v[92:95],a[216:219],v[32:35],0
cv v41,v106,v107
cv v42,v108,v109
cv v43,v110,v111
ar v96,a24
s_waitcnt lgkmcnt(0)
m v[64:67],a[220:223],v[4:7],v[64:67]
t a[204:205],v208,8192
t a[206:207],v208,10240
t a[208:209],v209,8192
m v[80:83],a[220:223],v[36:39],v[80:83]
t a[210:211],v209,10240
t a[212:213],v210,8192
t a[214:215],v210,10240
t a[216:217],v211,8192
m v[68:71],a[224:227],v[4:7],v[68:71]
t a[218:219],v211,10240
ar v97,a25
ar v98,a26
m v[84:87],a[224:227],v[36:39],v[84:87]
ar v99,a27
ar v100,a28
ar v101,a29
ar v102,a30
m v[72:75],a[228:231],v[4:7],v[72:75]
ar v103,a31
cv v12,v96,v97
cv v13,v98,v99
m v[88:91],a[228:231],v[36:39],v[88:91]
cv v14,v100,v101
cv v15,v102,v103
ar v104,a88
ar v105,a89
m v[76:79],a[232:235],v[4:7],v[76:79]
ar v106,a90
ar v107,a91
ar v108,a92
m v[92:95],a[232:235],v[36:39],v[92:95]
ar v109,a93
ar v110,a94
ar v111,a95
cv v44,v104,v105
s_waitcnt lgkmcnt(0)
m v[64:67],a[204:207],v[8:11],v[64:67]
t a[220:221],v208,12288
t a[222:223],v208,14336
t a[224:225],v209,12288
m v[80:83],a[204:207],v[40:43],v[80:83]
t a[226:227],v209,14336
t a[228:229],v210,12288
t a[230:231],v210,14336
t a[232:233],v211,12288
m v[68:71],a[208:211],v[8:11],v[68:71]
t a[234:235],v211,14336
cv v45,v106,v107
cv v46,v108,v109
m v[84:87],a[208:211],v[40:43],v[84:87]
cv v47,v110,v111
ar v96,a32
ar v97,a33
ar v98,a34
m v[72:75],a[212:215],v[8:11],v[72:75]
ar v99,a35
ar v100,a36
ar v101,a37
m v[88:91],a[212:215],v[40:43],v[88:91]
ar v102,a38
ar v103,a39
cv v16,v96,v97
cv v17,v98,v99
m v[76:79],a[216:219],v[8:11],v[76:79]
cv v18,v100,v101
cv v19,v102,v103
ar v104,a96
m v[92:95],a[216:219],v[40:43],v[92:95]
ar v105,a97
ar v106,a98
ar v107,a99
ar v108,a100
s_waitcnt lgkmcnt(0)
m v[64:67],a[220:223],v[12:15],v[64:67]
t a[204:205],v208,16384
t a[206:207],v208,18432
t a[208:209],v209,16384
m v[80:83],a[220:223],v[44:47],v[80:83]
t a[210:211],v209,18432
t a[212:213],v210,16384
t a[214:215],v210,18432
m v[68:71],a[224:227],v[12:15],v[68:71]
t a[216:217],v211,16384
t a[218:219],v211,18432
ar v109,a101
ar v110,a102
m v[84:87],a[224:227],v[44:47],v[84:87]
ar v111,a103
cv v48,v104,v105
cv v49,v106,v107
m v[72:75],a[228:231],v[12:15],v[72:75]
cv v50,v108,v109
cv v51,v110,v111
ar v96,a40
m v[88:91],a[228:231],v[44:47],v[88:91]
ar v97,a41
ar v98,a42
ar v99,a43
ar v100,a44
m v[76:79],a[232:235],v[12:15],v[76:79]
ar v101,a45
ar v102,a46
ar v103,a47
m v[92:95],a[232:235],v[44:47],v[92:95]
cv v20,v96,v97
cv v21,v98,v99
cv v22,v100,v101
cv v23,v102,v103
s_waitcnt lgkmcnt(0)
m v[64:67],a[204:207],v[16:19],v[64:67]
t a[220:221],v208,20480
t a[222:223],v208,22528
t a[224:225],v209,20480
m v[80:83],a[204:207],v[48:51],v[80:83]
t a[226:227],v209,22528
t a[228:229],v210,20480
t a[230:231],v210,22528
t a[232:233],v211,20480
m v[68:71],a[208:211],v[16:19],v[68:71]
t a[234:235],v211,22528
ar v104,a104
ar v105,a105
m v[84:87],a[208:211],v[48:51],v[84:87]
ar v106,a106
ar v107,a107
ar v108,a108
ar v109,a109
m v[72:75],a[212:215],v[16:19],v[72:75]
ar v110,a110
ar v111,a111
cv v52,v104,v105
m v[88:91],a[212:215],v[48:51],v[88:91]
cv v53,v106,v107
cv v54,v108,v109
cv v55,v110,v111
ar v96,a48
m v[76:79],a[216:219],v[16:19],v[76:79]
ar v97,a49
ar v98,a50
ar v99,a51
m v[92:95],a[216:219],v[48:51],v[92:95]
ar v100,a52
ar v101,a53
ar v102,a54
ar v103,a55
s_waitcnt lgkmcnt(0)
m v[64:67],a[220:223],v[20:23],v[64:67]
t a[204:205],v208,24576
t a[206:207],v208,26624
t a[208:209],v209,24576
m v[80:83],a[220:223],v[52:55],v[80:83]
t a[210:211],v209,26624
t a[212:213],v210,24576
t a[214:215],v210,26624
t a[216:217],v211,24576
m v[68:71],a[224:227],v[20:23],v[68:71]
t a[218:219],v211,26624
cv v24,v96,v97
cv v25,v98,v99
m v[84:87],a[224:227],v[52:55],v[84:87]
cv v26,v100,v101
cv v27,v102,v103
ar v104,a112
ar v105,a113
m v[72:75],a[228:231],v[20:23],v[72:75]
ar v106,a114
ar v107,a115
ar v108,a116
m v[88:91],a[228:231],v[52:55],v[88:91]
ar v109,a117
ar v110,a118
ar v111,a119
cv v56,v104,v105
m v[76:79],a[232:235],v[20:23],v[76:79]
cv v57,v106,v107
cv v58,v108,v109
cv v59,v110,v111
m v[92:95],a[232:235],v[52:55],v[92:95]
ar v96,a56
ar v97,a57
ar v98,a58
ar v99,a59
s_waitcnt lgkmcnt(0)
m v[64:67],a[204:207],v[24:27],v[64:67]
t a[220:221],v208,28672
t a[222:223],v208,30720
t a[224:225],v209,28672
t a[226:227],v209,30720
m v[80:83],a[204:207],v[56:59],v[80:83]
t a[228:229],v210,28672
t a[230:231],v210,30720
t a[232:233],v211,28672
t a[234:235],v211,30720
m v[68:71],a[208:211],v[24:27],v[68:71]
v_add_u32 v208,s48,v208
v_add_u32 v209,s48,v209
v_add_u32 v210,s48,v210
v_add_u32 v211,s48,v211
m v[84:87],a[208:211],v[56:59],v[84:87]
ar v100,a60
ar v101,a61
ar v102,a62
ar v103,a63
m v[72:75],a[212:215],v[24:27],v[72:75]
cv v28,v96,v97
cv v29,v98,v99
cv v30,v100,v101
cv v31,v102,v103
m v[88:91],a[212:215],v[56:59],v[88:91]
ar v104,a120
ar v105,a121
ar v106,a122
ar v107,a123
m v[76:79],a[216:219],v[24:27],v[76:79]
ar v108,a124
ar v109,a125
ar v110,a126
ar v111,a127
m v[92:95],a[216:219],v[56:59],v[92:95]
cv v60,v104,v105
cv v61,v106,v107
cv v62,v108,v109
cv v63,v110,v111
s_waitcnt lgkmcnt(0)
s_waitcnt vmcnt(48)
s_barrier
t a[204:205],v208,0
t a[206:207],v208,2048
t a[208:209],v209,0
t a[210:211],v209,2048
t a[212:213],v210,0
t a[214:215],v210,2048
t a[216:217],v211,0
t a[218:219],v211,2048
s_mov_b32 m0,0x10000
s_mov_b32 s45,0x280
dn v144,%4,s45,0
dn v145,%4,s45,1024
dn v146,%4,s45,2048
dn v147,%4,s45,3072
s_mov_b32 m0,0x11000
s_mov_b32 s45,0x7280
dn v144,%4,s45,0
dn v145,%4,s45,1024
dn v146,%4,s45,2048
dn v147,%4,s45,3072
s_mov_b32 m0,0x12000
s_mov_b32 s45,0xe280
dn v144,%4,s45,0
dn v145,%4,s45,1024
dn v146,%4,s45,2048
dn v147,%4,s45,3072
s_mov_b32 m0,0x13000
s_mov_b32 s45,0x15280
dn v144,%4,s45,0
dn v145,%4,s45,1024
dn v146,%4,s45,2048
dn v147,%4,s45,3072
m v[64:67],a[220:223],v[28:31],v[64:67]
m v[80:83],a[220:223],v[60:63],v[80:83]
m v[68:71],a[224:227],v[28:31],v[68:71]
m v[84:87],a[224:227],v[60:63],v[84:87]
m v[72:75],a[228:231],v[28:31],v[72:75]
m v[88:91],a[228:231],v[60:63],v[88:91]
m v[76:79],a[232:235],v[28:31],v[76:79]
m v[92:95],a[232:235],v[60:63],v[92:95]
s_waitcnt lgkmcnt(0)
m v[96:99],a[204:207],v[0:3],0
t a[220:221],v208,4096
t a[222:223],v208,6144
m v[112:115],a[204:207],v[32:35],0
t a[224:225],v209,4096
t a[226:227],v209,6144
m v[100:103],a[208:211],v[0:3],0
t a[228:229],v210,4096
t a[230:231],v210,6144
m v[116:119],a[208:211],v[32:35],0
t a[232:233],v211,4096
t a[234:235],v211,6144
m v[104:107],a[212:215],v[0:3],0
v_lshrrev_b32 v235,4,v214
v_bfe_u32 v236,v214,2,2
m v[120:123],a[212:215],v[32:35],0
m v[108:111],a[216:219],v[0:3],0
m v[124:127],a[216:219],v[32:35],0
s_waitcnt lgkmcnt(0)
m v[96:99],a[220:223],v[4:7],v[96:99]
t a[204:205],v208,8192
t a[206:207],v208,10240
t a[208:209],v209,8192
m v[112:115],a[220:223],v[36:39],v[112:115]
t a[210:211],v209,10240
t a[212:213],v210,8192
t a[214:215],v210,10240
m v[100:103],a[224:227],v[4:7],v[100:103]
t a[216:217],v211,8192
t a[218:219],v211,10240
s_add_u32 s55,s54,0
m v[116:119],a[224:227],v[36:39],v[116:119]
s_add_u32 s59,s55,0x1000
v_mul_f32 v64,v64,v216
v_mul_f32 v65,v65,v216
m v[104:107],a[228:231],v[4:7],v[104:107]
v_mul_f32 v66,v66,v216
v_mul_f32 v67,v67,v216
v_mul_f32 v68,v68,v216
m v[120:123],a[228:231],v[36:39],v[120:123]
v_mul_f32 v69,v69,v216
v_mul_f32 v70,v70,v216
v_mul_f32 v71,v71,v216
m v[108:111],a[232:235],v[4:7],v[108:111]
cv v64,v64,v65
cv v65,v66,v67
cv v66,v68,v69
m v[124:127],a[232:235],v[36:39],v[124:127]
v_cmp_eq_u32_e32 vcc,v235,v236
v_and_b32 v237,1,v214
v_lshlrev_b32 v237,4,v237
s_waitcnt lgkmcnt(0)
m v[96:99],a[204:207],v[8:11],v[96:99]
t a[220:221],v208,12288
t a[222:223],v208,14336
t a[224:225],v209,12288
m v[112:115],a[204:207],v[40:43],v[112:115]
t a[226:227],v209,14336
t a[228:229],v210,12288
t a[230:231],v210,14336
m v[100:103],a[208:211],v[8:11],v[100:103]
t a[232:233],v211,12288
t a[234:235],v211,14336
cv v67,v70,v71
m v[116:119],a[208:211],v[40:43],v[116:119]
buffer_store_dwordx4 v[64:67],v215,%5,s55 offen sc1
v_mul_f32 v72,v72,v216
v_mul_f32 v73,v73,v216
m v[104:107],a[212:215],v[8:11],v[104:107]
v_mul_f32 v74,v74,v216
v_mul_f32 v75,v75,v216
v_mul_f32 v76,v76,v216
m v[120:123],a[212:215],v[40:43],v[120:123]
v_mul_f32 v77,v77,v216
v_mul_f32 v78,v78,v216
v_mul_f32 v79,v79,v216
m v[108:111],a[216:219],v[8:11],v[108:111]
cv v72,v72,v73
cv v73,v74,v75
cv v74,v76,v77
m v[124:127],a[216:219],v[40:43],v[124:127]
cv v75,v78,v79
v_mov_b32 v238,0x3f80
v_lshlrev_b32 v238,v237,v238
s_waitcnt lgkmcnt(0)
m v[96:99],a[220:223],v[12:15],v[96:99]
t a[204:205],v208,16384
t a[206:207],v208,18432
t a[208:209],v209,16384
m v[112:115],a[220:223],v[44:47],v[112:115]
t a[210:211],v209,18432
t a[212:213],v210,16384
t a[214:215],v210,18432
m v[100:103],a[224:227],v[12:15],v[100:103]
t a[216:217],v211,16384
t a[218:219],v211,18432
buffer_store_dwordx4 v[72:75],v215,%5,s55 offen offset:1024 sc1
m v[116:119],a[224:227],v[44:47],v[116:119]
v_mul_f32 v80,v80,v217
v_mul_f32 v81,v81,v217
v_mul_f32 v82,v82,v217
m v[104:107],a[228:231],v[12:15],v[104:107]
v_mul_f32 v83,v83,v217
v_mul_f32 v84,v84,v217
v_mul_f32 v85,v85,v217
m v[120:123],a[228:231],v[44:47],v[120:123]
v_mul_f32 v86,v86,v217
v_mul_f32 v87,v87,v217
cv v80,v80,v81
m v[108:111],a[232:235],v[12:15],v[108:111]
cv v81,v82,v83
cv v82,v84,v85
cv v83,v86,v87
m v[124:127],a[232:235],v[44:47],v[124:127]
v_cndmask_b32_e32 v238,0,v238,vcc
v_bfe_u32 v237,v214,1,1
v_cmp_eq_u32_e32 vcc,0,v237
s_waitcnt lgkmcnt(0)
m v[96:99],a[204:207],v[16:19],v[96:99]
t a[220:221],v208,20480
t a[222:223],v208,22528
t a[224:225],v209,20480
m v[112:115],a[204:207],v[48:51],v[112:115]
t a[226:227],v209,22528
t a[228:229],v210,20480
t a[230:231],v210,22528
m v[100:103],a[208:211],v[16:19],v[100:103]
t a[232:233],v211,20480
t a[234:235],v211,22528
buffer_store_dwordx4 v[80:83],v215,%5,s55 offen offset:2048 sc1
m v[116:119],a[208:211],v[48:51],v[116:119]
v_mul_f32 v88,v88,v217
v_mul_f32 v89,v89,v217
v_mul_f32 v90,v90,v217
m v[104:107],a[212:215],v[16:19],v[104:107]
v_mul_f32 v91,v91,v217
v_mul_f32 v92,v92,v217
v_mul_f32 v93,v93,v217
m v[120:123],a[212:215],v[48:51],v[120:123]
v_mul_f32 v94,v94,v217
v_mul_f32 v95,v95,v217
cv v88,v88,v89
m v[108:111],a[216:219],v[16:19],v[108:111]
cv v89,v90,v91
cv v90,v92,v93
cv v91,v94,v95
m v[124:127],a[216:219],v[48:51],v[124:127]
buffer_store_dwordx4 v[88:91],v215,%5,s55 offen offset:3072 sc1
v_cndmask_b32_e32 v222,0,v238,vcc
v_sub_u32 v223,v238,v222
s_waitcnt lgkmcnt(0)
m v[96:99],a[220:223],v[20:23],v[96:99]
t a[204:205],v208,24576
t a[206:207],v208,26624
m v[112:115],a[220:223],v[52:55],v[112:115]
t a[208:209],v209,24576
t a[210:211],v209,26624
m v[100:103],a[224:227],v[20:23],v[100:103]
t a[212:213],v210,24576
t a[214:215],v210,26624
m v[116:119],a[224:227],v[52:55],v[116:119]
t a[216:217],v211,24576
t a[218:219],v211,26624
m v[104:107],a[228:231],v[20:23],v[104:107]
v_mov_b32 v220,0
v_mov_b32 v221,0
m v[120:123],a[228:231],v[52:55],v[120:123]
v_mov_b32 v224,0
m v[108:111],a[232:235],v[20:23],v[108:111]
m v[124:127],a[232:235],v[52:55],v[124:127]
s_waitcnt lgkmcnt(0)
m v[96:99],a[204:207],v[24:27],v[96:99]
t a[220:221],v208,28672
t a[222:223],v208,30720
m v[112:115],a[204:207],v[56:59],v[112:115]
t a[224:225],v209,28672
t a[226:227],v209,30720
m v[100:103],a[208:211],v[24:27],v[100:103]
t a[228:229],v210,28672
t a[230:231],v210,30720
m v[116:119],a[208:211],v[56:59],v[116:119]
t a[232:233],v211,28672
t a[234:235],v211,30720
m v[104:107],a[212:215],v[24:27],v[104:107]
v_add_u32 v208,s48,v208
v_add_u32 v209,s48,v209
m v[120:123],a[212:215],v[56:59],v[120:123]
v_add_u32 v210,s48,v210
v_add_u32 v211,s48,v211
m v[108:111],a[216:219],v[24:27],v[108:111]
v_mov_b32 v225,0
s_lshl_b32 s46,%11,3
m v[124:127],a[216:219],v[56:59],v[124:127]
s_waitcnt lgkmcnt(0)
s_waitcnt vmcnt(52)
s_barrier
t a[204:205],v208,0
t a[206:207],v208,2048
t a[208:209],v209,0
t a[210:211],v209,2048
t a[212:213],v210,0
t a[214:215],v210,2048
t a[216:217],v211,0
t a[218:219],v211,2048
s_mov_b32 m0,0x18000
s_mov_b32 s45,0x300
dn v144,%4,s45,0
dn v145,%4,s45,1024
dn v146,%4,s45,2048
dn v147,%4,s45,3072
s_mov_b32 m0,0x19000
s_mov_b32 s45,0x7300
dn v144,%4,s45,0
dn v145,%4,s45,1024
dn v146,%4,s45,2048
dn v147,%4,s45,3072
s_mov_b32 m0,0x1a000
s_mov_b32 s45,0xe300
dn v144,%4,s45,0
dn v145,%4,s45,1024
dn v146,%4,s45,2048
dn v147,%4,s45,3072
s_mov_b32 m0,0x1b000
s_mov_b32 s45,0x15300
dn v144,%4,s45,0
dn v145,%4,s45,1024
dn v146,%4,s45,2048
dn v147,%4,s45,3072
m v[96:99],a[220:223],v[28:31],v[96:99]
v_lshlrev_b32 v234,7,v214
v_add_u32 v234,s46,v234
m v[112:115],a[220:223],v[60:63],v[112:115]
v_add_u32 v234,4,v234
m v[100:103],a[224:227],v[28:31],v[100:103]
m v[116:119],a[224:227],v[60:63],v[116:119]
m v[104:107],a[228:231],v[28:31],v[104:107]
m v[120:123],a[228:231],v[60:63],v[120:123]
m v[108:111],a[232:235],v[28:31],v[108:111]
m v[124:127],a[232:235],v[60:63],v[124:127]
s_waitcnt lgkmcnt(0)
m v[64:67],a[204:207],v[0:3],0
t a[220:221],v208,4096
t a[222:223],v208,6144
m v[80:83],a[204:207],v[32:35],0
t a[224:225],v209,4096
t a[226:227],v209,6144
m v[68:71],a[208:211],v[0:3],0
t a[228:229],v210,4096
t a[230:231],v210,6144
m v[84:87],a[208:211],v[32:35],0
t a[232:233],v211,4096
t a[234:235],v211,6144
m v[72:75],a[212:215],v[0:3],0
v_cmp_gt_u32_e32 vcc,16,v214
v_mov_b32 v235,0x7fffff00
m v[88:91],a[212:215],v[32:35],0
m v[76:79],a[216:219],v[0:3],0
m v[92:95],a[216:219],v[32:35],0
s_waitcnt lgkmcnt(0)
m v[64:67],a[220:223],v[4:7],v[64:67]
t a[204:205],v208,8192
t a[206:207],v208,10240
t a[208:209],v209,8192
m v[80:83],a[220:223],v[36:39],v[80:83]
t a[210:211],v209,10240
t a[212:213],v210,8192
t a[214:215],v210,10240
m v[68:71],a[224:227],v[4:7],v[68:71]
t a[216:217],v211,8192
t a[218:219],v211,10240
s_add_u32 s55,s54,0x5000
m v[84:87],a[224:227],v[36:39],v[84:87]
s_add_u32 s59,s55,0x1000
v_mul_f32 v96,v96,v216
v_mul_f32 v97,v97,v216
m v[72:75],a[228:231],v[4:7],v[72:75]
v_mul_f32 v98,v98,v216
v_mul_f32 v99,v99,v216
v_mul_f32 v100,v100,v216
m v[88:91],a[228:231],v[36:39],v[88:91]
v_mul_f32 v101,v101,v216
v_mul_f32 v102,v102,v216
v_mul_f32 v103,v103,v216
m v[76:79],a[232:235],v[4:7],v[76:79]
cv v96,v96,v97
cv v97,v98,v99
cv v98,v100,v101
m v[92:95],a[232:235],v[36:39],v[92:95]
v_cndmask_b32_e32 v230,v235,v234,vcc
s_lshl_b32 s46,%11,7
v_and_b32 v234,15,v214
s_waitcnt lgkmcnt(0)
m v[64:67],a[204:207],v[8:11],v[64:67]
t a[220:221],v208,12288
t a[222:223],v208,14336
t a[224:225],v209,12288
m v[80:83],a[204:207],v[40:43],v[80:83]
t a[226:227],v209,14336
t a[228:229],v210,12288
t a[230:231],v210,14336
m v[68:71],a[208:211],v[8:11],v[68:71]
t a[232:233],v211,12288
t a[234:235],v211,14336
cv v99,v102,v103
m v[84:87],a[208:211],v[40:43],v[84:87]
buffer_store_dwordx4 v[96:99],v215,%5,s55 offen sc1
v_mul_f32 v104,v104,v216
v_mul_f32 v105,v105,v216
m v[72:75],a[212:215],v[8:11],v[72:75]
v_mul_f32 v106,v106,v216
v_mul_f32 v107,v107,v216
v_mul_f32 v108,v108,v216
m v[88:91],a[212:215],v[40:43],v[88:91]
v_mul_f32 v109,v109,v216
v_mul_f32 v110,v110,v216
v_mul_f32 v111,v111,v216
m v[76:79],a[216:219],v[8:11],v[76:79]
cv v104,v104,v105
cv v105,v106,v107
cv v106,v108,v109
m v[92:95],a[216:219],v[40:43],v[92:95]
cv v107,v110,v111
v_lshlrev_b32 v234,3,v234
v_add_u32 v234,4,v234
s_waitcnt lgkmcnt(0)
m v[64:67],a[220:223],v[12:15],v[64:67]
t a[204:205],v208,16384
t a[206:207],v208,18432
t a[208:209],v209,16384
m v[80:83],a[220:223],v[44:47],v[80:83]
t a[210:211],v209,18432
t a[212:213],v210,16384
t a[214:215],v210,18432
m v[68:71],a[224:227],v[12:15],v[68:71]
t a[216:217],v211,16384
t a[218:219],v211,18432
buffer_store_dwordx4 v[104:107],v215,%5,s55 offen offset:1024 sc1
m v[84:87],a[224:227],v[44:47],v[84:87]
v_mul_f32 v112,v112,v217
v_mul_f32 v113,v113,v217
v_mul_f32 v114,v114,v217
m v[72:75],a[228:231],v[12:15],v[72:75]
v_mul_f32 v115,v115,v217
v_mul_f32 v116,v116,v217
v_mul_f32 v117,v117,v217
m v[88:91],a[228:231],v[44:47],v[88:91]
v_mul_f32 v118,v118,v217
v_mul_f32 v119,v119,v217
cv v112,v112,v113
m v[76:79],a[232:235],v[12:15],v[76:79]
cv v113,v114,v115
cv v114,v116,v117
cv v115,v118,v119
m v[92:95],a[232:235],v[44:47],v[92:95]
v_add_u32 v231,s46,v234
s_mul_i32 s46,%11,0x50
v_add_u32 v234,s46,v214
s_waitcnt lgkmcnt(0)
m v[64:67],a[204:207],v[16:19],v[64:67]
t a[220:221],v208,20480
t a[222:223],v208,22528
t a[224:225],v209,20480
m v[80:83],a[204:207],v[48:51],v[80:83]
t a[226:227],v209,22528
t a[228:229],v210,20480
t a[230:231],v210,22528
m v[68:71],a[208:211],v[16:19],v[68:71]
t a[232:233],v211,20480
t a[234:235],v211,22528
buffer_store_dwordx4 v[112:115],v215,%5,s55 offen offset:2048 sc1
m v[84:87],a[208:211],v[48:51],v[84:87]
v_mul_f32 v120,v120,v217
v_mul_f32 v121,v121,v217
v_mul_f32 v122,v122,v217
m v[72:75],a[212:215],v[16:19],v[72:75]
v_mul_f32 v123,v123,v217
v_mul_f32 v124,v124,v217
v_mul_f32 v125,v125,v217
m v[88:91],a[212:215],v[48:51],v[88:91]
v_mul_f32 v126,v126,v217
v_mul_f32 v127,v127,v217
cv v120,v120,v121
m v[76:79],a[216:219],v[16:19],v[76:79]
cv v121,v122,v123
cv v122,v124,v125
cv v123,v126,v127
m v[92:95],a[216:219],v[48:51],v[92:95]
buffer_store_dwordx4 v[120:123],v215,%5,s55 offen offset:3072 sc1
v_lshlrev_b32 v226,4,v234
v_lshrrev_b32 v227,7,v234
s_waitcnt lgkmcnt(0)
m v[64:67],a[220:223],v[20:23],v[64:67]
t a[204:205],v208,24576
t a[206:207],v208,26624
m v[80:83],a[220:223],v[52:55],v[80:83]
t a[208:209],v209,24576
t a[210:211],v209,26624
m v[68:71],a[224:227],v[20:23],v[68:71]
t a[212:213],v210,24576
t a[214:215],v210,26624
m v[84:87],a[224:227],v[52:55],v[84:87]
t a[216:217],v211,24576
t a[218:219],v211,26624
m v[72:75],a[228:231],v[20:23],v[72:75]
v_lshlrev_b32 v227,4,v227
v_and_b32 v235,15,v234
m v[88:91],a[228:231],v[52:55],v[88:91]
v_add_u32 v227,v227,v235
m v[76:79],a[232:235],v[20:23],v[76:79]
m v[92:95],a[232:235],v[52:55],v[92:95]
s_waitcnt lgkmcnt(0)
m v[64:67],a[204:207],v[24:27],v[64:67]
t a[220:221],v208,28672
t a[222:223],v208,30720
m v[80:83],a[204:207],v[56:59],v[80:83]
t a[224:225],v209,28672
t a[226:227],v209,30720
m v[68:71],a[208:211],v[24:27],v[68:71]
t a[228:229],v210,28672
t a[230:231],v210,30720
m v[84:87],a[208:211],v[56:59],v[84:87]
t a[232:233],v211,28672
t a[234:235],v211,30720
m v[72:75],a[212:215],v[24:27],v[72:75]
v_add_u32 v208,s49,v208
v_add_u32 v209,s49,v209
m v[88:91],a[212:215],v[56:59],v[88:91]
v_add_u32 v210,s49,v210
v_add_u32 v211,s49,v211
m v[76:79],a[216:219],v[24:27],v[76:79]
v_mul_u32_u24 v227,0x700,v227
v_bfe_u32 v235,v234,6,1
m v[92:95],a[216:219],v[56:59],v[92:95]
s_waitcnt lgkmcnt(0)
s_waitcnt vmcnt(56)
s_waitcnt vmcnt(20)
s_barrier
m v[64:67],a[220:223],v[28:31],v[64:67]
t a[204:205],v208,0
t a[206:207],v208,2048
m v[80:83],a[220:223],v[60:63],v[80:83]
t a[208:209],v209,0
t a[210:211],v209,2048
m v[68:71],a[224:227],v[28:31],v[68:71]
t a[212:213],v210,0
t a[214:215],v210,2048
m v[84:87],a[224:227],v[60:63],v[84:87]
t a[216:217],v211,0
t a[218:219],v211,2048
m v[72:75],a[228:231],v[28:31],v[72:75]
v_lshl_add_u32 v227,v235,6,v227
v_bfe_u32 v235,v234,4,2
m v[88:91],a[228:231],v[60:63],v[88:91]
v_lshl_add_u32 v227,v235,3,v227
m v[76:79],a[232:235],v[28:31],v[76:79]
m v[92:95],a[232:235],v[60:63],v[92:95]
s_waitcnt lgkmcnt(0)
m v[96:99],a[204:207],v[0:3],0
t a[220:221],v208,4096
t a[222:223],v208,6144
m v[112:115],a[204:207],v[32:35],0
t a[224:225],v209,4096
t a[226:227],v209,6144
m v[100:103],a[208:211],v[0:3],0
t a[228:229],v210,4096
t a[230:231],v210,6144
m v[116:119],a[208:211],v[32:35],0
t a[232:233],v211,4096
t a[234:235],v211,6144
m v[104:107],a[212:215],v[0:3],0
m v[120:123],a[212:215],v[32:35],0
m v[108:111],a[216:219],v[0:3],0
m v[124:127],a[216:219],v[32:35],0
s_waitcnt lgkmcnt(0)
m v[96:99],a[220:223],v[4:7],v[96:99]
t a[204:205],v208,8192
t a[206:207],v208,10240
m v[112:115],a[220:223],v[36:39],v[112:115]
t a[208:209],v209,8192
t a[210:211],v209,10240
t a[212:213],v210,8192
m v[100:103],a[224:227],v[4:7],v[100:103]
t a[214:215],v210,10240
t a[216:217],v211,8192
m v[116:119],a[224:227],v[36:39],v[116:119]
t a[218:219],v211,10240
s_add_u32 s55,s54,0xa000
s_add_u32 s59,s55,0x1000
m v[104:107],a[228:231],v[4:7],v[104:107]
v_mul_f32 v64,v64,v216
v_mul_f32 v65,v65,v216
v_mul_f32 v66,v66,v216
m v[120:123],a[228:231],v[36:39],v[120:123]
v_mul_f32 v67,v67,v216
v_mul_f32 v68,v68,v216
m v[108:111],a[232:235],v[4:7],v[108:111]
v_mul_f32 v69,v69,v216
v_mul_f32 v70,v70,v216
v_mul_f32 v71,v71,v216
m v[124:127],a[232:235],v[36:39],v[124:127]
cv v64,v64,v65
cv v65,v66,v67
cv v66,v68,v69
s_waitcnt lgkmcnt(0)
m v[96:99],a[204:207],v[8:11],v[96:99]
t a[220:221],v208,12288
t a[222:223],v208,14336
m v[112:115],a[204:207],v[40:43],v[112:115]
t a[224:225],v209,12288
t a[226:227],v209,14336
t a[228:229],v210,12288
m v[100:103],a[208:211],v[8:11],v[100:103]
t a[230:231],v210,14336
t a[232:233],v211,12288
t a[234:235],v211,14336
m v[116:119],a[208:211],v[40:43],v[116:119]
cv v67,v70,v71
buffer_store_dwordx4 v[64:67],v215,%5,s55 offen sc1
v_mul_f32 v72,v72,v216
m v[104:107],a[212:215],v[8:11],v[104:107]
v_mul_f32 v73,v73,v216
v_mul_f32 v74,v74,v216
m v[120:123],a[212:215],v[40:43],v[120:123]
v_mul_f32 v75,v75,v216
v_mul_f32 v76,v76,v216
v_mul_f32 v77,v77,v216
m v[108:111],a[216:219],v[8:11],v[108:111]
v_mul_f32 v78,v78,v216
v_mul_f32 v79,v79,v216
cv v72,v72,v73
m v[124:127],a[216:219],v[40:43],v[124:127]
cv v73,v74,v75
cv v74,v76,v77
cv v75,v78,v79
s_waitcnt lgkmcnt(0)
m v[96:99],a[220:223],v[12:15],v[96:99]
t a[204:205],v208,16384
t a[206:207],v208,18432
m v[112:115],a[220:223],v[44:47],v[112:115]
t a[208:209],v209,16384
t a[210:211],v209,18432
t a[212:213],v210,16384
m v[100:103],a[224:227],v[12:15],v[100:103]
t a[214:215],v210,18432
t a[216:217],v211,16384
m v[116:119],a[224:227],v[44:47],v[116:119]
t a[218:219],v211,18432
buffer_store_dwordx4 v[72:75],v215,%5,s55 offen offset:1024 sc1
v_mul_f32 v80,v80,v217
m v[104:107],a[228:231],v[12:15],v[104:107]
v_mul_f32 v81,v81,v217
v_mul_f32 v82,v82,v217
v_mul_f32 v83,v83,v217
m v[120:123],a[228:231],v[44:47],v[120:123]
v_mul_f32 v84,v84,v217
v_mul_f32 v85,v85,v217
m v[108:111],a[232:235],v[12:15],v[108:111]
v_mul_f32 v86,v86,v217
v_mul_f32 v87,v87,v217
cv v80,v80,v81
m v[124:127],a[232:235],v[44:47],v[124:127]
cv v81,v82,v83
cv v82,v84,v85
cv v83,v86,v87
s_waitcnt lgkmcnt(0)
m v[96:99],a[204:207],v[16:19],v[96:99]
t a[220:221],v208,20480
t a[222:223],v208,22528
m v[112:115],a[204:207],v[48:51],v[112:115]
t a[224:225],v209,20480
t a[226:227],v209,22528
t a[228:229],v210,20480
m v[100:103],a[208:211],v[16:19],v[100:103]
t a[230:231],v210,22528
t a[232:233],v211,20480
t a[234:235],v211,22528
m v[116:119],a[208:211],v[48:51],v[116:119]
buffer_store_dwordx4 v[80:83],v215,%5,s55 offen offset:2048 sc1
v_mul_f32 v88,v88,v217
v_mul_f32 v89,v89,v217
m v[104:107],a[212:215],v[16:19],v[104:107]
v_mul_f32 v90,v90,v217
v_mul_f32 v91,v91,v217
m v[120:123],a[212:215],v[48:51],v[120:123]
v_mul_f32 v92,v92,v217
v_mul_f32 v93,v93,v217
v_mul_f32 v94,v94,v217
m v[108:111],a[216:219],v[16:19],v[108:111]
v_mul_f32 v95,v95,v217
cv v88,v88,v89
cv v89,v90,v91
m v[124:127],a[216:219],v[48:51],v[124:127]
cv v90,v92,v93
cv v91,v94,v95
buffer_store_dwordx4 v[88:91],v215,%5,s55 offen offset:3072 sc1
s_waitcnt lgkmcnt(0)
m v[96:99],a[220:223],v[20:23],v[96:99]
t a[204:205],v208,24576
t a[206:207],v208,26624
m v[112:115],a[220:223],v[52:55],v[112:115]
t a[208:209],v209,24576
t a[210:211],v209,26624
m v[100:103],a[224:227],v[20:23],v[100:103]
t a[212:213],v210,24576
t a[214:215],v210,26624
m v[116:119],a[224:227],v[52:55],v[116:119]
t a[216:217],v211,24576
t a[218:219],v211,26624
m v[104:107],a[228:231],v[20:23],v[104:107]
m v[120:123],a[228:231],v[52:55],v[120:123]
m v[108:111],a[232:235],v[20:23],v[108:111]
m v[124:127],a[232:235],v[52:55],v[124:127]
s_waitcnt lgkmcnt(0)
m v[96:99],a[204:207],v[24:27],v[96:99]
t a[220:221],v208,28672
t a[222:223],v208,30720
m v[112:115],a[204:207],v[56:59],v[112:115]
t a[224:225],v209,28672
t a[226:227],v209,30720
m v[100:103],a[208:211],v[24:27],v[100:103]
t a[228:229],v210,28672
t a[230:231],v210,30720
m v[116:119],a[208:211],v[56:59],v[116:119]
t a[232:233],v211,28672
t a[234:235],v211,30720
m v[104:107],a[212:215],v[24:27],v[104:107]
v_add_u32 v208,s48,v208
v_add_u32 v209,s48,v209
m v[120:123],a[212:215],v[56:59],v[120:123]
v_add_u32 v210,s48,v210
v_add_u32 v211,s48,v211
m v[108:111],a[216:219],v[24:27],v[108:111]
m v[124:127],a[216:219],v[56:59],v[124:127]
s_waitcnt lgkmcnt(0)
s_waitcnt vmcnt(4)
s_barrier
t a[204:205],v208,0
t a[206:207],v208,2048
t a[208:209],v209,0
t a[210:211],v209,2048
t a[212:213],v210,0
t a[214:215],v210,2048
t a[216:217],v211,0
t a[218:219],v211,2048
buffer_load_dword v233,v231,%5,%12 offen sc1
m v[96:99],a[220:223],v[28:31],v[96:99]
m v[112:115],a[220:223],v[60:63],v[112:115]
m v[100:103],a[224:227],v[28:31],v[100:103]
m v[116:119],a[224:227],v[60:63],v[116:119]
m v[104:107],a[228:231],v[28:31],v[104:107]
m v[120:123],a[228:231],v[60:63],v[120:123]
m v[108:111],a[232:235],v[28:31],v[108:111]
m v[124:127],a[232:235],v[60:63],v[124:127]
s_waitcnt lgkmcnt(0)
m v[64:67],a[204:207],v[0:3],0
t a[220:221],v208,4096
)ASM"
R"ASM(t a[222:223],v208,6144
m v[80:83],a[204:207],v[32:35],0
t a[224:225],v209,4096
t a[226:227],v209,6144
m v[68:71],a[208:211],v[0:3],0
t a[228:229],v210,4096
t a[230:231],v210,6144
m v[84:87],a[208:211],v[32:35],0
t a[232:233],v211,4096
t a[234:235],v211,6144
m v[72:75],a[212:215],v[0:3],0
m v[88:91],a[212:215],v[32:35],0
m v[76:79],a[216:219],v[0:3],0
m v[92:95],a[216:219],v[32:35],0
s_waitcnt lgkmcnt(0)
m v[64:67],a[220:223],v[4:7],v[64:67]
t a[204:205],v208,8192
t a[206:207],v208,10240
m v[80:83],a[220:223],v[36:39],v[80:83]
t a[208:209],v209,8192
t a[210:211],v209,10240
t a[212:213],v210,8192
m v[68:71],a[224:227],v[4:7],v[68:71]
t a[214:215],v210,10240
t a[216:217],v211,8192
m v[84:87],a[224:227],v[36:39],v[84:87]
t a[218:219],v211,10240
s_add_u32 s55,s54,0xf000
s_add_u32 s59,s55,0x1000
m v[72:75],a[228:231],v[4:7],v[72:75]
v_mul_f32 v96,v96,v216
v_mul_f32 v97,v97,v216
v_mul_f32 v98,v98,v216
m v[88:91],a[228:231],v[36:39],v[88:91]
v_mul_f32 v99,v99,v216
v_mul_f32 v100,v100,v216
m v[76:79],a[232:235],v[4:7],v[76:79]
v_mul_f32 v101,v101,v216
v_mul_f32 v102,v102,v216
v_mul_f32 v103,v103,v216
m v[92:95],a[232:235],v[36:39],v[92:95]
cv v96,v96,v97
cv v97,v98,v99
cv v98,v100,v101
s_waitcnt lgkmcnt(0)
m v[64:67],a[204:207],v[8:11],v[64:67]
t a[220:221],v208,12288
t a[222:223],v208,14336
m v[80:83],a[204:207],v[40:43],v[80:83]
t a[224:225],v209,12288
t a[226:227],v209,14336
t a[228:229],v210,12288
m v[68:71],a[208:211],v[8:11],v[68:71]
t a[230:231],v210,14336
t a[232:233],v211,12288
t a[234:235],v211,14336
m v[84:87],a[208:211],v[40:43],v[84:87]
cv v99,v102,v103
buffer_store_dwordx4 v[96:99],v215,%5,s55 offen sc1
v_mul_f32 v104,v104,v216
m v[72:75],a[212:215],v[8:11],v[72:75]
v_mul_f32 v105,v105,v216
v_mul_f32 v106,v106,v216
m v[88:91],a[212:215],v[40:43],v[88:91]
v_mul_f32 v107,v107,v216
v_mul_f32 v108,v108,v216
v_mul_f32 v109,v109,v216
m v[76:79],a[216:219],v[8:11],v[76:79]
v_mul_f32 v110,v110,v216
v_mul_f32 v111,v111,v216
cv v104,v104,v105
m v[92:95],a[216:219],v[40:43],v[92:95]
cv v105,v106,v107
cv v106,v108,v109
cv v107,v110,v111
s_waitcnt lgkmcnt(0)
m v[64:67],a[220:223],v[12:15],v[64:67]
t a[204:205],v208,16384
t a[206:207],v208,18432
m v[80:83],a[220:223],v[44:47],v[80:83]
t a[208:209],v209,16384
t a[210:211],v209,18432
t a[212:213],v210,16384
m v[68:71],a[224:227],v[12:15],v[68:71]
t a[214:215],v210,18432
t a[216:217],v211,16384
m v[84:87],a[224:227],v[44:47],v[84:87]
t a[218:219],v211,18432
buffer_store_dwordx4 v[104:107],v215,%5,s55 offen offset:1024 sc1
v_mul_f32 v112,v112,v217
m v[72:75],a[228:231],v[12:15],v[72:75]
v_mul_f32 v113,v113,v217
v_mul_f32 v114,v114,v217
v_mul_f32 v115,v115,v217
m v[88:91],a[228:231],v[44:47],v[88:91]
v_mul_f32 v116,v116,v217
v_mul_f32 v117,v117,v217
m v[76:79],a[232:235],v[12:15],v[76:79]
v_mul_f32 v118,v118,v217
v_mul_f32 v119,v119,v217
cv v112,v112,v113
m v[92:95],a[232:235],v[44:47],v[92:95]
cv v113,v114,v115
cv v114,v116,v117
cv v115,v118,v119
s_waitcnt lgkmcnt(0)
m v[64:67],a[204:207],v[16:19],v[64:67]
t a[220:221],v208,20480
t a[222:223],v208,22528
m v[80:83],a[204:207],v[48:51],v[80:83]
t a[224:225],v209,20480
t a[226:227],v209,22528
t a[228:229],v210,20480
m v[68:71],a[208:211],v[16:19],v[68:71]
t a[230:231],v210,22528
t a[232:233],v211,20480
t a[234:235],v211,22528
m v[84:87],a[208:211],v[48:51],v[84:87]
buffer_store_dwordx4 v[112:115],v215,%5,s55 offen offset:2048 sc1
v_mul_f32 v120,v120,v217
v_mul_f32 v121,v121,v217
m v[72:75],a[212:215],v[16:19],v[72:75]
v_mul_f32 v122,v122,v217
v_mul_f32 v123,v123,v217
m v[88:91],a[212:215],v[48:51],v[88:91]
v_mul_f32 v124,v124,v217
v_mul_f32 v125,v125,v217
v_mul_f32 v126,v126,v217
m v[76:79],a[216:219],v[16:19],v[76:79]
v_mul_f32 v127,v127,v217
cv v120,v120,v121
cv v121,v122,v123
m v[92:95],a[216:219],v[48:51],v[92:95]
cv v122,v124,v125
cv v123,v126,v127
buffer_store_dwordx4 v[120:123],v215,%5,s55 offen offset:3072 sc1
s_waitcnt lgkmcnt(0)
m v[64:67],a[220:223],v[20:23],v[64:67]
t a[204:205],v208,24576
t a[206:207],v208,26624
m v[80:83],a[220:223],v[52:55],v[80:83]
t a[208:209],v209,24576
t a[210:211],v209,26624
m v[68:71],a[224:227],v[20:23],v[68:71]
t a[212:213],v210,24576
t a[214:215],v210,26624
m v[84:87],a[224:227],v[52:55],v[84:87]
t a[216:217],v211,24576
t a[218:219],v211,26624
m v[72:75],a[228:231],v[20:23],v[72:75]
m v[88:91],a[228:231],v[52:55],v[88:91]
m v[76:79],a[232:235],v[20:23],v[76:79]
m v[92:95],a[232:235],v[52:55],v[92:95]
s_waitcnt lgkmcnt(0)
m v[64:67],a[204:207],v[24:27],v[64:67]
t a[220:221],v208,28672
t a[222:223],v208,30720
m v[80:83],a[204:207],v[56:59],v[80:83]
t a[224:225],v209,28672
t a[226:227],v209,30720
m v[68:71],a[208:211],v[24:27],v[68:71]
t a[228:229],v210,28672
t a[230:231],v210,30720
m v[84:87],a[208:211],v[56:59],v[84:87]
t a[232:233],v211,28672
t a[234:235],v211,30720
m v[72:75],a[212:215],v[24:27],v[72:75]
v_add_u32 v208,s48,v208
v_add_u32 v209,s48,v209
m v[88:91],a[212:215],v[56:59],v[88:91]
v_add_u32 v210,s48,v210
v_add_u32 v211,s48,v211
m v[76:79],a[216:219],v[24:27],v[76:79]
m v[92:95],a[216:219],v[56:59],v[92:95]
s_waitcnt lgkmcnt(0)
s_waitcnt vmcnt(5)
s_barrier
s_waitcnt vmcnt(4)
v_sub_u32 v234,v233,v219
v_add_u32 v234,-1,v234
v_cmp_lt_u32_e32 vcc,6,v234
s_nop 0
s_cbranch_vccnz LB_ps9%=
LB_pr9%=:
t a[204:205],v208,0
t a[206:207],v208,2048
t a[208:209],v209,0
t a[210:211],v209,2048
t a[212:213],v210,0
t a[214:215],v210,2048
t a[216:217],v211,0
t a[218:219],v211,2048
s_mov_b32 s45,0
buffer_load_dwordx4 a[0:3],v226,%5,s45 offen sc1
s_mov_b32 s45,0x118000
buffer_load_dwordx4 a[4:7],v226,%5,s45 offen sc1
s_mov_b32 s45,0x230000
buffer_load_dwordx4 a[8:11],v226,%5,s45 offen sc1
s_mov_b32 s45,0x348000
buffer_load_dwordx4 a[12:15],v226,%5,s45 offen sc1
s_mov_b32 s45,0x460000
buffer_load_dwordx4 a[16:19],v226,%5,s45 offen sc1
s_mov_b32 s45,0x578000
buffer_load_dwordx4 a[20:23],v226,%5,s45 offen sc1
s_mov_b32 s45,0x690000
buffer_load_dwordx4 a[24:27],v226,%5,s45 offen sc1
s_mov_b32 s45,0x7a8000
buffer_load_dwordx4 a[28:31],v226,%5,s45 offen sc1
s_mov_b32 s45,0x8c0000
buffer_load_dwordx4 a[32:35],v226,%5,s45 offen sc1
s_mov_b32 s45,0x9d8000
buffer_load_dwordx4 a[36:39],v226,%5,s45 offen sc1
s_mov_b32 s45,0xaf0000
buffer_load_dwordx4 a[40:43],v226,%5,s45 offen sc1
s_mov_b32 s45,0xc08000
buffer_load_dwordx4 a[44:47],v226,%5,s45 offen sc1
s_mov_b32 s45,0xd20000
buffer_load_dwordx4 a[48:51],v226,%5,s45 offen sc1
s_mov_b32 s45,0xe38000
buffer_load_dwordx4 a[52:55],v226,%5,s45 offen sc1
s_mov_b32 s45,0xf50000
buffer_load_dwordx4 a[56:59],v226,%5,s45 offen sc1
s_mov_b32 s45,0x1068000
buffer_load_dwordx4 a[60:63],v226,%5,s45 offen sc1
m v[64:67],a[220:223],v[28:31],v[64:67]
m v[80:83],a[220:223],v[60:63],v[80:83]
m v[68:71],a[224:227],v[28:31],v[68:71]
m v[84:87],a[224:227],v[60:63],v[84:87]
m v[72:75],a[228:231],v[28:31],v[72:75]
m v[88:91],a[228:231],v[60:63],v[88:91]
m v[76:79],a[232:235],v[28:31],v[76:79]
m v[92:95],a[232:235],v[60:63],v[92:95]
s_waitcnt lgkmcnt(0)
m v[96:99],a[204:207],v[0:3],0
t a[220:221],v208,4096
t a[222:223],v208,6144
m v[112:115],a[204:207],v[32:35],0
t a[224:225],v209,4096
t a[226:227],v209,6144
m v[100:103],a[208:211],v[0:3],0
t a[228:229],v210,4096
t a[230:231],v210,6144
m v[116:119],a[208:211],v[32:35],0
t a[232:233],v211,4096
t a[234:235],v211,6144
m v[104:107],a[212:215],v[0:3],0
m v[120:123],a[212:215],v[32:35],0
m v[108:111],a[216:219],v[0:3],0
m v[124:127],a[216:219],v[32:35],0
s_waitcnt lgkmcnt(0)
m v[96:99],a[220:223],v[4:7],v[96:99]
t a[204:205],v208,8192
t a[206:207],v208,10240
m v[112:115],a[220:223],v[36:39],v[112:115]
t a[208:209],v209,8192
t a[210:211],v209,10240
t a[212:213],v210,8192
m v[100:103],a[224:227],v[4:7],v[100:103]
t a[214:215],v210,10240
t a[216:217],v211,8192
m v[116:119],a[224:227],v[36:39],v[116:119]
t a[218:219],v211,10240
s_add_u32 s55,s54,0x14000
s_add_u32 s59,s55,0x1000
m v[104:107],a[228:231],v[4:7],v[104:107]
v_mul_f32 v64,v64,v216
v_mul_f32 v65,v65,v216
v_mul_f32 v66,v66,v216
m v[120:123],a[228:231],v[36:39],v[120:123]
v_mul_f32 v67,v67,v216
v_mul_f32 v68,v68,v216
m v[108:111],a[232:235],v[4:7],v[108:111]
v_mul_f32 v69,v69,v216
v_mul_f32 v70,v70,v216
v_mul_f32 v71,v71,v216
m v[124:127],a[232:235],v[36:39],v[124:127]
cv v64,v64,v65
cv v65,v66,v67
cv v66,v68,v69
s_waitcnt lgkmcnt(0)
m v[96:99],a[204:207],v[8:11],v[96:99]
t a[220:221],v208,12288
t a[222:223],v208,14336
m v[112:115],a[204:207],v[40:43],v[112:115]
t a[224:225],v209,12288
t a[226:227],v209,14336
t a[228:229],v210,12288
m v[100:103],a[208:211],v[8:11],v[100:103]
t a[230:231],v210,14336
t a[232:233],v211,12288
t a[234:235],v211,14336
m v[116:119],a[208:211],v[40:43],v[116:119]
cv v67,v70,v71
buffer_store_dwordx4 v[64:67],v215,%5,s55 offen sc1
v_mul_f32 v72,v72,v216
m v[104:107],a[212:215],v[8:11],v[104:107]
v_mul_f32 v73,v73,v216
v_mul_f32 v74,v74,v216
m v[120:123],a[212:215],v[40:43],v[120:123]
v_mul_f32 v75,v75,v216
v_mul_f32 v76,v76,v216
v_mul_f32 v77,v77,v216
m v[108:111],a[216:219],v[8:11],v[108:111]
v_mul_f32 v78,v78,v216
v_mul_f32 v79,v79,v216
cv v72,v72,v73
m v[124:127],a[216:219],v[40:43],v[124:127]
cv v73,v74,v75
cv v74,v76,v77
cv v75,v78,v79
s_waitcnt lgkmcnt(0)
m v[96:99],a[220:223],v[12:15],v[96:99]
t a[204:205],v208,16384
t a[206:207],v208,18432
m v[112:115],a[220:223],v[44:47],v[112:115]
t a[208:209],v209,16384
t a[210:211],v209,18432
t a[212:213],v210,16384
m v[100:103],a[224:227],v[12:15],v[100:103]
t a[214:215],v210,18432
t a[216:217],v211,16384
m v[116:119],a[224:227],v[44:47],v[116:119]
t a[218:219],v211,18432
buffer_store_dwordx4 v[72:75],v215,%5,s55 offen offset:1024 sc1
v_mul_f32 v80,v80,v217
m v[104:107],a[228:231],v[12:15],v[104:107]
v_mul_f32 v81,v81,v217
v_mul_f32 v82,v82,v217
v_mul_f32 v83,v83,v217
m v[120:123],a[228:231],v[44:47],v[120:123]
v_mul_f32 v84,v84,v217
v_mul_f32 v85,v85,v217
m v[108:111],a[232:235],v[12:15],v[108:111]
v_mul_f32 v86,v86,v217
v_mul_f32 v87,v87,v217
cv v80,v80,v81
m v[124:127],a[232:235],v[44:47],v[124:127]
cv v81,v82,v83
cv v82,v84,v85
cv v83,v86,v87
s_waitcnt lgkmcnt(0)
m v[96:99],a[204:207],v[16:19],v[96:99]
t a[220:221],v208,20480
t a[222:223],v208,22528
m v[112:115],a[204:207],v[48:51],v[112:115]
t a[224:225],v209,20480
t a[226:227],v209,22528
t a[228:229],v210,20480
m v[100:103],a[208:211],v[16:19],v[100:103]
t a[230:231],v210,22528
t a[232:233],v211,20480
t a[234:235],v211,22528
m v[116:119],a[208:211],v[48:51],v[116:119]
buffer_store_dwordx4 v[80:83],v215,%5,s55 offen offset:2048 sc1
v_mul_f32 v88,v88,v217
v_mul_f32 v89,v89,v217
m v[104:107],a[212:215],v[16:19],v[104:107]
v_mul_f32 v90,v90,v217
v_mul_f32 v91,v91,v217
m v[120:123],a[212:215],v[48:51],v[120:123]
v_mul_f32 v92,v92,v217
v_mul_f32 v93,v93,v217
v_mul_f32 v94,v94,v217
m v[108:111],a[216:219],v[16:19],v[108:111]
v_mul_f32 v95,v95,v217
cv v88,v88,v89
cv v89,v90,v91
m v[124:127],a[216:219],v[48:51],v[124:127]
cv v90,v92,v93
cv v91,v94,v95
buffer_store_dwordx4 v[88:91],v215,%5,s55 offen offset:3072 sc1
s_waitcnt lgkmcnt(0)
m v[96:99],a[220:223],v[20:23],v[96:99]
t a[204:205],v208,24576
t a[206:207],v208,26624
m v[112:115],a[220:223],v[52:55],v[112:115]
t a[208:209],v209,24576
t a[210:211],v209,26624
m v[100:103],a[224:227],v[20:23],v[100:103]
t a[212:213],v210,24576
t a[214:215],v210,26624
m v[116:119],a[224:227],v[52:55],v[116:119]
t a[216:217],v211,24576
t a[218:219],v211,26624
m v[104:107],a[228:231],v[20:23],v[104:107]
m v[120:123],a[228:231],v[52:55],v[120:123]
m v[108:111],a[232:235],v[20:23],v[108:111]
m v[124:127],a[232:235],v[52:55],v[124:127]
s_waitcnt lgkmcnt(0)
m v[96:99],a[204:207],v[24:27],v[96:99]
t a[220:221],v208,28672
t a[222:223],v208,30720
m v[112:115],a[204:207],v[56:59],v[112:115]
t a[224:225],v209,28672
t a[226:227],v209,30720
m v[100:103],a[208:211],v[24:27],v[100:103]
t a[228:229],v210,28672
t a[230:231],v210,30720
m v[116:119],a[208:211],v[56:59],v[116:119]
t a[232:233],v211,28672
t a[234:235],v211,30720
m v[104:107],a[212:215],v[24:27],v[104:107]
v_add_u32 v208,s48,v208
v_add_u32 v209,s48,v209
m v[120:123],a[212:215],v[56:59],v[120:123]
v_add_u32 v210,s48,v210
v_add_u32 v211,s48,v211
m v[108:111],a[216:219],v[24:27],v[108:111]
m v[124:127],a[216:219],v[56:59],v[124:127]
s_waitcnt lgkmcnt(0)
s_waitcnt vmcnt(20)
s_barrier
t a[204:205],v208,0
t a[206:207],v208,2048
t a[208:209],v209,0
t a[210:211],v209,2048
t a[212:213],v210,0
t a[214:215],v210,2048
t a[216:217],v211,0
t a[218:219],v211,2048
buffer_load_dword v233,v231,%5,%12 offen sc1
m v[96:99],a[220:223],v[28:31],v[96:99]
m v[112:115],a[220:223],v[60:63],v[112:115]
m v[100:103],a[224:227],v[28:31],v[100:103]
m v[116:119],a[224:227],v[60:63],v[116:119]
m v[104:107],a[228:231],v[28:31],v[104:107]
m v[120:123],a[228:231],v[60:63],v[120:123]
m v[108:111],a[232:235],v[28:31],v[108:111]
m v[124:127],a[232:235],v[60:63],v[124:127]
s_waitcnt lgkmcnt(0)
m v[64:67],a[204:207],v[0:3],0
t a[220:221],v208,4096
t a[222:223],v208,6144
m v[80:83],a[204:207],v[32:35],0
t a[224:225],v209,4096
t a[226:227],v209,6144
m v[68:71],a[208:211],v[0:3],0
t a[228:229],v210,4096
t a[230:231],v210,6144
m v[84:87],a[208:211],v[32:35],0
t a[232:233],v211,4096
t a[234:235],v211,6144
m v[72:75],a[212:215],v[0:3],0
m v[88:91],a[212:215],v[32:35],0
m v[76:79],a[216:219],v[0:3],0
m v[92:95],a[216:219],v[32:35],0
s_waitcnt lgkmcnt(0)
m v[64:67],a[220:223],v[4:7],v[64:67]
t a[204:205],v208,8192
t a[206:207],v208,10240
m v[80:83],a[220:223],v[36:39],v[80:83]
t a[208:209],v209,8192
t a[210:211],v209,10240
t a[212:213],v210,8192
m v[68:71],a[224:227],v[4:7],v[68:71]
t a[214:215],v210,10240
t a[216:217],v211,8192
m v[84:87],a[224:227],v[36:39],v[84:87]
t a[218:219],v211,10240
s_add_u32 s55,s54,0x19000
s_add_u32 s59,s55,0x1000
m v[72:75],a[228:231],v[4:7],v[72:75]
v_mul_f32 v96,v96,v216
v_mul_f32 v97,v97,v216
v_mul_f32 v98,v98,v216
m v[88:91],a[228:231],v[36:39],v[88:91]
v_mul_f32 v99,v99,v216
v_mul_f32 v100,v100,v216
m v[76:79],a[232:235],v[4:7],v[76:79]
v_mul_f32 v101,v101,v216
v_mul_f32 v102,v102,v216
v_mul_f32 v103,v103,v216
m v[92:95],a[232:235],v[36:39],v[92:95]
cv v96,v96,v97
cv v97,v98,v99
cv v98,v100,v101
s_waitcnt lgkmcnt(0)
m v[64:67],a[204:207],v[8:11],v[64:67]
t a[220:221],v208,12288
t a[222:223],v208,14336
m v[80:83],a[204:207],v[40:43],v[80:83]
t a[224:225],v209,12288
t a[226:227],v209,14336
t a[228:229],v210,12288
m v[68:71],a[208:211],v[8:11],v[68:71]
t a[230:231],v210,14336
t a[232:233],v211,12288
t a[234:235],v211,14336
m v[84:87],a[208:211],v[40:43],v[84:87]
cv v99,v102,v103
buffer_store_dwordx4 v[96:99],v215,%5,s55 offen sc1
v_mul_f32 v104,v104,v216
m v[72:75],a[212:215],v[8:11],v[72:75]
v_mul_f32 v105,v105,v216
v_mul_f32 v106,v106,v216
m v[88:91],a[212:215],v[40:43],v[88:91]
v_mul_f32 v107,v107,v216
v_mul_f32 v108,v108,v216
v_mul_f32 v109,v109,v216
m v[76:79],a[216:219],v[8:11],v[76:79]
v_mul_f32 v110,v110,v216
v_mul_f32 v111,v111,v216
cv v104,v104,v105
m v[92:95],a[216:219],v[40:43],v[92:95]
cv v105,v106,v107
cv v106,v108,v109
cv v107,v110,v111
s_waitcnt lgkmcnt(0)
m v[64:67],a[220:223],v[12:15],v[64:67]
t a[204:205],v208,16384
t a[206:207],v208,18432
m v[80:83],a[220:223],v[44:47],v[80:83]
t a[208:209],v209,16384
t a[210:211],v209,18432
t a[212:213],v210,16384
m v[68:71],a[224:227],v[12:15],v[68:71]
t a[214:215],v210,18432
t a[216:217],v211,16384
m v[84:87],a[224:227],v[44:47],v[84:87]
t a[218:219],v211,18432
buffer_store_dwordx4 v[104:107],v215,%5,s55 offen offset:1024 sc1
v_mul_f32 v112,v112,v217
m v[72:75],a[228:231],v[12:15],v[72:75]
v_mul_f32 v113,v113,v217
v_mul_f32 v114,v114,v217
v_mul_f32 v115,v115,v217
m v[88:91],a[228:231],v[44:47],v[88:91]
v_mul_f32 v116,v116,v217
v_mul_f32 v117,v117,v217
m v[76:79],a[232:235],v[12:15],v[76:79]
v_mul_f32 v118,v118,v217
v_mul_f32 v119,v119,v217
cv v112,v112,v113
m v[92:95],a[232:235],v[44:47],v[92:95]
cv v113,v114,v115
cv v114,v116,v117
cv v115,v118,v119
s_waitcnt lgkmcnt(0)
m v[64:67],a[204:207],v[16:19],v[64:67]
t a[220:221],v208,20480
t a[222:223],v208,22528
m v[80:83],a[204:207],v[48:51],v[80:83]
t a[224:225],v209,20480
t a[226:227],v209,22528
t a[228:229],v210,20480
m v[68:71],a[208:211],v[16:19],v[68:71]
t a[230:231],v210,22528
t a[232:233],v211,20480
t a[234:235],v211,22528
m v[84:87],a[208:211],v[48:51],v[84:87]
buffer_store_dwordx4 v[112:115],v215,%5,s55 offen offset:2048 sc1
v_mul_f32 v120,v120,v217
v_mul_f32 v121,v121,v217
m v[72:75],a[212:215],v[16:19],v[72:75]
v_mul_f32 v122,v122,v217
v_mul_f32 v123,v123,v217
m v[88:91],a[212:215],v[48:51],v[88:91]
v_mul_f32 v124,v124,v217
v_mul_f32 v125,v125,v217
v_mul_f32 v126,v126,v217
m v[76:79],a[216:219],v[16:19],v[76:79]
v_mul_f32 v127,v127,v217
cv v120,v120,v121
cv v121,v122,v123
m v[92:95],a[216:219],v[48:51],v[92:95]
cv v122,v124,v125
cv v123,v126,v127
buffer_store_dwordx4 v[120:123],v215,%5,s55 offen offset:3072 sc1
s_waitcnt lgkmcnt(0)
m v[64:67],a[220:223],v[20:23],v[64:67]
t a[204:205],v208,24576
t a[206:207],v208,26624
m v[80:83],a[220:223],v[52:55],v[80:83]
t a[208:209],v209,24576
t a[210:211],v209,26624
m v[68:71],a[224:227],v[20:23],v[68:71]
t a[212:213],v210,24576
t a[214:215],v210,26624
m v[84:87],a[224:227],v[52:55],v[84:87]
t a[216:217],v211,24576
t a[218:219],v211,26624
m v[72:75],a[228:231],v[20:23],v[72:75]
m v[88:91],a[228:231],v[52:55],v[88:91]
m v[76:79],a[232:235],v[20:23],v[76:79]
m v[92:95],a[232:235],v[52:55],v[92:95]
s_waitcnt lgkmcnt(0)
m v[64:67],a[204:207],v[24:27],v[64:67]
t a[220:221],v208,28672
t a[222:223],v208,30720
m v[80:83],a[204:207],v[56:59],v[80:83]
t a[224:225],v209,28672
t a[226:227],v209,30720
m v[68:71],a[208:211],v[24:27],v[68:71]
t a[228:229],v210,28672
t a[230:231],v210,30720
m v[84:87],a[208:211],v[56:59],v[84:87]
t a[232:233],v211,28672
t a[234:235],v211,30720
m v[72:75],a[212:215],v[24:27],v[72:75]
m v[88:91],a[212:215],v[56:59],v[88:91]
m v[76:79],a[216:219],v[24:27],v[76:79]
m v[92:95],a[216:219],v[56:59],v[92:95]
s_waitcnt lgkmcnt(0)
m v[64:67],a[220:223],v[28:31],v[64:67]
m v[80:83],a[220:223],v[60:63],v[80:83]
m v[68:71],a[224:227],v[28:31],v[68:71]
m v[84:87],a[224:227],v[60:63],v[84:87]
m v[72:75],a[228:231],v[28:31],v[72:75]
m v[88:91],a[228:231],v[60:63],v[88:91]
m v[76:79],a[232:235],v[28:31],v[76:79]
m v[92:95],a[232:235],v[60:63],v[92:95]
s_add_u32 s55,s54,0x1e000
s_add_u32 s59,s55,0x1000
v_mul_f32 v64,v64,v216
v_mul_f32 v65,v65,v216
v_mul_f32 v66,v66,v216
v_mul_f32 v67,v67,v216
v_mul_f32 v68,v68,v216
v_mul_f32 v69,v69,v216
v_mul_f32 v70,v70,v216
v_mul_f32 v71,v71,v216
cv v64,v64,v65
cv v65,v66,v67
cv v66,v68,v69
cv v67,v70,v71
buffer_store_dwordx4 v[64:67],v215,%5,s55 offen sc1
v_mul_f32 v72,v72,v216
v_mul_f32 v73,v73,v216
v_mul_f32 v74,v74,v216
v_mul_f32 v75,v75,v216
v_mul_f32 v76,v76,v216
v_mul_f32 v77,v77,v216
v_mul_f32 v78,v78,v216
v_mul_f32 v79,v79,v216
cv v72,v72,v73
cv v73,v74,v75
cv v74,v76,v77
cv v75,v78,v79
buffer_store_dwordx4 v[72:75],v215,%5,s55 offen offset:1024 sc1
v_mul_f32 v80,v80,v217
v_mul_f32 v81,v81,v217
v_mul_f32 v82,v82,v217
v_mul_f32 v83,v83,v217
v_mul_f32 v84,v84,v217
v_mul_f32 v85,v85,v217
v_mul_f32 v86,v86,v217
v_mul_f32 v87,v87,v217
cv v80,v80,v81
cv v81,v82,v83
cv v82,v84,v85
cv v83,v86,v87
buffer_store_dwordx4 v[80:83],v215,%5,s55 offen offset:2048 sc1
v_mul_f32 v88,v88,v217
v_mul_f32 v89,v89,v217
v_mul_f32 v90,v90,v217
v_mul_f32 v91,v91,v217
v_mul_f32 v92,v92,v217
v_mul_f32 v93,v93,v217
v_mul_f32 v94,v94,v217
v_mul_f32 v95,v95,v217
cv v88,v88,v89
cv v89,v90,v91
cv v90,v92,v93
cv v91,v94,v95
buffer_store_dwordx4 v[88:91],v215,%5,s55 offen offset:3072 sc1
s_waitcnt vmcnt(8)
v_sub_u32 v234,v233,v219
v_add_u32 v234,-3,v234
v_cmp_lt_u32_e32 vcc,4,v234
s_nop 0
s_cbranch_vccnz LB_ps11%=
LB_pr11%=:
s_mov_b32 s45,0xa000
buffer_load_dwordx4 a[64:67],v226,%5,s45 offen sc1
s_mov_b32 s45,0x122000
buffer_load_dwordx4 a[68:71],v226,%5,s45 offen sc1
s_mov_b32 s45,0x23a000
buffer_load_dwordx4 a[72:75],v226,%5,s45 offen sc1
s_mov_b32 s45,0x352000
buffer_load_dwordx4 a[76:79],v226,%5,s45 offen sc1
s_mov_b32 s45,0x46a000
buffer_load_dwordx4 a[80:83],v226,%5,s45 offen sc1
s_mov_b32 s45,0x582000
buffer_load_dwordx4 a[84:87],v226,%5,s45 offen sc1
s_mov_b32 s45,0x69a000
buffer_load_dwordx4 a[88:91],v226,%5,s45 offen sc1
s_mov_b32 s45,0x7b2000
buffer_load_dwordx4 a[92:95],v226,%5,s45 offen sc1
s_mov_b32 s45,0x8ca000
buffer_load_dwordx4 a[96:99],v226,%5,s45 offen sc1
s_mov_b32 s45,0x9e2000
buffer_load_dwordx4 a[100:103],v226,%5,s45 offen sc1
s_mov_b32 s45,0xafa000
buffer_load_dwordx4 a[104:107],v226,%5,s45 offen sc1
s_mov_b32 s45,0xc12000
buffer_load_dwordx4 a[108:111],v226,%5,s45 offen sc1
s_mov_b32 s45,0xd2a000
buffer_load_dwordx4 a[112:115],v226,%5,s45 offen sc1
s_mov_b32 s45,0xe42000
buffer_load_dwordx4 a[116:119],v226,%5,s45 offen sc1
s_mov_b32 s45,0xf5a000
buffer_load_dwordx4 a[120:123],v226,%5,s45 offen sc1
s_mov_b32 s45,0x1072000
buffer_load_dwordx4 a[124:127],v226,%5,s45 offen sc1
s_waitcnt vmcnt(16)
s_barrier
m v[240:243],v[222:225],a[0:3],0
m v[244:247],v[220:223],a[0:3],0
m v[240:243],v[222:225],a[4:7],v[240:243]
m v[244:247],v[220:223],a[4:7],v[244:247]
m v[240:243],v[222:225],a[8:11],v[240:243]
m v[244:247],v[220:223],a[8:11],v[244:247]
m v[240:243],v[222:225],a[12:15],v[240:243]
m v[244:247],v[220:223],a[12:15],v[244:247]
m v[240:243],v[222:225],a[16:19],v[240:243]
m v[244:247],v[220:223],a[16:19],v[244:247]
m v[240:243],v[222:225],a[20:23],v[240:243]
m v[244:247],v[220:223],a[20:23],v[244:247]
m v[240:243],v[222:225],a[24:27],v[240:243]
m v[244:247],v[220:223],a[24:27],v[244:247]
m v[240:243],v[222:225],a[28:31],v[240:243]
m v[244:247],v[220:223],a[28:31],v[244:247]
m v[240:243],v[222:225],a[32:35],v[240:243]
m v[244:247],v[220:223],a[32:35],v[244:247]
m v[240:243],v[222:225],a[36:39],v[240:243]
m v[244:247],v[220:223],a[36:39],v[244:247]
m v[240:243],v[222:225],a[40:43],v[240:243]
m v[244:247],v[220:223],a[40:43],v[244:247]
m v[240:243],v[222:225],a[44:47],v[240:243]
m v[244:247],v[220:223],a[44:47],v[244:247]
m v[240:243],v[222:225],a[48:51],v[240:243]
m v[244:247],v[220:223],a[48:51],v[244:247]
m v[240:243],v[222:225],a[52:55],v[240:243]
m v[244:247],v[220:223],a[52:55],v[244:247]
m v[240:243],v[222:225],a[56:59],v[240:243]
m v[244:247],v[220:223],a[56:59],v[244:247]
m v[240:243],v[222:225],a[60:63],v[240:243]
m v[244:247],v[220:223],a[60:63],v[244:247]
s_nop 6
cv v248,v240,v241
cv v249,v242,v243
cv v250,v244,v245
cv v251,v246,v247
s_mov_b32 s45,0
buffer_store_dwordx2 v[248:249],v227,%10,s45 offen
buffer_store_dwordx2 v[250:251],v227,%10,s45 offen offset:32
s_waitcnt vmcnt(17)
m v[240:243],v[222:225],a[64:67],0
m v[244:247],v[220:223],a[64:67],0
s_waitcnt vmcnt(16)
m v[240:243],v[222:225],a[68:71],v[240:243]
m v[244:247],v[220:223],a[68:71],v[244:247]
s_waitcnt vmcnt(15)
m v[240:243],v[222:225],a[72:75],v[240:243]
m v[244:247],v[220:223],a[72:75],v[244:247]
s_waitcnt vmcnt(14)
m v[240:243],v[222:225],a[76:79],v[240:243]
m v[244:247],v[220:223],a[76:79],v[244:247]
s_waitcnt vmcnt(13)
m v[240:243],v[222:225],a[80:83],v[240:243]
m v[244:247],v[220:223],a[80:83],v[244:247]
s_waitcnt vmcnt(12)
m v[240:243],v[222:225],a[84:87],v[240:243]
m v[244:247],v[220:223],a[84:87],v[244:247]
s_waitcnt vmcnt(11)
m v[240:243],v[222:225],a[88:91],v[240:243]
m v[244:247],v[220:223],a[88:91],v[244:247]
s_waitcnt vmcnt(10)
m v[240:243],v[222:225],a[92:95],v[240:243]
m v[244:247],v[220:223],a[92:95],v[244:247]
s_waitcnt vmcnt(9)
m v[240:243],v[222:225],a[96:99],v[240:243]
m v[244:247],v[220:223],a[96:99],v[244:247]
s_waitcnt vmcnt(8)
m v[240:243],v[222:225],a[100:103],v[240:243]
m v[244:247],v[220:223],a[100:103],v[244:247]
s_waitcnt vmcnt(7)
m v[240:243],v[222:225],a[104:107],v[240:243]
m v[244:247],v[220:223],a[104:107],v[244:247]
s_waitcnt vmcnt(6)
m v[240:243],v[222:225],a[108:111],v[240:243]
m v[244:247],v[220:223],a[108:111],v[244:247]
s_waitcnt vmcnt(5)
m v[240:243],v[222:225],a[112:115],v[240:243]
m v[244:247],v[220:223],a[112:115],v[244:247]
s_waitcnt vmcnt(4)
m v[240:243],v[222:225],a[116:119],v[240:243]
m v[244:247],v[220:223],a[116:119],v[244:247]
s_waitcnt vmcnt(3)
m v[240:243],v[222:225],a[120:123],v[240:243]
m v[244:247],v[220:223],a[120:123],v[244:247]
s_waitcnt vmcnt(2)
m v[240:243],v[222:225],a[124:127],v[240:243]
m v[244:247],v[220:223],a[124:127],v[244:247]
s_nop 6
cv v248,v240,v241
cv v249,v242,v243
cv v250,v244,v245
cv v251,v246,v247
s_mov_b32 s45,0x100
buffer_store_dwordx2 v[248:249],v227,%10,s45 offen
buffer_store_dwordx2 v[250:251],v227,%10,s45 offen offset:32
s_mov_b32 s44,0x186a0
LB_pl13%=:
buffer_load_dword v233,v231,%5,%12 offen sc1
s_waitcnt vmcnt(0)
v_sub_u32 v234,v233,v219
v_add_u32 v234,-7,v234
v_cmp_lt_u32_e32 vcc,0,v234
s_nop 0
s_cbranch_vccz LB_pd13%=
s_sleep 2
s_sub_u32 s44,s44,1
s_cmp_lg_u32 s44,0
s_cbranch_scc1 LB_pl13%=
LB_pd13%=:
s_mov_b32 s45,0x1e000
buffer_load_dwordx4 a[128:131],v226,%5,s45 offen sc1
s_mov_b32 s45,0x136000
buffer_load_dwordx4 a[132:135],v226,%5,s45 offen sc1
s_mov_b32 s45,0x24e000
buffer_load_dwordx4 a[136:139],v226,%5,s45 offen sc1
s_mov_b32 s45,0x366000
buffer_load_dwordx4 a[140:143],v226,%5,s45 offen sc1
s_mov_b32 s45,0x47e000
buffer_load_dwordx4 a[144:147],v226,%5,s45 offen sc1
s_mov_b32 s45,0x596000
buffer_load_dwordx4 a[148:151],v226,%5,s45 offen sc1
s_mov_b32 s45,0x6ae000
buffer_load_dwordx4 a[152:155],v226,%5,s45 offen sc1
s_mov_b32 s45,0x7c6000
buffer_load_dwordx4 a[156:159],v226,%5,s45 offen sc1
s_mov_b32 s45,0x8de000
buffer_load_dwordx4 a[160:163],v226,%5,s45 offen sc1
s_mov_b32 s45,0x9f6000
buffer_load_dwordx4 a[164:167],v226,%5,s45 offen sc1
s_mov_b32 s45,0xb0e000
buffer_load_dwordx4 a[168:171],v226,%5,s45 offen sc1
s_mov_b32 s45,0xc26000
buffer_load_dwordx4 a[172:175],v226,%5,s45 offen sc1
s_mov_b32 s45,0xd3e000
buffer_load_dwordx4 a[176:179],v226,%5,s45 offen sc1
s_mov_b32 s45,0xe56000
buffer_load_dwordx4 a[180:183],v226,%5,s45 offen sc1
s_mov_b32 s45,0xf6e000
buffer_load_dwordx4 a[184:187],v226,%5,s45 offen sc1
s_mov_b32 s45,0x1086000
buffer_load_dwordx4 a[188:191],v226,%5,s45 offen sc1
s_waitcnt vmcnt(15)
m v[240:243],v[222:225],a[128:131],0
m v[244:247],v[220:223],a[128:131],0
s_waitcnt vmcnt(14)
m v[240:243],v[222:225],a[132:135],v[240:243]
m v[244:247],v[220:223],a[132:135],v[244:247]
s_waitcnt vmcnt(13)
m v[240:243],v[222:225],a[136:139],v[240:243]
m v[244:247],v[220:223],a[136:139],v[244:247]
s_waitcnt vmcnt(12)
m v[240:243],v[222:225],a[140:143],v[240:243]
m v[244:247],v[220:223],a[140:143],v[244:247]
s_waitcnt vmcnt(11)
m v[240:243],v[222:225],a[144:147],v[240:243]
m v[244:247],v[220:223],a[144:147],v[244:247]
s_waitcnt vmcnt(10)
m v[240:243],v[222:225],a[148:151],v[240:243]
m v[244:247],v[220:223],a[148:151],v[244:247]
s_waitcnt vmcnt(9)
m v[240:243],v[222:225],a[152:155],v[240:243]
m v[244:247],v[220:223],a[152:155],v[244:247]
s_waitcnt vmcnt(8)
m v[240:243],v[222:225],a[156:159],v[240:243]
m v[244:247],v[220:223],a[156:159],v[244:247]
s_waitcnt vmcnt(7)
m v[240:243],v[222:225],a[160:163],v[240:243]
m v[244:247],v[220:223],a[160:163],v[244:247]
s_waitcnt vmcnt(6)
m v[240:243],v[222:225],a[164:167],v[240:243]
m v[244:247],v[220:223],a[164:167],v[244:247]
s_waitcnt vmcnt(5)
m v[240:243],v[222:225],a[168:171],v[240:243]
m v[244:247],v[220:223],a[168:171],v[244:247]
s_waitcnt vmcnt(4)
m v[240:243],v[222:225],a[172:175],v[240:243]
m v[244:247],v[220:223],a[172:175],v[244:247]
s_waitcnt vmcnt(3)
m v[240:243],v[222:225],a[176:179],v[240:243]
m v[244:247],v[220:223],a[176:179],v[244:247]
s_waitcnt vmcnt(2)
m v[240:243],v[222:225],a[180:183],v[240:243]
m v[244:247],v[220:223],a[180:183],v[244:247]
s_waitcnt vmcnt(1)
m v[240:243],v[222:225],a[184:187],v[240:243]
m v[244:247],v[220:223],a[184:187],v[244:247]
s_waitcnt vmcnt(0)
m v[240:243],v[222:225],a[188:191],v[240:243]
m v[244:247],v[220:223],a[188:191],v[244:247]
s_nop 6
cv v248,v240,v241
cv v249,v242,v243
cv v250,v244,v245
cv v251,v246,v247
s_mov_b32 s45,0x300
buffer_store_dwordx2 v[248:249],v227,%10,s45 offen
buffer_store_dwordx2 v[250:251],v227,%10,s45 offen offset:32
s_waitcnt vmcnt(0)
s_endpgm
LB_stub1%=:
s_call_b64 s[50:51],LB_rare0%=
s_branch LB_ret1%=
LB_stub2%=:
s_call_b64 s[50:51],LB_rare1%=
s_branch LB_ret2%=
LB_stub3%=:
s_call_b64 s[50:51],LB_rare0%=
s_branch LB_ret3%=
LB_stub4%=:
s_call_b64 s[50:51],LB_rare1%=
s_branch LB_ret4%=
LB_stub5%=:
s_call_b64 s[50:51],LB_rare0%=
s_branch LB_ret5%=
LB_stub6%=:
s_call_b64 s[50:51],LB_rare1%=
s_branch LB_ret6%=
LB_stub7%=:
s_call_b64 s[50:51],LB_rare0%=
s_branch LB_ret7%=
LB_stub8%=:
s_call_b64 s[50:51],LB_rare1%=
s_branch LB_ret8%=
LB_ps9%=:
s_mov_b32 s44,0x186a0
LB_pl10%=:
buffer_load_dword v233,v231,%5,%12 offen sc1
s_waitcnt vmcnt(0)
v_sub_u32 v234,v233,v219
v_add_u32 v234,-1,v234
v_cmp_lt_u32_e32 vcc,6,v234
s_nop 0
s_cbranch_vccz LB_pr9%=
s_sleep 2
s_sub_u32 s44,s44,1
s_cmp_lg_u32 s44,0
s_cbranch_scc1 LB_pl10%=
s_branch LB_pr9%=
LB_ps11%=:
s_mov_b32 s44,0x186a0
LB_pl12%=:
buffer_load_dword v233,v231,%5,%12 offen sc1
s_waitcnt vmcnt(0)
v_sub_u32 v234,v233,v219
v_add_u32 v234,-3,v234
v_cmp_lt_u32_e32 vcc,4,v234
s_nop 0
s_cbranch_vccz LB_pr11%=
s_sleep 2
s_sub_u32 s44,s44,1
s_cmp_lg_u32 s44,0
s_cbranch_scc1 LB_pl12%=
s_branch LB_pr11%=
LB_rare0%=:
s_nop 7
s_nop 1
v_max3_f32 v220,v4,v5,v6
v_max3_f32 v220,v220,v7,v12
v_max3_f32 v220,v220,v13,v14
v_max_f32 v220,v220,v15
v_mov_b32 v225,v220
s_nop 1
v_permlane32_swap_b32 v220,v225
v_max_f32 v220,v220,v225
v_mov_b32 v225,v220
s_nop 1
v_permlane16_swap_b32 v220,v225
v_max_f32 v220,v220,v225
v_add_f32 v220,0x40e00000,v220
v_max_f32 v220,0,v220
v_sub_f32 v224,0,v220
v_exp_f32 v224,v224
v_sub_f32 v4,v4,v220
v_sub_f32 v5,v5,v220
v_sub_f32 v6,v6,v220
v_sub_f32 v7,v7,v220
v_sub_f32 v12,v12,v220
v_sub_f32 v13,v13,v220
v_sub_f32 v14,v14,v220
v_sub_f32 v15,v15,v220
v_sub_f32 v36,v36,v220
v_sub_f32 v37,v37,v220
v_sub_f32 v38,v38,v220
v_sub_f32 v39,v39,v220
v_sub_f32 v44,v44,v220
v_sub_f32 v45,v45,v220
v_sub_f32 v46,v46,v220
v_sub_f32 v47,v47,v220
v_sub_f32 v84,v84,v220
v_sub_f32 v85,v85,v220
v_sub_f32 v86,v86,v220
v_sub_f32 v87,v87,v220
ar v230,a0
ar v231,a1
ar v232,a2
ar v233,a3
v_mul_f32 v230,v230,v224
v_mul_f32 v231,v231,v224
v_mul_f32 v232,v232,v224
v_mul_f32 v233,v233,v224
v_accvgpr_write_b32 a0,v230
v_accvgpr_write_b32 a1,v231
v_accvgpr_write_b32 a2,v232
v_accvgpr_write_b32 a3,v233
ar v230,a4
ar v231,a5
ar v232,a6
ar v233,a7
v_mul_f32 v230,v230,v224
v_mul_f32 v231,v231,v224
v_mul_f32 v232,v232,v224
v_mul_f32 v233,v233,v224
v_accvgpr_write_b32 a4,v230
v_accvgpr_write_b32 a5,v231
v_accvgpr_write_b32 a6,v232
v_accvgpr_write_b32 a7,v233
ar v230,a8
ar v231,a9
ar v232,a10
ar v233,a11
v_mul_f32 v230,v230,v224
v_mul_f32 v231,v231,v224
v_mul_f32 v232,v232,v224
v_mul_f32 v233,v233,v224
v_accvgpr_write_b32 a8,v230
v_accvgpr_write_b32 a9,v231
v_accvgpr_write_b32 a10,v232
v_accvgpr_write_b32 a11,v233
ar v230,a12
ar v231,a13
ar v232,a14
ar v233,a15
v_mul_f32 v230,v230,v224
v_mul_f32 v231,v231,v224
v_mul_f32 v232,v232,v224
v_mul_f32 v233,v233,v224
v_accvgpr_write_b32 a12,v230
v_accvgpr_write_b32 a13,v231
v_accvgpr_write_b32 a14,v232
v_accvgpr_write_b32 a15,v233
ar v230,a16
ar v231,a17
ar v232,a18
ar v233,a19
v_mul_f32 v230,v230,v224
v_mul_f32 v231,v231,v224
v_mul_f32 v232,v232,v224
v_mul_f32 v233,v233,v224
v_accvgpr_write_b32 a16,v230
v_accvgpr_write_b32 a17,v231
v_accvgpr_write_b32 a18,v232
v_accvgpr_write_b32 a19,v233
ar v230,a20
ar v231,a21
ar v232,a22
ar v233,a23
v_mul_f32 v230,v230,v224
v_mul_f32 v231,v231,v224
v_mul_f32 v232,v232,v224
v_mul_f32 v233,v233,v224
v_accvgpr_write_b32 a20,v230
v_accvgpr_write_b32 a21,v231
v_accvgpr_write_b32 a22,v232
v_accvgpr_write_b32 a23,v233
ar v230,a24
ar v231,a25
ar v232,a26
ar v233,a27
v_mul_f32 v230,v230,v224
v_mul_f32 v231,v231,v224
v_mul_f32 v232,v232,v224
v_mul_f32 v233,v233,v224
v_accvgpr_write_b32 a24,v230
v_accvgpr_write_b32 a25,v231
v_accvgpr_write_b32 a26,v232
v_accvgpr_write_b32 a27,v233
ar v230,a28
ar v231,a29
ar v232,a30
ar v233,a31
v_mul_f32 v230,v230,v224
v_mul_f32 v231,v231,v224
v_mul_f32 v232,v232,v224
v_mul_f32 v233,v233,v224
v_accvgpr_write_b32 a28,v230
v_accvgpr_write_b32 a29,v231
v_accvgpr_write_b32 a30,v232
v_accvgpr_write_b32 a31,v233
ar v230,a32
ar v231,a33
ar v232,a34
ar v233,a35
v_mul_f32 v230,v230,v224
v_mul_f32 v231,v231,v224
v_mul_f32 v232,v232,v224
v_mul_f32 v233,v233,v224
v_accvgpr_write_b32 a32,v230
v_accvgpr_write_b32 a33,v231
v_accvgpr_write_b32 a34,v232
v_accvgpr_write_b32 a35,v233
ar v230,a36
ar v231,a37
ar v232,a38
ar v233,a39
v_mul_f32 v230,v230,v224
v_mul_f32 v231,v231,v224
v_mul_f32 v232,v232,v224
v_mul_f32 v233,v233,v224
v_accvgpr_write_b32 a36,v230
v_accvgpr_write_b32 a37,v231
v_accvgpr_write_b32 a38,v232
v_accvgpr_write_b32 a39,v233
ar v230,a40
ar v231,a41
ar v232,a42
ar v233,a43
v_mul_f32 v230,v230,v224
v_mul_f32 v231,v231,v224
v_mul_f32 v232,v232,v224
v_mul_f32 v233,v233,v224
v_accvgpr_write_b32 a40,v230
v_accvgpr_write_b32 a41,v231
v_accvgpr_write_b32 a42,v232
v_accvgpr_write_b32 a43,v233
ar v230,a44
ar v231,a45
ar v232,a46
ar v233,a47
v_mul_f32 v230,v230,v224
v_mul_f32 v231,v231,v224
v_mul_f32 v232,v232,v224
v_mul_f32 v233,v233,v224
v_accvgpr_write_b32 a44,v230
v_accvgpr_write_b32 a45,v231
v_accvgpr_write_b32 a46,v232
v_accvgpr_write_b32 a47,v233
ar v230,a48
ar v231,a49
ar v232,a50
ar v233,a51
v_mul_f32 v230,v230,v224
v_mul_f32 v231,v231,v224
v_mul_f32 v232,v232,v224
v_mul_f32 v233,v233,v224
v_accvgpr_write_b32 a48,v230
v_accvgpr_write_b32 a49,v231
v_accvgpr_write_b32 a50,v232
v_accvgpr_write_b32 a51,v233
ar v230,a52
ar v231,a53
ar v232,a54
ar v233,a55
v_mul_f32 v230,v230,v224
v_mul_f32 v231,v231,v224
v_mul_f32 v232,v232,v224
v_mul_f32 v233,v233,v224
v_accvgpr_write_b32 a52,v230
v_accvgpr_write_b32 a53,v231
v_accvgpr_write_b32 a54,v232
v_accvgpr_write_b32 a55,v233
ar v230,a56
ar v231,a57
ar v232,a58
ar v233,a59
v_mul_f32 v230,v230,v224
v_mul_f32 v231,v231,v224
v_mul_f32 v232,v232,v224
v_mul_f32 v233,v233,v224
v_accvgpr_write_b32 a56,v230
v_accvgpr_write_b32 a57,v231
v_accvgpr_write_b32 a58,v232
v_accvgpr_write_b32 a59,v233
ar v230,a60
ar v231,a61
ar v232,a62
ar v233,a63
v_mul_f32 v230,v230,v224
v_mul_f32 v231,v231,v224
v_mul_f32 v232,v232,v224
v_mul_f32 v233,v233,v224
v_accvgpr_write_b32 a60,v230
v_accvgpr_write_b32 a61,v231
v_accvgpr_write_b32 a62,v232
v_accvgpr_write_b32 a63,v233
ar v230,a192
ar v231,a193
ar v232,a194
ar v233,a195
v_mul_f32 v230,v230,v224
v_mul_f32 v231,v231,v224
v_mul_f32 v232,v232,v224
v_mul_f32 v233,v233,v224
v_accvgpr_write_b32 a192,v230
v_accvgpr_write_b32 a193,v231
v_accvgpr_write_b32 a194,v232
v_accvgpr_write_b32 a195,v233
v_max3_f32 v221,v20,v21,v22
v_max3_f32 v221,v221,v23,v28
v_max3_f32 v221,v221,v29,v30
v_max_f32 v221,v221,v31
v_mov_b32 v225,v221
s_nop 1
v_permlane32_swap_b32 v221,v225
v_max_f32 v221,v221,v225
v_mov_b32 v225,v221
s_nop 1
v_permlane16_swap_b32 v221,v225
v_max_f32 v221,v221,v225
v_add_f32 v221,0x40e00000,v221
v_max_f32 v221,0,v221
v_sub_f32 v224,0,v221
v_exp_f32 v224,v224
v_sub_f32 v20,v20,v221
v_sub_f32 v21,v21,v221
v_sub_f32 v22,v22,v221
v_sub_f32 v23,v23,v221
v_sub_f32 v28,v28,v221
v_sub_f32 v29,v29,v221
v_sub_f32 v30,v30,v221
v_sub_f32 v31,v31,v221
v_sub_f32 v52,v52,v221
v_sub_f32 v53,v53,v221
v_sub_f32 v54,v54,v221
v_sub_f32 v55,v55,v221
v_sub_f32 v60,v60,v221
v_sub_f32 v61,v61,v221
v_sub_f32 v62,v62,v221
v_sub_f32 v63,v63,v221
v_sub_f32 v92,v92,v221
v_sub_f32 v93,v93,v221
v_sub_f32 v94,v94,v221
v_sub_f32 v95,v95,v221
ar v230,a64
ar v231,a65
ar v232,a66
ar v233,a67
v_mul_f32 v230,v230,v224
v_mul_f32 v231,v231,v224
v_mul_f32 v232,v232,v224
v_mul_f32 v233,v233,v224
v_accvgpr_write_b32 a64,v230
v_accvgpr_write_b32 a65,v231
v_accvgpr_write_b32 a66,v232
v_accvgpr_write_b32 a67,v233
ar v230,a68
ar v231,a69
ar v232,a70
ar v233,a71
v_mul_f32 v230,v230,v224
v_mul_f32 v231,v231,v224
v_mul_f32 v232,v232,v224
v_mul_f32 v233,v233,v224
v_accvgpr_write_b32 a68,v230
v_accvgpr_write_b32 a69,v231
v_accvgpr_write_b32 a70,v232
v_accvgpr_write_b32 a71,v233
ar v230,a72
ar v231,a73
ar v232,a74
ar v233,a75
v_mul_f32 v230,v230,v224
v_mul_f32 v231,v231,v224
v_mul_f32 v232,v232,v224
v_mul_f32 v233,v233,v224
v_accvgpr_write_b32 a72,v230
v_accvgpr_write_b32 a73,v231
v_accvgpr_write_b32 a74,v232
v_accvgpr_write_b32 a75,v233
ar v230,a76
ar v231,a77
ar v232,a78
ar v233,a79
v_mul_f32 v230,v230,v224
v_mul_f32 v231,v231,v224
v_mul_f32 v232,v232,v224
v_mul_f32 v233,v233,v224
v_accvgpr_write_b32 a76,v230
v_accvgpr_write_b32 a77,v231
v_accvgpr_write_b32 a78,v232
v_accvgpr_write_b32 a79,v233
ar v230,a80
ar v231,a81
ar v232,a82
ar v233,a83
v_mul_f32 v230,v230,v224
v_mul_f32 v231,v231,v224
v_mul_f32 v232,v232,v224
v_mul_f32 v233,v233,v224
v_accvgpr_write_b32 a80,v230
v_accvgpr_write_b32 a81,v231
v_accvgpr_write_b32 a82,v232
v_accvgpr_write_b32 a83,v233
ar v230,a84
ar v231,a85
ar v232,a86
ar v233,a87
v_mul_f32 v230,v230,v224
v_mul_f32 v231,v231,v224
v_mul_f32 v232,v232,v224
v_mul_f32 v233,v233,v224
v_accvgpr_write_b32 a84,v230
v_accvgpr_write_b32 a85,v231
v_accvgpr_write_b32 a86,v232
v_accvgpr_write_b32 a87,v233
ar v230,a88
ar v231,a89
ar v232,a90
ar v233,a91
v_mul_f32 v230,v230,v224
v_mul_f32 v231,v231,v224
v_mul_f32 v232,v232,v224
v_mul_f32 v233,v233,v224
v_accvgpr_write_b32 a88,v230
v_accvgpr_write_b32 a89,v231
v_accvgpr_write_b32 a90,v232
v_accvgpr_write_b32 a91,v233
ar v230,a92
ar v231,a93
ar v232,a94
ar v233,a95
v_mul_f32 v230,v230,v224
v_mul_f32 v231,v231,v224
v_mul_f32 v232,v232,v224
v_mul_f32 v233,v233,v224
v_accvgpr_write_b32 a92,v230
v_accvgpr_write_b32 a93,v231
v_accvgpr_write_b32 a94,v232
v_accvgpr_write_b32 a95,v233
ar v230,a96
ar v231,a97
ar v232,a98
ar v233,a99
v_mul_f32 v230,v230,v224
v_mul_f32 v231,v231,v224
v_mul_f32 v232,v232,v224
v_mul_f32 v233,v233,v224
v_accvgpr_write_b32 a96,v230
v_accvgpr_write_b32 a97,v231
v_accvgpr_write_b32 a98,v232
v_accvgpr_write_b32 a99,v233
ar v230,a100
ar v231,a101
ar v232,a102
ar v233,a103
v_mul_f32 v230,v230,v224
v_mul_f32 v231,v231,v224
v_mul_f32 v232,v232,v224
v_mul_f32 v233,v233,v224
v_accvgpr_write_b32 a100,v230
v_accvgpr_write_b32 a101,v231
v_accvgpr_write_b32 a102,v232
v_accvgpr_write_b32 a103,v233
ar v230,a104
ar v231,a105
ar v232,a106
ar v233,a107
v_mul_f32 v230,v230,v224
v_mul_f32 v231,v231,v224
v_mul_f32 v232,v232,v224
v_mul_f32 v233,v233,v224
v_accvgpr_write_b32 a104,v230
v_accvgpr_write_b32 a105,v231
v_accvgpr_write_b32 a106,v232
v_accvgpr_write_b32 a107,v233
ar v230,a108
ar v231,a109
ar v232,a110
ar v233,a111
v_mul_f32 v230,v230,v224
v_mul_f32 v231,v231,v224
v_mul_f32 v232,v232,v224
v_mul_f32 v233,v233,v224
v_accvgpr_write_b32 a108,v230
v_accvgpr_write_b32 a109,v231
v_accvgpr_write_b32 a110,v232
v_accvgpr_write_b32 a111,v233
ar v230,a112
ar v231,a113
ar v232,a114
ar v233,a115
v_mul_f32 v230,v230,v224
v_mul_f32 v231,v231,v224
v_mul_f32 v232,v232,v224
v_mul_f32 v233,v233,v224
v_accvgpr_write_b32 a112,v230
v_accvgpr_write_b32 a113,v231
v_accvgpr_write_b32 a114,v232
v_accvgpr_write_b32 a115,v233
ar v230,a116
ar v231,a117
ar v232,a118
ar v233,a119
v_mul_f32 v230,v230,v224
v_mul_f32 v231,v231,v224
v_mul_f32 v232,v232,v224
v_mul_f32 v233,v233,v224
v_accvgpr_write_b32 a116,v230
v_accvgpr_write_b32 a117,v231
v_accvgpr_write_b32 a118,v232
v_accvgpr_write_b32 a119,v233
ar v230,a120
ar v231,a121
ar v232,a122
ar v233,a123
v_mul_f32 v230,v230,v224
v_mul_f32 v231,v231,v224
v_mul_f32 v232,v232,v224
v_mul_f32 v233,v233,v224
v_accvgpr_write_b32 a120,v230
v_accvgpr_write_b32 a121,v231
v_accvgpr_write_b32 a122,v232
v_accvgpr_write_b32 a123,v233
ar v230,a124
ar v231,a125
ar v232,a126
ar v233,a127
v_mul_f32 v230,v230,v224
v_mul_f32 v231,v231,v224
v_mul_f32 v232,v232,v224
v_mul_f32 v233,v233,v224
v_accvgpr_write_b32 a124,v230
v_accvgpr_write_b32 a125,v231
v_accvgpr_write_b32 a126,v232
v_accvgpr_write_b32 a127,v233
ar v230,a196
ar v231,a197
ar v232,a198
ar v233,a199
v_mul_f32 v230,v230,v224
v_mul_f32 v231,v231,v224
v_mul_f32 v232,v232,v224
v_mul_f32 v233,v233,v224
v_accvgpr_write_b32 a196,v230
v_accvgpr_write_b32 a197,v231
v_accvgpr_write_b32 a198,v232
v_accvgpr_write_b32 a199,v233
v_exp_f32 v220,v4
v_exp_f32 v221,v5
v_exp_f32 v222,v6
v_exp_f32 v223,v7
cv v68,v220,v221
v_exp_f32 v224,v12
v_exp_f32 v225,v13
cv v69,v222,v223
v_exp_f32 v220,v14
v_exp_f32 v221,v15
cv v70,v224,v225
v_exp_f32 v222,v20
v_exp_f32 v223,v21
cv v71,v220,v221
v_exp_f32 v224,v22
v_exp_f32 v225,v23
cv v76,v222,v223
v_exp_f32 v220,v28
v_exp_f32 v221,v29
cv v77,v224,v225
v_exp_f32 v222,v30
v_exp_f32 v223,v31
cv v78,v220,v221
cv v79,v222,v223
v_or3_b32 v228,v68,v69,v70
v_or3_b32 v228,v228,v71,v76
v_or3_b32 v228,v228,v77,v78
v_or_b32 v228,v228,v79
v_and_b32 v228,0x40004000,v228
s_nop 7
s_setpc_b64 s[50:51]
LB_rare1%=:
s_nop 7
s_nop 1
v_max3_f32 v220,v36,v37,v38
v_max3_f32 v220,v220,v39,v44
v_max3_f32 v220,v220,v45,v46
v_max_f32 v220,v220,v47
v_mov_b32 v225,v220
s_nop 1
v_permlane32_swap_b32 v220,v225
v_max_f32 v220,v220,v225
v_mov_b32 v225,v220
s_nop 1
v_permlane16_swap_b32 v220,v225
v_max_f32 v220,v220,v225
v_add_f32 v220,0x40e00000,v220
v_max_f32 v220,0,v220
v_sub_f32 v224,0,v220
v_exp_f32 v224,v224
v_sub_f32 v4,v4,v220
v_sub_f32 v5,v5,v220
v_sub_f32 v6,v6,v220
v_sub_f32 v7,v7,v220
v_sub_f32 v12,v12,v220
v_sub_f32 v13,v13,v220
v_sub_f32 v14,v14,v220
v_sub_f32 v15,v15,v220
v_sub_f32 v36,v36,v220
v_sub_f32 v37,v37,v220
v_sub_f32 v38,v38,v220
v_sub_f32 v39,v39,v220
v_sub_f32 v44,v44,v220
v_sub_f32 v45,v45,v220
v_sub_f32 v46,v46,v220
v_sub_f32 v47,v47,v220
v_sub_f32 v84,v84,v220
v_sub_f32 v85,v85,v220
v_sub_f32 v86,v86,v220
v_sub_f32 v87,v87,v220
ar v230,a0
ar v231,a1
ar v232,a2
ar v233,a3
v_mul_f32 v230,v230,v224
v_mul_f32 v231,v231,v224
v_mul_f32 v232,v232,v224
v_mul_f32 v233,v233,v224
v_accvgpr_write_b32 a0,v230
v_accvgpr_write_b32 a1,v231
v_accvgpr_write_b32 a2,v232
v_accvgpr_write_b32 a3,v233
ar v230,a4
ar v231,a5
ar v232,a6
ar v233,a7
v_mul_f32 v230,v230,v224
v_mul_f32 v231,v231,v224
v_mul_f32 v232,v232,v224
v_mul_f32 v233,v233,v224
v_accvgpr_write_b32 a4,v230
v_accvgpr_write_b32 a5,v231
v_accvgpr_write_b32 a6,v232
v_accvgpr_write_b32 a7,v233
ar v230,a8
ar v231,a9
ar v232,a10
ar v233,a11
v_mul_f32 v230,v230,v224
v_mul_f32 v231,v231,v224
v_mul_f32 v232,v232,v224
v_mul_f32 v233,v233,v224
v_accvgpr_write_b32 a8,v230
v_accvgpr_write_b32 a9,v231
v_accvgpr_write_b32 a10,v232
v_accvgpr_write_b32 a11,v233
ar v230,a12
ar v231,a13
ar v232,a14
ar v233,a15
v_mul_f32 v230,v230,v224
v_mul_f32 v231,v231,v224
v_mul_f32 v232,v232,v224
v_mul_f32 v233,v233,v224
v_accvgpr_write_b32 a12,v230
v_accvgpr_write_b32 a13,v231
v_accvgpr_write_b32 a14,v232
v_accvgpr_write_b32 a15,v233
ar v230,a16
ar v231,a17
ar v232,a18
ar v233,a19
v_mul_f32 v230,v230,v224
v_mul_f32 v231,v231,v224
v_mul_f32 v232,v232,v224
v_mul_f32 v233,v233,v224
v_accvgpr_write_b32 a16,v230
v_accvgpr_write_b32 a17,v231
v_accvgpr_write_b32 a18,v232
v_accvgpr_write_b32 a19,v233
ar v230,a20
ar v231,a21
ar v232,a22
ar v233,a23
v_mul_f32 v230,v230,v224
v_mul_f32 v231,v231,v224
v_mul_f32 v232,v232,v224
v_mul_f32 v233,v233,v224
v_accvgpr_write_b32 a20,v230
v_accvgpr_write_b32 a21,v231
v_accvgpr_write_b32 a22,v232
v_accvgpr_write_b32 a23,v233
ar v230,a24
ar v231,a25
ar v232,a26
ar v233,a27
v_mul_f32 v230,v230,v224
v_mul_f32 v231,v231,v224
v_mul_f32 v232,v232,v224
v_mul_f32 v233,v233,v224
v_accvgpr_write_b32 a24,v230
v_accvgpr_write_b32 a25,v231
v_accvgpr_write_b32 a26,v232
v_accvgpr_write_b32 a27,v233
ar v230,a28
ar v231,a29
ar v232,a30
ar v233,a31
v_mul_f32 v230,v230,v224
v_mul_f32 v231,v231,v224
v_mul_f32 v232,v232,v224
v_mul_f32 v233,v233,v224
v_accvgpr_write_b32 a28,v230
v_accvgpr_write_b32 a29,v231
v_accvgpr_write_b32 a30,v232
v_accvgpr_write_b32 a31,v233
ar v230,a32
ar v231,a33
ar v232,a34
ar v233,a35
v_mul_f32 v230,v230,v224
v_mul_f32 v231,v231,v224
v_mul_f32 v232,v232,v224
v_mul_f32 v233,v233,v224
v_accvgpr_write_b32 a32,v230
v_accvgpr_write_b32 a33,v231
v_accvgpr_write_b32 a34,v232
v_accvgpr_write_b32 a35,v233
ar v230,a36
ar v231,a37
ar v232,a38
ar v233,a39
v_mul_f32 v230,v230,v224
v_mul_f32 v231,v231,v224
v_mul_f32 v232,v232,v224
v_mul_f32 v233,v233,v224
v_accvgpr_write_b32 a36,v230
v_accvgpr_write_b32 a37,v231
v_accvgpr_write_b32 a38,v232
v_accvgpr_write_b32 a39,v233
ar v230,a40
ar v231,a41
ar v232,a42
ar v233,a43
v_mul_f32 v230,v230,v224
v_mul_f32 v231,v231,v224
v_mul_f32 v232,v232,v224
v_mul_f32 v233,v233,v224
v_accvgpr_write_b32 a40,v230
v_accvgpr_write_b32 a41,v231
v_accvgpr_write_b32 a42,v232
v_accvgpr_write_b32 a43,v233
ar v230,a44
ar v231,a45
ar v232,a46
ar v233,a47
v_mul_f32 v230,v230,v224
v_mul_f32 v231,v231,v224
v_mul_f32 v232,v232,v224
v_mul_f32 v233,v233,v224
v_accvgpr_write_b32 a44,v230
v_accvgpr_write_b32 a45,v231
v_accvgpr_write_b32 a46,v232
v_accvgpr_write_b32 a47,v233
ar v230,a48
ar v231,a49
ar v232,a50
ar v233,a51
v_mul_f32 v230,v230,v224
v_mul_f32 v231,v231,v224
v_mul_f32 v232,v232,v224
v_mul_f32 v233,v233,v224
v_accvgpr_write_b32 a48,v230
v_accvgpr_write_b32 a49,v231
v_accvgpr_write_b32 a50,v232
v_accvgpr_write_b32 a51,v233
ar v230,a52
ar v231,a53
ar v232,a54
ar v233,a55
v_mul_f32 v230,v230,v224
v_mul_f32 v231,v231,v224
v_mul_f32 v232,v232,v224
v_mul_f32 v233,v233,v224
v_accvgpr_write_b32 a52,v230
v_accvgpr_write_b32 a53,v231
v_accvgpr_write_b32 a54,v232
v_accvgpr_write_b32 a55,v233
ar v230,a56
ar v231,a57
ar v232,a58
ar v233,a59
v_mul_f32 v230,v230,v224
v_mul_f32 v231,v231,v224
v_mul_f32 v232,v232,v224
v_mul_f32 v233,v233,v224
v_accvgpr_write_b32 a56,v230
v_accvgpr_write_b32 a57,v231
v_accvgpr_write_b32 a58,v232
v_accvgpr_write_b32 a59,v233
ar v230,a60
ar v231,a61
ar v232,a62
ar v233,a63
v_mul_f32 v230,v230,v224
v_mul_f32 v231,v231,v224
v_mul_f32 v232,v232,v224
v_mul_f32 v233,v233,v224
v_accvgpr_write_b32 a60,v230
v_accvgpr_write_b32 a61,v231
v_accvgpr_write_b32 a62,v232
v_accvgpr_write_b32 a63,v233
ar v230,a192
ar v231,a193
ar v232,a194
ar v233,a195
v_mul_f32 v230,v230,v224
v_mul_f32 v231,v231,v224
v_mul_f32 v232,v232,v224
v_mul_f32 v233,v233,v224
v_accvgpr_write_b32 a192,v230
v_accvgpr_write_b32 a193,v231
v_accvgpr_write_b32 a194,v232
v_accvgpr_write_b32 a195,v233
v_max3_f32 v221,v52,v53,v54
v_max3_f32 v221,v221,v55,v60
v_max3_f32 v221,v221,v61,v62
v_max_f32 v221,v221,v63
v_mov_b32 v225,v221
s_nop 1
v_permlane32_swap_b32 v221,v225
v_max_f32 v221,v221,v225
v_mov_b32 v225,v221
s_nop 1
v_permlane16_swap_b32 v221,v225
v_max_f32 v221,v221,v225
v_add_f32 v221,0x40e00000,v221
v_max_f32 v221,0,v221
v_sub_f32 v224,0,v221
v_exp_f32 v224,v224
v_sub_f32 v20,v20,v221
v_sub_f32 v21,v21,v221
v_sub_f32 v22,v22,v221
v_sub_f32 v23,v23,v221
v_sub_f32 v28,v28,v221
v_sub_f32 v29,v29,v221
v_sub_f32 v30,v30,v221
v_sub_f32 v31,v31,v221
v_sub_f32 v52,v52,v221
v_sub_f32 v53,v53,v221
v_sub_f32 v54,v54,v221
v_sub_f32 v55,v55,v221
v_sub_f32 v60,v60,v221
v_sub_f32 v61,v61,v221
v_sub_f32 v62,v62,v221
v_sub_f32 v63,v63,v221
v_sub_f32 v92,v92,v221
v_sub_f32 v93,v93,v221
v_sub_f32 v94,v94,v221
v_sub_f32 v95,v95,v221
ar v230,a64
ar v231,a65
ar v232,a66
ar v233,a67
v_mul_f32 v230,v230,v224
v_mul_f32 v231,v231,v224
v_mul_f32 v232,v232,v224
v_mul_f32 v233,v233,v224
v_accvgpr_write_b32 a64,v230
v_accvgpr_write_b32 a65,v231
v_accvgpr_write_b32 a66,v232
v_accvgpr_write_b32 a67,v233
ar v230,a68
ar v231,a69
ar v232,a70
ar v233,a71
v_mul_f32 v230,v230,v224
v_mul_f32 v231,v231,v224
v_mul_f32 v232,v232,v224
v_mul_f32 v233,v233,v224
v_accvgpr_write_b32 a68,v230
v_accvgpr_write_b32 a69,v231
v_accvgpr_write_b32 a70,v232
v_accvgpr_write_b32 a71,v233
ar v230,a72
ar v231,a73
ar v232,a74
ar v233,a75
v_mul_f32 v230,v230,v224
v_mul_f32 v231,v231,v224
v_mul_f32 v232,v232,v224
v_mul_f32 v233,v233,v224
v_accvgpr_write_b32 a72,v230
v_accvgpr_write_b32 a73,v231
v_accvgpr_write_b32 a74,v232
v_accvgpr_write_b32 a75,v233
ar v230,a76
ar v231,a77
ar v232,a78
ar v233,a79
v_mul_f32 v230,v230,v224
v_mul_f32 v231,v231,v224
v_mul_f32 v232,v232,v224
v_mul_f32 v233,v233,v224
v_accvgpr_write_b32 a76,v230
v_accvgpr_write_b32 a77,v231
v_accvgpr_write_b32 a78,v232
v_accvgpr_write_b32 a79,v233
ar v230,a80
ar v231,a81
ar v232,a82
ar v233,a83
v_mul_f32 v230,v230,v224
v_mul_f32 v231,v231,v224
v_mul_f32 v232,v232,v224
v_mul_f32 v233,v233,v224
v_accvgpr_write_b32 a80,v230
v_accvgpr_write_b32 a81,v231
v_accvgpr_write_b32 a82,v232
v_accvgpr_write_b32 a83,v233
ar v230,a84
ar v231,a85
ar v232,a86
ar v233,a87
v_mul_f32 v230,v230,v224
v_mul_f32 v231,v231,v224
v_mul_f32 v232,v232,v224
v_mul_f32 v233,v233,v224
v_accvgpr_write_b32 a84,v230
v_accvgpr_write_b32 a85,v231
v_accvgpr_write_b32 a86,v232
v_accvgpr_write_b32 a87,v233
ar v230,a88
ar v231,a89
ar v232,a90
ar v233,a91
v_mul_f32 v230,v230,v224
v_mul_f32 v231,v231,v224
v_mul_f32 v232,v232,v224
v_mul_f32 v233,v233,v224
v_accvgpr_write_b32 a88,v230
v_accvgpr_write_b32 a89,v231
v_accvgpr_write_b32 a90,v232
v_accvgpr_write_b32 a91,v233
ar v230,a92
ar v231,a93
ar v232,a94
ar v233,a95
v_mul_f32 v230,v230,v224
v_mul_f32 v231,v231,v224
v_mul_f32 v232,v232,v224
v_mul_f32 v233,v233,v224
v_accvgpr_write_b32 a92,v230
v_accvgpr_write_b32 a93,v231
v_accvgpr_write_b32 a94,v232
v_accvgpr_write_b32 a95,v233
ar v230,a96
ar v231,a97
ar v232,a98
ar v233,a99
v_mul_f32 v230,v230,v224
v_mul_f32 v231,v231,v224
v_mul_f32 v232,v232,v224
v_mul_f32 v233,v233,v224
v_accvgpr_write_b32 a96,v230
v_accvgpr_write_b32 a97,v231
v_accvgpr_write_b32 a98,v232
v_accvgpr_write_b32 a99,v233
ar v230,a100
ar v231,a101
ar v232,a102
ar v233,a103
v_mul_f32 v230,v230,v224
v_mul_f32 v231,v231,v224
v_mul_f32 v232,v232,v224
v_mul_f32 v233,v233,v224
v_accvgpr_write_b32 a100,v230
v_accvgpr_write_b32 a101,v231
v_accvgpr_write_b32 a102,v232
v_accvgpr_write_b32 a103,v233
ar v230,a104
ar v231,a105
ar v232,a106
ar v233,a107
v_mul_f32 v230,v230,v224
v_mul_f32 v231,v231,v224
v_mul_f32 v232,v232,v224
v_mul_f32 v233,v233,v224
v_accvgpr_write_b32 a104,v230
v_accvgpr_write_b32 a105,v231
v_accvgpr_write_b32 a106,v232
v_accvgpr_write_b32 a107,v233
ar v230,a108
ar v231,a109
ar v232,a110
ar v233,a111
v_mul_f32 v230,v230,v224
v_mul_f32 v231,v231,v224
v_mul_f32 v232,v232,v224
v_mul_f32 v233,v233,v224
v_accvgpr_write_b32 a108,v230
v_accvgpr_write_b32 a109,v231
v_accvgpr_write_b32 a110,v232
v_accvgpr_write_b32 a111,v233
ar v230,a112
ar v231,a113
ar v232,a114
ar v233,a115
v_mul_f32 v230,v230,v224
v_mul_f32 v231,v231,v224
v_mul_f32 v232,v232,v224
v_mul_f32 v233,v233,v224
v_accvgpr_write_b32 a112,v230
v_accvgpr_write_b32 a113,v231
v_accvgpr_write_b32 a114,v232
v_accvgpr_write_b32 a115,v233
ar v230,a116
ar v231,a117
ar v232,a118
ar v233,a119
v_mul_f32 v230,v230,v224
v_mul_f32 v231,v231,v224
v_mul_f32 v232,v232,v224
v_mul_f32 v233,v233,v224
v_accvgpr_write_b32 a116,v230
v_accvgpr_write_b32 a117,v231
v_accvgpr_write_b32 a118,v232
v_accvgpr_write_b32 a119,v233
ar v230,a120
ar v231,a121
ar v232,a122
ar v233,a123
v_mul_f32 v230,v230,v224
v_mul_f32 v231,v231,v224
v_mul_f32 v232,v232,v224
v_mul_f32 v233,v233,v224
v_accvgpr_write_b32 a120,v230
v_accvgpr_write_b32 a121,v231
v_accvgpr_write_b32 a122,v232
v_accvgpr_write_b32 a123,v233
ar v230,a124
ar v231,a125
ar v232,a126
ar v233,a127
v_mul_f32 v230,v230,v224
v_mul_f32 v231,v231,v224
v_mul_f32 v232,v232,v224
v_mul_f32 v233,v233,v224
v_accvgpr_write_b32 a124,v230
v_accvgpr_write_b32 a125,v231
v_accvgpr_write_b32 a126,v232
v_accvgpr_write_b32 a127,v233
ar v230,a196
ar v231,a197
ar v232,a198
ar v233,a199
v_mul_f32 v230,v230,v224
v_mul_f32 v231,v231,v224
v_mul_f32 v232,v232,v224
v_mul_f32 v233,v233,v224
v_accvgpr_write_b32 a196,v230
v_accvgpr_write_b32 a197,v231
v_accvgpr_write_b32 a198,v232
v_accvgpr_write_b32 a199,v233
v_exp_f32 v220,v36
v_exp_f32 v221,v37
v_exp_f32 v222,v38
v_exp_f32 v223,v39
cv v68,v220,v221
v_exp_f32 v224,v44
v_exp_f32 v225,v45
cv v69,v222,v223
v_exp_f32 v220,v46
v_exp_f32 v221,v47
cv v70,v224,v225
v_exp_f32 v222,v52
v_exp_f32 v223,v53
cv v71,v220,v221
v_exp_f32 v224,v54
v_exp_f32 v225,v55
cv v76,v222,v223
v_exp_f32 v220,v60
v_exp_f32 v221,v61
cv v77,v224,v225
v_exp_f32 v222,v62
v_exp_f32 v223,v63
cv v78,v220,v221
cv v79,v222,v223
v_or3_b32 v228,v68,v69,v70
v_or3_b32 v228,v228,v71,v76
v_or3_b32 v228,v228,v77,v78
v_or_b32 v228,v228,v79
v_and_b32 v228,0x40004000,v228
s_nop 7
s_setpc_b64 s[50:51]
LC_start%=:
s_mov_b32 s43,0x14000
s_lshl_b32 s57,%9,14
s_add_u32 s43,s43,s57
s_and_b32 s43,s43,0x7ffff
v_and_b32 v155,15,v214
s_lshl_b32 s46,%11,7
v_lshlrev_b32 v152,3,v155
v_add_u32 v152,s46,v152
s_lshl_b32 s46,%11,3
v_lshlrev_b32 v155,7,v155
v_add_u32 v155,s46,v155
)ASM"
R"ASM(v_cmp_gt_u32_e32 vcc,16,v214
v_mov_b32 v153,0x7fffff00
s_nop 0
v_cndmask_b32_e32 v153,v153,v155,vcc
buffer_load_dwordx2 v[150:151],v152,%5,%12 offen sc1
buffer_load_dword v154,v155,%5,%12 offen sc1
s_waitcnt lgkmcnt(0)
s_waitcnt vmcnt(11)
s_barrier
r a[0:3],v212,40960
r a[56:59],v212,43008
t a[204:205],v204,16384
t a[206:207],v204,16896
t a[208:209],v205,16384
t a[210:211],v205,16896
t a[212:213],v206,16384
t a[214:215],v206,16896
t a[216:217],v207,16384
t a[218:219],v207,16896
s_mov_b32 m0,0xd000
s_lshr_b32 s45,s87,12
s_add_u32 s45,s45,0
dt v220,%0,s45,0
dt v221,%0,s45,1024
dt v222,%0,s45,2048
dt v223,%0,s45,3072
s_mov_b32 m0,0xe000
s_lshr_b32 s45,s87,12
s_add_u32 s45,s45,0x7000
dt v220,%0,s45,0
dt v221,%0,s45,1024
dt v222,%0,s45,2048
dt v223,%0,s45,3072
s_mov_b32 m0,0xf000
s_lshr_b32 s45,s87,12
s_add_u32 s45,s45,0xe000
dt v220,%0,s45,0
dt v221,%0,s45,1024
dt v222,%0,s45,2048
dt v223,%0,s45,3072
s_mov_b32 m0,0x10000
s_lshr_b32 s45,s87,12
s_add_u32 s45,s45,0x15000
dt v220,%0,s45,0
dt v221,%0,s45,1024
dt v222,%0,s45,2048
dt v223,%0,s45,3072
s_mov_b32 m0,0x11000
s_lshr_b32 s45,s87,12
s_add_u32 s45,s45,0x1c000
dt v220,%0,s45,0
dt v221,%0,s45,1024
dt v222,%0,s45,2048
dt v223,%0,s45,3072
s_waitcnt lgkmcnt(0)
m v[0:3],a[204:207],a[0:3],0
t a[220:221],v204,20480
t a[222:223],v204,20992
m v[16:19],a[204:207],a[56:59],0
t a[224:225],v205,20480
t a[226:227],v205,20992
m v[4:7],a[208:211],a[0:3],0
t a[228:229],v206,20480
t a[230:231],v206,20992
t a[232:233],v207,20480
m v[20:23],a[208:211],a[56:59],0
t a[234:235],v207,20992
v_lshrrev_b32 v236,5,v214
m v[8:11],a[212:215],a[0:3],0
v_and_b32 v237,31,v214
v_xor_b32 v237,v237,v236
m v[24:27],a[212:215],a[56:59],0
v_lshlrev_b32 v237,4,v237
v_lshlrev_b32 v236,9,v236
v_add_u32 v128,v236,v237
m v[12:15],a[216:219],a[0:3],0
v_xor_b32 v129,32,v128
v_xor_b32 v130,64,v128
m v[28:31],a[216:219],a[56:59],0
v_xor_b32 v131,0x60,v128
v_xor_b32 v132,0x80,v128
v_xor_b32 v133,0xa0,v128
s_waitcnt lgkmcnt(0)
m v[32:35],a[220:223],a[0:3],0
r a[4:7],v213,40960
r a[60:63],v213,43008
m v[48:51],a[220:223],a[56:59],0
t a[204:205],v204,24576
t a[206:207],v204,25088
t a[208:209],v205,24576
m v[36:39],a[224:227],a[0:3],0
t a[210:211],v205,25088
t a[212:213],v206,24576
m v[52:55],a[224:227],a[56:59],0
t a[214:215],v206,25088
t a[216:217],v207,24576
t a[218:219],v207,25088
m v[40:43],a[228:231],a[0:3],0
v_xor_b32 v134,0xc0,v128
v_xor_b32 v135,0xe0,v128
v_lshrrev_b32 v236,5,v214
m v[56:59],a[228:231],a[56:59],0
v_and_b32 v237,31,v214
v_lshlrev_b32 v238,1,v236
m v[44:47],a[232:235],a[0:3],0
v_xor_b32 v237,v237,v238
v_lshlrev_b32 v237,4,v237
v_lshlrev_b32 v236,9,v236
m v[60:63],a[232:235],a[56:59],0
v_add_u32 v136,v236,v237
v_xor_b32 v137,64,v136
v_xor_b32 v138,0x80,v136
s_waitcnt lgkmcnt(0)
m v[0:3],a[204:207],a[4:7],v[0:3]
t a[220:221],v204,28672
t a[222:223],v204,29184
m v[16:19],a[204:207],a[60:63],v[16:19]
t a[224:225],v205,28672
t a[226:227],v205,29184
m v[4:7],a[208:211],a[4:7],v[4:7]
t a[228:229],v206,28672
t a[230:231],v206,29184
t a[232:233],v207,28672
m v[20:23],a[208:211],a[60:63],v[20:23]
t a[234:235],v207,29184
v_xor_b32 v139,0xc0,v136
m v[8:11],a[212:215],a[4:7],v[8:11]
v_lshrrev_b32 v236,3,v214
v_and_b32 v237,6,v236
m v[24:27],a[212:215],a[60:63],v[24:27]
v_and_b32 v238,7,v214
v_xor_b32 v237,v237,v238
v_lshlrev_b32 v237,4,v237
m v[12:15],a[216:219],a[4:7],v[12:15]
v_mul_u32_u24 v236,0x380,v236
v_add_u32 v144,v236,v237
m v[28:31],a[216:219],a[60:63],v[28:31]
v_add_u32 v145,0x1800,v144
v_add_u32 v146,0x3000,v144
v_add_u32 v147,0x4800,v144
s_waitcnt lgkmcnt(0)
s_waitcnt vmcnt(22)
s_barrier
m v[32:35],a[220:223],a[4:7],v[32:35]
r a[8:11],v212,0
r a[64:67],v212,2048
t a[204:205],v204,49152
t a[206:207],v204,49664
t a[208:209],v205,49152
t a[210:211],v205,49664
t a[212:213],v206,49152
m v[48:51],a[220:223],a[60:63],v[48:51]
t a[214:215],v206,49664
t a[216:217],v207,49152
t a[218:219],v207,49664
s_mov_b32 m0,0x12000
s_lshr_b32 s45,s88,12
s_add_u32 s45,s45,0
m v[36:39],a[224:227],a[4:7],v[36:39]
dt v220,%0,s45,0
dt v221,%0,s45,1024
m v[52:55],a[224:227],a[60:63],v[52:55]
dt v222,%0,s45,2048
dt v223,%0,s45,3072
m v[40:43],a[228:231],a[4:7],v[40:43]
s_mov_b32 m0,0x13000
s_lshr_b32 s45,s88,12
s_add_u32 s45,s45,0x7000
dt v220,%0,s45,0
m v[56:59],a[228:231],a[60:63],v[56:59]
dt v221,%0,s45,1024
dt v222,%0,s45,2048
m v[44:47],a[232:235],a[4:7],v[44:47]
dt v223,%0,s45,3072
s_mov_b32 m0,0x14000
s_lshr_b32 s45,s88,12
s_add_u32 s45,s45,0xe000
m v[60:63],a[232:235],a[60:63],v[60:63]
dt v220,%0,s45,0
dt v221,%0,s45,1024
s_waitcnt lgkmcnt(0)
m v[0:3],a[204:207],a[8:11],v[0:3]
t a[220:221],v204,53248
t a[222:223],v204,53760
t a[224:225],v205,53248
t a[226:227],v205,53760
t a[228:229],v206,53248
t a[230:231],v206,53760
t a[232:233],v207,53248
m v[16:19],a[204:207],a[64:67],v[16:19]
t a[234:235],v207,53760
dt v222,%0,s45,2048
m v[4:7],a[208:211],a[8:11],v[4:7]
dt v223,%0,s45,3072
s_mov_b32 m0,0x15000
s_lshr_b32 s45,s88,12
s_add_u32 s45,s45,0x15000
m v[20:23],a[208:211],a[64:67],v[20:23]
dt v220,%0,s45,0
dt v221,%0,s45,1024
m v[8:11],a[212:215],a[8:11],v[8:11]
dt v222,%0,s45,2048
dt v223,%0,s45,3072
m v[24:27],a[212:215],a[64:67],v[24:27]
s_mov_b32 m0,0x16000
s_lshr_b32 s45,s88,12
s_add_u32 s45,s45,0x1c000
dt v220,%0,s45,0
m v[12:15],a[216:219],a[8:11],v[12:15]
dt v221,%0,s45,1024
dt v222,%0,s45,2048
m v[28:31],a[216:219],a[64:67],v[28:31]
dt v223,%0,s45,3072
s_waitcnt lgkmcnt(0)
m v[32:35],a[220:223],a[8:11],v[32:35]
r a[12:15],v213,0
r a[68:71],v213,2048
m v[48:51],a[220:223],a[64:67],v[48:51]
t a[204:205],v204,57344
t a[206:207],v204,57856
m v[36:39],a[224:227],a[8:11],v[36:39]
t a[208:209],v205,57344
t a[210:211],v205,57856
m v[52:55],a[224:227],a[64:67],v[52:55]
t a[212:213],v206,57344
t a[214:215],v206,57856
m v[40:43],a[228:231],a[8:11],v[40:43]
t a[216:217],v207,57344
t a[218:219],v207,57856
m v[56:59],a[228:231],a[64:67],v[56:59]
m v[44:47],a[232:235],a[8:11],v[44:47]
m v[60:63],a[232:235],a[64:67],v[60:63]
s_waitcnt lgkmcnt(0)
m v[0:3],a[204:207],a[12:15],v[0:3]
t a[220:221],v204,61440
t a[222:223],v204,61952
m v[16:19],a[204:207],a[68:71],v[16:19]
t a[224:225],v205,61440
t a[226:227],v205,61952
m v[4:7],a[208:211],a[12:15],v[4:7]
t a[228:229],v206,61440
t a[230:231],v206,61952
m v[20:23],a[208:211],a[68:71],v[20:23]
t a[232:233],v207,61440
t a[234:235],v207,61952
m v[8:11],a[212:215],a[12:15],v[8:11]
m v[24:27],a[212:215],a[68:71],v[24:27]
m v[12:15],a[216:219],a[12:15],v[12:15]
m v[28:31],a[216:219],a[68:71],v[28:31]
s_waitcnt lgkmcnt(0)
s_waitcnt vmcnt(20)
s_barrier
r a[16:19],v212,20480
r a[72:75],v212,22528
t a[204:205],v204,0
t a[206:207],v204,512
t a[208:209],v205,0
t a[210:211],v205,512
t a[212:213],v206,0
t a[214:215],v206,512
t a[216:217],v207,0
t a[218:219],v207,512
v_add_u32 v148,8,v154
v_sub_u32 v155,v151,v148
v_cmp_ge_u32_e32 vcc,7,v155
v_add_u32 v155,1,v151
s_nop 0
v_cndmask_b32_e32 v148,v148,v155,vcc
v_mov_b32 v149,v150
buffer_store_dwordx2 v[148:149],v153,%5,%12 offen sc1
v_lshlrev_b32 v155,2,v214
v_add_u32 v155,0x17000,v155
ds_write_b32 v155,v148
m v[32:35],a[220:223],a[12:15],v[32:35]
s_mov_b32 m0,0x8000
s_lshr_b32 s45,s89,12
s_add_u32 s45,s45,0
dt v220,%0,s45,0
m v[48:51],a[220:223],a[68:71],v[48:51]
dt v221,%0,s45,1024
m v[36:39],a[224:227],a[12:15],v[36:39]
dt v222,%0,s45,2048
dt v223,%0,s45,3072
m v[52:55],a[224:227],a[68:71],v[52:55]
s_mov_b32 m0,0x9000
s_lshr_b32 s45,s89,12
s_add_u32 s45,s45,0x7000
dt v220,%0,s45,0
m v[40:43],a[228:231],a[12:15],v[40:43]
dt v221,%0,s45,1024
m v[56:59],a[228:231],a[68:71],v[56:59]
dt v222,%0,s45,2048
dt v223,%0,s45,3072
m v[44:47],a[232:235],a[12:15],v[44:47]
s_mov_b32 m0,0xa000
s_lshr_b32 s45,s89,12
s_add_u32 s45,s45,0xe000
dt v220,%0,s45,0
m v[60:63],a[232:235],a[68:71],v[60:63]
dt v221,%0,s45,1024
s_waitcnt lgkmcnt(0)
m v[0:3],a[204:207],a[16:19],v[0:3]
t a[220:221],v204,4096
t a[222:223],v204,4608
t a[224:225],v205,4096
t a[226:227],v205,4608
t a[228:229],v206,4096
t a[230:231],v206,4608
t a[232:233],v207,4096
m v[16:19],a[204:207],a[72:75],v[16:19]
t a[234:235],v207,4608
dt v222,%0,s45,2048
m v[4:7],a[208:211],a[16:19],v[4:7]
dt v223,%0,s45,3072
s_mov_b32 m0,0xb000
s_lshr_b32 s45,s89,12
s_add_u32 s45,s45,0x15000
m v[20:23],a[208:211],a[72:75],v[20:23]
dt v220,%0,s45,0
dt v221,%0,s45,1024
m v[8:11],a[212:215],a[16:19],v[8:11]
dt v222,%0,s45,2048
dt v223,%0,s45,3072
m v[24:27],a[212:215],a[72:75],v[24:27]
s_mov_b32 m0,0xc000
s_lshr_b32 s45,s89,12
s_add_u32 s45,s45,0x1c000
dt v220,%0,s45,0
m v[12:15],a[216:219],a[16:19],v[12:15]
dt v221,%0,s45,1024
dt v222,%0,s45,2048
m v[28:31],a[216:219],a[72:75],v[28:31]
dt v223,%0,s45,3072
s_waitcnt lgkmcnt(0)
m v[32:35],a[220:223],a[16:19],v[32:35]
r a[20:23],v213,20480
r a[76:79],v213,22528
m v[48:51],a[220:223],a[72:75],v[48:51]
t a[204:205],v204,8192
t a[206:207],v204,8704
m v[36:39],a[224:227],a[16:19],v[36:39]
t a[208:209],v205,8192
t a[210:211],v205,8704
m v[52:55],a[224:227],a[72:75],v[52:55]
t a[212:213],v206,8192
t a[214:215],v206,8704
m v[40:43],a[228:231],a[16:19],v[40:43]
t a[216:217],v207,8192
t a[218:219],v207,8704
m v[56:59],a[228:231],a[72:75],v[56:59]
m v[44:47],a[232:235],a[16:19],v[44:47]
m v[60:63],a[232:235],a[72:75],v[60:63]
s_waitcnt lgkmcnt(0)
m v[0:3],a[204:207],a[20:23],v[0:3]
t a[220:221],v204,12288
t a[222:223],v204,12800
m v[16:19],a[204:207],a[76:79],v[16:19]
t a[224:225],v205,12288
t a[226:227],v205,12800
m v[4:7],a[208:211],a[20:23],v[4:7]
t a[228:229],v206,12288
t a[230:231],v206,12800
m v[20:23],a[208:211],a[76:79],v[20:23]
t a[232:233],v207,12288
t a[234:235],v207,12800
m v[8:11],a[212:215],a[20:23],v[8:11]
m v[24:27],a[212:215],a[76:79],v[24:27]
m v[12:15],a[216:219],a[20:23],v[12:15]
m v[28:31],a[216:219],a[76:79],v[28:31]
s_waitcnt lgkmcnt(0)
s_waitcnt vmcnt(21)
s_barrier
m v[32:35],a[220:223],a[20:23],v[32:35]
r a[24:27],v212,40960
r a[80:83],v212,43008
t a[204:205],v204,16384
t a[206:207],v204,16896
t a[208:209],v205,16384
t a[210:211],v205,16896
t a[212:213],v206,16384
m v[48:51],a[220:223],a[76:79],v[48:51]
t a[214:215],v206,16896
t a[216:217],v207,16384
t a[218:219],v207,16896
s_mov_b32 m0,0xd000
s_lshr_b32 s45,s90,12
s_add_u32 s45,s45,0
m v[36:39],a[224:227],a[20:23],v[36:39]
dt v220,%0,s45,0
dt v221,%0,s45,1024
m v[52:55],a[224:227],a[76:79],v[52:55]
dt v222,%0,s45,2048
dt v223,%0,s45,3072
m v[40:43],a[228:231],a[20:23],v[40:43]
s_mov_b32 m0,0xe000
s_lshr_b32 s45,s90,12
s_add_u32 s45,s45,0x7000
dt v220,%0,s45,0
m v[56:59],a[228:231],a[76:79],v[56:59]
dt v221,%0,s45,1024
dt v222,%0,s45,2048
m v[44:47],a[232:235],a[20:23],v[44:47]
dt v223,%0,s45,3072
s_mov_b32 m0,0xf000
s_lshr_b32 s45,s90,12
s_add_u32 s45,s45,0xe000
m v[60:63],a[232:235],a[76:79],v[60:63]
dt v220,%0,s45,0
dt v221,%0,s45,1024
s_waitcnt lgkmcnt(0)
m v[0:3],a[204:207],a[24:27],v[0:3]
t a[220:221],v204,20480
t a[222:223],v204,20992
t a[224:225],v205,20480
t a[226:227],v205,20992
t a[228:229],v206,20480
t a[230:231],v206,20992
t a[232:233],v207,20480
m v[16:19],a[204:207],a[80:83],v[16:19]
t a[234:235],v207,20992
dt v222,%0,s45,2048
m v[4:7],a[208:211],a[24:27],v[4:7]
dt v223,%0,s45,3072
s_mov_b32 m0,0x10000
s_lshr_b32 s45,s90,12
s_add_u32 s45,s45,0x15000
m v[20:23],a[208:211],a[80:83],v[20:23]
dt v220,%0,s45,0
dt v221,%0,s45,1024
m v[8:11],a[212:215],a[24:27],v[8:11]
dt v222,%0,s45,2048
dt v223,%0,s45,3072
m v[24:27],a[212:215],a[80:83],v[24:27]
s_mov_b32 m0,0x11000
s_lshr_b32 s45,s90,12
s_add_u32 s45,s45,0x1c000
dt v220,%0,s45,0
m v[12:15],a[216:219],a[24:27],v[12:15]
dt v221,%0,s45,1024
dt v222,%0,s45,2048
m v[28:31],a[216:219],a[80:83],v[28:31]
dt v223,%0,s45,3072
s_waitcnt lgkmcnt(0)
m v[32:35],a[220:223],a[24:27],v[32:35]
r a[28:31],v213,40960
r a[84:87],v213,43008
m v[48:51],a[220:223],a[80:83],v[48:51]
t a[204:205],v204,24576
t a[206:207],v204,25088
m v[36:39],a[224:227],a[24:27],v[36:39]
t a[208:209],v205,24576
t a[210:211],v205,25088
m v[52:55],a[224:227],a[80:83],v[52:55]
t a[212:213],v206,24576
t a[214:215],v206,25088
m v[40:43],a[228:231],a[24:27],v[40:43]
t a[216:217],v207,24576
t a[218:219],v207,25088
m v[56:59],a[228:231],a[80:83],v[56:59]
m v[44:47],a[232:235],a[24:27],v[44:47]
m v[60:63],a[232:235],a[80:83],v[60:63]
s_waitcnt lgkmcnt(0)
m v[0:3],a[204:207],a[28:31],v[0:3]
t a[220:221],v204,28672
t a[222:223],v204,29184
m v[16:19],a[204:207],a[84:87],v[16:19]
t a[224:225],v205,28672
t a[226:227],v205,29184
m v[4:7],a[208:211],a[28:31],v[4:7]
t a[228:229],v206,28672
t a[230:231],v206,29184
m v[20:23],a[208:211],a[84:87],v[20:23]
t a[232:233],v207,28672
t a[234:235],v207,29184
m v[8:11],a[212:215],a[28:31],v[8:11]
m v[24:27],a[212:215],a[84:87],v[24:27]
m v[12:15],a[216:219],a[28:31],v[12:15]
m v[28:31],a[216:219],a[84:87],v[28:31]
s_waitcnt lgkmcnt(0)
s_waitcnt vmcnt(20)
s_barrier
m v[32:35],a[220:223],a[28:31],v[32:35]
r a[32:35],v212,0
r a[88:91],v212,2048
t a[204:205],v204,49152
t a[206:207],v204,49664
t a[208:209],v205,49152
t a[210:211],v205,49664
t a[212:213],v206,49152
m v[48:51],a[220:223],a[84:87],v[48:51]
t a[214:215],v206,49664
t a[216:217],v207,49152
t a[218:219],v207,49664
s_mov_b32 m0,0x12000
s_lshr_b32 s45,s91,12
s_add_u32 s45,s45,0
m v[36:39],a[224:227],a[28:31],v[36:39]
dt v220,%0,s45,0
dt v221,%0,s45,1024
m v[52:55],a[224:227],a[84:87],v[52:55]
dt v222,%0,s45,2048
dt v223,%0,s45,3072
m v[40:43],a[228:231],a[28:31],v[40:43]
s_mov_b32 m0,0x13000
s_lshr_b32 s45,s91,12
s_add_u32 s45,s45,0x7000
dt v220,%0,s45,0
m v[56:59],a[228:231],a[84:87],v[56:59]
dt v221,%0,s45,1024
dt v222,%0,s45,2048
m v[44:47],a[232:235],a[28:31],v[44:47]
dt v223,%0,s45,3072
s_mov_b32 m0,0x14000
s_lshr_b32 s45,s91,12
s_add_u32 s45,s45,0xe000
m v[60:63],a[232:235],a[84:87],v[60:63]
dt v220,%0,s45,0
dt v221,%0,s45,1024
s_waitcnt lgkmcnt(0)
m v[0:3],a[204:207],a[32:35],v[0:3]
t a[220:221],v204,53248
t a[222:223],v204,53760
t a[224:225],v205,53248
t a[226:227],v205,53760
t a[228:229],v206,53248
t a[230:231],v206,53760
t a[232:233],v207,53248
m v[16:19],a[204:207],a[88:91],v[16:19]
t a[234:235],v207,53760
dt v222,%0,s45,2048
m v[4:7],a[208:211],a[32:35],v[4:7]
dt v223,%0,s45,3072
s_mov_b32 m0,0x15000
s_lshr_b32 s45,s91,12
s_add_u32 s45,s45,0x15000
m v[20:23],a[208:211],a[88:91],v[20:23]
dt v220,%0,s45,0
dt v221,%0,s45,1024
m v[8:11],a[212:215],a[32:35],v[8:11]
dt v222,%0,s45,2048
dt v223,%0,s45,3072
m v[24:27],a[212:215],a[88:91],v[24:27]
s_mov_b32 m0,0x16000
s_lshr_b32 s45,s91,12
s_add_u32 s45,s45,0x1c000
dt v220,%0,s45,0
m v[12:15],a[216:219],a[32:35],v[12:15]
dt v221,%0,s45,1024
dt v222,%0,s45,2048
m v[28:31],a[216:219],a[88:91],v[28:31]
dt v223,%0,s45,3072
s_waitcnt lgkmcnt(0)
m v[32:35],a[220:223],a[32:35],v[32:35]
r a[36:39],v213,0
r a[92:95],v213,2048
m v[48:51],a[220:223],a[88:91],v[48:51]
t a[204:205],v204,57344
t a[206:207],v204,57856
m v[36:39],a[224:227],a[32:35],v[36:39]
t a[208:209],v205,57344
t a[210:211],v205,57856
m v[52:55],a[224:227],a[88:91],v[52:55]
t a[212:213],v206,57344
t a[214:215],v206,57856
m v[40:43],a[228:231],a[32:35],v[40:43]
t a[216:217],v207,57344
t a[218:219],v207,57856
m v[56:59],a[228:231],a[88:91],v[56:59]
m v[44:47],a[232:235],a[32:35],v[44:47]
m v[60:63],a[232:235],a[88:91],v[60:63]
s_waitcnt lgkmcnt(0)
m v[0:3],a[204:207],a[36:39],v[0:3]
t a[220:221],v204,61440
t a[222:223],v204,61952
m v[16:19],a[204:207],a[92:95],v[16:19]
t a[224:225],v205,61440
t a[226:227],v205,61952
m v[4:7],a[208:211],a[36:39],v[4:7]
t a[228:229],v206,61440
t a[230:231],v206,61952
m v[20:23],a[208:211],a[92:95],v[20:23]
t a[232:233],v207,61440
t a[234:235],v207,61952
m v[8:11],a[212:215],a[36:39],v[8:11]
m v[24:27],a[212:215],a[92:95],v[24:27]
m v[12:15],a[216:219],a[36:39],v[12:15]
m v[28:31],a[216:219],a[92:95],v[28:31]
s_waitcnt lgkmcnt(0)
s_waitcnt vmcnt(20)
s_barrier
m v[32:35],a[220:223],a[36:39],v[32:35]
r a[40:43],v212,20480
r a[96:99],v212,22528
m v[48:51],a[220:223],a[92:95],v[48:51]
t a[204:205],v204,0
t a[206:207],v204,512
m v[36:39],a[224:227],a[36:39],v[36:39]
t a[208:209],v205,0
t a[210:211],v205,512
m v[52:55],a[224:227],a[92:95],v[52:55]
t a[212:213],v206,0
t a[214:215],v206,512
m v[40:43],a[228:231],a[36:39],v[40:43]
t a[216:217],v207,0
t a[218:219],v207,512
m v[56:59],a[228:231],a[92:95],v[56:59]
m v[44:47],a[232:235],a[36:39],v[44:47]
m v[60:63],a[232:235],a[92:95],v[60:63]
s_waitcnt lgkmcnt(0)
m v[0:3],a[204:207],a[40:43],v[0:3]
t a[220:221],v204,4096
t a[222:223],v204,4608
t a[224:225],v205,4096
t a[226:227],v205,4608
t a[228:229],v206,4096
m v[16:19],a[204:207],a[96:99],v[16:19]
t a[230:231],v206,4608
t a[232:233],v207,4096
t a[234:235],v207,4608
s_mov_b32 m0,0
s_add_u32 s45,s57,0x4000
m v[4:7],a[208:211],a[40:43],v[4:7]
s_and_b32 s45,s45,0x7ffff
dn v128,%2,s45,0
m v[20:23],a[208:211],a[96:99],v[20:23]
dn v129,%2,s45,1024
m v[8:11],a[212:215],a[40:43],v[8:11]
dn v130,%2,s45,2048
m v[24:27],a[212:215],a[96:99],v[24:27]
dn v131,%2,s45,3072
s_mov_b32 m0,0x1000
s_add_u32 s45,s57,0x5000
m v[12:15],a[216:219],a[40:43],v[12:15]
s_and_b32 s45,s45,0x7ffff
dn v132,%2,s45,0
m v[28:31],a[216:219],a[96:99],v[28:31]
dn v133,%2,s45,1024
s_waitcnt lgkmcnt(0)
m v[32:35],a[220:223],a[40:43],v[32:35]
r a[44:47],v213,20480
r a[100:103],v213,22528
t a[204:205],v204,8192
t a[206:207],v204,8704
t a[208:209],v205,8192
m v[48:51],a[220:223],a[96:99],v[48:51]
t a[210:211],v205,8704
t a[212:213],v206,8192
t a[214:215],v206,8704
t a[216:217],v207,8192
t a[218:219],v207,8704
m v[36:39],a[224:227],a[40:43],v[36:39]
dn v134,%2,s45,2048
m v[52:55],a[224:227],a[96:99],v[52:55]
dn v135,%2,s45,3072
s_mov_b32 m0,0x2000
s_add_u32 s45,s57,0x6000
m v[40:43],a[228:231],a[40:43],v[40:43]
s_and_b32 s45,s45,0x7ffff
dn v128,%2,s45,0
m v[56:59],a[228:231],a[96:99],v[56:59]
dn v129,%2,s45,1024
m v[44:47],a[232:235],a[40:43],v[44:47]
dn v130,%2,s45,2048
m v[60:63],a[232:235],a[96:99],v[60:63]
dn v131,%2,s45,3072
s_waitcnt lgkmcnt(0)
m v[0:3],a[204:207],a[44:47],v[0:3]
t a[220:221],v204,12288
t a[222:223],v204,12800
m v[16:19],a[204:207],a[100:103],v[16:19]
t a[224:225],v205,12288
t a[226:227],v205,12800
m v[4:7],a[208:211],a[44:47],v[4:7]
t a[228:229],v206,12288
t a[230:231],v206,12800
m v[20:23],a[208:211],a[100:103],v[20:23]
t a[232:233],v207,12288
t a[234:235],v207,12800
m v[8:11],a[212:215],a[44:47],v[8:11]
m v[24:27],a[212:215],a[100:103],v[24:27]
m v[12:15],a[216:219],a[44:47],v[12:15]
m v[28:31],a[216:219],a[100:103],v[28:31]
s_waitcnt lgkmcnt(0)
s_waitcnt vmcnt(12)
s_barrier
r a[48:51],v212,40960
r a[104:107],v212,43008
t a[204:205],v204,16384
t a[206:207],v204,16896
t a[208:209],v205,16384
t a[210:211],v205,16896
t a[212:213],v206,16384
t a[214:215],v206,16896
t a[216:217],v207,16384
t a[218:219],v207,16896
buffer_load_dwordx2 v[150:151],v152,%5,%12 offen sc1
m v[32:35],a[220:223],a[44:47],v[32:35]
m v[48:51],a[220:223],a[100:103],v[48:51]
m v[36:39],a[224:227],a[44:47],v[36:39]
m v[52:55],a[224:227],a[100:103],v[52:55]
m v[40:43],a[228:231],a[44:47],v[40:43]
m v[56:59],a[228:231],a[100:103],v[56:59]
m v[44:47],a[232:235],a[44:47],v[44:47]
m v[60:63],a[232:235],a[100:103],v[60:63]
s_waitcnt lgkmcnt(0)
m v[0:3],a[204:207],a[48:51],v[0:3]
t a[220:221],v204,20480
t a[222:223],v204,20992
t a[224:225],v205,20480
t a[226:227],v205,20992
t a[228:229],v206,20480
m v[16:19],a[204:207],a[104:107],v[16:19]
t a[230:231],v206,20992
t a[232:233],v207,20480
t a[234:235],v207,20992
s_mov_b32 m0,0x3000
s_add_u32 s45,s57,0x7000
m v[4:7],a[208:211],a[48:51],v[4:7]
s_and_b32 s45,s45,0x7ffff
dn v132,%2,s45,0
m v[20:23],a[208:211],a[104:107],v[20:23]
dn v133,%2,s45,1024
m v[8:11],a[212:215],a[48:51],v[8:11]
dn v134,%2,s45,2048
m v[24:27],a[212:215],a[104:107],v[24:27]
dn v135,%2,s45,3072
s_mov_b32 m0,0x4000
s_add_u32 s45,s57,0
m v[12:15],a[216:219],a[48:51],v[12:15]
s_and_b32 s45,s45,0x7ffff
dn v136,%3,s45,0
m v[28:31],a[216:219],a[104:107],v[28:31]
dn v137,%3,s45,1024
s_waitcnt lgkmcnt(0)
m v[32:35],a[220:223],a[48:51],v[32:35]
r a[52:55],v213,40960
r a[108:111],v213,43008
t a[204:205],v204,24576
t a[206:207],v204,25088
t a[208:209],v205,24576
m v[48:51],a[220:223],a[104:107],v[48:51]
t a[210:211],v205,25088
t a[212:213],v206,24576
t a[214:215],v206,25088
t a[216:217],v207,24576
t a[218:219],v207,25088
m v[36:39],a[224:227],a[48:51],v[36:39]
dn v138,%3,s45,2048
m v[52:55],a[224:227],a[104:107],v[52:55]
dn v139,%3,s45,3072
s_mov_b32 m0,0x5000
s_add_u32 s45,s57,0x1000
m v[40:43],a[228:231],a[48:51],v[40:43]
s_and_b32 s45,s45,0x7ffff
dn v136,%3,s45,0
m v[56:59],a[228:231],a[104:107],v[56:59]
dn v137,%3,s45,1024
m v[44:47],a[232:235],a[48:51],v[44:47]
dn v138,%3,s45,2048
m v[60:63],a[232:235],a[104:107],v[60:63]
dn v139,%3,s45,3072
s_waitcnt lgkmcnt(0)
m v[0:3],a[204:207],a[52:55],v[0:3]
t a[220:221],v204,28672
t a[222:223],v204,29184
m v[16:19],a[204:207],a[108:111],v[16:19]
t a[224:225],v205,28672
t a[226:227],v205,29184
m v[4:7],a[208:211],a[52:55],v[4:7]
t a[228:229],v206,28672
t a[230:231],v206,29184
m v[20:23],a[208:211],a[108:111],v[20:23]
t a[232:233],v207,28672
t a[234:235],v207,29184
m v[8:11],a[212:215],a[52:55],v[8:11]
m v[24:27],a[212:215],a[108:111],v[24:27]
m v[12:15],a[216:219],a[52:55],v[12:15]
m v[28:31],a[216:219],a[108:111],v[28:31]
s_waitcnt lgkmcnt(0)
s_barrier
m v[32:35],a[220:223],a[52:55],v[32:35]
t a[204:205],v204,49152
t a[206:207],v204,49664
m v[48:51],a[220:223],a[108:111],v[48:51]
t a[208:209],v205,49152
t a[210:211],v205,49664
m v[36:39],a[224:227],a[52:55],v[36:39]
t a[212:213],v206,49152
t a[214:215],v206,49664
m v[52:55],a[224:227],a[108:111],v[52:55]
t a[216:217],v207,49152
t a[218:219],v207,49664
m v[40:43],a[228:231],a[52:55],v[40:43]
m v[56:59],a[228:231],a[108:111],v[56:59]
m v[44:47],a[232:235],a[52:55],v[44:47]
m v[60:63],a[232:235],a[108:111],v[60:63]
s_waitcnt lgkmcnt(0)
m v[64:67],a[204:207],a[0:3],0
t a[220:221],v204,53248
t a[222:223],v204,53760
t a[224:225],v205,53248
t a[226:227],v205,53760
t a[228:229],v206,53248
m v[80:83],a[204:207],a[56:59],0
t a[230:231],v206,53760
t a[232:233],v207,53248
t a[234:235],v207,53760
s_mov_b32 m0,0x6000
s_add_u32 s45,s57,0x2000
m v[68:71],a[208:211],a[0:3],0
s_and_b32 s45,s45,0x7ffff
dn v136,%3,s45,0
m v[84:87],a[208:211],a[56:59],0
dn v137,%3,s45,1024
m v[72:75],a[212:215],a[0:3],0
dn v138,%3,s45,2048
m v[88:91],a[212:215],a[56:59],0
dn v139,%3,s45,3072
s_mov_b32 m0,0x7000
s_add_u32 s45,s57,0x3000
m v[76:79],a[216:219],a[0:3],0
s_and_b32 s45,s45,0x7ffff
dn v136,%3,s45,0
m v[92:95],a[216:219],a[56:59],0
dn v137,%3,s45,1024
s_waitcnt lgkmcnt(0)
m v[64:67],a[220:223],a[4:7],v[64:67]
t a[204:205],v204,57344
t a[206:207],v204,57856
t a[208:209],v205,57344
t a[210:211],v205,57856
t a[212:213],v206,57344
t a[214:215],v206,57856
m v[80:83],a[220:223],a[60:63],v[80:83]
t a[216:217],v207,57344
t a[218:219],v207,57856
dn v138,%3,s45,2048
m v[68:71],a[224:227],a[4:7],v[68:71]
dn v139,%3,s45,3072
s_mov_b32 m0,0x8000
s_add_u32 s45,s57,0x8000
m v[84:87],a[224:227],a[60:63],v[84:87]
s_and_b32 s45,s45,0x7ffff
dn v128,%2,s45,0
m v[72:75],a[228:231],a[4:7],v[72:75]
dn v129,%2,s45,1024
m v[88:91],a[228:231],a[60:63],v[88:91]
dn v130,%2,s45,2048
m v[76:79],a[232:235],a[4:7],v[76:79]
dn v131,%2,s45,3072
v_mul_f32 v0,s47,v0
v_mul_f32 v1,s47,v1
m v[92:95],a[232:235],a[60:63],v[92:95]
v_mul_f32 v2,s47,v2
v_mul_f32 v3,s47,v3
v_mul_f32 v4,s47,v4
v_mul_f32 v5,s47,v5
v_mul_f32 v6,s47,v6
v_mul_f32 v7,s47,v7
cv v0,v0,v1
s_waitcnt lgkmcnt(0)
m v[64:67],a[204:207],a[8:11],v[64:67]
t a[220:221],v204,61440
t a[222:223],v204,61952
m v[80:83],a[204:207],a[64:67],v[80:83]
t a[224:225],v205,61440
t a[226:227],v205,61952
m v[68:71],a[208:211],a[8:11],v[68:71]
t a[228:229],v206,61440
t a[230:231],v206,61952
m v[84:87],a[208:211],a[64:67],v[84:87]
t a[232:233],v207,61440
t a[234:235],v207,61952
cv v1,v2,v3
m v[72:75],a[212:215],a[8:11],v[72:75]
cv v2,v4,v5
cv v3,v6,v7
m v[88:91],a[212:215],a[64:67],v[88:91]
v_mul_f32 v8,s47,v8
v_mul_f32 v9,s47,v9
m v[76:79],a[216:219],a[8:11],v[76:79]
v_mul_f32 v10,s47,v10
v_mul_f32 v11,s47,v11
m v[92:95],a[216:219],a[64:67],v[92:95]
v_mul_f32 v12,s47,v12
v_mul_f32 v13,s47,v13
v_mul_f32 v14,s47,v14
s_waitcnt lgkmcnt(0)
s_barrier
m v[64:67],a[220:223],a[12:15],v[64:67]
t a[204:205],v204,0
t a[206:207],v204,512
m v[80:83],a[220:223],a[68:71],v[80:83]
t a[208:209],v205,0
t a[210:211],v205,512
m v[68:71],a[224:227],a[12:15],v[68:71]
t a[212:213],v206,0
t a[214:215],v206,512
m v[84:87],a[224:227],a[68:71],v[84:87]
t a[216:217],v207,0
t a[218:219],v207,512
v_mul_f32 v15,s47,v15
m v[72:75],a[228:231],a[12:15],v[72:75]
cv v8,v8,v9
cv v9,v10,v11
m v[88:91],a[228:231],a[68:71],v[88:91]
cv v10,v12,v13
cv v11,v14,v15
m v[76:79],a[232:235],a[12:15],v[76:79]
v_mul_f32 v16,s47,v16
v_mul_f32 v17,s47,v17
m v[92:95],a[232:235],a[68:71],v[92:95]
v_mul_f32 v18,s47,v18
v_mul_f32 v19,s47,v19
v_mul_f32 v20,s47,v20
s_waitcnt lgkmcnt(0)
m v[64:67],a[204:207],a[16:19],v[64:67]
t a[220:221],v204,4096
t a[222:223],v204,4608
t a[224:225],v205,4096
t a[226:227],v205,4608
t a[228:229],v206,4096
t a[230:231],v206,4608
t a[232:233],v207,4096
m v[80:83],a[204:207],a[72:75],v[80:83]
t a[234:235],v207,4608
s_mov_b32 m0,0x9000
s_add_u32 s45,s57,0x9000
s_and_b32 s45,s45,0x7ffff
m v[68:71],a[208:211],a[16:19],v[68:71]
dn v132,%2,s45,0
m v[84:87],a[208:211],a[72:75],v[84:87]
dn v133,%2,s45,1024
dn v134,%2,s45,2048
m v[72:75],a[212:215],a[16:19],v[72:75]
dn v135,%2,s45,3072
s_mov_b32 m0,0xa000
s_add_u32 s45,s57,0xa000
s_and_b32 s45,s45,0x7ffff
m v[88:91],a[212:215],a[72:75],v[88:91]
dn v128,%2,s45,0
m v[76:79],a[216:219],a[16:19],v[76:79]
dn v129,%2,s45,1024
v_mul_f32 v21,s47,v21
v_mul_f32 v22,s47,v22
v_mul_f32 v23,s47,v23
m v[92:95],a[216:219],a[72:75],v[92:95]
cv v16,v16,v17
cv v17,v18,v19
cv v18,v20,v21
cv v19,v22,v23
v_mul_f32 v24,s47,v24
v_mul_f32 v25,s47,v25
v_mul_f32 v26,s47,v26
s_waitcnt lgkmcnt(0)
m v[64:67],a[220:223],a[20:23],v[64:67]
t a[204:205],v204,8192
t a[206:207],v204,8704
t a[208:209],v205,8192
t a[210:211],v205,8704
t a[212:213],v206,8192
t a[214:215],v206,8704
m v[80:83],a[220:223],a[76:79],v[80:83]
t a[216:217],v207,8192
t a[218:219],v207,8704
dn v130,%2,s45,2048
m v[68:71],a[224:227],a[20:23],v[68:71]
dn v131,%2,s45,3072
s_mov_b32 m0,0xb000
s_add_u32 s45,s57,0xb000
m v[84:87],a[224:227],a[76:79],v[84:87]
s_and_b32 s45,s45,0x7ffff
dn v132,%2,s45,0
m v[72:75],a[228:231],a[20:23],v[72:75]
dn v133,%2,s45,1024
m v[88:91],a[228:231],a[76:79],v[88:91]
dn v134,%2,s45,2048
m v[76:79],a[232:235],a[20:23],v[76:79]
dn v135,%2,s45,3072
v_mul_f32 v27,s47,v27
v_mul_f32 v28,s47,v28
v_mul_f32 v29,s47,v29
m v[92:95],a[232:235],a[76:79],v[92:95]
v_mul_f32 v30,s47,v30
v_mul_f32 v31,s47,v31
cv v24,v24,v25
cv v25,v26,v27
cv v26,v28,v29
cv v27,v30,v31
v_permlane32_swap_b32 v0,v2
s_waitcnt lgkmcnt(0)
m v[64:67],a[204:207],a[24:27],v[64:67]
t a[220:221],v204,12288
t a[222:223],v204,12800
m v[80:83],a[204:207],a[80:83],v[80:83]
t a[224:225],v205,12288
t a[226:227],v205,12800
m v[68:71],a[208:211],a[24:27],v[68:71]
t a[228:229],v206,12288
t a[230:231],v206,12800
m v[84:87],a[208:211],a[80:83],v[84:87]
t a[232:233],v207,12288
t a[234:235],v207,12800
v_permlane32_swap_b32 v1,v3
m v[72:75],a[212:215],a[24:27],v[72:75]
v_permlane32_swap_b32 v8,v10
v_permlane32_swap_b32 v9,v11
m v[88:91],a[212:215],a[80:83],v[88:91]
v_permlane32_swap_b32 v16,v18
v_permlane32_swap_b32 v17,v19
m v[76:79],a[216:219],a[24:27],v[76:79]
v_permlane32_swap_b32 v24,v26
v_permlane32_swap_b32 v25,v27
m v[92:95],a[216:219],a[80:83],v[92:95]
v_permlane16_swap_b32 v0,v2
v_permlane16_swap_b32 v1,v3
v_permlane16_swap_b32 v8,v10
s_waitcnt lgkmcnt(0)
s_barrier
s_waitcnt vmcnt(36)
t a[204:205],v204,16384
t a[206:207],v204,16896
t a[208:209],v205,16384
t a[210:211],v205,16896
t a[212:213],v206,16384
t a[214:215],v206,16896
t a[216:217],v207,16384
t a[218:219],v207,16896
v_mov_b32 v149,v150
buffer_store_dwordx2 v[148:149],v153,%5,%12 offen sc1
v_sub_u32 v155,v151,v148
v_cmp_lt_u32_e32 vcc,7,v155
s_nop 0
s_cmp_eq_u64 vcc,0
s_cselect_b32 s58,1,0
m v[64:67],a[220:223],a[28:31],v[64:67]
v_permlane16_swap_b32 v9,v11
v_permlane16_swap_b32 v16,v18
m v[80:83],a[220:223],a[84:87],v[80:83]
v_permlane16_swap_b32 v17,v19
v_permlane16_swap_b32 v24,v26
m v[68:71],a[224:227],a[28:31],v[68:71]
v_permlane16_swap_b32 v25,v27
v_mul_f32 v32,s47,v32
m v[84:87],a[224:227],a[84:87],v[84:87]
v_mul_f32 v33,s47,v33
v_mul_f32 v34,s47,v34
m v[72:75],a[228:231],a[28:31],v[72:75]
v_mul_f32 v35,s47,v35
m v[88:91],a[228:231],a[84:87],v[88:91]
m v[76:79],a[232:235],a[28:31],v[76:79]
m v[92:95],a[232:235],a[84:87],v[92:95]
s_waitcnt lgkmcnt(0)
m v[64:67],a[204:207],a[32:35],v[64:67]
t a[220:221],v204,20480
t a[222:223],v204,20992
t a[224:225],v205,20480
t a[226:227],v205,20992
t a[228:229],v206,20480
t a[230:231],v206,20992
t a[232:233],v207,20480
m v[80:83],a[204:207],a[88:91],v[80:83]
t a[234:235],v207,20992
s_mov_b32 m0,0xc000
s_add_u32 s45,s57,0x4000
s_and_b32 s45,s45,0x7ffff
m v[68:71],a[208:211],a[32:35],v[68:71]
dn v136,%3,s45,0
m v[84:87],a[208:211],a[88:91],v[84:87]
dn v137,%3,s45,1024
dn v138,%3,s45,2048
m v[72:75],a[212:215],a[32:35],v[72:75]
dn v139,%3,s45,3072
s_mov_b32 m0,0xd000
s_add_u32 s45,s57,0x5000
s_and_b32 s45,s45,0x7ffff
m v[88:91],a[212:215],a[88:91],v[88:91]
dn v136,%3,s45,0
m v[76:79],a[216:219],a[32:35],v[76:79]
dn v137,%3,s45,1024
v_mul_f32 v36,s47,v36
v_mul_f32 v37,s47,v37
v_mul_f32 v38,s47,v38
m v[92:95],a[216:219],a[88:91],v[92:95]
v_mul_f32 v39,s47,v39
cv v32,v32,v33
cv v33,v34,v35
cv v34,v36,v37
cv v35,v38,v39
v_mul_f32 v40,s47,v40
v_mul_f32 v41,s47,v41
s_waitcnt lgkmcnt(0)
m v[64:67],a[220:223],a[36:39],v[64:67]
t a[204:205],v204,24576
t a[206:207],v204,25088
t a[208:209],v205,24576
t a[210:211],v205,25088
t a[212:213],v206,24576
t a[214:215],v206,25088
m v[80:83],a[220:223],a[92:95],v[80:83]
t a[216:217],v207,24576
t a[218:219],v207,25088
dn v138,%3,s45,2048
m v[68:71],a[224:227],a[36:39],v[68:71]
dn v139,%3,s45,3072
s_mov_b32 m0,0xe000
s_add_u32 s45,s57,0x6000
m v[84:87],a[224:227],a[92:95],v[84:87]
s_and_b32 s45,s45,0x7ffff
dn v136,%3,s45,0
m v[72:75],a[228:231],a[36:39],v[72:75]
dn v137,%3,s45,1024
m v[88:91],a[228:231],a[92:95],v[88:91]
dn v138,%3,s45,2048
m v[76:79],a[232:235],a[36:39],v[76:79]
dn v139,%3,s45,3072
v_mul_f32 v42,s47,v42
v_mul_f32 v43,s47,v43
v_mul_f32 v44,s47,v44
m v[92:95],a[232:235],a[92:95],v[92:95]
v_mul_f32 v45,s47,v45
v_mul_f32 v46,s47,v46
v_mul_f32 v47,s47,v47
cv v40,v40,v41
cv v41,v42,v43
cv v42,v44,v45
cv v43,v46,v47
s_waitcnt lgkmcnt(0)
m v[64:67],a[204:207],a[40:43],v[64:67]
t a[220:221],v204,28672
t a[222:223],v204,29184
m v[80:83],a[204:207],a[96:99],v[80:83]
t a[224:225],v205,28672
t a[226:227],v205,29184
m v[68:71],a[208:211],a[40:43],v[68:71]
t a[228:229],v206,28672
t a[230:231],v206,29184
m v[84:87],a[208:211],a[96:99],v[84:87]
t a[232:233],v207,28672
t a[234:235],v207,29184
v_mul_f32 v48,s47,v48
m v[72:75],a[212:215],a[40:43],v[72:75]
v_mul_f32 v49,s47,v49
v_mul_f32 v50,s47,v50
m v[88:91],a[212:215],a[96:99],v[88:91]
v_mul_f32 v51,s47,v51
v_mul_f32 v52,s47,v52
m v[76:79],a[216:219],a[40:43],v[76:79]
v_mul_f32 v53,s47,v53
v_mul_f32 v54,s47,v54
m v[92:95],a[216:219],a[96:99],v[92:95]
v_mul_f32 v55,s47,v55
cv v48,v48,v49
cv v49,v50,v51
s_waitcnt lgkmcnt(0)
s_barrier
m v[64:67],a[220:223],a[44:47],v[64:67]
t a[204:205],v204,49152
t a[206:207],v204,49664
m v[80:83],a[220:223],a[100:103],v[80:83]
t a[208:209],v205,49152
t a[210:211],v205,49664
m v[68:71],a[224:227],a[44:47],v[68:71]
t a[212:213],v206,49152
t a[214:215],v206,49664
m v[84:87],a[224:227],a[100:103],v[84:87]
t a[216:217],v207,49152
t a[218:219],v207,49664
cv v50,v52,v53
m v[72:75],a[228:231],a[44:47],v[72:75]
cv v51,v54,v55
v_mul_f32 v56,s47,v56
m v[88:91],a[228:231],a[100:103],v[88:91]
v_mul_f32 v57,s47,v57
v_mul_f32 v58,s47,v58
m v[76:79],a[232:235],a[44:47],v[76:79]
v_mul_f32 v59,s47,v59
v_mul_f32 v60,s47,v60
m v[92:95],a[232:235],a[100:103],v[92:95]
v_mul_f32 v61,s47,v61
v_mul_f32 v62,s47,v62
v_mul_f32 v63,s47,v63
s_waitcnt lgkmcnt(0)
m v[64:67],a[204:207],a[48:51],v[64:67]
t a[220:221],v204,53248
t a[222:223],v204,53760
t a[224:225],v205,53248
t a[226:227],v205,53760
t a[228:229],v206,53248
t a[230:231],v206,53760
t a[232:233],v207,53248
m v[80:83],a[204:207],a[104:107],v[80:83]
t a[234:235],v207,53760
s_mov_b32 m0,0xf000
s_add_u32 s45,s57,0x7000
s_and_b32 s45,s45,0x7ffff
m v[68:71],a[208:211],a[48:51],v[68:71]
dn v136,%3,s45,0
m v[84:87],a[208:211],a[104:107],v[84:87]
dn v137,%3,s45,1024
dn v138,%3,s45,2048
m v[72:75],a[212:215],a[48:51],v[72:75]
dn v139,%3,s45,3072
s_mov_b32 m0,0x10000
s_add_u32 s45,s57,0xc000
s_and_b32 s45,s45,0x7ffff
m v[88:91],a[212:215],a[104:107],v[88:91]
dn v128,%2,s45,0
m v[76:79],a[216:219],a[48:51],v[76:79]
dn v129,%2,s45,1024
cv v56,v56,v57
cv v57,v58,v59
cv v58,v60,v61
m v[92:95],a[216:219],a[104:107],v[92:95]
cv v59,v62,v63
v_permlane32_swap_b32 v32,v34
v_permlane32_swap_b32 v33,v35
v_permlane32_swap_b32 v40,v42
v_permlane32_swap_b32 v41,v43
v_permlane32_swap_b32 v48,v50
v_permlane32_swap_b32 v49,v51
s_waitcnt lgkmcnt(0)
m v[64:67],a[220:223],a[52:55],v[64:67]
t a[204:205],v204,57344
t a[206:207],v204,57856
t a[208:209],v205,57344
t a[210:211],v205,57856
t a[212:213],v206,57344
t a[214:215],v206,57856
m v[80:83],a[220:223],a[108:111],v[80:83]
t a[216:217],v207,57344
t a[218:219],v207,57856
dn v130,%2,s45,2048
m v[68:71],a[224:227],a[52:55],v[68:71]
dn v131,%2,s45,3072
s_mov_b32 m0,0x11000
s_add_u32 s45,s57,0xd000
m v[84:87],a[224:227],a[108:111],v[84:87]
s_and_b32 s45,s45,0x7ffff
dn v132,%2,s45,0
m v[72:75],a[228:231],a[52:55],v[72:75]
dn v133,%2,s45,1024
m v[88:91],a[228:231],a[108:111],v[88:91]
dn v134,%2,s45,2048
m v[76:79],a[232:235],a[52:55],v[76:79]
dn v135,%2,s45,3072
v_permlane32_swap_b32 v56,v58
v_permlane32_swap_b32 v57,v59
v_permlane16_swap_b32 v32,v34
m v[92:95],a[232:235],a[108:111],v[92:95]
v_permlane16_swap_b32 v33,v35
v_permlane16_swap_b32 v40,v42
v_permlane16_swap_b32 v41,v43
v_permlane16_swap_b32 v48,v50
v_permlane16_swap_b32 v49,v51
v_permlane16_swap_b32 v56,v58
v_permlane16_swap_b32 v57,v59
s_waitcnt lgkmcnt(0)
m v[96:99],a[204:207],a[0:3],0
t a[220:221],v204,61440
t a[222:223],v204,61952
m v[112:115],a[204:207],a[56:59],0
t a[224:225],v205,61440
t a[226:227],v205,61952
m v[100:103],a[208:211],a[0:3],0
t a[228:229],v206,61440
t a[230:231],v206,61952
m v[116:119],a[208:211],a[56:59],0
t a[232:233],v207,61440
t a[234:235],v207,61952
m v[104:107],a[212:215],a[0:3],0
m v[120:123],a[212:215],a[56:59],0
m v[108:111],a[216:219],a[0:3],0
m v[124:127],a[216:219],a[56:59],0
s_waitcnt lgkmcnt(0)
s_barrier
m v[96:99],a[220:223],a[4:7],v[96:99]
t a[204:205],v204,0
t a[206:207],v204,512
m v[112:115],a[220:223],a[60:63],v[112:115]
t a[208:209],v205,0
t a[210:211],v205,512
m v[100:103],a[224:227],a[4:7],v[100:103]
t a[212:213],v206,0
t a[214:215],v206,512
m v[116:119],a[224:227],a[60:63],v[116:119]
t a[216:217],v207,0
t a[218:219],v207,512
m v[104:107],a[228:231],a[4:7],v[104:107]
v_mul_f32 v64,s47,v64
v_mul_f32 v65,s47,v65
m v[120:123],a[228:231],a[60:63],v[120:123]
v_mul_f32 v66,s47,v66
v_mul_f32 v67,s47,v67
m v[108:111],a[232:235],a[4:7],v[108:111]
m v[124:127],a[232:235],a[60:63],v[124:127]
s_waitcnt lgkmcnt(0)
m v[96:99],a[204:207],a[8:11],v[96:99]
t a[220:221],v204,4096
t a[222:223],v204,4608
t a[224:225],v205,4096
t a[226:227],v205,4608
t a[228:229],v206,4096
t a[230:231],v206,4608
t a[232:233],v207,4096
m v[112:115],a[204:207],a[64:67],v[112:115]
t a[234:235],v207,4608
s_mov_b32 m0,0x12000
s_add_u32 s45,s57,0xe000
s_and_b32 s45,s45,0x7ffff
m v[100:103],a[208:211],a[8:11],v[100:103]
dn v128,%2,s45,0
m v[116:119],a[208:211],a[64:67],v[116:119]
dn v129,%2,s45,1024
dn v130,%2,s45,2048
m v[104:107],a[212:215],a[8:11],v[104:107]
dn v131,%2,s45,3072
s_mov_b32 m0,0x13000
s_add_u32 s45,s57,0xf000
s_and_b32 s45,s45,0x7ffff
m v[120:123],a[212:215],a[64:67],v[120:123]
dn v132,%2,s45,0
m v[108:111],a[216:219],a[8:11],v[108:111]
dn v133,%2,s45,1024
v_mul_f32 v68,s47,v68
v_mul_f32 v69,s47,v69
v_mul_f32 v70,s47,v70
m v[124:127],a[216:219],a[64:67],v[124:127]
v_mul_f32 v71,s47,v71
cv v64,v64,v65
s_waitcnt lgkmcnt(0)
m v[96:99],a[220:223],a[12:15],v[96:99]
t a[204:205],v204,8192
t a[206:207],v204,8704
t a[208:209],v205,8192
t a[210:211],v205,8704
t a[212:213],v206,8192
t a[214:215],v206,8704
m v[112:115],a[220:223],a[68:71],v[112:115]
t a[216:217],v207,8192
t a[218:219],v207,8704
dn v134,%2,s45,2048
m v[100:103],a[224:227],a[12:15],v[100:103]
dn v135,%2,s45,3072
s_mov_b32 m0,0x14000
s_add_u32 s45,s57,0x8000
m v[116:119],a[224:227],a[68:71],v[116:119]
s_and_b32 s45,s45,0x7ffff
dn v136,%3,s45,0
m v[104:107],a[228:231],a[12:15],v[104:107]
dn v137,%3,s45,1024
m v[120:123],a[228:231],a[68:71],v[120:123]
dn v138,%3,s45,2048
m v[108:111],a[232:235],a[12:15],v[108:111]
dn v139,%3,s45,3072
cv v65,v66,v67
cv v66,v68,v69
m v[124:127],a[232:235],a[68:71],v[124:127]
cv v67,v70,v71
v_mul_f32 v72,s47,v72
v_mul_f32 v73,s47,v73
s_waitcnt lgkmcnt(0)
m v[96:99],a[204:207],a[16:19],v[96:99]
t a[220:221],v204,12288
t a[222:223],v204,12800
m v[112:115],a[204:207],a[72:75],v[112:115]
t a[224:225],v205,12288
t a[226:227],v205,12800
m v[100:103],a[208:211],a[16:19],v[100:103]
t a[228:229],v206,12288
t a[230:231],v206,12800
m v[116:119],a[208:211],a[72:75],v[116:119]
t a[232:233],v207,12288
t a[234:235],v207,12800
m v[104:107],a[212:215],a[16:19],v[104:107]
v_mul_f32 v74,s47,v74
v_mul_f32 v75,s47,v75
m v[120:123],a[212:215],a[72:75],v[120:123]
v_mul_f32 v76,s47,v76
v_mul_f32 v77,s47,v77
m v[108:111],a[216:219],a[16:19],v[108:111]
v_mul_f32 v78,s47,v78
m v[124:127],a[216:219],a[72:75],v[124:127]
s_waitcnt lgkmcnt(0)
s_barrier
t a[204:205],v204,16384
t a[206:207],v204,16896
t a[208:209],v205,16384
t a[210:211],v205,16896
t a[212:213],v206,16384
t a[214:215],v206,16896
t a[216:217],v207,16384
t a[218:219],v207,16896
buffer_load_dwordx2 v[150:151],v152,%5,%12 offen sc1
m v[96:99],a[220:223],a[20:23],v[96:99]
v_mul_f32 v79,s47,v79
cv v72,v72,v73
m v[112:115],a[220:223],a[76:79],v[112:115]
cv v73,v74,v75
cv v74,v76,v77
m v[100:103],a[224:227],a[20:23],v[100:103]
cv v75,v78,v79
m v[116:119],a[224:227],a[76:79],v[116:119]
m v[104:107],a[228:231],a[20:23],v[104:107]
m v[120:123],a[228:231],a[76:79],v[120:123]
m v[108:111],a[232:235],a[20:23],v[108:111]
m v[124:127],a[232:235],a[76:79],v[124:127]
s_waitcnt lgkmcnt(0)
m v[96:99],a[204:207],a[24:27],v[96:99]
t a[220:221],v204,20480
t a[222:223],v204,20992
t a[224:225],v205,20480
t a[226:227],v205,20992
t a[228:229],v206,20480
t a[230:231],v206,20992
t a[232:233],v207,20480
m v[112:115],a[204:207],a[80:83],v[112:115]
t a[234:235],v207,20992
s_mov_b32 m0,0x15000
s_add_u32 s45,s57,0x9000
s_and_b32 s45,s45,0x7ffff
m v[100:103],a[208:211],a[24:27],v[100:103]
dn v136,%3,s45,0
m v[116:119],a[208:211],a[80:83],v[116:119]
dn v137,%3,s45,1024
dn v138,%3,s45,2048
m v[104:107],a[212:215],a[24:27],v[104:107]
dn v139,%3,s45,3072
s_mov_b32 m0,0x16000
s_add_u32 s45,s57,0xa000
s_and_b32 s45,s45,0x7ffff
m v[120:123],a[212:215],a[80:83],v[120:123]
dn v136,%3,s45,0
m v[108:111],a[216:219],a[24:27],v[108:111]
dn v137,%3,s45,1024
v_mul_f32 v80,s47,v80
v_mul_f32 v81,s47,v81
v_mul_f32 v82,s47,v82
m v[124:127],a[216:219],a[80:83],v[124:127]
v_mul_f32 v83,s47,v83
v_mul_f32 v84,s47,v84
s_waitcnt lgkmcnt(0)
m v[96:99],a[220:223],a[28:31],v[96:99]
t a[204:205],v204,24576
t a[206:207],v204,25088
t a[208:209],v205,24576
t a[210:211],v205,25088
t a[212:213],v206,24576
t a[214:215],v206,25088
m v[112:115],a[220:223],a[84:87],v[112:115]
t a[216:217],v207,24576
t a[218:219],v207,25088
dn v138,%3,s45,2048
m v[100:103],a[224:227],a[28:31],v[100:103]
dn v139,%3,s45,3072
s_mov_b32 m0,0x17000
s_add_u32 s45,s57,0xb000
m v[116:119],a[224:227],a[84:87],v[116:119]
s_and_b32 s45,s45,0x7ffff
dn v136,%3,s45,0
m v[104:107],a[228:231],a[28:31],v[104:107]
dn v137,%3,s45,1024
m v[120:123],a[228:231],a[84:87],v[120:123]
dn v138,%3,s45,2048
m v[108:111],a[232:235],a[28:31],v[108:111]
dn v139,%3,s45,3072
v_mul_f32 v85,s47,v85
v_mul_f32 v86,s47,v86
m v[124:127],a[232:235],a[84:87],v[124:127]
v_mul_f32 v87,s47,v87
cv v80,v80,v81
cv v81,v82,v83
s_waitcnt lgkmcnt(0)
m v[96:99],a[204:207],a[32:35],v[96:99]
t a[220:221],v204,28672
t a[222:223],v204,29184
m v[112:115],a[204:207],a[88:91],v[112:115]
t a[224:225],v205,28672
t a[226:227],v205,29184
m v[100:103],a[208:211],a[32:35],v[100:103]
t a[228:229],v206,28672
t a[230:231],v206,29184
m v[116:119],a[208:211],a[88:91],v[116:119]
t a[232:233],v207,28672
t a[234:235],v207,29184
m v[104:107],a[212:215],a[32:35],v[104:107]
cv v82,v84,v85
cv v83,v86,v87
m v[120:123],a[212:215],a[88:91],v[120:123]
v_mul_f32 v88,s47,v88
v_mul_f32 v89,s47,v89
m v[108:111],a[216:219],a[32:35],v[108:111]
v_mul_f32 v90,s47,v90
m v[124:127],a[216:219],a[88:91],v[124:127]
s_waitcnt lgkmcnt(0)
s_barrier
m v[96:99],a[220:223],a[36:39],v[96:99]
t a[204:205],v204,49152
t a[206:207],v204,49664
m v[112:115],a[220:223],a[92:95],v[112:115]
t a[208:209],v205,49152
t a[210:211],v205,49664
m v[100:103],a[224:227],a[36:39],v[100:103]
t a[212:213],v206,49152
t a[214:215],v206,49664
m v[116:119],a[224:227],a[92:95],v[116:119]
t a[216:217],v207,49152
t a[218:219],v207,49664
m v[104:107],a[228:231],a[36:39],v[104:107]
v_mul_f32 v91,s47,v91
v_mul_f32 v92,s47,v92
m v[120:123],a[228:231],a[92:95],v[120:123]
v_mul_f32 v93,s47,v93
v_mul_f32 v94,s47,v94
m v[108:111],a[232:235],a[36:39],v[108:111]
v_mul_f32 v95,s47,v95
m v[124:127],a[232:235],a[92:95],v[124:127]
s_waitcnt lgkmcnt(0)
m v[96:99],a[204:207],a[40:43],v[96:99]
t a[220:221],v204,53248
t a[222:223],v204,53760
t a[224:225],v205,53248
t a[226:227],v205,53760
t a[228:229],v206,53248
t a[230:231],v206,53760
t a[232:233],v207,53248
m v[112:115],a[204:207],a[96:99],v[112:115]
t a[234:235],v207,53760
s_mov_b32 m0,0x1c000
s_add_u32 s45,s57,0xc000
s_and_b32 s45,s45,0x7ffff
dn v136,%3,s45,0
m v[100:103],a[208:211],a[40:43],v[100:103]
dn v137,%3,s45,1024
m v[116:119],a[208:211],a[96:99],v[116:119]
dn v138,%3,s45,2048
dn v139,%3,s45,3072
m v[104:107],a[212:215],a[40:43],v[104:107]
s_mov_b32 m0,0x1d000
s_add_u32 s45,s57,0xd000
s_and_b32 s45,s45,0x7ffff
dn v136,%3,s45,0
m v[120:123],a[212:215],a[96:99],v[120:123]
dn v137,%3,s45,1024
dn v138,%3,s45,2048
m v[108:111],a[216:219],a[40:43],v[108:111]
dn v139,%3,s45,3072
cv v88,v88,v89
cv v89,v90,v91
cv v90,v92,v93
m v[124:127],a[216:219],a[96:99],v[124:127]
cv v91,v94,v95
v_permlane32_swap_b32 v64,v66
s_waitcnt lgkmcnt(0)
m v[96:99],a[220:223],a[44:47],v[96:99]
t a[204:205],v204,57344
t a[206:207],v204,57856
t a[208:209],v205,57344
t a[210:211],v205,57856
t a[212:213],v206,57344
t a[214:215],v206,57856
t a[216:217],v207,57344
m v[112:115],a[220:223],a[100:103],v[112:115]
t a[218:219],v207,57856
s_mov_b32 m0,0x1e000
s_add_u32 s45,s57,0xe000
s_and_b32 s45,s45,0x7ffff
dn v136,%3,s45,0
m v[100:103],a[224:227],a[44:47],v[100:103]
dn v137,%3,s45,1024
m v[116:119],a[224:227],a[100:103],v[116:119]
dn v138,%3,s45,2048
dn v139,%3,s45,3072
m v[104:107],a[228:231],a[44:47],v[104:107]
s_mov_b32 m0,0x1f000
s_add_u32 s45,s57,0xf000
s_and_b32 s45,s45,0x7ffff
dn v136,%3,s45,0
m v[120:123],a[228:231],a[100:103],v[120:123]
dn v137,%3,s45,1024
dn v138,%3,s45,2048
m v[108:111],a[232:235],a[44:47],v[108:111]
dn v139,%3,s45,3072
v_permlane32_swap_b32 v65,v67
v_permlane32_swap_b32 v72,v74
v_permlane32_swap_b32 v73,v75
m v[124:127],a[232:235],a[100:103],v[124:127]
v_permlane32_swap_b32 v80,v82
v_permlane32_swap_b32 v81,v83
s_waitcnt lgkmcnt(0)
m v[96:99],a[204:207],a[48:51],v[96:99]
t a[220:221],v204,61440
t a[222:223],v204,61952
m v[112:115],a[204:207],a[104:107],v[112:115]
t a[224:225],v205,61440
t a[226:227],v205,61952
m v[100:103],a[208:211],a[48:51],v[100:103]
t a[228:229],v206,61440
t a[230:231],v206,61952
m v[116:119],a[208:211],a[104:107],v[116:119]
t a[232:233],v207,61440
t a[234:235],v207,61952
m v[104:107],a[212:215],a[48:51],v[104:107]
v_permlane32_swap_b32 v88,v90
v_permlane32_swap_b32 v89,v91
m v[120:123],a[212:215],a[104:107],v[120:123]
v_permlane16_swap_b32 v64,v66
v_permlane16_swap_b32 v65,v67
m v[108:111],a[216:219],a[48:51],v[108:111]
v_permlane16_swap_b32 v72,v74
m v[124:127],a[216:219],a[104:107],v[124:127]
s_waitcnt lgkmcnt(0)
s_barrier
r a[204:207],v192,0
r a[208:211],v192,8192
r a[212:215],v193,0
r a[216:219],v193,8192
s_mov_b32 m0,0x24000
s_add_u32 s45,s57,0x10000
s_and_b32 s45,s45,0x7ffff
dn v136,%3,s45,0
dn v137,%3,s45,1024
dn v138,%3,s45,2048
dn v139,%3,s45,3072
s_mov_b32 m0,0x25000
s_add_u32 s45,s57,0x11000
s_and_b32 s45,s45,0x7ffff
dn v136,%3,s45,0
dn v137,%3,s45,1024
dn v138,%3,s45,2048
dn v139,%3,s45,3072
s_mov_b32 m0,0x26000
s_add_u32 s45,s57,0x12000
s_and_b32 s45,s45,0x7ffff
dn v136,%3,s45,0
dn v137,%3,s45,1024
dn v138,%3,s45,2048
dn v139,%3,s45,3072
s_mov_b32 m0,0x27000
s_add_u32 s45,s57,0x13000
s_and_b32 s45,s45,0x7ffff
dn v136,%3,s45,0
dn v137,%3,s45,1024
dn v138,%3,s45,2048
dn v139,%3,s45,3072
m v[96:99],a[220:223],a[52:55],v[96:99]
v_permlane16_swap_b32 v73,v75
v_permlane16_swap_b32 v80,v82
m v[112:115],a[220:223],a[108:111],v[112:115]
v_permlane16_swap_b32 v81,v83
v_permlane16_swap_b32 v88,v90
m v[100:103],a[224:227],a[52:55],v[100:103]
v_permlane16_swap_b32 v89,v91
m v[116:119],a[224:227],a[108:111],v[116:119]
m v[104:107],a[228:231],a[52:55],v[104:107]
m v[120:123],a[228:231],a[108:111],v[120:123]
m v[108:111],a[232:235],a[52:55],v[108:111]
m v[124:127],a[232:235],a[108:111],v[124:127]
s_waitcnt lgkmcnt(0)
m v[4:7],a[204:207],v[0:3],0
r a[220:223],v194,0
r a[224:227],v194,8192
r a[228:231],v195,0
m v[20:23],a[204:207],v[16:19],0
r a[232:235],v195,8192
v_mul_f32 v96,s47,v96
v_mul_f32 v97,s47,v97
m v[12:15],a[208:211],v[0:3],0
v_mul_f32 v98,s47,v98
v_mul_f32 v99,s47,v99
v_mul_f32 v100,s47,v100
m v[28:31],a[208:211],v[16:19],0
v_mul_f32 v101,s47,v101
v_mul_f32 v102,s47,v102
v_mul_f32 v103,s47,v103
m v[4:7],a[212:215],v[8:11],v[4:7]
cv v96,v96,v97
cv v97,v98,v99
cv v98,v100,v101
m v[20:23],a[212:215],v[24:27],v[20:23]
cv v99,v102,v103
v_mul_f32 v104,s47,v104
v_mul_f32 v105,s47,v105
m v[12:15],a[216:219],v[8:11],v[12:15]
v_mul_f32 v106,s47,v106
v_mul_f32 v107,s47,v107
v_mul_f32 v108,s47,v108
m v[28:31],a[216:219],v[24:27],v[28:31]
v_mul_f32 v109,s47,v109
v_mul_f32 v110,s47,v110
v_mul_f32 v111,s47,v111
cv v104,v104,v105
s_waitcnt lgkmcnt(0)
m v[4:7],a[220:223],v[32:35],v[4:7]
r a[204:207],v192,256
r a[208:211],v192,8448
r a[212:215],v193,256
m v[20:23],a[220:223],v[48:51],v[20:23]
r a[216:219],v193,8448
cv v105,v106,v107
cv v106,v108,v109
m v[12:15],a[224:227],v[32:35],v[12:15]
cv v107,v110,v111
v_mul_f32 v112,s47,v112
v_mul_f32 v113,s47,v113
m v[28:31],a[224:227],v[48:51],v[28:31]
v_mul_f32 v114,s47,v114
v_mul_f32 v115,s47,v115
v_mul_f32 v116,s47,v116
m v[4:7],a[228:231],v[40:43],v[4:7]
v_mul_f32 v117,s47,v117
v_mul_f32 v118,s47,v118
v_mul_f32 v119,s47,v119
m v[20:23],a[228:231],v[56:59],v[20:23]
cv v112,v112,v113
cv v113,v114,v115
cv v114,v116,v117
m v[12:15],a[232:235],v[40:43],v[12:15]
cv v115,v118,v119
v_mul_f32 v120,s47,v120
v_mul_f32 v121,s47,v121
m v[28:31],a[232:235],v[56:59],v[28:31]
v_mul_f32 v122,s47,v122
v_mul_f32 v123,s47,v123
v_mul_f32 v124,s47,v124
v_mul_f32 v125,s47,v125
s_waitcnt lgkmcnt(0)
m v[4:7],a[204:207],v[64:67],v[4:7]
r a[220:223],v194,256
r a[224:227],v194,8448
r a[228:231],v195,256
r a[232:235],v195,8448
m v[20:23],a[204:207],v[80:83],v[20:23]
v_mul_f32 v126,s47,v126
v_mul_f32 v127,s47,v127
cv v120,v120,v121
cv v121,v122,v123
m v[12:15],a[208:211],v[64:67],v[12:15]
cv v122,v124,v125
cv v123,v126,v127
v_permlane32_swap_b32 v96,v98
v_permlane32_swap_b32 v97,v99
v_permlane32_swap_b32 v104,v106
m v[28:31],a[208:211],v[80:83],v[28:31]
v_permlane32_swap_b32 v105,v107
v_permlane32_swap_b32 v112,v114
v_permlane32_swap_b32 v113,v115
v_permlane32_swap_b32 v120,v122
m v[4:7],a[212:215],v[72:75],v[4:7]
v_permlane32_swap_b32 v121,v123
v_permlane16_swap_b32 v96,v98
v_permlane16_swap_b32 v97,v99
v_permlane16_swap_b32 v104,v106
m v[20:23],a[212:215],v[88:91],v[20:23]
v_permlane16_swap_b32 v105,v107
v_permlane16_swap_b32 v112,v114
v_permlane16_swap_b32 v113,v115
v_permlane16_swap_b32 v120,v122
v_permlane16_swap_b32 v121,v123
m v[12:15],a[216:219],v[72:75],v[12:15]
s_mov_b32 s41,s40
s_add_u32 s40,s40,0x8000
s_cmp_eq_u32 s40,0x28000
s_cselect_b32 s42,s49,s48
m v[28:31],a[216:219],v[88:91],v[28:31]
s_cselect_b32 s40,0,s40
v_add_u32 v192,s42,v192
v_add_u32 v193,s42,v193
v_add_u32 v194,s42,v194
v_add_u32 v195,s42,v195
s_waitcnt lgkmcnt(0)
s_waitcnt vmcnt(63)
s_barrier
m v[4:7],a[220:223],v[96:99],v[4:7]
r a[204:207],v192,0
r a[208:211],v192,8192
m v[20:23],a[220:223],v[112:115],v[20:23]
r a[212:215],v193,0
r a[216:219],v193,8192
m v[12:15],a[224:227],v[96:99],v[12:15]
v_mov_b32 v100,0x3f803f80
v_mov_b32 v101,0x3f803f80
m v[28:31],a[224:227],v[112:115],v[28:31]
v_mov_b32 v102,0x3f803f80
v_mov_b32 v103,0x3f803f80
m v[4:7],a[228:231],v[104:107],v[4:7]
m v[20:23],a[228:231],v[120:123],v[20:23]
m v[12:15],a[232:235],v[104:107],v[12:15]
m v[28:31],a[232:235],v[120:123],v[28:31]
s_nop 4
v_max3_f32 v220,v4,v5,v6
v_max3_f32 v221,v20,v21,v22
v_max3_f32 v220,v220,v7,v12
v_max3_f32 v221,v221,v23,v28
v_max3_f32 v220,v220,v13,v14
v_max3_f32 v221,v221,v29,v30
v_max_f32 v220,v220,v15
v_max_f32 v221,v221,v31
v_mov_b32 v224,v220
v_mov_b32 v225,v221
s_nop 0
v_permlane32_swap_b32 v220,v224
v_permlane32_swap_b32 v221,v225
v_max_f32 v220,v220,v224
v_max_f32 v221,v221,v225
v_mov_b32 v224,v220
v_mov_b32 v225,v221
s_nop 0
v_permlane16_swap_b32 v220,v224
v_permlane16_swap_b32 v221,v225
v_max_f32 v220,v220,v224
v_max_f32 v221,v221,v225
v_add_f32 v220,0x40e00000,v220
v_add_f32 v221,0x40e00000,v221
v_sub_f32 v84,0,v220
v_sub_f32 v85,0,v220
v_sub_f32 v86,0,v220
v_sub_f32 v87,0,v220
v_sub_f32 v92,0,v221
v_sub_f32 v93,0,v221
v_sub_f32 v94,0,v221
v_sub_f32 v95,0,v221
v_sub_f32 v4,v4,v220
v_sub_f32 v5,v5,v220
v_sub_f32 v6,v6,v220
v_sub_f32 v7,v7,v220
v_sub_f32 v12,v12,v220
v_sub_f32 v13,v13,v220
v_sub_f32 v14,v14,v220
v_sub_f32 v15,v15,v220
v_sub_f32 v20,v20,v221
v_sub_f32 v21,v21,v221
v_sub_f32 v22,v22,v221
v_sub_f32 v23,v23,v221
v_sub_f32 v28,v28,v221
v_sub_f32 v29,v29,v221
v_sub_f32 v30,v30,v221
v_sub_f32 v31,v31,v221
s_waitcnt lgkmcnt(0)
m v[36:39],a[204:207],v[0:3],v[84:87]
r a[220:223],v194,0
r a[224:227],v194,8192
r a[228:231],v195,0
m v[52:55],a[204:207],v[16:19],v[92:95]
r a[232:235],v195,8192
v_add_u32 v196,s42,v196
v_add_u32 v197,s42,v197
m v[44:47],a[208:211],v[0:3],v[84:87]
v_add_u32 v198,s42,v198
v_add_u32 v199,s42,v199
v_add_u32 v200,s42,v200
v_add_u32 v201,s42,v201
m v[60:63],a[208:211],v[16:19],v[92:95]
v_add_u32 v202,s42,v202
v_add_u32 v203,s42,v203
m v[36:39],a[212:215],v[8:11],v[36:39]
v_exp_f32 v220,v4
m v[52:55],a[212:215],v[24:27],v[52:55]
v_exp_f32 v221,v5
v_exp_f32 v222,v6
m v[44:47],a[216:219],v[8:11],v[44:47]
v_exp_f32 v223,v7
cv v68,v220,v221
m v[60:63],a[216:219],v[24:27],v[60:63]
v_exp_f32 v224,v12
v_exp_f32 v225,v13
s_waitcnt lgkmcnt(0)
m v[36:39],a[220:223],v[32:35],v[36:39]
r a[204:207],v192,256
r a[208:211],v192,8448
m v[52:55],a[220:223],v[48:51],v[52:55]
r a[212:215],v193,256
r a[216:219],v193,8448
m v[44:47],a[224:227],v[32:35],v[44:47]
cv v69,v222,v223
m v[60:63],a[224:227],v[48:51],v[60:63]
v_exp_f32 v220,v14
m v[36:39],a[228:231],v[40:43],v[36:39]
v_exp_f32 v221,v15
m v[52:55],a[228:231],v[56:59],v[52:55]
cv v70,v224,v225
m v[44:47],a[232:235],v[40:43],v[44:47]
v_exp_f32 v222,v20
m v[60:63],a[232:235],v[56:59],v[60:63]
v_exp_f32 v223,v21
cv v71,v220,v221
s_waitcnt lgkmcnt(0)
m v[36:39],a[204:207],v[64:67],v[36:39]
r a[220:223],v194,256
r a[224:227],v194,8448
m v[52:55],a[204:207],v[80:83],v[52:55]
r a[228:231],v195,256
r a[232:235],v195,8448
m v[44:47],a[208:211],v[64:67],v[44:47]
v_exp_f32 v224,v22
m v[60:63],a[208:211],v[80:83],v[60:63]
v_exp_f32 v225,v23
m v[36:39],a[212:215],v[72:75],v[36:39]
cv v76,v222,v223
m v[52:55],a[212:215],v[88:91],v[52:55]
v_exp_f32 v220,v28
m v[44:47],a[216:219],v[72:75],v[44:47]
v_exp_f32 v221,v29
m v[60:63],a[216:219],v[88:91],v[60:63]
cv v77,v224,v225
v_exp_f32 v222,v30
s_waitcnt lgkmcnt(0)
m v[36:39],a[220:223],v[96:99],v[36:39]
t a[204:205],v196,0
t a[206:207],v196,8192
m v[52:55],a[220:223],v[112:115],v[52:55]
t a[208:209],v197,0
t a[210:211],v197,8192
m v[44:47],a[224:227],v[96:99],v[44:47]
t a[212:213],v198,0
t a[214:215],v198,8192
m v[60:63],a[224:227],v[112:115],v[60:63]
t a[216:217],v199,0
t a[218:219],v199,8192
m v[36:39],a[228:231],v[104:107],v[36:39]
v_exp_f32 v223,v31
m v[52:55],a[228:231],v[120:123],v[52:55]
cv v78,v220,v221
cv v79,v222,v223
m v[44:47],a[232:235],v[104:107],v[44:47]
v_or3_b32 v228,v68,v69,v70
v_or3_b32 v228,v228,v71,v76
m v[60:63],a[232:235],v[120:123],v[60:63]
v_or3_b32 v228,v228,v77,v78
v_or_b32 v228,v228,v79
v_and_b32 v228,0x40004000,v228
v_cmp_ne_u32_e32 vcc,0,v228
s_nop 0
s_cbranch_vccnz LC_stub1%=
LC_ret1%=:
s_waitcnt lgkmcnt(0)
m a[192:195],v[100:103],v[68:71],0
t a[220:221],v200,0
t a[222:223],v200,8192
m a[196:199],v[100:103],v[76:79],0
t a[224:225],v201,0
t a[226:227],v201,8192
m a[0:3],a[204:207],v[68:71],0
t a[228:229],v202,0
t a[230:231],v202,8192
m a[64:67],a[204:207],v[76:79],0
t a[232:233],v203,0
t a[234:235],v203,8192
m a[4:7],a[208:211],v[68:71],0
m a[68:71],a[208:211],v[76:79],0
m a[8:11],a[212:215],v[68:71],0
m a[72:75],a[212:215],v[76:79],0
m a[12:15],a[216:219],v[68:71],0
m a[76:79],a[216:219],v[76:79],0
s_waitcnt lgkmcnt(0)
m a[16:19],a[220:223],v[68:71],0
t a[204:205],v196,256
t a[206:207],v196,8448
m a[80:83],a[220:223],v[76:79],0
t a[208:209],v197,256
t a[210:211],v197,8448
m a[20:23],a[224:227],v[68:71],0
t a[212:213],v198,256
t a[214:215],v198,8448
m a[84:87],a[224:227],v[76:79],0
t a[216:217],v199,256
t a[218:219],v199,8448
m a[24:27],a[228:231],v[68:71],0
s_mov_b32 s41,s40
s_add_u32 s40,s40,0x8000
m a[88:91],a[228:231],v[76:79],0
s_cmp_eq_u32 s40,0x28000
s_cselect_b32 s42,s49,s48
m a[28:31],a[232:235],v[68:71],0
s_cselect_b32 s40,0,s40
v_add_u32 v192,s42,v192
m a[92:95],a[232:235],v[76:79],0
v_add_u32 v193,s42,v193
v_add_u32 v194,s42,v194
v_add_u32 v195,s42,v195
s_waitcnt lgkmcnt(0)
m a[32:35],a[204:207],v[68:71],0
t a[220:221],v200,256
t a[222:223],v200,8448
m a[96:99],a[204:207],v[76:79],0
t a[224:225],v201,256
t a[226:227],v201,8448
m a[36:39],a[208:211],v[68:71],0
t a[228:229],v202,256
t a[230:231],v202,8448
m a[100:103],a[208:211],v[76:79],0
t a[232:233],v203,256
t a[234:235],v203,8448
m a[40:43],a[212:215],v[68:71],0
m a[104:107],a[212:215],v[76:79],0
m a[44:47],a[216:219],v[68:71],0
m a[108:111],a[216:219],v[76:79],0
s_waitcnt lgkmcnt(0)
s_barrier
s_waitcnt vmcnt(44)
)ASM"
R"ASM(v_mov_b32 v149,v150
buffer_store_dwordx2 v[148:149],v153,%5,%12 offen sc1
v_sub_u32 v155,v151,v148
v_cmp_lt_u32_e32 vcc,7,v155
s_nop 0
s_cmp_eq_u64 vcc,0
s_cselect_b32 s58,1,0
buffer_load_dwordx2 v[150:151],v152,%5,%12 offen sc1
r a[204:207],v192,0
r a[208:211],v192,8192
r a[212:215],v193,0
r a[216:219],v193,8192
s_add_u32 m0,s41,0x4000
s_add_u32 s45,s43,0
dn v136,%3,s45,0
dn v137,%3,s45,1024
dn v138,%3,s45,2048
dn v139,%3,s45,3072
s_add_u32 m0,s41,0x5000
s_add_u32 s45,s43,0x1000
dn v136,%3,s45,0
dn v137,%3,s45,1024
dn v138,%3,s45,2048
dn v139,%3,s45,3072
s_add_u32 m0,s41,0x6000
s_add_u32 s45,s43,0x2000
dn v136,%3,s45,0
dn v137,%3,s45,1024
dn v138,%3,s45,2048
dn v139,%3,s45,3072
s_add_u32 m0,s41,0x7000
s_add_u32 s45,s43,0x3000
dn v136,%3,s45,0
dn v137,%3,s45,1024
dn v138,%3,s45,2048
dn v139,%3,s45,3072
s_add_u32 s43,s43,0x4000
s_and_b32 s43,s43,0x7ffff
m a[48:51],a[220:223],v[68:71],0
m a[112:115],a[220:223],v[76:79],0
m a[52:55],a[224:227],v[68:71],0
m a[116:119],a[224:227],v[76:79],0
m a[56:59],a[228:231],v[68:71],0
m a[120:123],a[228:231],v[76:79],0
m a[60:63],a[232:235],v[68:71],0
m a[124:127],a[232:235],v[76:79],0
s_waitcnt lgkmcnt(0)
m v[4:7],a[204:207],v[0:3],v[84:87]
r a[220:223],v194,0
r a[224:227],v194,8192
r a[228:231],v195,0
m v[20:23],a[204:207],v[16:19],v[92:95]
r a[232:235],v195,8192
v_add_u32 v196,s42,v196
v_add_u32 v197,s42,v197
m v[12:15],a[208:211],v[0:3],v[84:87]
v_add_u32 v198,s42,v198
v_add_u32 v199,s42,v199
v_add_u32 v200,s42,v200
v_add_u32 v201,s42,v201
m v[28:31],a[208:211],v[16:19],v[92:95]
v_add_u32 v202,s42,v202
v_add_u32 v203,s42,v203
m v[4:7],a[212:215],v[8:11],v[4:7]
v_exp_f32 v220,v36
m v[20:23],a[212:215],v[24:27],v[20:23]
v_exp_f32 v221,v37
v_exp_f32 v222,v38
m v[12:15],a[216:219],v[8:11],v[12:15]
v_exp_f32 v223,v39
cv v68,v220,v221
m v[28:31],a[216:219],v[24:27],v[28:31]
v_exp_f32 v224,v44
v_exp_f32 v225,v45
s_waitcnt lgkmcnt(0)
m v[4:7],a[220:223],v[32:35],v[4:7]
r a[204:207],v192,256
r a[208:211],v192,8448
m v[20:23],a[220:223],v[48:51],v[20:23]
r a[212:215],v193,256
r a[216:219],v193,8448
m v[12:15],a[224:227],v[32:35],v[12:15]
cv v69,v222,v223
m v[28:31],a[224:227],v[48:51],v[28:31]
v_exp_f32 v220,v46
m v[4:7],a[228:231],v[40:43],v[4:7]
v_exp_f32 v221,v47
m v[20:23],a[228:231],v[56:59],v[20:23]
cv v70,v224,v225
m v[12:15],a[232:235],v[40:43],v[12:15]
v_exp_f32 v222,v52
m v[28:31],a[232:235],v[56:59],v[28:31]
v_exp_f32 v223,v53
cv v71,v220,v221
s_waitcnt lgkmcnt(0)
m v[4:7],a[204:207],v[64:67],v[4:7]
r a[220:223],v194,256
r a[224:227],v194,8448
m v[20:23],a[204:207],v[80:83],v[20:23]
r a[228:231],v195,256
r a[232:235],v195,8448
m v[12:15],a[208:211],v[64:67],v[12:15]
v_exp_f32 v224,v54
m v[28:31],a[208:211],v[80:83],v[28:31]
v_exp_f32 v225,v55
m v[4:7],a[212:215],v[72:75],v[4:7]
cv v76,v222,v223
m v[20:23],a[212:215],v[88:91],v[20:23]
v_exp_f32 v220,v60
m v[12:15],a[216:219],v[72:75],v[12:15]
v_exp_f32 v221,v61
m v[28:31],a[216:219],v[88:91],v[28:31]
cv v77,v224,v225
v_exp_f32 v222,v62
s_waitcnt lgkmcnt(0)
m v[4:7],a[220:223],v[96:99],v[4:7]
t a[204:205],v196,0
t a[206:207],v196,8192
m v[20:23],a[220:223],v[112:115],v[20:23]
t a[208:209],v197,0
t a[210:211],v197,8192
m v[12:15],a[224:227],v[96:99],v[12:15]
t a[212:213],v198,0
t a[214:215],v198,8192
m v[28:31],a[224:227],v[112:115],v[28:31]
t a[216:217],v199,0
t a[218:219],v199,8192
m v[4:7],a[228:231],v[104:107],v[4:7]
v_exp_f32 v223,v63
m v[20:23],a[228:231],v[120:123],v[20:23]
cv v78,v220,v221
cv v79,v222,v223
m v[12:15],a[232:235],v[104:107],v[12:15]
v_or3_b32 v228,v68,v69,v70
v_or3_b32 v228,v228,v71,v76
m v[28:31],a[232:235],v[120:123],v[28:31]
v_or3_b32 v228,v228,v77,v78
v_or_b32 v228,v228,v79
v_and_b32 v228,0x40004000,v228
v_cmp_ne_u32_e32 vcc,0,v228
s_nop 0
s_cbranch_vccnz LC_stub2%=
LC_ret2%=:
s_waitcnt lgkmcnt(0)
m a[192:195],v[100:103],v[68:71],a[192:195]
t a[220:221],v200,0
t a[222:223],v200,8192
m a[196:199],v[100:103],v[76:79],a[196:199]
t a[224:225],v201,0
t a[226:227],v201,8192
m a[0:3],a[204:207],v[68:71],a[0:3]
t a[228:229],v202,0
t a[230:231],v202,8192
m a[64:67],a[204:207],v[76:79],a[64:67]
t a[232:233],v203,0
t a[234:235],v203,8192
m a[4:7],a[208:211],v[68:71],a[4:7]
m a[68:71],a[208:211],v[76:79],a[68:71]
m a[8:11],a[212:215],v[68:71],a[8:11]
m a[72:75],a[212:215],v[76:79],a[72:75]
m a[12:15],a[216:219],v[68:71],a[12:15]
m a[76:79],a[216:219],v[76:79],a[76:79]
s_waitcnt lgkmcnt(0)
m a[16:19],a[220:223],v[68:71],a[16:19]
t a[204:205],v196,256
t a[206:207],v196,8448
m a[80:83],a[220:223],v[76:79],a[80:83]
t a[208:209],v197,256
t a[210:211],v197,8448
m a[20:23],a[224:227],v[68:71],a[20:23]
t a[212:213],v198,256
t a[214:215],v198,8448
m a[84:87],a[224:227],v[76:79],a[84:87]
t a[216:217],v199,256
t a[218:219],v199,8448
m a[24:27],a[228:231],v[68:71],a[24:27]
s_mov_b32 s41,s40
s_add_u32 s40,s40,0x8000
m a[88:91],a[228:231],v[76:79],a[88:91]
s_cmp_eq_u32 s40,0x28000
s_cselect_b32 s42,s49,s48
m a[28:31],a[232:235],v[68:71],a[28:31]
s_cselect_b32 s40,0,s40
v_add_u32 v192,s42,v192
m a[92:95],a[232:235],v[76:79],a[92:95]
v_add_u32 v193,s42,v193
v_add_u32 v194,s42,v194
v_add_u32 v195,s42,v195
s_waitcnt lgkmcnt(0)
m a[32:35],a[204:207],v[68:71],a[32:35]
t a[220:221],v200,256
t a[222:223],v200,8448
m a[96:99],a[204:207],v[76:79],a[96:99]
t a[224:225],v201,256
t a[226:227],v201,8448
m a[36:39],a[208:211],v[68:71],a[36:39]
t a[228:229],v202,256
t a[230:231],v202,8448
m a[100:103],a[208:211],v[76:79],a[100:103]
t a[232:233],v203,256
t a[234:235],v203,8448
m a[40:43],a[212:215],v[68:71],a[40:43]
m a[104:107],a[212:215],v[76:79],a[104:107]
m a[44:47],a[216:219],v[68:71],a[44:47]
m a[108:111],a[216:219],v[76:79],a[108:111]
s_mov_b32 s44,13
LC_loop%=:
s_waitcnt lgkmcnt(0)
s_waitcnt vmcnt(48)
s_barrier
r a[204:207],v192,0
r a[208:211],v192,8192
r a[212:215],v193,0
r a[216:219],v193,8192
s_add_u32 m0,s41,0x4000
s_add_u32 s45,s43,0
dn v136,%3,s45,0
dn v137,%3,s45,1024
dn v138,%3,s45,2048
dn v139,%3,s45,3072
s_add_u32 m0,s41,0x5000
s_add_u32 s45,s43,0x1000
dn v136,%3,s45,0
dn v137,%3,s45,1024
dn v138,%3,s45,2048
dn v139,%3,s45,3072
s_add_u32 m0,s41,0x6000
s_add_u32 s45,s43,0x2000
dn v136,%3,s45,0
dn v137,%3,s45,1024
dn v138,%3,s45,2048
dn v139,%3,s45,3072
s_add_u32 m0,s41,0x7000
s_add_u32 s45,s43,0x3000
dn v136,%3,s45,0
dn v137,%3,s45,1024
dn v138,%3,s45,2048
dn v139,%3,s45,3072
s_add_u32 s43,s43,0x4000
s_and_b32 s43,s43,0x7ffff
m a[48:51],a[220:223],v[68:71],a[48:51]
m a[112:115],a[220:223],v[76:79],a[112:115]
m a[52:55],a[224:227],v[68:71],a[52:55]
m a[116:119],a[224:227],v[76:79],a[116:119]
m a[56:59],a[228:231],v[68:71],a[56:59]
m a[120:123],a[228:231],v[76:79],a[120:123]
m a[60:63],a[232:235],v[68:71],a[60:63]
m a[124:127],a[232:235],v[76:79],a[124:127]
s_waitcnt lgkmcnt(0)
m v[36:39],a[204:207],v[0:3],v[84:87]
r a[220:223],v194,0
r a[224:227],v194,8192
r a[228:231],v195,0
m v[52:55],a[204:207],v[16:19],v[92:95]
r a[232:235],v195,8192
v_add_u32 v196,s42,v196
v_add_u32 v197,s42,v197
m v[44:47],a[208:211],v[0:3],v[84:87]
v_add_u32 v198,s42,v198
v_add_u32 v199,s42,v199
v_add_u32 v200,s42,v200
v_add_u32 v201,s42,v201
m v[60:63],a[208:211],v[16:19],v[92:95]
v_add_u32 v202,s42,v202
v_add_u32 v203,s42,v203
m v[36:39],a[212:215],v[8:11],v[36:39]
v_exp_f32 v220,v4
m v[52:55],a[212:215],v[24:27],v[52:55]
v_exp_f32 v221,v5
v_exp_f32 v222,v6
m v[44:47],a[216:219],v[8:11],v[44:47]
v_exp_f32 v223,v7
cv v68,v220,v221
m v[60:63],a[216:219],v[24:27],v[60:63]
v_exp_f32 v224,v12
v_exp_f32 v225,v13
s_waitcnt lgkmcnt(0)
m v[36:39],a[220:223],v[32:35],v[36:39]
r a[204:207],v192,256
r a[208:211],v192,8448
m v[52:55],a[220:223],v[48:51],v[52:55]
r a[212:215],v193,256
r a[216:219],v193,8448
m v[44:47],a[224:227],v[32:35],v[44:47]
cv v69,v222,v223
m v[60:63],a[224:227],v[48:51],v[60:63]
v_exp_f32 v220,v14
m v[36:39],a[228:231],v[40:43],v[36:39]
v_exp_f32 v221,v15
m v[52:55],a[228:231],v[56:59],v[52:55]
cv v70,v224,v225
m v[44:47],a[232:235],v[40:43],v[44:47]
v_exp_f32 v222,v20
m v[60:63],a[232:235],v[56:59],v[60:63]
v_exp_f32 v223,v21
cv v71,v220,v221
s_waitcnt lgkmcnt(0)
m v[36:39],a[204:207],v[64:67],v[36:39]
r a[220:223],v194,256
r a[224:227],v194,8448
m v[52:55],a[204:207],v[80:83],v[52:55]
r a[228:231],v195,256
r a[232:235],v195,8448
m v[44:47],a[208:211],v[64:67],v[44:47]
v_exp_f32 v224,v22
m v[60:63],a[208:211],v[80:83],v[60:63]
v_exp_f32 v225,v23
m v[36:39],a[212:215],v[72:75],v[36:39]
cv v76,v222,v223
m v[52:55],a[212:215],v[88:91],v[52:55]
v_exp_f32 v220,v28
m v[44:47],a[216:219],v[72:75],v[44:47]
v_exp_f32 v221,v29
m v[60:63],a[216:219],v[88:91],v[60:63]
cv v77,v224,v225
v_exp_f32 v222,v30
s_waitcnt lgkmcnt(0)
m v[36:39],a[220:223],v[96:99],v[36:39]
t a[204:205],v196,0
t a[206:207],v196,8192
m v[52:55],a[220:223],v[112:115],v[52:55]
t a[208:209],v197,0
t a[210:211],v197,8192
m v[44:47],a[224:227],v[96:99],v[44:47]
t a[212:213],v198,0
t a[214:215],v198,8192
m v[60:63],a[224:227],v[112:115],v[60:63]
t a[216:217],v199,0
t a[218:219],v199,8192
m v[36:39],a[228:231],v[104:107],v[36:39]
v_exp_f32 v223,v31
m v[52:55],a[228:231],v[120:123],v[52:55]
cv v78,v220,v221
cv v79,v222,v223
m v[44:47],a[232:235],v[104:107],v[44:47]
v_or3_b32 v228,v68,v69,v70
v_or3_b32 v228,v228,v71,v76
m v[60:63],a[232:235],v[120:123],v[60:63]
v_or3_b32 v228,v228,v77,v78
v_or_b32 v228,v228,v79
v_and_b32 v228,0x40004000,v228
v_cmp_ne_u32_e32 vcc,0,v228
s_nop 0
s_cbranch_vccnz LC_stub3%=
LC_ret3%=:
s_waitcnt lgkmcnt(0)
m a[192:195],v[100:103],v[68:71],a[192:195]
t a[220:221],v200,0
t a[222:223],v200,8192
m a[196:199],v[100:103],v[76:79],a[196:199]
t a[224:225],v201,0
t a[226:227],v201,8192
m a[0:3],a[204:207],v[68:71],a[0:3]
t a[228:229],v202,0
t a[230:231],v202,8192
m a[64:67],a[204:207],v[76:79],a[64:67]
t a[232:233],v203,0
t a[234:235],v203,8192
m a[4:7],a[208:211],v[68:71],a[4:7]
m a[68:71],a[208:211],v[76:79],a[68:71]
m a[8:11],a[212:215],v[68:71],a[8:11]
m a[72:75],a[212:215],v[76:79],a[72:75]
m a[12:15],a[216:219],v[68:71],a[12:15]
m a[76:79],a[216:219],v[76:79],a[76:79]
s_waitcnt lgkmcnt(0)
m a[16:19],a[220:223],v[68:71],a[16:19]
t a[204:205],v196,256
t a[206:207],v196,8448
m a[80:83],a[220:223],v[76:79],a[80:83]
t a[208:209],v197,256
t a[210:211],v197,8448
m a[20:23],a[224:227],v[68:71],a[20:23]
t a[212:213],v198,256
t a[214:215],v198,8448
m a[84:87],a[224:227],v[76:79],a[84:87]
t a[216:217],v199,256
t a[218:219],v199,8448
m a[24:27],a[228:231],v[68:71],a[24:27]
s_mov_b32 s41,s40
s_add_u32 s40,s40,0x8000
m a[88:91],a[228:231],v[76:79],a[88:91]
s_cmp_eq_u32 s40,0x28000
s_cselect_b32 s42,s49,s48
m a[28:31],a[232:235],v[68:71],a[28:31]
s_cselect_b32 s40,0,s40
v_add_u32 v192,s42,v192
m a[92:95],a[232:235],v[76:79],a[92:95]
v_add_u32 v193,s42,v193
v_add_u32 v194,s42,v194
v_add_u32 v195,s42,v195
s_waitcnt lgkmcnt(0)
m a[32:35],a[204:207],v[68:71],a[32:35]
t a[220:221],v200,256
t a[222:223],v200,8448
m a[96:99],a[204:207],v[76:79],a[96:99]
t a[224:225],v201,256
t a[226:227],v201,8448
m a[36:39],a[208:211],v[68:71],a[36:39]
t a[228:229],v202,256
t a[230:231],v202,8448
m a[100:103],a[208:211],v[76:79],a[100:103]
t a[232:233],v203,256
t a[234:235],v203,8448
m a[40:43],a[212:215],v[68:71],a[40:43]
m a[104:107],a[212:215],v[76:79],a[104:107]
m a[44:47],a[216:219],v[68:71],a[44:47]
m a[108:111],a[216:219],v[76:79],a[108:111]
s_waitcnt lgkmcnt(0)
s_waitcnt vmcnt(48)
s_barrier
r a[204:207],v192,0
r a[208:211],v192,8192
r a[212:215],v193,0
r a[216:219],v193,8192
s_add_u32 m0,s41,0x4000
s_add_u32 s45,s43,0
dn v136,%3,s45,0
dn v137,%3,s45,1024
dn v138,%3,s45,2048
dn v139,%3,s45,3072
s_add_u32 m0,s41,0x5000
s_add_u32 s45,s43,0x1000
dn v136,%3,s45,0
dn v137,%3,s45,1024
dn v138,%3,s45,2048
dn v139,%3,s45,3072
s_add_u32 m0,s41,0x6000
s_add_u32 s45,s43,0x2000
dn v136,%3,s45,0
dn v137,%3,s45,1024
dn v138,%3,s45,2048
dn v139,%3,s45,3072
s_add_u32 m0,s41,0x7000
s_add_u32 s45,s43,0x3000
dn v136,%3,s45,0
dn v137,%3,s45,1024
dn v138,%3,s45,2048
dn v139,%3,s45,3072
s_add_u32 s43,s43,0x4000
s_and_b32 s43,s43,0x7ffff
m a[48:51],a[220:223],v[68:71],a[48:51]
m a[112:115],a[220:223],v[76:79],a[112:115]
m a[52:55],a[224:227],v[68:71],a[52:55]
m a[116:119],a[224:227],v[76:79],a[116:119]
m a[56:59],a[228:231],v[68:71],a[56:59]
m a[120:123],a[228:231],v[76:79],a[120:123]
m a[60:63],a[232:235],v[68:71],a[60:63]
m a[124:127],a[232:235],v[76:79],a[124:127]
s_waitcnt lgkmcnt(0)
m v[4:7],a[204:207],v[0:3],v[84:87]
r a[220:223],v194,0
r a[224:227],v194,8192
r a[228:231],v195,0
m v[20:23],a[204:207],v[16:19],v[92:95]
r a[232:235],v195,8192
v_add_u32 v196,s42,v196
v_add_u32 v197,s42,v197
m v[12:15],a[208:211],v[0:3],v[84:87]
v_add_u32 v198,s42,v198
v_add_u32 v199,s42,v199
v_add_u32 v200,s42,v200
v_add_u32 v201,s42,v201
m v[28:31],a[208:211],v[16:19],v[92:95]
v_add_u32 v202,s42,v202
v_add_u32 v203,s42,v203
m v[4:7],a[212:215],v[8:11],v[4:7]
v_exp_f32 v220,v36
m v[20:23],a[212:215],v[24:27],v[20:23]
v_exp_f32 v221,v37
v_exp_f32 v222,v38
m v[12:15],a[216:219],v[8:11],v[12:15]
v_exp_f32 v223,v39
cv v68,v220,v221
m v[28:31],a[216:219],v[24:27],v[28:31]
v_exp_f32 v224,v44
v_exp_f32 v225,v45
s_waitcnt lgkmcnt(0)
m v[4:7],a[220:223],v[32:35],v[4:7]
r a[204:207],v192,256
r a[208:211],v192,8448
m v[20:23],a[220:223],v[48:51],v[20:23]
r a[212:215],v193,256
r a[216:219],v193,8448
m v[12:15],a[224:227],v[32:35],v[12:15]
cv v69,v222,v223
m v[28:31],a[224:227],v[48:51],v[28:31]
v_exp_f32 v220,v46
m v[4:7],a[228:231],v[40:43],v[4:7]
v_exp_f32 v221,v47
m v[20:23],a[228:231],v[56:59],v[20:23]
cv v70,v224,v225
m v[12:15],a[232:235],v[40:43],v[12:15]
v_exp_f32 v222,v52
m v[28:31],a[232:235],v[56:59],v[28:31]
v_exp_f32 v223,v53
cv v71,v220,v221
s_waitcnt lgkmcnt(0)
m v[4:7],a[204:207],v[64:67],v[4:7]
r a[220:223],v194,256
r a[224:227],v194,8448
m v[20:23],a[204:207],v[80:83],v[20:23]
r a[228:231],v195,256
r a[232:235],v195,8448
m v[12:15],a[208:211],v[64:67],v[12:15]
v_exp_f32 v224,v54
m v[28:31],a[208:211],v[80:83],v[28:31]
v_exp_f32 v225,v55
m v[4:7],a[212:215],v[72:75],v[4:7]
cv v76,v222,v223
m v[20:23],a[212:215],v[88:91],v[20:23]
v_exp_f32 v220,v60
m v[12:15],a[216:219],v[72:75],v[12:15]
v_exp_f32 v221,v61
m v[28:31],a[216:219],v[88:91],v[28:31]
cv v77,v224,v225
v_exp_f32 v222,v62
s_waitcnt lgkmcnt(0)
m v[4:7],a[220:223],v[96:99],v[4:7]
t a[204:205],v196,0
t a[206:207],v196,8192
m v[20:23],a[220:223],v[112:115],v[20:23]
t a[208:209],v197,0
t a[210:211],v197,8192
m v[12:15],a[224:227],v[96:99],v[12:15]
t a[212:213],v198,0
t a[214:215],v198,8192
m v[28:31],a[224:227],v[112:115],v[28:31]
t a[216:217],v199,0
t a[218:219],v199,8192
m v[4:7],a[228:231],v[104:107],v[4:7]
v_exp_f32 v223,v63
m v[20:23],a[228:231],v[120:123],v[20:23]
cv v78,v220,v221
cv v79,v222,v223
m v[12:15],a[232:235],v[104:107],v[12:15]
v_or3_b32 v228,v68,v69,v70
v_or3_b32 v228,v228,v71,v76
m v[28:31],a[232:235],v[120:123],v[28:31]
v_or3_b32 v228,v228,v77,v78
v_or_b32 v228,v228,v79
v_and_b32 v228,0x40004000,v228
v_cmp_ne_u32_e32 vcc,0,v228
s_nop 0
s_cbranch_vccnz LC_stub4%=
LC_ret4%=:
s_waitcnt lgkmcnt(0)
m a[192:195],v[100:103],v[68:71],a[192:195]
t a[220:221],v200,0
t a[222:223],v200,8192
m a[196:199],v[100:103],v[76:79],a[196:199]
t a[224:225],v201,0
t a[226:227],v201,8192
m a[0:3],a[204:207],v[68:71],a[0:3]
t a[228:229],v202,0
t a[230:231],v202,8192
m a[64:67],a[204:207],v[76:79],a[64:67]
t a[232:233],v203,0
t a[234:235],v203,8192
m a[4:7],a[208:211],v[68:71],a[4:7]
m a[68:71],a[208:211],v[76:79],a[68:71]
m a[8:11],a[212:215],v[68:71],a[8:11]
m a[72:75],a[212:215],v[76:79],a[72:75]
m a[12:15],a[216:219],v[68:71],a[12:15]
m a[76:79],a[216:219],v[76:79],a[76:79]
s_waitcnt lgkmcnt(0)
m a[16:19],a[220:223],v[68:71],a[16:19]
t a[204:205],v196,256
t a[206:207],v196,8448
m a[80:83],a[220:223],v[76:79],a[80:83]
t a[208:209],v197,256
t a[210:211],v197,8448
m a[20:23],a[224:227],v[68:71],a[20:23]
t a[212:213],v198,256
t a[214:215],v198,8448
m a[84:87],a[224:227],v[76:79],a[84:87]
t a[216:217],v199,256
t a[218:219],v199,8448
m a[24:27],a[228:231],v[68:71],a[24:27]
s_mov_b32 s41,s40
s_add_u32 s40,s40,0x8000
m a[88:91],a[228:231],v[76:79],a[88:91]
s_cmp_eq_u32 s40,0x28000
s_cselect_b32 s42,s49,s48
m a[28:31],a[232:235],v[68:71],a[28:31]
s_cselect_b32 s40,0,s40
v_add_u32 v192,s42,v192
m a[92:95],a[232:235],v[76:79],a[92:95]
v_add_u32 v193,s42,v193
v_add_u32 v194,s42,v194
v_add_u32 v195,s42,v195
s_waitcnt lgkmcnt(0)
m a[32:35],a[204:207],v[68:71],a[32:35]
t a[220:221],v200,256
t a[222:223],v200,8448
m a[96:99],a[204:207],v[76:79],a[96:99]
t a[224:225],v201,256
t a[226:227],v201,8448
m a[36:39],a[208:211],v[68:71],a[36:39]
t a[228:229],v202,256
t a[230:231],v202,8448
m a[100:103],a[208:211],v[76:79],a[100:103]
t a[232:233],v203,256
t a[234:235],v203,8448
m a[40:43],a[212:215],v[68:71],a[40:43]
m a[104:107],a[212:215],v[76:79],a[104:107]
m a[44:47],a[216:219],v[68:71],a[44:47]
m a[108:111],a[216:219],v[76:79],a[108:111]
s_sub_u32 s44,s44,1
s_cmp_lg_u32 s44,0
s_cbranch_scc1 LC_loop%=
s_waitcnt lgkmcnt(0)
s_waitcnt vmcnt(48)
s_barrier
v_mov_b32 v149,v150
buffer_store_dwordx2 v[148:149],v153,%5,%12 offen sc1
v_sub_u32 v155,v151,v148
v_cmp_lt_u32_e32 vcc,7,v155
s_nop 0
s_cmp_eq_u64 vcc,0
s_cselect_b32 s58,1,0
s_cmp_eq_u32 s58,1
s_cbranch_scc0 LC_hs6%=
LC_hr6%=:
r a[204:207],v192,0
r a[208:211],v192,8192
r a[212:215],v193,0
r a[216:219],v193,8192
s_mov_b32 m0,0x14000
s_mov_b32 s45,0x1c000
dn v144,%4,s45,0
dn v145,%4,s45,1024
dn v146,%4,s45,2048
dn v147,%4,s45,3072
s_mov_b32 m0,0x15000
s_mov_b32 s45,0x23000
dn v144,%4,s45,0
dn v145,%4,s45,1024
dn v146,%4,s45,2048
dn v147,%4,s45,3072
s_mov_b32 m0,0x16000
s_mov_b32 s45,0x2a000
dn v144,%4,s45,0
dn v145,%4,s45,1024
dn v146,%4,s45,2048
dn v147,%4,s45,3072
s_mov_b32 m0,0x17000
s_mov_b32 s45,0x31000
dn v144,%4,s45,0
dn v145,%4,s45,1024
dn v146,%4,s45,2048
dn v147,%4,s45,3072
m a[48:51],a[220:223],v[68:71],a[48:51]
m a[112:115],a[220:223],v[76:79],a[112:115]
m a[52:55],a[224:227],v[68:71],a[52:55]
m a[116:119],a[224:227],v[76:79],a[116:119]
m a[56:59],a[228:231],v[68:71],a[56:59]
m a[120:123],a[228:231],v[76:79],a[120:123]
m a[60:63],a[232:235],v[68:71],a[60:63]
m a[124:127],a[232:235],v[76:79],a[124:127]
s_waitcnt lgkmcnt(0)
m v[36:39],a[204:207],v[0:3],v[84:87]
r a[220:223],v194,0
r a[224:227],v194,8192
r a[228:231],v195,0
m v[52:55],a[204:207],v[16:19],v[92:95]
r a[232:235],v195,8192
v_add_u32 v196,s42,v196
v_add_u32 v197,s42,v197
m v[44:47],a[208:211],v[0:3],v[84:87]
v_add_u32 v198,s42,v198
v_add_u32 v199,s42,v199
v_add_u32 v200,s42,v200
v_add_u32 v201,s42,v201
m v[60:63],a[208:211],v[16:19],v[92:95]
v_add_u32 v202,s42,v202
v_add_u32 v203,s42,v203
m v[36:39],a[212:215],v[8:11],v[36:39]
v_exp_f32 v220,v4
m v[52:55],a[212:215],v[24:27],v[52:55]
v_exp_f32 v221,v5
v_exp_f32 v222,v6
m v[44:47],a[216:219],v[8:11],v[44:47]
v_exp_f32 v223,v7
cv v68,v220,v221
m v[60:63],a[216:219],v[24:27],v[60:63]
v_exp_f32 v224,v12
v_exp_f32 v225,v13
s_waitcnt lgkmcnt(0)
m v[36:39],a[220:223],v[32:35],v[36:39]
r a[204:207],v192,256
r a[208:211],v192,8448
m v[52:55],a[220:223],v[48:51],v[52:55]
r a[212:215],v193,256
r a[216:219],v193,8448
m v[44:47],a[224:227],v[32:35],v[44:47]
cv v69,v222,v223
m v[60:63],a[224:227],v[48:51],v[60:63]
v_exp_f32 v220,v14
m v[36:39],a[228:231],v[40:43],v[36:39]
v_exp_f32 v221,v15
m v[52:55],a[228:231],v[56:59],v[52:55]
cv v70,v224,v225
m v[44:47],a[232:235],v[40:43],v[44:47]
v_exp_f32 v222,v20
m v[60:63],a[232:235],v[56:59],v[60:63]
v_exp_f32 v223,v21
cv v71,v220,v221
s_waitcnt lgkmcnt(0)
m v[36:39],a[204:207],v[64:67],v[36:39]
r a[220:223],v194,256
r a[224:227],v194,8448
m v[52:55],a[204:207],v[80:83],v[52:55]
r a[228:231],v195,256
r a[232:235],v195,8448
m v[44:47],a[208:211],v[64:67],v[44:47]
v_exp_f32 v224,v22
m v[60:63],a[208:211],v[80:83],v[60:63]
v_exp_f32 v225,v23
m v[36:39],a[212:215],v[72:75],v[36:39]
cv v76,v222,v223
m v[52:55],a[212:215],v[88:91],v[52:55]
v_exp_f32 v220,v28
m v[44:47],a[216:219],v[72:75],v[44:47]
v_exp_f32 v221,v29
m v[60:63],a[216:219],v[88:91],v[60:63]
cv v77,v224,v225
v_exp_f32 v222,v30
s_waitcnt lgkmcnt(0)
m v[36:39],a[220:223],v[96:99],v[36:39]
t a[204:205],v196,0
t a[206:207],v196,8192
m v[52:55],a[220:223],v[112:115],v[52:55]
t a[208:209],v197,0
t a[210:211],v197,8192
m v[44:47],a[224:227],v[96:99],v[44:47]
t a[212:213],v198,0
t a[214:215],v198,8192
m v[60:63],a[224:227],v[112:115],v[60:63]
t a[216:217],v199,0
t a[218:219],v199,8192
m v[36:39],a[228:231],v[104:107],v[36:39]
v_exp_f32 v223,v31
m v[52:55],a[228:231],v[120:123],v[52:55]
cv v78,v220,v221
cv v79,v222,v223
m v[44:47],a[232:235],v[104:107],v[44:47]
v_or3_b32 v228,v68,v69,v70
v_or3_b32 v228,v228,v71,v76
m v[60:63],a[232:235],v[120:123],v[60:63]
v_or3_b32 v228,v228,v77,v78
v_or_b32 v228,v228,v79
v_and_b32 v228,0x40004000,v228
v_cmp_ne_u32_e32 vcc,0,v228
s_nop 0
s_cbranch_vccnz LC_stub5%=
LC_ret5%=:
s_waitcnt lgkmcnt(0)
m a[192:195],v[100:103],v[68:71],a[192:195]
t a[220:221],v200,0
t a[222:223],v200,8192
m a[196:199],v[100:103],v[76:79],a[196:199]
t a[224:225],v201,0
t a[226:227],v201,8192
m a[0:3],a[204:207],v[68:71],a[0:3]
t a[228:229],v202,0
t a[230:231],v202,8192
m a[64:67],a[204:207],v[76:79],a[64:67]
t a[232:233],v203,0
t a[234:235],v203,8192
m a[4:7],a[208:211],v[68:71],a[4:7]
m a[68:71],a[208:211],v[76:79],a[68:71]
m a[8:11],a[212:215],v[68:71],a[8:11]
m a[72:75],a[212:215],v[76:79],a[72:75]
m a[12:15],a[216:219],v[68:71],a[12:15]
m a[76:79],a[216:219],v[76:79],a[76:79]
s_waitcnt lgkmcnt(0)
m a[16:19],a[220:223],v[68:71],a[16:19]
t a[204:205],v196,256
t a[206:207],v196,8448
m a[80:83],a[220:223],v[76:79],a[80:83]
t a[208:209],v197,256
t a[210:211],v197,8448
m a[20:23],a[224:227],v[68:71],a[20:23]
t a[212:213],v198,256
t a[214:215],v198,8448
m a[84:87],a[224:227],v[76:79],a[84:87]
t a[216:217],v199,256
t a[218:219],v199,8448
m a[24:27],a[228:231],v[68:71],a[24:27]
s_mov_b32 s41,s40
s_add_u32 s40,s40,0x8000
m a[88:91],a[228:231],v[76:79],a[88:91]
s_cmp_eq_u32 s40,0x28000
s_cselect_b32 s42,s49,s48
m a[28:31],a[232:235],v[68:71],a[28:31]
s_cselect_b32 s40,0,s40
v_add_u32 v192,s42,v192
m a[92:95],a[232:235],v[76:79],a[92:95]
v_add_u32 v193,s42,v193
v_add_u32 v194,s42,v194
v_add_u32 v195,s42,v195
s_waitcnt lgkmcnt(0)
m a[32:35],a[204:207],v[68:71],a[32:35]
t a[220:221],v200,256
t a[222:223],v200,8448
m a[96:99],a[204:207],v[76:79],a[96:99]
t a[224:225],v201,256
t a[226:227],v201,8448
m a[36:39],a[208:211],v[68:71],a[36:39]
t a[228:229],v202,256
t a[230:231],v202,8448
m a[100:103],a[208:211],v[76:79],a[100:103]
t a[232:233],v203,256
t a[234:235],v203,8448
m a[40:43],a[212:215],v[68:71],a[40:43]
m a[104:107],a[212:215],v[76:79],a[104:107]
m a[44:47],a[216:219],v[68:71],a[44:47]
m a[108:111],a[216:219],v[76:79],a[108:111]
s_waitcnt lgkmcnt(0)
s_waitcnt vmcnt(49)
s_barrier
r a[204:207],v192,0
r a[208:211],v192,8192
r a[212:215],v193,0
r a[216:219],v193,8192
s_mov_b32 m0,0x1c000
s_mov_b32 s45,0x1c080
dn v144,%4,s45,0
dn v145,%4,s45,1024
dn v146,%4,s45,2048
dn v147,%4,s45,3072
s_mov_b32 m0,0x1d000
s_mov_b32 s45,0x23080
dn v144,%4,s45,0
dn v145,%4,s45,1024
dn v146,%4,s45,2048
dn v147,%4,s45,3072
s_mov_b32 m0,0x1e000
s_mov_b32 s45,0x2a080
dn v144,%4,s45,0
dn v145,%4,s45,1024
dn v146,%4,s45,2048
dn v147,%4,s45,3072
s_mov_b32 m0,0x1f000
s_mov_b32 s45,0x31080
dn v144,%4,s45,0
dn v145,%4,s45,1024
dn v146,%4,s45,2048
dn v147,%4,s45,3072
m a[48:51],a[220:223],v[68:71],a[48:51]
m a[112:115],a[220:223],v[76:79],a[112:115]
m a[52:55],a[224:227],v[68:71],a[52:55]
m a[116:119],a[224:227],v[76:79],a[116:119]
m a[56:59],a[228:231],v[68:71],a[56:59]
m a[120:123],a[228:231],v[76:79],a[120:123]
m a[60:63],a[232:235],v[68:71],a[60:63]
m a[124:127],a[232:235],v[76:79],a[124:127]
s_waitcnt lgkmcnt(0)
m v[4:7],a[204:207],v[0:3],v[84:87]
r a[220:223],v194,0
r a[224:227],v194,8192
r a[228:231],v195,0
m v[20:23],a[204:207],v[16:19],v[92:95]
r a[232:235],v195,8192
v_add_u32 v196,s42,v196
v_add_u32 v197,s42,v197
m v[12:15],a[208:211],v[0:3],v[84:87]
v_add_u32 v198,s42,v198
v_add_u32 v199,s42,v199
v_add_u32 v200,s42,v200
v_add_u32 v201,s42,v201
m v[28:31],a[208:211],v[16:19],v[92:95]
v_add_u32 v202,s42,v202
v_add_u32 v203,s42,v203
m v[4:7],a[212:215],v[8:11],v[4:7]
v_exp_f32 v220,v36
m v[20:23],a[212:215],v[24:27],v[20:23]
v_exp_f32 v221,v37
v_exp_f32 v222,v38
m v[12:15],a[216:219],v[8:11],v[12:15]
v_exp_f32 v223,v39
cv v68,v220,v221
m v[28:31],a[216:219],v[24:27],v[28:31]
v_exp_f32 v224,v44
v_exp_f32 v225,v45
s_waitcnt lgkmcnt(0)
m v[4:7],a[220:223],v[32:35],v[4:7]
r a[204:207],v192,256
r a[208:211],v192,8448
m v[20:23],a[220:223],v[48:51],v[20:23]
r a[212:215],v193,256
r a[216:219],v193,8448
m v[12:15],a[224:227],v[32:35],v[12:15]
cv v69,v222,v223
m v[28:31],a[224:227],v[48:51],v[28:31]
v_exp_f32 v220,v46
m v[4:7],a[228:231],v[40:43],v[4:7]
v_exp_f32 v221,v47
m v[20:23],a[228:231],v[56:59],v[20:23]
cv v70,v224,v225
m v[12:15],a[232:235],v[40:43],v[12:15]
v_exp_f32 v222,v52
m v[28:31],a[232:235],v[56:59],v[28:31]
v_exp_f32 v223,v53
cv v71,v220,v221
s_waitcnt lgkmcnt(0)
m v[4:7],a[204:207],v[64:67],v[4:7]
r a[220:223],v194,256
r a[224:227],v194,8448
m v[20:23],a[204:207],v[80:83],v[20:23]
r a[228:231],v195,256
r a[232:235],v195,8448
m v[12:15],a[208:211],v[64:67],v[12:15]
v_exp_f32 v224,v54
m v[28:31],a[208:211],v[80:83],v[28:31]
v_exp_f32 v225,v55
m v[4:7],a[212:215],v[72:75],v[4:7]
cv v76,v222,v223
m v[20:23],a[212:215],v[88:91],v[20:23]
v_exp_f32 v220,v60
m v[12:15],a[216:219],v[72:75],v[12:15]
v_exp_f32 v221,v61
m v[28:31],a[216:219],v[88:91],v[28:31]
cv v77,v224,v225
v_exp_f32 v222,v62
s_waitcnt lgkmcnt(0)
m v[4:7],a[220:223],v[96:99],v[4:7]
t a[204:205],v196,0
t a[206:207],v196,8192
m v[20:23],a[220:223],v[112:115],v[20:23]
t a[208:209],v197,0
t a[210:211],v197,8192
m v[12:15],a[224:227],v[96:99],v[12:15]
t a[212:213],v198,0
t a[214:215],v198,8192
m v[28:31],a[224:227],v[112:115],v[28:31]
t a[216:217],v199,0
t a[218:219],v199,8192
m v[4:7],a[228:231],v[104:107],v[4:7]
v_exp_f32 v223,v63
m v[20:23],a[228:231],v[120:123],v[20:23]
cv v78,v220,v221
cv v79,v222,v223
m v[12:15],a[232:235],v[104:107],v[12:15]
v_or3_b32 v228,v68,v69,v70
v_or3_b32 v228,v228,v71,v76
m v[28:31],a[232:235],v[120:123],v[28:31]
v_or3_b32 v228,v228,v77,v78
v_or_b32 v228,v228,v79
v_and_b32 v228,0x40004000,v228
v_cmp_ne_u32_e32 vcc,0,v228
s_nop 0
s_cbranch_vccnz LC_stub7%=
LC_ret7%=:
s_waitcnt lgkmcnt(0)
m a[192:195],v[100:103],v[68:71],a[192:195]
t a[220:221],v200,0
t a[222:223],v200,8192
m a[196:199],v[100:103],v[76:79],a[196:199]
t a[224:225],v201,0
t a[226:227],v201,8192
m a[0:3],a[204:207],v[68:71],a[0:3]
t a[228:229],v202,0
t a[230:231],v202,8192
m a[64:67],a[204:207],v[76:79],a[64:67]
t a[232:233],v203,0
t a[234:235],v203,8192
m a[4:7],a[208:211],v[68:71],a[4:7]
m a[68:71],a[208:211],v[76:79],a[68:71]
m a[8:11],a[212:215],v[68:71],a[8:11]
m a[72:75],a[212:215],v[76:79],a[72:75]
m a[12:15],a[216:219],v[68:71],a[12:15]
m a[76:79],a[216:219],v[76:79],a[76:79]
s_waitcnt lgkmcnt(0)
m a[16:19],a[220:223],v[68:71],a[16:19]
t a[204:205],v196,256
t a[206:207],v196,8448
m a[80:83],a[220:223],v[76:79],a[80:83]
t a[208:209],v197,256
t a[210:211],v197,8448
m a[20:23],a[224:227],v[68:71],a[20:23]
t a[212:213],v198,256
t a[214:215],v198,8448
m a[84:87],a[224:227],v[76:79],a[84:87]
t a[216:217],v199,256
t a[218:219],v199,8448
m a[24:27],a[228:231],v[68:71],a[24:27]
s_mov_b32 s41,s40
s_add_u32 s40,s40,0x8000
m a[88:91],a[228:231],v[76:79],a[88:91]
s_cmp_eq_u32 s40,0x28000
s_cselect_b32 s42,s49,s48
m a[28:31],a[232:235],v[68:71],a[28:31]
s_cselect_b32 s40,0,s40
v_add_u32 v192,s42,v192
m a[92:95],a[232:235],v[76:79],a[92:95]
v_add_u32 v193,s42,v193
v_add_u32 v194,s42,v194
v_add_u32 v195,s42,v195
s_waitcnt lgkmcnt(0)
m a[32:35],a[204:207],v[68:71],a[32:35]
t a[220:221],v200,256
t a[222:223],v200,8448
m a[96:99],a[204:207],v[76:79],a[96:99]
t a[224:225],v201,256
t a[226:227],v201,8448
m a[36:39],a[208:211],v[68:71],a[36:39]
t a[228:229],v202,256
t a[230:231],v202,8448
m a[100:103],a[208:211],v[76:79],a[100:103]
t a[232:233],v203,256
t a[234:235],v203,8448
m a[40:43],a[212:215],v[68:71],a[40:43]
m a[104:107],a[212:215],v[76:79],a[104:107]
m a[44:47],a[216:219],v[68:71],a[44:47]
m a[108:111],a[216:219],v[76:79],a[108:111]
s_waitcnt lgkmcnt(0)
s_waitcnt vmcnt(49)
s_barrier
r a[204:207],v192,0
r a[208:211],v192,8192
r a[212:215],v193,0
r a[216:219],v193,8192
s_mov_b32 m0,0x24000
s_mov_b32 s45,0x1c100
dn v144,%4,s45,0
dn v145,%4,s45,1024
dn v146,%4,s45,2048
dn v147,%4,s45,3072
s_mov_b32 m0,0x25000
s_mov_b32 s45,0x23100
dn v144,%4,s45,0
dn v145,%4,s45,1024
dn v146,%4,s45,2048
dn v147,%4,s45,3072
s_mov_b32 m0,0x26000
s_mov_b32 s45,0x2a100
dn v144,%4,s45,0
dn v145,%4,s45,1024
dn v146,%4,s45,2048
dn v147,%4,s45,3072
s_mov_b32 m0,0x27000
s_mov_b32 s45,0x31100
dn v144,%4,s45,0
dn v145,%4,s45,1024
dn v146,%4,s45,2048
dn v147,%4,s45,3072
m a[48:51],a[220:223],v[68:71],a[48:51]
m a[112:115],a[220:223],v[76:79],a[112:115]
m a[52:55],a[224:227],v[68:71],a[52:55]
m a[116:119],a[224:227],v[76:79],a[116:119]
m a[56:59],a[228:231],v[68:71],a[56:59]
m a[120:123],a[228:231],v[76:79],a[120:123]
m a[60:63],a[232:235],v[68:71],a[60:63]
m a[124:127],a[232:235],v[76:79],a[124:127]
s_waitcnt lgkmcnt(0)
m v[36:39],a[204:207],v[0:3],v[84:87]
r a[220:223],v194,0
r a[224:227],v194,8192
r a[228:231],v195,0
m v[52:55],a[204:207],v[16:19],v[92:95]
r a[232:235],v195,8192
v_add_u32 v196,s42,v196
v_add_u32 v197,s42,v197
m v[44:47],a[208:211],v[0:3],v[84:87]
v_add_u32 v198,s42,v198
v_add_u32 v199,s42,v199
v_add_u32 v200,s42,v200
v_add_u32 v201,s42,v201
m v[60:63],a[208:211],v[16:19],v[92:95]
v_add_u32 v202,s42,v202
v_add_u32 v203,s42,v203
m v[36:39],a[212:215],v[8:11],v[36:39]
v_exp_f32 v220,v4
m v[52:55],a[212:215],v[24:27],v[52:55]
v_exp_f32 v221,v5
v_exp_f32 v222,v6
m v[44:47],a[216:219],v[8:11],v[44:47]
v_exp_f32 v223,v7
cv v68,v220,v221
m v[60:63],a[216:219],v[24:27],v[60:63]
v_exp_f32 v224,v12
v_exp_f32 v225,v13
s_waitcnt lgkmcnt(0)
m v[36:39],a[220:223],v[32:35],v[36:39]
r a[204:207],v192,256
r a[208:211],v192,8448
m v[52:55],a[220:223],v[48:51],v[52:55]
r a[212:215],v193,256
r a[216:219],v193,8448
m v[44:47],a[224:227],v[32:35],v[44:47]
cv v69,v222,v223
m v[60:63],a[224:227],v[48:51],v[60:63]
v_exp_f32 v220,v14
m v[36:39],a[228:231],v[40:43],v[36:39]
v_exp_f32 v221,v15
m v[52:55],a[228:231],v[56:59],v[52:55]
cv v70,v224,v225
m v[44:47],a[232:235],v[40:43],v[44:47]
v_exp_f32 v222,v20
m v[60:63],a[232:235],v[56:59],v[60:63]
v_exp_f32 v223,v21
cv v71,v220,v221
s_waitcnt lgkmcnt(0)
m v[36:39],a[204:207],v[64:67],v[36:39]
r a[220:223],v194,256
r a[224:227],v194,8448
m v[52:55],a[204:207],v[80:83],v[52:55]
r a[228:231],v195,256
r a[232:235],v195,8448
m v[44:47],a[208:211],v[64:67],v[44:47]
v_exp_f32 v224,v22
m v[60:63],a[208:211],v[80:83],v[60:63]
v_exp_f32 v225,v23
m v[36:39],a[212:215],v[72:75],v[36:39]
cv v76,v222,v223
m v[52:55],a[212:215],v[88:91],v[52:55]
v_exp_f32 v220,v28
m v[44:47],a[216:219],v[72:75],v[44:47]
v_exp_f32 v221,v29
m v[60:63],a[216:219],v[88:91],v[60:63]
cv v77,v224,v225
v_exp_f32 v222,v30
s_waitcnt lgkmcnt(0)
m v[36:39],a[220:223],v[96:99],v[36:39]
t a[204:205],v196,0
t a[206:207],v196,8192
m v[52:55],a[220:223],v[112:115],v[52:55]
t a[208:209],v197,0
t a[210:211],v197,8192
m v[44:47],a[224:227],v[96:99],v[44:47]
t a[212:213],v198,0
t a[214:215],v198,8192
m v[60:63],a[224:227],v[112:115],v[60:63]
t a[216:217],v199,0
t a[218:219],v199,8192
m v[36:39],a[228:231],v[104:107],v[36:39]
v_exp_f32 v223,v31
m v[52:55],a[228:231],v[120:123],v[52:55]
cv v78,v220,v221
cv v79,v222,v223
m v[44:47],a[232:235],v[104:107],v[44:47]
v_or3_b32 v228,v68,v69,v70
v_or3_b32 v228,v228,v71,v76
m v[60:63],a[232:235],v[120:123],v[60:63]
v_or3_b32 v228,v228,v77,v78
v_or_b32 v228,v228,v79
v_and_b32 v228,0x40004000,v228
v_cmp_ne_u32_e32 vcc,0,v228
s_nop 0
s_cbranch_vccnz LC_stub8%=
LC_ret8%=:
s_waitcnt lgkmcnt(0)
m a[192:195],v[100:103],v[68:71],a[192:195]
t a[220:221],v200,0
t a[222:223],v200,8192
m a[196:199],v[100:103],v[76:79],a[196:199]
t a[224:225],v201,0
t a[226:227],v201,8192
m a[0:3],a[204:207],v[68:71],a[0:3]
t a[228:229],v202,0
t a[230:231],v202,8192
m a[64:67],a[204:207],v[76:79],a[64:67]
t a[232:233],v203,0
t a[234:235],v203,8192
m a[4:7],a[208:211],v[68:71],a[4:7]
m a[68:71],a[208:211],v[76:79],a[68:71]
m a[8:11],a[212:215],v[68:71],a[8:11]
m a[72:75],a[212:215],v[76:79],a[72:75]
m a[12:15],a[216:219],v[68:71],a[12:15]
m a[76:79],a[216:219],v[76:79],a[76:79]
s_waitcnt lgkmcnt(0)
m a[16:19],a[220:223],v[68:71],a[16:19]
t a[204:205],v196,256
t a[206:207],v196,8448
m a[80:83],a[220:223],v[76:79],a[80:83]
t a[208:209],v197,256
t a[210:211],v197,8448
m a[20:23],a[224:227],v[68:71],a[20:23]
t a[212:213],v198,256
t a[214:215],v198,8448
m a[84:87],a[224:227],v[76:79],a[84:87]
t a[216:217],v199,256
t a[218:219],v199,8448
m a[24:27],a[228:231],v[68:71],a[24:27]
s_mov_b32 s41,s40
s_add_u32 s40,s40,0x8000
m a[88:91],a[228:231],v[76:79],a[88:91]
s_cmp_eq_u32 s40,0x28000
s_cselect_b32 s42,s49,s48
m a[28:31],a[232:235],v[68:71],a[28:31]
s_cselect_b32 s40,0,s40
v_add_u32 v192,s42,v192
m a[92:95],a[232:235],v[76:79],a[92:95]
v_add_u32 v193,s42,v193
v_add_u32 v194,s42,v194
v_add_u32 v195,s42,v195
s_waitcnt lgkmcnt(0)
m a[32:35],a[204:207],v[68:71],a[32:35]
t a[220:221],v200,256
t a[222:223],v200,8448
m a[96:99],a[204:207],v[76:79],a[96:99]
t a[224:225],v201,256
t a[226:227],v201,8448
m a[36:39],a[208:211],v[68:71],a[36:39]
t a[228:229],v202,256
t a[230:231],v202,8448
m a[100:103],a[208:211],v[76:79],a[100:103]
t a[232:233],v203,256
t a[234:235],v203,8448
m a[40:43],a[212:215],v[68:71],a[40:43]
v_add_u32 v196,s42,v196
v_add_u32 v197,s42,v197
m a[104:107],a[212:215],v[76:79],a[104:107]
v_add_u32 v198,s42,v198
v_add_u32 v199,s42,v199
m a[44:47],a[216:219],v[68:71],a[44:47]
v_add_u32 v200,s42,v200
v_add_u32 v201,s42,v201
m a[108:111],a[216:219],v[76:79],a[108:111]
v_add_u32 v202,s42,v202
v_add_u32 v203,s42,v203
s_waitcnt lgkmcnt(0)
s_waitcnt vmcnt(49)
s_barrier
t a[204:205],v196,0
t a[206:207],v196,8192
t a[208:209],v197,0
t a[210:211],v197,8192
t a[212:213],v198,0
t a[214:215],v198,8192
t a[216:217],v199,0
t a[218:219],v199,8192
s_mov_b32 m0,0x4000
s_mov_b32 s45,0x1c180
dn v144,%4,s45,0
dn v145,%4,s45,1024
dn v146,%4,s45,2048
dn v147,%4,s45,3072
s_mov_b32 m0,0x5000
s_mov_b32 s45,0x23180
dn v144,%4,s45,0
dn v145,%4,s45,1024
dn v146,%4,s45,2048
dn v147,%4,s45,3072
s_mov_b32 m0,0x6000
s_mov_b32 s45,0x2a180
dn v144,%4,s45,0
dn v145,%4,s45,1024
dn v146,%4,s45,2048
dn v147,%4,s45,3072
s_mov_b32 m0,0x7000
s_mov_b32 s45,0x31180
dn v144,%4,s45,0
dn v145,%4,s45,1024
dn v146,%4,s45,2048
dn v147,%4,s45,3072
m a[48:51],a[220:223],v[68:71],a[48:51]
m a[112:115],a[220:223],v[76:79],a[112:115]
m a[52:55],a[224:227],v[68:71],a[52:55]
m a[116:119],a[224:227],v[76:79],a[116:119]
m a[56:59],a[228:231],v[68:71],a[56:59]
m a[120:123],a[228:231],v[76:79],a[120:123]
m a[60:63],a[232:235],v[68:71],a[60:63]
m a[124:127],a[232:235],v[76:79],a[124:127]
v_exp_f32 v220,v36
v_exp_f32 v221,v37
v_exp_f32 v222,v38
v_exp_f32 v223,v39
cv v68,v220,v221
v_exp_f32 v224,v44
v_exp_f32 v225,v45
cv v69,v222,v223
v_exp_f32 v220,v46
v_exp_f32 v221,v47
cv v70,v224,v225
v_exp_f32 v222,v52
v_exp_f32 v223,v53
cv v71,v220,v221
v_exp_f32 v224,v54
v_exp_f32 v225,v55
cv v76,v222,v223
v_exp_f32 v220,v60
v_exp_f32 v221,v61
cv v77,v224,v225
v_exp_f32 v222,v62
v_exp_f32 v223,v63
cv v78,v220,v221
cv v79,v222,v223
v_or3_b32 v228,v68,v69,v70
v_or3_b32 v228,v228,v71,v76
v_or3_b32 v228,v228,v77,v78
v_or_b32 v228,v228,v79
v_and_b32 v228,0x40004000,v228
v_cmp_ne_u32_e32 vcc,0,v228
s_nop 0
s_cbranch_vccnz LC_stub9%=
LC_ret9%=:
s_waitcnt lgkmcnt(0)
m a[192:195],v[100:103],v[68:71],a[192:195]
t a[220:221],v200,0
t a[222:223],v200,8192
m a[196:199],v[100:103],v[76:79],a[196:199]
t a[224:225],v201,0
t a[226:227],v201,8192
m a[0:3],a[204:207],v[68:71],a[0:3]
t a[228:229],v202,0
t a[230:231],v202,8192
m a[64:67],a[204:207],v[76:79],a[64:67]
t a[232:233],v203,0
t a[234:235],v203,8192
m a[4:7],a[208:211],v[68:71],a[4:7]
m a[68:71],a[208:211],v[76:79],a[68:71]
m a[8:11],a[212:215],v[68:71],a[8:11]
m a[72:75],a[212:215],v[76:79],a[72:75]
m a[12:15],a[216:219],v[68:71],a[12:15]
m a[76:79],a[216:219],v[76:79],a[76:79]
s_waitcnt lgkmcnt(0)
m a[16:19],a[220:223],v[68:71],a[16:19]
t a[204:205],v196,256
t a[206:207],v196,8448
t a[208:209],v197,256
m a[80:83],a[220:223],v[76:79],a[80:83]
t a[210:211],v197,8448
t a[212:213],v198,256
t a[214:215],v198,8448
t a[216:217],v199,256
m a[20:23],a[224:227],v[68:71],a[20:23]
t a[218:219],v199,8448
ar v216,a192
ar v217,a196
m a[84:87],a[224:227],v[76:79],a[84:87]
v_rcp_f32 v216,v216
v_rcp_f32 v217,v217
m a[24:27],a[228:231],v[68:71],a[24:27]
ar v96,a0
ar v97,a1
ar v98,a2
ar v99,a3
m a[88:91],a[228:231],v[76:79],a[88:91]
ar v100,a4
ar v101,a5
ar v102,a6
m a[28:31],a[232:235],v[68:71],a[28:31]
ar v103,a7
cv v0,v96,v97
cv v1,v98,v99
cv v2,v100,v101
m a[92:95],a[232:235],v[76:79],a[92:95]
cv v3,v102,v103
ar v104,a64
ar v105,a65
ar v106,a66
s_waitcnt lgkmcnt(0)
m a[32:35],a[204:207],v[68:71],a[32:35]
t a[220:221],v200,256
t a[222:223],v200,8448
t a[224:225],v201,256
m a[96:99],a[204:207],v[76:79],a[96:99]
t a[226:227],v201,8448
t a[228:229],v202,256
t a[230:231],v202,8448
m a[36:39],a[208:211],v[68:71],a[36:39]
t a[232:233],v203,256
t a[234:235],v203,8448
ar v107,a67
ar v108,a68
m a[100:103],a[208:211],v[76:79],a[100:103]
ar v109,a69
ar v110,a70
ar v111,a71
m a[40:43],a[212:215],v[68:71],a[40:43]
cv v32,v104,v105
cv v33,v106,v107
cv v34,v108,v109
m a[104:107],a[212:215],v[76:79],a[104:107]
cv v35,v110,v111
ar v96,a8
ar v97,a9
ar v98,a10
m a[44:47],a[216:219],v[68:71],a[44:47]
ar v99,a11
ar v100,a12
ar v101,a13
m a[108:111],a[216:219],v[76:79],a[108:111]
ar v102,a14
ar v103,a15
cv v4,v96,v97
cv v5,v98,v99
s_waitcnt lgkmcnt(0)
s_waitcnt vmcnt(48)
s_barrier
t a[204:205],v208,0
t a[206:207],v208,2048
t a[208:209],v209,0
t a[210:211],v209,2048
t a[212:213],v210,0
t a[214:215],v210,2048
t a[216:217],v211,0
t a[218:219],v211,2048
s_mov_b32 m0,0xc000
s_mov_b32 s45,0x1c200
dn v144,%4,s45,0
dn v145,%4,s45,1024
dn v146,%4,s45,2048
dn v147,%4,s45,3072
s_mov_b32 m0,0xd000
s_mov_b32 s45,0x23200
dn v144,%4,s45,0
dn v145,%4,s45,1024
dn v146,%4,s45,2048
dn v147,%4,s45,3072
s_mov_b32 m0,0xe000
s_mov_b32 s45,0x2a200
dn v144,%4,s45,0
dn v145,%4,s45,1024
dn v146,%4,s45,2048
dn v147,%4,s45,3072
s_mov_b32 m0,0xf000
s_mov_b32 s45,0x31200
dn v144,%4,s45,0
dn v145,%4,s45,1024
dn v146,%4,s45,2048
dn v147,%4,s45,3072
m a[48:51],a[220:223],v[68:71],a[48:51]
cv v6,v100,v101
cv v7,v102,v103
m a[112:115],a[220:223],v[76:79],a[112:115]
ar v104,a72
ar v105,a73
ar v106,a74
m a[52:55],a[224:227],v[68:71],a[52:55]
ar v107,a75
ar v108,a76
m a[116:119],a[224:227],v[76:79],a[116:119]
ar v109,a77
ar v110,a78
ar v111,a79
m a[56:59],a[228:231],v[68:71],a[56:59]
cv v36,v104,v105
cv v37,v106,v107
m a[120:123],a[228:231],v[76:79],a[120:123]
cv v38,v108,v109
cv v39,v110,v111
ar v96,a16
m a[60:63],a[232:235],v[68:71],a[60:63]
ar v97,a17
ar v98,a18
m a[124:127],a[232:235],v[76:79],a[124:127]
ar v99,a19
ar v100,a20
ar v101,a21
s_waitcnt lgkmcnt(0)
m v[64:67],a[204:207],v[0:3],0
t a[220:221],v208,4096
t a[222:223],v208,6144
t a[224:225],v209,4096
m v[80:83],a[204:207],v[32:35],0
t a[226:227],v209,6144
t a[228:229],v210,4096
t a[230:231],v210,6144
m v[68:71],a[208:211],v[0:3],0
t a[232:233],v211,4096
t a[234:235],v211,6144
ar v102,a22
ar v103,a23
m v[84:87],a[208:211],v[32:35],0
cv v8,v96,v97
cv v9,v98,v99
cv v10,v100,v101
m v[72:75],a[212:215],v[0:3],0
cv v11,v102,v103
ar v104,a80
ar v105,a81
m v[88:91],a[212:215],v[32:35],0
ar v106,a82
ar v107,a83
ar v108,a84
ar v109,a85
m v[76:79],a[216:219],v[0:3],0
ar v110,a86
ar v111,a87
cv v40,v104,v105
m v[92:95],a[216:219],v[32:35],0
cv v41,v106,v107
cv v42,v108,v109
cv v43,v110,v111
ar v96,a24
s_waitcnt lgkmcnt(0)
m v[64:67],a[220:223],v[4:7],v[64:67]
t a[204:205],v208,8192
t a[206:207],v208,10240
t a[208:209],v209,8192
m v[80:83],a[220:223],v[36:39],v[80:83]
t a[210:211],v209,10240
t a[212:213],v210,8192
t a[214:215],v210,10240
t a[216:217],v211,8192
m v[68:71],a[224:227],v[4:7],v[68:71]
t a[218:219],v211,10240
ar v97,a25
ar v98,a26
m v[84:87],a[224:227],v[36:39],v[84:87]
ar v99,a27
ar v100,a28
ar v101,a29
ar v102,a30
m v[72:75],a[228:231],v[4:7],v[72:75]
ar v103,a31
cv v12,v96,v97
cv v13,v98,v99
m v[88:91],a[228:231],v[36:39],v[88:91]
cv v14,v100,v101
cv v15,v102,v103
ar v104,a88
ar v105,a89
m v[76:79],a[232:235],v[4:7],v[76:79]
ar v106,a90
ar v107,a91
ar v108,a92
m v[92:95],a[232:235],v[36:39],v[92:95]
ar v109,a93
ar v110,a94
ar v111,a95
cv v44,v104,v105
s_waitcnt lgkmcnt(0)
m v[64:67],a[204:207],v[8:11],v[64:67]
t a[220:221],v208,12288
t a[222:223],v208,14336
t a[224:225],v209,12288
m v[80:83],a[204:207],v[40:43],v[80:83]
t a[226:227],v209,14336
t a[228:229],v210,12288
t a[230:231],v210,14336
t a[232:233],v211,12288
m v[68:71],a[208:211],v[8:11],v[68:71]
t a[234:235],v211,14336
cv v45,v106,v107
cv v46,v108,v109
m v[84:87],a[208:211],v[40:43],v[84:87]
cv v47,v110,v111
ar v96,a32
ar v97,a33
ar v98,a34
m v[72:75],a[212:215],v[8:11],v[72:75]
ar v99,a35
ar v100,a36
ar v101,a37
m v[88:91],a[212:215],v[40:43],v[88:91]
ar v102,a38
ar v103,a39
cv v16,v96,v97
cv v17,v98,v99
m v[76:79],a[216:219],v[8:11],v[76:79]
cv v18,v100,v101
cv v19,v102,v103
ar v104,a96
m v[92:95],a[216:219],v[40:43],v[92:95]
ar v105,a97
ar v106,a98
ar v107,a99
ar v108,a100
s_waitcnt lgkmcnt(0)
m v[64:67],a[220:223],v[12:15],v[64:67]
t a[204:205],v208,16384
t a[206:207],v208,18432
t a[208:209],v209,16384
m v[80:83],a[220:223],v[44:47],v[80:83]
t a[210:211],v209,18432
t a[212:213],v210,16384
t a[214:215],v210,18432
m v[68:71],a[224:227],v[12:15],v[68:71]
t a[216:217],v211,16384
t a[218:219],v211,18432
ar v109,a101
ar v110,a102
m v[84:87],a[224:227],v[44:47],v[84:87]
ar v111,a103
cv v48,v104,v105
cv v49,v106,v107
m v[72:75],a[228:231],v[12:15],v[72:75]
cv v50,v108,v109
cv v51,v110,v111
ar v96,a40
m v[88:91],a[228:231],v[44:47],v[88:91]
ar v97,a41
ar v98,a42
ar v99,a43
ar v100,a44
m v[76:79],a[232:235],v[12:15],v[76:79]
ar v101,a45
ar v102,a46
ar v103,a47
m v[92:95],a[232:235],v[44:47],v[92:95]
cv v20,v96,v97
cv v21,v98,v99
cv v22,v100,v101
cv v23,v102,v103
s_waitcnt lgkmcnt(0)
m v[64:67],a[204:207],v[16:19],v[64:67]
t a[220:221],v208,20480
t a[222:223],v208,22528
t a[224:225],v209,20480
m v[80:83],a[204:207],v[48:51],v[80:83]
t a[226:227],v209,22528
t a[228:229],v210,20480
t a[230:231],v210,22528
t a[232:233],v211,20480
m v[68:71],a[208:211],v[16:19],v[68:71]
t a[234:235],v211,22528
ar v104,a104
ar v105,a105
m v[84:87],a[208:211],v[48:51],v[84:87]
ar v106,a106
ar v107,a107
ar v108,a108
ar v109,a109
m v[72:75],a[212:215],v[16:19],v[72:75]
ar v110,a110
ar v111,a111
cv v52,v104,v105
m v[88:91],a[212:215],v[48:51],v[88:91]
cv v53,v106,v107
cv v54,v108,v109
cv v55,v110,v111
ar v96,a48
m v[76:79],a[216:219],v[16:19],v[76:79]
ar v97,a49
ar v98,a50
ar v99,a51
m v[92:95],a[216:219],v[48:51],v[92:95]
ar v100,a52
ar v101,a53
ar v102,a54
ar v103,a55
s_waitcnt lgkmcnt(0)
m v[64:67],a[220:223],v[20:23],v[64:67]
t a[204:205],v208,24576
t a[206:207],v208,26624
t a[208:209],v209,24576
m v[80:83],a[220:223],v[52:55],v[80:83]
t a[210:211],v209,26624
t a[212:213],v210,24576
t a[214:215],v210,26624
t a[216:217],v211,24576
m v[68:71],a[224:227],v[20:23],v[68:71]
t a[218:219],v211,26624
cv v24,v96,v97
cv v25,v98,v99
m v[84:87],a[224:227],v[52:55],v[84:87]
cv v26,v100,v101
cv v27,v102,v103
ar v104,a112
ar v105,a113
m v[72:75],a[228:231],v[20:23],v[72:75]
ar v106,a114
ar v107,a115
ar v108,a116
m v[88:91],a[228:231],v[52:55],v[88:91]
ar v109,a117
ar v110,a118
ar v111,a119
cv v56,v104,v105
m v[76:79],a[232:235],v[20:23],v[76:79]
cv v57,v106,v107
cv v58,v108,v109
cv v59,v110,v111
m v[92:95],a[232:235],v[52:55],v[92:95]
ar v96,a56
ar v97,a57
ar v98,a58
ar v99,a59
s_waitcnt lgkmcnt(0)
m v[64:67],a[204:207],v[24:27],v[64:67]
t a[220:221],v208,28672
t a[222:223],v208,30720
t a[224:225],v209,28672
t a[226:227],v209,30720
m v[80:83],a[204:207],v[56:59],v[80:83]
t a[228:229],v210,28672
t a[230:231],v210,30720
t a[232:233],v211,28672
t a[234:235],v211,30720
m v[68:71],a[208:211],v[24:27],v[68:71]
v_add_u32 v208,s48,v208
v_add_u32 v209,s48,v209
v_add_u32 v210,s48,v210
v_add_u32 v211,s48,v211
m v[84:87],a[208:211],v[56:59],v[84:87]
ar v100,a60
ar v101,a61
ar v102,a62
ar v103,a63
m v[72:75],a[212:215],v[24:27],v[72:75]
cv v28,v96,v97
cv v29,v98,v99
cv v30,v100,v101
cv v31,v102,v103
m v[88:91],a[212:215],v[56:59],v[88:91]
ar v104,a120
ar v105,a121
ar v106,a122
ar v107,a123
m v[76:79],a[216:219],v[24:27],v[76:79]
ar v108,a124
ar v109,a125
ar v110,a126
ar v111,a127
m v[92:95],a[216:219],v[56:59],v[92:95]
cv v60,v104,v105
cv v61,v106,v107
cv v62,v108,v109
cv v63,v110,v111
s_waitcnt lgkmcnt(0)
s_waitcnt vmcnt(48)
s_barrier
t a[204:205],v208,0
t a[206:207],v208,2048
t a[208:209],v209,0
t a[210:211],v209,2048
t a[212:213],v210,0
t a[214:215],v210,2048
t a[216:217],v211,0
t a[218:219],v211,2048
s_mov_b32 m0,0x14000
s_mov_b32 s45,0x1c280
dn v144,%4,s45,0
dn v145,%4,s45,1024
dn v146,%4,s45,2048
dn v147,%4,s45,3072
s_mov_b32 m0,0x15000
s_mov_b32 s45,0x23280
dn v144,%4,s45,0
dn v145,%4,s45,1024
dn v146,%4,s45,2048
dn v147,%4,s45,3072
s_mov_b32 m0,0x16000
s_mov_b32 s45,0x2a280
dn v144,%4,s45,0
dn v145,%4,s45,1024
dn v146,%4,s45,2048
dn v147,%4,s45,3072
s_mov_b32 m0,0x17000
s_mov_b32 s45,0x31280
dn v144,%4,s45,0
dn v145,%4,s45,1024
dn v146,%4,s45,2048
dn v147,%4,s45,3072
m v[64:67],a[220:223],v[28:31],v[64:67]
m v[80:83],a[220:223],v[60:63],v[80:83]
m v[68:71],a[224:227],v[28:31],v[68:71]
m v[84:87],a[224:227],v[60:63],v[84:87]
m v[72:75],a[228:231],v[28:31],v[72:75]
m v[88:91],a[228:231],v[60:63],v[88:91]
m v[76:79],a[232:235],v[28:31],v[76:79]
m v[92:95],a[232:235],v[60:63],v[92:95]
s_waitcnt lgkmcnt(0)
m v[96:99],a[204:207],v[0:3],0
t a[220:221],v208,4096
t a[222:223],v208,6144
m v[112:115],a[204:207],v[32:35],0
t a[224:225],v209,4096
t a[226:227],v209,6144
m v[100:103],a[208:211],v[0:3],0
t a[228:229],v210,4096
t a[230:231],v210,6144
m v[116:119],a[208:211],v[32:35],0
t a[232:233],v211,4096
t a[234:235],v211,6144
m v[104:107],a[212:215],v[0:3],0
v_lshrrev_b32 v235,4,v214
v_bfe_u32 v236,v214,2,2
m v[120:123],a[212:215],v[32:35],0
v_cmp_eq_u32_e32 vcc,v235,v236
m v[108:111],a[216:219],v[0:3],0
m v[124:127],a[216:219],v[32:35],0
s_waitcnt lgkmcnt(0)
m v[96:99],a[220:223],v[4:7],v[96:99]
t a[204:205],v208,8192
t a[206:207],v208,10240
t a[208:209],v209,8192
m v[112:115],a[220:223],v[36:39],v[112:115]
t a[210:211],v209,10240
t a[212:213],v210,8192
t a[214:215],v210,10240
m v[100:103],a[224:227],v[4:7],v[100:103]
t a[216:217],v211,8192
t a[218:219],v211,10240
s_add_u32 s55,s54,0
m v[116:119],a[224:227],v[36:39],v[116:119]
s_add_u32 s59,s55,0x1000
v_mul_f32 v64,v64,v216
v_mul_f32 v65,v65,v216
m v[104:107],a[228:231],v[4:7],v[104:107]
v_mul_f32 v66,v66,v216
v_mul_f32 v67,v67,v216
v_mul_f32 v68,v68,v216
m v[120:123],a[228:231],v[36:39],v[120:123]
v_mul_f32 v69,v69,v216
v_mul_f32 v70,v70,v216
v_mul_f32 v71,v71,v216
m v[108:111],a[232:235],v[4:7],v[108:111]
cv v64,v64,v65
cv v65,v66,v67
cv v66,v68,v69
m v[124:127],a[232:235],v[36:39],v[124:127]
v_and_b32 v237,1,v214
v_lshlrev_b32 v237,4,v237
v_mov_b32 v238,0x3f80
v_lshlrev_b32 v238,v237,v238
s_waitcnt lgkmcnt(0)
m v[96:99],a[204:207],v[8:11],v[96:99]
t a[220:221],v208,12288
t a[222:223],v208,14336
t a[224:225],v209,12288
m v[112:115],a[204:207],v[40:43],v[112:115]
t a[226:227],v209,14336
t a[228:229],v210,12288
t a[230:231],v210,14336
m v[100:103],a[208:211],v[8:11],v[100:103]
t a[232:233],v211,12288
t a[234:235],v211,14336
cv v67,v70,v71
m v[116:119],a[208:211],v[40:43],v[116:119]
buffer_store_dwordx4 v[64:67],v215,%5,s55 offen sc1
v_mul_f32 v72,v72,v216
v_mul_f32 v73,v73,v216
v_mul_f32 v74,v74,v216
m v[104:107],a[212:215],v[8:11],v[104:107]
v_mul_f32 v75,v75,v216
v_mul_f32 v76,v76,v216
v_mul_f32 v77,v77,v216
m v[120:123],a[212:215],v[40:43],v[120:123]
v_mul_f32 v78,v78,v216
v_mul_f32 v79,v79,v216
cv v72,v72,v73
m v[108:111],a[216:219],v[8:11],v[108:111]
cv v73,v74,v75
cv v74,v76,v77
cv v75,v78,v79
m v[124:127],a[216:219],v[40:43],v[124:127]
v_cndmask_b32_e32 v238,0,v238,vcc
v_bfe_u32 v237,v214,1,1
v_cmp_eq_u32_e32 vcc,0,v237
s_nop 1
v_cndmask_b32_e32 v222,0,v238,vcc
s_waitcnt lgkmcnt(0)
m v[96:99],a[220:223],v[12:15],v[96:99]
t a[204:205],v208,16384
t a[206:207],v208,18432
t a[208:209],v209,16384
m v[112:115],a[220:223],v[44:47],v[112:115]
t a[210:211],v209,18432
t a[212:213],v210,16384
t a[214:215],v210,18432
m v[100:103],a[224:227],v[12:15],v[100:103]
t a[216:217],v211,16384
t a[218:219],v211,18432
buffer_store_dwordx4 v[72:75],v215,%5,s55 offen offset:1024 sc1
m v[116:119],a[224:227],v[44:47],v[116:119]
v_mul_f32 v80,v80,v217
v_mul_f32 v81,v81,v217
v_mul_f32 v82,v82,v217
m v[104:107],a[228:231],v[12:15],v[104:107]
v_mul_f32 v83,v83,v217
v_mul_f32 v84,v84,v217
v_mul_f32 v85,v85,v217
m v[120:123],a[228:231],v[44:47],v[120:123]
v_mul_f32 v86,v86,v217
v_mul_f32 v87,v87,v217
cv v80,v80,v81
m v[108:111],a[232:235],v[12:15],v[108:111]
cv v81,v82,v83
cv v82,v84,v85
cv v83,v86,v87
m v[124:127],a[232:235],v[44:47],v[124:127]
v_sub_u32 v223,v238,v222
v_mov_b32 v220,0
v_mov_b32 v221,0
v_mov_b32 v224,0
s_waitcnt lgkmcnt(0)
m v[96:99],a[204:207],v[16:19],v[96:99]
t a[220:221],v208,20480
t a[222:223],v208,22528
t a[224:225],v209,20480
m v[112:115],a[204:207],v[48:51],v[112:115]
t a[226:227],v209,22528
t a[228:229],v210,20480
t a[230:231],v210,22528
m v[100:103],a[208:211],v[16:19],v[100:103]
t a[232:233],v211,20480
t a[234:235],v211,22528
buffer_store_dwordx4 v[80:83],v215,%5,s55 offen offset:2048 sc1
m v[116:119],a[208:211],v[48:51],v[116:119]
v_mul_f32 v88,v88,v217
v_mul_f32 v89,v89,v217
v_mul_f32 v90,v90,v217
v_mul_f32 v91,v91,v217
m v[104:107],a[212:215],v[16:19],v[104:107]
v_mul_f32 v92,v92,v217
v_mul_f32 v93,v93,v217
v_mul_f32 v94,v94,v217
m v[120:123],a[212:215],v[48:51],v[120:123]
v_mul_f32 v95,v95,v217
cv v88,v88,v89
cv v89,v90,v91
m v[108:111],a[216:219],v[16:19],v[108:111]
cv v90,v92,v93
cv v91,v94,v95
buffer_store_dwordx4 v[88:91],v215,%5,s55 offen offset:3072 sc1
m v[124:127],a[216:219],v[48:51],v[124:127]
v_mov_b32 v225,0
s_lshl_b32 s46,%11,3
v_lshlrev_b32 v234,7,v214
v_add_u32 v234,s46,v234
s_waitcnt lgkmcnt(0)
m v[96:99],a[220:223],v[20:23],v[96:99]
t a[204:205],v208,24576
t a[206:207],v208,26624
m v[112:115],a[220:223],v[52:55],v[112:115]
t a[208:209],v209,24576
t a[210:211],v209,26624
m v[100:103],a[224:227],v[20:23],v[100:103]
t a[212:213],v210,24576
t a[214:215],v210,26624
m v[116:119],a[224:227],v[52:55],v[116:119]
t a[216:217],v211,24576
t a[218:219],v211,26624
m v[104:107],a[228:231],v[20:23],v[104:107]
v_add_u32 v234,4,v234
v_cmp_gt_u32_e32 vcc,16,v214
m v[120:123],a[228:231],v[52:55],v[120:123]
v_mov_b32 v235,0x7fffff00
m v[108:111],a[232:235],v[20:23],v[108:111]
m v[124:127],a[232:235],v[52:55],v[124:127]
s_waitcnt lgkmcnt(0)
m v[96:99],a[204:207],v[24:27],v[96:99]
t a[220:221],v208,28672
t a[222:223],v208,30720
m v[112:115],a[204:207],v[56:59],v[112:115]
t a[224:225],v209,28672
t a[226:227],v209,30720
m v[100:103],a[208:211],v[24:27],v[100:103]
t a[228:229],v210,28672
t a[230:231],v210,30720
m v[116:119],a[208:211],v[56:59],v[116:119]
t a[232:233],v211,28672
t a[234:235],v211,30720
m v[104:107],a[212:215],v[24:27],v[104:107]
v_add_u32 v208,s48,v208
v_add_u32 v209,s48,v209
m v[120:123],a[212:215],v[56:59],v[120:123]
v_add_u32 v210,s48,v210
v_add_u32 v211,s48,v211
m v[108:111],a[216:219],v[24:27],v[108:111]
v_cndmask_b32_e32 v230,v235,v234,vcc
s_lshl_b32 s46,%11,7
m v[124:127],a[216:219],v[56:59],v[124:127]
v_and_b32 v234,15,v214
v_lshlrev_b32 v234,3,v234
s_waitcnt lgkmcnt(0)
s_waitcnt vmcnt(52)
s_barrier
t a[204:205],v208,0
t a[206:207],v208,2048
t a[208:209],v209,0
t a[210:211],v209,2048
t a[212:213],v210,0
t a[214:215],v210,2048
t a[216:217],v211,0
t a[218:219],v211,2048
s_mov_b32 m0,0x1c000
s_mov_b32 s45,0x1c300
dn v144,%4,s45,0
dn v145,%4,s45,1024
dn v146,%4,s45,2048
dn v147,%4,s45,3072
s_mov_b32 m0,0x1d000
s_mov_b32 s45,0x23300
dn v144,%4,s45,0
dn v145,%4,s45,1024
dn v146,%4,s45,2048
dn v147,%4,s45,3072
s_mov_b32 m0,0x1e000
s_mov_b32 s45,0x2a300
dn v144,%4,s45,0
dn v145,%4,s45,1024
dn v146,%4,s45,2048
dn v147,%4,s45,3072
s_mov_b32 m0,0x1f000
s_mov_b32 s45,0x31300
dn v144,%4,s45,0
dn v145,%4,s45,1024
dn v146,%4,s45,2048
dn v147,%4,s45,3072
m v[96:99],a[220:223],v[28:31],v[96:99]
v_add_u32 v234,4,v234
v_add_u32 v231,s46,v234
m v[112:115],a[220:223],v[60:63],v[112:115]
s_mul_i32 s46,%11,0x50
)ASM"
R"ASM(v_add_u32 v234,s46,v214
m v[100:103],a[224:227],v[28:31],v[100:103]
m v[116:119],a[224:227],v[60:63],v[116:119]
m v[104:107],a[228:231],v[28:31],v[104:107]
m v[120:123],a[228:231],v[60:63],v[120:123]
m v[108:111],a[232:235],v[28:31],v[108:111]
m v[124:127],a[232:235],v[60:63],v[124:127]
s_waitcnt lgkmcnt(0)
m v[64:67],a[204:207],v[0:3],0
t a[220:221],v208,4096
t a[222:223],v208,6144
m v[80:83],a[204:207],v[32:35],0
t a[224:225],v209,4096
t a[226:227],v209,6144
m v[68:71],a[208:211],v[0:3],0
t a[228:229],v210,4096
t a[230:231],v210,6144
m v[84:87],a[208:211],v[32:35],0
t a[232:233],v211,4096
t a[234:235],v211,6144
m v[72:75],a[212:215],v[0:3],0
v_lshlrev_b32 v226,4,v234
v_lshrrev_b32 v227,7,v234
m v[88:91],a[212:215],v[32:35],0
v_lshlrev_b32 v227,4,v227
v_and_b32 v235,15,v234
m v[76:79],a[216:219],v[0:3],0
m v[92:95],a[216:219],v[32:35],0
s_waitcnt lgkmcnt(0)
m v[64:67],a[220:223],v[4:7],v[64:67]
t a[204:205],v208,8192
t a[206:207],v208,10240
t a[208:209],v209,8192
m v[80:83],a[220:223],v[36:39],v[80:83]
t a[210:211],v209,10240
t a[212:213],v210,8192
t a[214:215],v210,10240
m v[68:71],a[224:227],v[4:7],v[68:71]
t a[216:217],v211,8192
t a[218:219],v211,10240
s_add_u32 s55,s54,0x5000
m v[84:87],a[224:227],v[36:39],v[84:87]
s_add_u32 s59,s55,0x1000
v_mul_f32 v96,v96,v216
v_mul_f32 v97,v97,v216
m v[72:75],a[228:231],v[4:7],v[72:75]
v_mul_f32 v98,v98,v216
v_mul_f32 v99,v99,v216
v_mul_f32 v100,v100,v216
m v[88:91],a[228:231],v[36:39],v[88:91]
v_mul_f32 v101,v101,v216
v_mul_f32 v102,v102,v216
v_mul_f32 v103,v103,v216
m v[76:79],a[232:235],v[4:7],v[76:79]
cv v96,v96,v97
cv v97,v98,v99
cv v98,v100,v101
m v[92:95],a[232:235],v[36:39],v[92:95]
v_add_u32 v227,v227,v235
v_mul_u32_u24 v227,0x700,v227
v_bfe_u32 v235,v234,6,1
v_lshl_add_u32 v227,v235,6,v227
s_waitcnt lgkmcnt(0)
m v[64:67],a[204:207],v[8:11],v[64:67]
t a[220:221],v208,12288
t a[222:223],v208,14336
t a[224:225],v209,12288
m v[80:83],a[204:207],v[40:43],v[80:83]
t a[226:227],v209,14336
t a[228:229],v210,12288
t a[230:231],v210,14336
m v[68:71],a[208:211],v[8:11],v[68:71]
t a[232:233],v211,12288
t a[234:235],v211,14336
cv v99,v102,v103
m v[84:87],a[208:211],v[40:43],v[84:87]
buffer_store_dwordx4 v[96:99],v215,%5,s55 offen sc1
v_mul_f32 v104,v104,v216
v_mul_f32 v105,v105,v216
m v[72:75],a[212:215],v[8:11],v[72:75]
v_mul_f32 v106,v106,v216
v_mul_f32 v107,v107,v216
v_mul_f32 v108,v108,v216
m v[88:91],a[212:215],v[40:43],v[88:91]
v_mul_f32 v109,v109,v216
v_mul_f32 v110,v110,v216
v_mul_f32 v111,v111,v216
m v[76:79],a[216:219],v[8:11],v[76:79]
cv v104,v104,v105
cv v105,v106,v107
cv v106,v108,v109
m v[92:95],a[216:219],v[40:43],v[92:95]
cv v107,v110,v111
v_bfe_u32 v235,v234,4,2
v_lshl_add_u32 v227,v235,3,v227
v_and_b32 v234,15,v214
s_waitcnt lgkmcnt(0)
m v[64:67],a[220:223],v[12:15],v[64:67]
t a[204:205],v208,16384
t a[206:207],v208,18432
t a[208:209],v209,16384
m v[80:83],a[220:223],v[44:47],v[80:83]
t a[210:211],v209,18432
t a[212:213],v210,16384
t a[214:215],v210,18432
m v[68:71],a[224:227],v[12:15],v[68:71]
t a[216:217],v211,16384
t a[218:219],v211,18432
buffer_store_dwordx4 v[104:107],v215,%5,s55 offen offset:1024 sc1
m v[84:87],a[224:227],v[44:47],v[84:87]
v_mul_f32 v112,v112,v217
v_mul_f32 v113,v113,v217
v_mul_f32 v114,v114,v217
m v[72:75],a[228:231],v[12:15],v[72:75]
v_mul_f32 v115,v115,v217
v_mul_f32 v116,v116,v217
v_mul_f32 v117,v117,v217
m v[88:91],a[228:231],v[44:47],v[88:91]
v_mul_f32 v118,v118,v217
v_mul_f32 v119,v119,v217
cv v112,v112,v113
m v[76:79],a[232:235],v[12:15],v[76:79]
cv v113,v114,v115
cv v114,v116,v117
cv v115,v118,v119
m v[92:95],a[232:235],v[44:47],v[92:95]
v_add_u32 v234,s46,v234
v_add_u32 v234,64,v234
v_lshrrev_b32 v229,7,v234
v_lshlrev_b32 v229,4,v229
s_waitcnt lgkmcnt(0)
m v[64:67],a[204:207],v[16:19],v[64:67]
t a[220:221],v208,20480
t a[222:223],v208,22528
t a[224:225],v209,20480
m v[80:83],a[204:207],v[48:51],v[80:83]
t a[226:227],v209,22528
t a[228:229],v210,20480
t a[230:231],v210,22528
m v[68:71],a[208:211],v[16:19],v[68:71]
t a[232:233],v211,20480
t a[234:235],v211,22528
buffer_store_dwordx4 v[112:115],v215,%5,s55 offen offset:2048 sc1
m v[84:87],a[208:211],v[48:51],v[84:87]
v_mul_f32 v120,v120,v217
v_mul_f32 v121,v121,v217
v_mul_f32 v122,v122,v217
v_mul_f32 v123,v123,v217
m v[72:75],a[212:215],v[16:19],v[72:75]
v_mul_f32 v124,v124,v217
v_mul_f32 v125,v125,v217
v_mul_f32 v126,v126,v217
m v[88:91],a[212:215],v[48:51],v[88:91]
v_mul_f32 v127,v127,v217
cv v120,v120,v121
cv v121,v122,v123
m v[76:79],a[216:219],v[16:19],v[76:79]
cv v122,v124,v125
cv v123,v126,v127
buffer_store_dwordx4 v[120:123],v215,%5,s55 offen offset:3072 sc1
m v[92:95],a[216:219],v[48:51],v[92:95]
v_and_b32 v235,15,v234
v_add_u32 v229,v229,v235
v_mul_u32_u24 v229,0x700,v229
v_bfe_u32 v235,v234,6,1
s_waitcnt lgkmcnt(0)
m v[64:67],a[220:223],v[20:23],v[64:67]
t a[204:205],v208,24576
t a[206:207],v208,26624
m v[80:83],a[220:223],v[52:55],v[80:83]
t a[208:209],v209,24576
t a[210:211],v209,26624
m v[68:71],a[224:227],v[20:23],v[68:71]
t a[212:213],v210,24576
t a[214:215],v210,26624
m v[84:87],a[224:227],v[52:55],v[84:87]
t a[216:217],v211,24576
t a[218:219],v211,26624
m v[72:75],a[228:231],v[20:23],v[72:75]
v_lshl_add_u32 v229,v235,6,v229
v_bfe_u32 v235,v234,4,2
m v[88:91],a[228:231],v[52:55],v[88:91]
v_lshl_add_u32 v229,v235,3,v229
v_lshrrev_b32 v236,4,v214
m v[76:79],a[232:235],v[20:23],v[76:79]
m v[92:95],a[232:235],v[52:55],v[92:95]
s_waitcnt lgkmcnt(0)
m v[64:67],a[204:207],v[24:27],v[64:67]
t a[220:221],v208,28672
t a[222:223],v208,30720
m v[80:83],a[204:207],v[56:59],v[80:83]
t a[224:225],v209,28672
t a[226:227],v209,30720
m v[68:71],a[208:211],v[24:27],v[68:71]
t a[228:229],v210,28672
t a[230:231],v210,30720
m v[84:87],a[208:211],v[56:59],v[84:87]
t a[232:233],v211,28672
t a[234:235],v211,30720
m v[72:75],a[212:215],v[24:27],v[72:75]
v_add_u32 v208,s49,v208
v_add_u32 v209,s49,v209
m v[88:91],a[212:215],v[56:59],v[88:91]
v_add_u32 v210,s49,v210
v_add_u32 v211,s49,v211
m v[76:79],a[216:219],v[24:27],v[76:79]
v_add_u32 v236,4,v236
v_lshl_add_u32 v229,v236,7,v229
m v[92:95],a[216:219],v[56:59],v[92:95]
v_mul_u32_u24 v237,0x5000,v236
v_lshl_add_u32 v228,v234,4,v237
s_waitcnt lgkmcnt(0)
s_waitcnt vmcnt(56)
s_waitcnt vmcnt(20)
s_barrier
v_add_u32 v232,1,v150
buffer_store_dword v232,v230,%5,%12 offen sc1
m v[64:67],a[220:223],v[28:31],v[64:67]
t a[204:205],v208,0
t a[206:207],v208,2048
m v[80:83],a[220:223],v[60:63],v[80:83]
t a[208:209],v209,0
t a[210:211],v209,2048
m v[68:71],a[224:227],v[28:31],v[68:71]
t a[212:213],v210,0
t a[214:215],v210,2048
m v[84:87],a[224:227],v[60:63],v[84:87]
t a[216:217],v211,0
t a[218:219],v211,2048
m v[72:75],a[228:231],v[28:31],v[72:75]
v_cmp_gt_u32_e32 vcc,7,v236
v_mov_b32 v235,0x7fffff00
m v[88:91],a[228:231],v[60:63],v[88:91]
v_cndmask_b32_e32 v228,v235,v228,vcc
v_cndmask_b32_e32 v229,v235,v229,vcc
m v[76:79],a[232:235],v[28:31],v[76:79]
m v[92:95],a[232:235],v[60:63],v[92:95]
s_waitcnt lgkmcnt(0)
m v[96:99],a[204:207],v[0:3],0
t a[220:221],v208,4096
t a[222:223],v208,6144
m v[112:115],a[204:207],v[32:35],0
t a[224:225],v209,4096
t a[226:227],v209,6144
m v[100:103],a[208:211],v[0:3],0
t a[228:229],v210,4096
t a[230:231],v210,6144
m v[116:119],a[208:211],v[32:35],0
t a[232:233],v211,4096
t a[234:235],v211,6144
m v[104:107],a[212:215],v[0:3],0
m v[120:123],a[212:215],v[32:35],0
m v[108:111],a[216:219],v[0:3],0
m v[124:127],a[216:219],v[32:35],0
s_waitcnt lgkmcnt(0)
m v[96:99],a[220:223],v[4:7],v[96:99]
t a[204:205],v208,8192
t a[206:207],v208,10240
m v[112:115],a[220:223],v[36:39],v[112:115]
t a[208:209],v209,8192
t a[210:211],v209,10240
t a[212:213],v210,8192
m v[100:103],a[224:227],v[4:7],v[100:103]
t a[214:215],v210,10240
t a[216:217],v211,8192
m v[116:119],a[224:227],v[36:39],v[116:119]
t a[218:219],v211,10240
s_add_u32 s55,s54,0xa000
s_add_u32 s59,s55,0x1000
m v[104:107],a[228:231],v[4:7],v[104:107]
v_mul_f32 v64,v64,v216
v_mul_f32 v65,v65,v216
v_mul_f32 v66,v66,v216
m v[120:123],a[228:231],v[36:39],v[120:123]
v_mul_f32 v67,v67,v216
v_mul_f32 v68,v68,v216
m v[108:111],a[232:235],v[4:7],v[108:111]
v_mul_f32 v69,v69,v216
v_mul_f32 v70,v70,v216
v_mul_f32 v71,v71,v216
m v[124:127],a[232:235],v[36:39],v[124:127]
cv v64,v64,v65
cv v65,v66,v67
cv v66,v68,v69
s_waitcnt lgkmcnt(0)
m v[96:99],a[204:207],v[8:11],v[96:99]
t a[220:221],v208,12288
t a[222:223],v208,14336
m v[112:115],a[204:207],v[40:43],v[112:115]
t a[224:225],v209,12288
t a[226:227],v209,14336
t a[228:229],v210,12288
m v[100:103],a[208:211],v[8:11],v[100:103]
t a[230:231],v210,14336
t a[232:233],v211,12288
t a[234:235],v211,14336
m v[116:119],a[208:211],v[40:43],v[116:119]
cv v67,v70,v71
buffer_store_dwordx4 v[64:67],v215,%5,s55 offen sc1
v_mul_f32 v72,v72,v216
m v[104:107],a[212:215],v[8:11],v[104:107]
v_mul_f32 v73,v73,v216
v_mul_f32 v74,v74,v216
m v[120:123],a[212:215],v[40:43],v[120:123]
v_mul_f32 v75,v75,v216
v_mul_f32 v76,v76,v216
v_mul_f32 v77,v77,v216
m v[108:111],a[216:219],v[8:11],v[108:111]
v_mul_f32 v78,v78,v216
v_mul_f32 v79,v79,v216
cv v72,v72,v73
m v[124:127],a[216:219],v[40:43],v[124:127]
cv v73,v74,v75
cv v74,v76,v77
cv v75,v78,v79
s_waitcnt lgkmcnt(0)
m v[96:99],a[220:223],v[12:15],v[96:99]
t a[204:205],v208,16384
t a[206:207],v208,18432
m v[112:115],a[220:223],v[44:47],v[112:115]
t a[208:209],v209,16384
t a[210:211],v209,18432
t a[212:213],v210,16384
m v[100:103],a[224:227],v[12:15],v[100:103]
t a[214:215],v210,18432
t a[216:217],v211,16384
m v[116:119],a[224:227],v[44:47],v[116:119]
t a[218:219],v211,18432
buffer_store_dwordx4 v[72:75],v215,%5,s55 offen offset:1024 sc1
v_mul_f32 v80,v80,v217
m v[104:107],a[228:231],v[12:15],v[104:107]
v_mul_f32 v81,v81,v217
v_mul_f32 v82,v82,v217
v_mul_f32 v83,v83,v217
m v[120:123],a[228:231],v[44:47],v[120:123]
v_mul_f32 v84,v84,v217
v_mul_f32 v85,v85,v217
m v[108:111],a[232:235],v[12:15],v[108:111]
v_mul_f32 v86,v86,v217
v_mul_f32 v87,v87,v217
cv v80,v80,v81
m v[124:127],a[232:235],v[44:47],v[124:127]
cv v81,v82,v83
cv v82,v84,v85
cv v83,v86,v87
s_waitcnt lgkmcnt(0)
m v[96:99],a[204:207],v[16:19],v[96:99]
t a[220:221],v208,20480
t a[222:223],v208,22528
m v[112:115],a[204:207],v[48:51],v[112:115]
t a[224:225],v209,20480
t a[226:227],v209,22528
t a[228:229],v210,20480
m v[100:103],a[208:211],v[16:19],v[100:103]
t a[230:231],v210,22528
t a[232:233],v211,20480
t a[234:235],v211,22528
m v[116:119],a[208:211],v[48:51],v[116:119]
buffer_store_dwordx4 v[80:83],v215,%5,s55 offen offset:2048 sc1
v_mul_f32 v88,v88,v217
v_mul_f32 v89,v89,v217
m v[104:107],a[212:215],v[16:19],v[104:107]
v_mul_f32 v90,v90,v217
v_mul_f32 v91,v91,v217
m v[120:123],a[212:215],v[48:51],v[120:123]
v_mul_f32 v92,v92,v217
v_mul_f32 v93,v93,v217
v_mul_f32 v94,v94,v217
m v[108:111],a[216:219],v[16:19],v[108:111]
v_mul_f32 v95,v95,v217
cv v88,v88,v89
cv v89,v90,v91
m v[124:127],a[216:219],v[48:51],v[124:127]
cv v90,v92,v93
cv v91,v94,v95
buffer_store_dwordx4 v[88:91],v215,%5,s55 offen offset:3072 sc1
s_waitcnt lgkmcnt(0)
m v[96:99],a[220:223],v[20:23],v[96:99]
t a[204:205],v208,24576
t a[206:207],v208,26624
m v[112:115],a[220:223],v[52:55],v[112:115]
t a[208:209],v209,24576
t a[210:211],v209,26624
m v[100:103],a[224:227],v[20:23],v[100:103]
t a[212:213],v210,24576
t a[214:215],v210,26624
m v[116:119],a[224:227],v[52:55],v[116:119]
t a[216:217],v211,24576
t a[218:219],v211,26624
m v[104:107],a[228:231],v[20:23],v[104:107]
m v[120:123],a[228:231],v[52:55],v[120:123]
m v[108:111],a[232:235],v[20:23],v[108:111]
m v[124:127],a[232:235],v[52:55],v[124:127]
s_waitcnt lgkmcnt(0)
m v[96:99],a[204:207],v[24:27],v[96:99]
t a[220:221],v208,28672
t a[222:223],v208,30720
m v[112:115],a[204:207],v[56:59],v[112:115]
t a[224:225],v209,28672
t a[226:227],v209,30720
m v[100:103],a[208:211],v[24:27],v[100:103]
t a[228:229],v210,28672
t a[230:231],v210,30720
m v[116:119],a[208:211],v[56:59],v[116:119]
t a[232:233],v211,28672
t a[234:235],v211,30720
m v[104:107],a[212:215],v[24:27],v[104:107]
v_add_u32 v208,s48,v208
v_add_u32 v209,s48,v209
m v[120:123],a[212:215],v[56:59],v[120:123]
v_add_u32 v210,s48,v210
v_add_u32 v211,s48,v211
m v[108:111],a[216:219],v[24:27],v[108:111]
m v[124:127],a[216:219],v[56:59],v[124:127]
s_waitcnt lgkmcnt(0)
s_waitcnt vmcnt(5)
s_barrier
v_add_u32 v232,2,v150
buffer_store_dword v232,v230,%5,%12 offen sc1
m v[96:99],a[220:223],v[28:31],v[96:99]
t a[204:205],v208,0
t a[206:207],v208,2048
m v[112:115],a[220:223],v[60:63],v[112:115]
t a[208:209],v209,0
t a[210:211],v209,2048
m v[100:103],a[224:227],v[28:31],v[100:103]
t a[212:213],v210,0
t a[214:215],v210,2048
m v[116:119],a[224:227],v[60:63],v[116:119]
t a[216:217],v211,0
t a[218:219],v211,2048
m v[104:107],a[228:231],v[28:31],v[104:107]
m v[120:123],a[228:231],v[60:63],v[120:123]
m v[108:111],a[232:235],v[28:31],v[108:111]
m v[124:127],a[232:235],v[60:63],v[124:127]
s_waitcnt lgkmcnt(0)
m v[64:67],a[204:207],v[0:3],0
t a[220:221],v208,4096
t a[222:223],v208,6144
m v[80:83],a[204:207],v[32:35],0
t a[224:225],v209,4096
t a[226:227],v209,6144
m v[68:71],a[208:211],v[0:3],0
t a[228:229],v210,4096
t a[230:231],v210,6144
m v[84:87],a[208:211],v[32:35],0
t a[232:233],v211,4096
t a[234:235],v211,6144
m v[72:75],a[212:215],v[0:3],0
m v[88:91],a[212:215],v[32:35],0
m v[76:79],a[216:219],v[0:3],0
m v[92:95],a[216:219],v[32:35],0
s_waitcnt lgkmcnt(0)
m v[64:67],a[220:223],v[4:7],v[64:67]
t a[204:205],v208,8192
t a[206:207],v208,10240
m v[80:83],a[220:223],v[36:39],v[80:83]
t a[208:209],v209,8192
t a[210:211],v209,10240
t a[212:213],v210,8192
m v[68:71],a[224:227],v[4:7],v[68:71]
t a[214:215],v210,10240
t a[216:217],v211,8192
m v[84:87],a[224:227],v[36:39],v[84:87]
t a[218:219],v211,10240
s_add_u32 s55,s54,0xf000
s_add_u32 s59,s55,0x1000
m v[72:75],a[228:231],v[4:7],v[72:75]
v_mul_f32 v96,v96,v216
v_mul_f32 v97,v97,v216
v_mul_f32 v98,v98,v216
m v[88:91],a[228:231],v[36:39],v[88:91]
v_mul_f32 v99,v99,v216
v_mul_f32 v100,v100,v216
m v[76:79],a[232:235],v[4:7],v[76:79]
v_mul_f32 v101,v101,v216
v_mul_f32 v102,v102,v216
v_mul_f32 v103,v103,v216
m v[92:95],a[232:235],v[36:39],v[92:95]
cv v96,v96,v97
cv v97,v98,v99
cv v98,v100,v101
s_waitcnt lgkmcnt(0)
m v[64:67],a[204:207],v[8:11],v[64:67]
t a[220:221],v208,12288
t a[222:223],v208,14336
m v[80:83],a[204:207],v[40:43],v[80:83]
t a[224:225],v209,12288
t a[226:227],v209,14336
t a[228:229],v210,12288
m v[68:71],a[208:211],v[8:11],v[68:71]
t a[230:231],v210,14336
t a[232:233],v211,12288
t a[234:235],v211,14336
m v[84:87],a[208:211],v[40:43],v[84:87]
cv v99,v102,v103
buffer_store_dwordx4 v[96:99],v215,%5,s55 offen sc1
v_mul_f32 v104,v104,v216
m v[72:75],a[212:215],v[8:11],v[72:75]
v_mul_f32 v105,v105,v216
v_mul_f32 v106,v106,v216
m v[88:91],a[212:215],v[40:43],v[88:91]
v_mul_f32 v107,v107,v216
v_mul_f32 v108,v108,v216
v_mul_f32 v109,v109,v216
m v[76:79],a[216:219],v[8:11],v[76:79]
v_mul_f32 v110,v110,v216
v_mul_f32 v111,v111,v216
cv v104,v104,v105
m v[92:95],a[216:219],v[40:43],v[92:95]
cv v105,v106,v107
cv v106,v108,v109
cv v107,v110,v111
s_waitcnt lgkmcnt(0)
m v[64:67],a[220:223],v[12:15],v[64:67]
t a[204:205],v208,16384
t a[206:207],v208,18432
m v[80:83],a[220:223],v[44:47],v[80:83]
t a[208:209],v209,16384
t a[210:211],v209,18432
t a[212:213],v210,16384
m v[68:71],a[224:227],v[12:15],v[68:71]
t a[214:215],v210,18432
t a[216:217],v211,16384
m v[84:87],a[224:227],v[44:47],v[84:87]
t a[218:219],v211,18432
buffer_store_dwordx4 v[104:107],v215,%5,s55 offen offset:1024 sc1
v_mul_f32 v112,v112,v217
m v[72:75],a[228:231],v[12:15],v[72:75]
v_mul_f32 v113,v113,v217
v_mul_f32 v114,v114,v217
v_mul_f32 v115,v115,v217
m v[88:91],a[228:231],v[44:47],v[88:91]
v_mul_f32 v116,v116,v217
v_mul_f32 v117,v117,v217
m v[76:79],a[232:235],v[12:15],v[76:79]
v_mul_f32 v118,v118,v217
v_mul_f32 v119,v119,v217
cv v112,v112,v113
m v[92:95],a[232:235],v[44:47],v[92:95]
cv v113,v114,v115
cv v114,v116,v117
cv v115,v118,v119
s_waitcnt lgkmcnt(0)
m v[64:67],a[204:207],v[16:19],v[64:67]
t a[220:221],v208,20480
t a[222:223],v208,22528
m v[80:83],a[204:207],v[48:51],v[80:83]
t a[224:225],v209,20480
t a[226:227],v209,22528
t a[228:229],v210,20480
m v[68:71],a[208:211],v[16:19],v[68:71]
t a[230:231],v210,22528
t a[232:233],v211,20480
t a[234:235],v211,22528
m v[84:87],a[208:211],v[48:51],v[84:87]
buffer_store_dwordx4 v[112:115],v215,%5,s55 offen offset:2048 sc1
v_mul_f32 v120,v120,v217
v_mul_f32 v121,v121,v217
m v[72:75],a[212:215],v[16:19],v[72:75]
v_mul_f32 v122,v122,v217
v_mul_f32 v123,v123,v217
m v[88:91],a[212:215],v[48:51],v[88:91]
v_mul_f32 v124,v124,v217
v_mul_f32 v125,v125,v217
v_mul_f32 v126,v126,v217
m v[76:79],a[216:219],v[16:19],v[76:79]
v_mul_f32 v127,v127,v217
cv v120,v120,v121
cv v121,v122,v123
m v[92:95],a[216:219],v[48:51],v[92:95]
cv v122,v124,v125
cv v123,v126,v127
buffer_store_dwordx4 v[120:123],v215,%5,s55 offen offset:3072 sc1
s_waitcnt lgkmcnt(0)
m v[64:67],a[220:223],v[20:23],v[64:67]
t a[204:205],v208,24576
t a[206:207],v208,26624
m v[80:83],a[220:223],v[52:55],v[80:83]
t a[208:209],v209,24576
t a[210:211],v209,26624
m v[68:71],a[224:227],v[20:23],v[68:71]
t a[212:213],v210,24576
t a[214:215],v210,26624
m v[84:87],a[224:227],v[52:55],v[84:87]
t a[216:217],v211,24576
t a[218:219],v211,26624
m v[72:75],a[228:231],v[20:23],v[72:75]
m v[88:91],a[228:231],v[52:55],v[88:91]
m v[76:79],a[232:235],v[20:23],v[76:79]
m v[92:95],a[232:235],v[52:55],v[92:95]
s_waitcnt lgkmcnt(0)
m v[64:67],a[204:207],v[24:27],v[64:67]
t a[220:221],v208,28672
t a[222:223],v208,30720
m v[80:83],a[204:207],v[56:59],v[80:83]
t a[224:225],v209,28672
t a[226:227],v209,30720
m v[68:71],a[208:211],v[24:27],v[68:71]
t a[228:229],v210,28672
t a[230:231],v210,30720
m v[84:87],a[208:211],v[56:59],v[84:87]
t a[232:233],v211,28672
t a[234:235],v211,30720
m v[72:75],a[212:215],v[24:27],v[72:75]
v_add_u32 v208,s48,v208
v_add_u32 v209,s48,v209
m v[88:91],a[212:215],v[56:59],v[88:91]
v_add_u32 v210,s48,v210
v_add_u32 v211,s48,v211
m v[76:79],a[216:219],v[24:27],v[76:79]
m v[92:95],a[216:219],v[56:59],v[92:95]
s_waitcnt lgkmcnt(0)
s_waitcnt vmcnt(5)
s_barrier
v_add_u32 v232,3,v150
buffer_store_dword v232,v230,%5,%12 offen sc1
t a[204:205],v208,0
t a[206:207],v208,2048
t a[208:209],v209,0
t a[210:211],v209,2048
t a[212:213],v210,0
t a[214:215],v210,2048
t a[216:217],v211,0
t a[218:219],v211,2048
buffer_load_dword v233,v231,%5,%12 offen sc1
m v[64:67],a[220:223],v[28:31],v[64:67]
m v[80:83],a[220:223],v[60:63],v[80:83]
m v[68:71],a[224:227],v[28:31],v[68:71]
m v[84:87],a[224:227],v[60:63],v[84:87]
m v[72:75],a[228:231],v[28:31],v[72:75]
m v[88:91],a[228:231],v[60:63],v[88:91]
m v[76:79],a[232:235],v[28:31],v[76:79]
m v[92:95],a[232:235],v[60:63],v[92:95]
s_waitcnt lgkmcnt(0)
m v[96:99],a[204:207],v[0:3],0
t a[220:221],v208,4096
t a[222:223],v208,6144
m v[112:115],a[204:207],v[32:35],0
t a[224:225],v209,4096
t a[226:227],v209,6144
m v[100:103],a[208:211],v[0:3],0
t a[228:229],v210,4096
t a[230:231],v210,6144
m v[116:119],a[208:211],v[32:35],0
t a[232:233],v211,4096
t a[234:235],v211,6144
m v[104:107],a[212:215],v[0:3],0
m v[120:123],a[212:215],v[32:35],0
m v[108:111],a[216:219],v[0:3],0
m v[124:127],a[216:219],v[32:35],0
s_waitcnt lgkmcnt(0)
m v[96:99],a[220:223],v[4:7],v[96:99]
t a[204:205],v208,8192
t a[206:207],v208,10240
m v[112:115],a[220:223],v[36:39],v[112:115]
t a[208:209],v209,8192
t a[210:211],v209,10240
t a[212:213],v210,8192
m v[100:103],a[224:227],v[4:7],v[100:103]
t a[214:215],v210,10240
t a[216:217],v211,8192
m v[116:119],a[224:227],v[36:39],v[116:119]
t a[218:219],v211,10240
s_add_u32 s55,s54,0x14000
s_add_u32 s59,s55,0x1000
m v[104:107],a[228:231],v[4:7],v[104:107]
v_mul_f32 v64,v64,v216
v_mul_f32 v65,v65,v216
v_mul_f32 v66,v66,v216
m v[120:123],a[228:231],v[36:39],v[120:123]
v_mul_f32 v67,v67,v216
v_mul_f32 v68,v68,v216
m v[108:111],a[232:235],v[4:7],v[108:111]
v_mul_f32 v69,v69,v216
v_mul_f32 v70,v70,v216
v_mul_f32 v71,v71,v216
m v[124:127],a[232:235],v[36:39],v[124:127]
cv v64,v64,v65
cv v65,v66,v67
cv v66,v68,v69
s_waitcnt lgkmcnt(0)
m v[96:99],a[204:207],v[8:11],v[96:99]
t a[220:221],v208,12288
t a[222:223],v208,14336
m v[112:115],a[204:207],v[40:43],v[112:115]
t a[224:225],v209,12288
t a[226:227],v209,14336
t a[228:229],v210,12288
m v[100:103],a[208:211],v[8:11],v[100:103]
t a[230:231],v210,14336
t a[232:233],v211,12288
t a[234:235],v211,14336
m v[116:119],a[208:211],v[40:43],v[116:119]
cv v67,v70,v71
buffer_store_dwordx4 v[64:67],v215,%5,s55 offen sc1
v_mul_f32 v72,v72,v216
m v[104:107],a[212:215],v[8:11],v[104:107]
v_mul_f32 v73,v73,v216
v_mul_f32 v74,v74,v216
m v[120:123],a[212:215],v[40:43],v[120:123]
v_mul_f32 v75,v75,v216
v_mul_f32 v76,v76,v216
v_mul_f32 v77,v77,v216
m v[108:111],a[216:219],v[8:11],v[108:111]
v_mul_f32 v78,v78,v216
v_mul_f32 v79,v79,v216
cv v72,v72,v73
m v[124:127],a[216:219],v[40:43],v[124:127]
cv v73,v74,v75
cv v74,v76,v77
cv v75,v78,v79
s_waitcnt lgkmcnt(0)
m v[96:99],a[220:223],v[12:15],v[96:99]
t a[204:205],v208,16384
t a[206:207],v208,18432
m v[112:115],a[220:223],v[44:47],v[112:115]
t a[208:209],v209,16384
t a[210:211],v209,18432
t a[212:213],v210,16384
m v[100:103],a[224:227],v[12:15],v[100:103]
t a[214:215],v210,18432
t a[216:217],v211,16384
m v[116:119],a[224:227],v[44:47],v[116:119]
t a[218:219],v211,18432
buffer_store_dwordx4 v[72:75],v215,%5,s55 offen offset:1024 sc1
v_mul_f32 v80,v80,v217
m v[104:107],a[228:231],v[12:15],v[104:107]
v_mul_f32 v81,v81,v217
v_mul_f32 v82,v82,v217
v_mul_f32 v83,v83,v217
m v[120:123],a[228:231],v[44:47],v[120:123]
v_mul_f32 v84,v84,v217
v_mul_f32 v85,v85,v217
m v[108:111],a[232:235],v[12:15],v[108:111]
v_mul_f32 v86,v86,v217
v_mul_f32 v87,v87,v217
cv v80,v80,v81
m v[124:127],a[232:235],v[44:47],v[124:127]
cv v81,v82,v83
cv v82,v84,v85
cv v83,v86,v87
s_waitcnt lgkmcnt(0)
m v[96:99],a[204:207],v[16:19],v[96:99]
t a[220:221],v208,20480
t a[222:223],v208,22528
m v[112:115],a[204:207],v[48:51],v[112:115]
t a[224:225],v209,20480
t a[226:227],v209,22528
t a[228:229],v210,20480
m v[100:103],a[208:211],v[16:19],v[100:103]
t a[230:231],v210,22528
t a[232:233],v211,20480
t a[234:235],v211,22528
m v[116:119],a[208:211],v[48:51],v[116:119]
buffer_store_dwordx4 v[80:83],v215,%5,s55 offen offset:2048 sc1
v_mul_f32 v88,v88,v217
v_mul_f32 v89,v89,v217
m v[104:107],a[212:215],v[16:19],v[104:107]
v_mul_f32 v90,v90,v217
v_mul_f32 v91,v91,v217
m v[120:123],a[212:215],v[48:51],v[120:123]
v_mul_f32 v92,v92,v217
v_mul_f32 v93,v93,v217
v_mul_f32 v94,v94,v217
m v[108:111],a[216:219],v[16:19],v[108:111]
v_mul_f32 v95,v95,v217
cv v88,v88,v89
cv v89,v90,v91
m v[124:127],a[216:219],v[48:51],v[124:127]
cv v90,v92,v93
cv v91,v94,v95
buffer_store_dwordx4 v[88:91],v215,%5,s55 offen offset:3072 sc1
s_waitcnt lgkmcnt(0)
m v[96:99],a[220:223],v[20:23],v[96:99]
t a[204:205],v208,24576
t a[206:207],v208,26624
m v[112:115],a[220:223],v[52:55],v[112:115]
t a[208:209],v209,24576
t a[210:211],v209,26624
m v[100:103],a[224:227],v[20:23],v[100:103]
t a[212:213],v210,24576
t a[214:215],v210,26624
m v[116:119],a[224:227],v[52:55],v[116:119]
t a[216:217],v211,24576
t a[218:219],v211,26624
m v[104:107],a[228:231],v[20:23],v[104:107]
m v[120:123],a[228:231],v[52:55],v[120:123]
m v[108:111],a[232:235],v[20:23],v[108:111]
m v[124:127],a[232:235],v[52:55],v[124:127]
s_waitcnt lgkmcnt(0)
m v[96:99],a[204:207],v[24:27],v[96:99]
t a[220:221],v208,28672
t a[222:223],v208,30720
m v[112:115],a[204:207],v[56:59],v[112:115]
t a[224:225],v209,28672
t a[226:227],v209,30720
m v[100:103],a[208:211],v[24:27],v[100:103]
t a[228:229],v210,28672
t a[230:231],v210,30720
m v[116:119],a[208:211],v[56:59],v[116:119]
t a[232:233],v211,28672
t a[234:235],v211,30720
m v[104:107],a[212:215],v[24:27],v[104:107]
v_add_u32 v208,s48,v208
v_add_u32 v209,s48,v209
m v[120:123],a[212:215],v[56:59],v[120:123]
v_add_u32 v210,s48,v210
v_add_u32 v211,s48,v211
m v[108:111],a[216:219],v[24:27],v[108:111]
m v[124:127],a[216:219],v[56:59],v[124:127]
s_waitcnt lgkmcnt(0)
s_waitcnt vmcnt(6)
s_barrier
v_add_u32 v232,4,v150
buffer_store_dword v232,v230,%5,%12 offen sc1
s_waitcnt vmcnt(5)
v_sub_u32 v234,v233,v148
v_add_u32 v234,-2,v234
v_cmp_lt_u32_e32 vcc,5,v234
s_nop 0
s_cbranch_vccnz LC_ps10%=
LC_pr10%=:
t a[204:205],v208,0
t a[206:207],v208,2048
t a[208:209],v209,0
t a[210:211],v209,2048
t a[212:213],v210,0
t a[214:215],v210,2048
t a[216:217],v211,0
t a[218:219],v211,2048
s_mov_b32 s45,0x5000
buffer_load_dwordx4 a[0:3],v226,%5,s45 offen sc1
s_mov_b32 s45,0x11d000
buffer_load_dwordx4 a[4:7],v226,%5,s45 offen sc1
s_mov_b32 s45,0x235000
buffer_load_dwordx4 a[8:11],v226,%5,s45 offen sc1
s_mov_b32 s45,0x34d000
buffer_load_dwordx4 a[12:15],v226,%5,s45 offen sc1
s_mov_b32 s45,0x465000
buffer_load_dwordx4 a[16:19],v226,%5,s45 offen sc1
s_mov_b32 s45,0x57d000
buffer_load_dwordx4 a[20:23],v226,%5,s45 offen sc1
s_mov_b32 s45,0x695000
buffer_load_dwordx4 a[24:27],v226,%5,s45 offen sc1
s_mov_b32 s45,0x7ad000
buffer_load_dwordx4 a[28:31],v226,%5,s45 offen sc1
s_mov_b32 s45,0x8c5000
buffer_load_dwordx4 a[32:35],v226,%5,s45 offen sc1
s_mov_b32 s45,0x9dd000
buffer_load_dwordx4 a[36:39],v226,%5,s45 offen sc1
s_mov_b32 s45,0xaf5000
buffer_load_dwordx4 a[40:43],v226,%5,s45 offen sc1
s_mov_b32 s45,0xc0d000
buffer_load_dwordx4 a[44:47],v226,%5,s45 offen sc1
s_mov_b32 s45,0xd25000
buffer_load_dwordx4 a[48:51],v226,%5,s45 offen sc1
s_mov_b32 s45,0xe3d000
buffer_load_dwordx4 a[52:55],v226,%5,s45 offen sc1
s_mov_b32 s45,0xf55000
buffer_load_dwordx4 a[56:59],v226,%5,s45 offen sc1
s_mov_b32 s45,0x106d000
buffer_load_dwordx4 a[60:63],v226,%5,s45 offen sc1
m v[96:99],a[220:223],v[28:31],v[96:99]
m v[112:115],a[220:223],v[60:63],v[112:115]
m v[100:103],a[224:227],v[28:31],v[100:103]
m v[116:119],a[224:227],v[60:63],v[116:119]
m v[104:107],a[228:231],v[28:31],v[104:107]
m v[120:123],a[228:231],v[60:63],v[120:123]
m v[108:111],a[232:235],v[28:31],v[108:111]
m v[124:127],a[232:235],v[60:63],v[124:127]
s_waitcnt lgkmcnt(0)
m v[64:67],a[204:207],v[0:3],0
t a[220:221],v208,4096
t a[222:223],v208,6144
m v[80:83],a[204:207],v[32:35],0
t a[224:225],v209,4096
t a[226:227],v209,6144
m v[68:71],a[208:211],v[0:3],0
t a[228:229],v210,4096
t a[230:231],v210,6144
m v[84:87],a[208:211],v[32:35],0
t a[232:233],v211,4096
t a[234:235],v211,6144
m v[72:75],a[212:215],v[0:3],0
m v[88:91],a[212:215],v[32:35],0
m v[76:79],a[216:219],v[0:3],0
m v[92:95],a[216:219],v[32:35],0
s_waitcnt lgkmcnt(0)
m v[64:67],a[220:223],v[4:7],v[64:67]
t a[204:205],v208,8192
t a[206:207],v208,10240
m v[80:83],a[220:223],v[36:39],v[80:83]
t a[208:209],v209,8192
t a[210:211],v209,10240
t a[212:213],v210,8192
m v[68:71],a[224:227],v[4:7],v[68:71]
t a[214:215],v210,10240
t a[216:217],v211,8192
m v[84:87],a[224:227],v[36:39],v[84:87]
t a[218:219],v211,10240
s_add_u32 s55,s54,0x19000
s_add_u32 s59,s55,0x1000
m v[72:75],a[228:231],v[4:7],v[72:75]
v_mul_f32 v96,v96,v216
v_mul_f32 v97,v97,v216
v_mul_f32 v98,v98,v216
m v[88:91],a[228:231],v[36:39],v[88:91]
v_mul_f32 v99,v99,v216
v_mul_f32 v100,v100,v216
m v[76:79],a[232:235],v[4:7],v[76:79]
v_mul_f32 v101,v101,v216
v_mul_f32 v102,v102,v216
v_mul_f32 v103,v103,v216
m v[92:95],a[232:235],v[36:39],v[92:95]
cv v96,v96,v97
cv v97,v98,v99
cv v98,v100,v101
s_waitcnt lgkmcnt(0)
m v[64:67],a[204:207],v[8:11],v[64:67]
t a[220:221],v208,12288
t a[222:223],v208,14336
m v[80:83],a[204:207],v[40:43],v[80:83]
t a[224:225],v209,12288
t a[226:227],v209,14336
t a[228:229],v210,12288
m v[68:71],a[208:211],v[8:11],v[68:71]
t a[230:231],v210,14336
t a[232:233],v211,12288
t a[234:235],v211,14336
m v[84:87],a[208:211],v[40:43],v[84:87]
cv v99,v102,v103
buffer_store_dwordx4 v[96:99],v215,%5,s55 offen sc1
v_mul_f32 v104,v104,v216
m v[72:75],a[212:215],v[8:11],v[72:75]
v_mul_f32 v105,v105,v216
v_mul_f32 v106,v106,v216
m v[88:91],a[212:215],v[40:43],v[88:91]
v_mul_f32 v107,v107,v216
v_mul_f32 v108,v108,v216
v_mul_f32 v109,v109,v216
m v[76:79],a[216:219],v[8:11],v[76:79]
v_mul_f32 v110,v110,v216
v_mul_f32 v111,v111,v216
cv v104,v104,v105
m v[92:95],a[216:219],v[40:43],v[92:95]
cv v105,v106,v107
cv v106,v108,v109
cv v107,v110,v111
s_waitcnt lgkmcnt(0)
m v[64:67],a[220:223],v[12:15],v[64:67]
t a[204:205],v208,16384
t a[206:207],v208,18432
m v[80:83],a[220:223],v[44:47],v[80:83]
t a[208:209],v209,16384
t a[210:211],v209,18432
t a[212:213],v210,16384
m v[68:71],a[224:227],v[12:15],v[68:71]
t a[214:215],v210,18432
t a[216:217],v211,16384
m v[84:87],a[224:227],v[44:47],v[84:87]
t a[218:219],v211,18432
buffer_store_dwordx4 v[104:107],v215,%5,s55 offen offset:1024 sc1
v_mul_f32 v112,v112,v217
m v[72:75],a[228:231],v[12:15],v[72:75]
v_mul_f32 v113,v113,v217
v_mul_f32 v114,v114,v217
v_mul_f32 v115,v115,v217
m v[88:91],a[228:231],v[44:47],v[88:91]
v_mul_f32 v116,v116,v217
v_mul_f32 v117,v117,v217
m v[76:79],a[232:235],v[12:15],v[76:79]
v_mul_f32 v118,v118,v217
v_mul_f32 v119,v119,v217
cv v112,v112,v113
m v[92:95],a[232:235],v[44:47],v[92:95]
cv v113,v114,v115
cv v114,v116,v117
cv v115,v118,v119
s_waitcnt lgkmcnt(0)
m v[64:67],a[204:207],v[16:19],v[64:67]
t a[220:221],v208,20480
t a[222:223],v208,22528
m v[80:83],a[204:207],v[48:51],v[80:83]
t a[224:225],v209,20480
t a[226:227],v209,22528
t a[228:229],v210,20480
m v[68:71],a[208:211],v[16:19],v[68:71]
t a[230:231],v210,22528
t a[232:233],v211,20480
t a[234:235],v211,22528
m v[84:87],a[208:211],v[48:51],v[84:87]
buffer_store_dwordx4 v[112:115],v215,%5,s55 offen offset:2048 sc1
v_mul_f32 v120,v120,v217
v_mul_f32 v121,v121,v217
m v[72:75],a[212:215],v[16:19],v[72:75]
v_mul_f32 v122,v122,v217
v_mul_f32 v123,v123,v217
m v[88:91],a[212:215],v[48:51],v[88:91]
v_mul_f32 v124,v124,v217
v_mul_f32 v125,v125,v217
v_mul_f32 v126,v126,v217
m v[76:79],a[216:219],v[16:19],v[76:79]
v_mul_f32 v127,v127,v217
cv v120,v120,v121
cv v121,v122,v123
m v[92:95],a[216:219],v[48:51],v[92:95]
cv v122,v124,v125
cv v123,v126,v127
buffer_store_dwordx4 v[120:123],v215,%5,s55 offen offset:3072 sc1
s_waitcnt lgkmcnt(0)
m v[64:67],a[220:223],v[20:23],v[64:67]
t a[204:205],v208,24576
t a[206:207],v208,26624
m v[80:83],a[220:223],v[52:55],v[80:83]
t a[208:209],v209,24576
t a[210:211],v209,26624
m v[68:71],a[224:227],v[20:23],v[68:71]
t a[212:213],v210,24576
t a[214:215],v210,26624
m v[84:87],a[224:227],v[52:55],v[84:87]
t a[216:217],v211,24576
t a[218:219],v211,26624
m v[72:75],a[228:231],v[20:23],v[72:75]
m v[88:91],a[228:231],v[52:55],v[88:91]
m v[76:79],a[232:235],v[20:23],v[76:79]
m v[92:95],a[232:235],v[52:55],v[92:95]
s_waitcnt lgkmcnt(0)
m v[64:67],a[204:207],v[24:27],v[64:67]
t a[220:221],v208,28672
t a[222:223],v208,30720
m v[80:83],a[204:207],v[56:59],v[80:83]
t a[224:225],v209,28672
t a[226:227],v209,30720
m v[68:71],a[208:211],v[24:27],v[68:71]
t a[228:229],v210,28672
t a[230:231],v210,30720
m v[84:87],a[208:211],v[56:59],v[84:87]
t a[232:233],v211,28672
t a[234:235],v211,30720
m v[72:75],a[212:215],v[24:27],v[72:75]
m v[88:91],a[212:215],v[56:59],v[88:91]
m v[76:79],a[216:219],v[24:27],v[76:79]
m v[92:95],a[216:219],v[56:59],v[92:95]
s_waitcnt lgkmcnt(0)
m v[64:67],a[220:223],v[28:31],v[64:67]
m v[80:83],a[220:223],v[60:63],v[80:83]
m v[68:71],a[224:227],v[28:31],v[68:71]
m v[84:87],a[224:227],v[60:63],v[84:87]
m v[72:75],a[228:231],v[28:31],v[72:75]
m v[88:91],a[228:231],v[60:63],v[88:91]
m v[76:79],a[232:235],v[28:31],v[76:79]
m v[92:95],a[232:235],v[60:63],v[92:95]
s_add_u32 s55,s54,0x1e000
s_add_u32 s59,s55,0x1000
v_mul_f32 v64,v64,v216
v_mul_f32 v65,v65,v216
v_mul_f32 v66,v66,v216
v_mul_f32 v67,v67,v216
v_mul_f32 v68,v68,v216
v_mul_f32 v69,v69,v216
v_mul_f32 v70,v70,v216
v_mul_f32 v71,v71,v216
cv v64,v64,v65
cv v65,v66,v67
cv v66,v68,v69
cv v67,v70,v71
buffer_store_dwordx4 v[64:67],v215,%5,s55 offen sc1
v_mul_f32 v72,v72,v216
v_mul_f32 v73,v73,v216
v_mul_f32 v74,v74,v216
v_mul_f32 v75,v75,v216
v_mul_f32 v76,v76,v216
v_mul_f32 v77,v77,v216
v_mul_f32 v78,v78,v216
v_mul_f32 v79,v79,v216
cv v72,v72,v73
cv v73,v74,v75
cv v74,v76,v77
cv v75,v78,v79
buffer_store_dwordx4 v[72:75],v215,%5,s55 offen offset:1024 sc1
v_mul_f32 v80,v80,v217
v_mul_f32 v81,v81,v217
v_mul_f32 v82,v82,v217
v_mul_f32 v83,v83,v217
v_mul_f32 v84,v84,v217
v_mul_f32 v85,v85,v217
v_mul_f32 v86,v86,v217
v_mul_f32 v87,v87,v217
cv v80,v80,v81
cv v81,v82,v83
cv v82,v84,v85
cv v83,v86,v87
buffer_store_dwordx4 v[80:83],v215,%5,s55 offen offset:2048 sc1
v_mul_f32 v88,v88,v217
v_mul_f32 v89,v89,v217
v_mul_f32 v90,v90,v217
v_mul_f32 v91,v91,v217
v_mul_f32 v92,v92,v217
v_mul_f32 v93,v93,v217
v_mul_f32 v94,v94,v217
v_mul_f32 v95,v95,v217
cv v88,v88,v89
cv v89,v90,v91
cv v90,v92,v93
cv v91,v94,v95
buffer_store_dwordx4 v[88:91],v215,%5,s55 offen offset:3072 sc1
s_waitcnt vmcnt(0)
s_barrier
v_add_u32 v232,7,v150
buffer_store_dword v232,v230,%5,%12 offen sc1
m v[240:243],v[222:225],a[0:3],0
m v[244:247],v[220:223],a[0:3],0
m v[240:243],v[222:225],a[4:7],v[240:243]
m v[244:247],v[220:223],a[4:7],v[244:247]
m v[240:243],v[222:225],a[8:11],v[240:243]
m v[244:247],v[220:223],a[8:11],v[244:247]
m v[240:243],v[222:225],a[12:15],v[240:243]
m v[244:247],v[220:223],a[12:15],v[244:247]
m v[240:243],v[222:225],a[16:19],v[240:243]
m v[244:247],v[220:223],a[16:19],v[244:247]
m v[240:243],v[222:225],a[20:23],v[240:243]
m v[244:247],v[220:223],a[20:23],v[244:247]
m v[240:243],v[222:225],a[24:27],v[240:243]
m v[244:247],v[220:223],a[24:27],v[244:247]
m v[240:243],v[222:225],a[28:31],v[240:243]
m v[244:247],v[220:223],a[28:31],v[244:247]
m v[240:243],v[222:225],a[32:35],v[240:243]
m v[244:247],v[220:223],a[32:35],v[244:247]
m v[240:243],v[222:225],a[36:39],v[240:243]
m v[244:247],v[220:223],a[36:39],v[244:247]
m v[240:243],v[222:225],a[40:43],v[240:243]
m v[244:247],v[220:223],a[40:43],v[244:247]
m v[240:243],v[222:225],a[44:47],v[240:243]
m v[244:247],v[220:223],a[44:47],v[244:247]
m v[240:243],v[222:225],a[48:51],v[240:243]
m v[244:247],v[220:223],a[48:51],v[244:247]
m v[240:243],v[222:225],a[52:55],v[240:243]
m v[244:247],v[220:223],a[52:55],v[244:247]
m v[240:243],v[222:225],a[56:59],v[240:243]
m v[244:247],v[220:223],a[56:59],v[244:247]
m v[240:243],v[222:225],a[60:63],v[240:243]
m v[244:247],v[220:223],a[60:63],v[244:247]
s_nop 6
cv v248,v240,v241
cv v249,v242,v243
cv v250,v244,v245
cv v251,v246,v247
s_mov_b32 s45,0x80
buffer_store_dwordx2 v[248:249],v227,%10,s45 offen
buffer_store_dwordx2 v[250:251],v227,%10,s45 offen offset:32
s_mov_b32 s44,0x186a0
LC_pl12%=:
buffer_load_dword v233,v231,%5,%12 offen sc1
s_waitcnt vmcnt(0)
v_sub_u32 v234,v233,v148
v_add_u32 v234,-7,v234
v_cmp_lt_u32_e32 vcc,0,v234
s_nop 0
s_cbranch_vccz LC_pd12%=
s_sleep 2
s_sub_u32 s44,s44,1
s_cmp_lg_u32 s44,0
s_cbranch_scc1 LC_pl12%=
LC_pd12%=:
s_mov_b32 s45,0
buffer_load_dwordx4 a[64:67],v228,%5,s45 offen sc1
s_mov_b32 s45,0x118000
buffer_load_dwordx4 a[68:71],v228,%5,s45 offen sc1
s_mov_b32 s45,0x230000
buffer_load_dwordx4 a[72:75],v228,%5,s45 offen sc1
s_mov_b32 s45,0x348000
buffer_load_dwordx4 a[76:79],v228,%5,s45 offen sc1
s_mov_b32 s45,0x460000
buffer_load_dwordx4 a[80:83],v228,%5,s45 offen sc1
s_mov_b32 s45,0x578000
buffer_load_dwordx4 a[84:87],v228,%5,s45 offen sc1
s_mov_b32 s45,0x690000
buffer_load_dwordx4 a[88:91],v228,%5,s45 offen sc1
s_mov_b32 s45,0x7a8000
buffer_load_dwordx4 a[92:95],v228,%5,s45 offen sc1
s_mov_b32 s45,0x8c0000
buffer_load_dwordx4 a[96:99],v228,%5,s45 offen sc1
s_mov_b32 s45,0x9d8000
buffer_load_dwordx4 a[100:103],v228,%5,s45 offen sc1
s_mov_b32 s45,0xaf0000
buffer_load_dwordx4 a[104:107],v228,%5,s45 offen sc1
s_mov_b32 s45,0xc08000
buffer_load_dwordx4 a[108:111],v228,%5,s45 offen sc1
s_mov_b32 s45,0xd20000
buffer_load_dwordx4 a[112:115],v228,%5,s45 offen sc1
s_mov_b32 s45,0xe38000
buffer_load_dwordx4 a[116:119],v228,%5,s45 offen sc1
s_mov_b32 s45,0xf50000
buffer_load_dwordx4 a[120:123],v228,%5,s45 offen sc1
s_mov_b32 s45,0x1068000
buffer_load_dwordx4 a[124:127],v228,%5,s45 offen sc1
s_waitcnt vmcnt(15)
m v[240:243],v[222:225],a[64:67],0
m v[244:247],v[220:223],a[64:67],0
s_waitcnt vmcnt(14)
m v[240:243],v[222:225],a[68:71],v[240:243]
m v[244:247],v[220:223],a[68:71],v[244:247]
s_waitcnt vmcnt(13)
m v[240:243],v[222:225],a[72:75],v[240:243]
m v[244:247],v[220:223],a[72:75],v[244:247]
s_waitcnt vmcnt(12)
m v[240:243],v[222:225],a[76:79],v[240:243]
m v[244:247],v[220:223],a[76:79],v[244:247]
s_waitcnt vmcnt(11)
m v[240:243],v[222:225],a[80:83],v[240:243]
m v[244:247],v[220:223],a[80:83],v[244:247]
s_waitcnt vmcnt(10)
m v[240:243],v[222:225],a[84:87],v[240:243]
m v[244:247],v[220:223],a[84:87],v[244:247]
s_waitcnt vmcnt(9)
m v[240:243],v[222:225],a[88:91],v[240:243]
m v[244:247],v[220:223],a[88:91],v[244:247]
s_waitcnt vmcnt(8)
m v[240:243],v[222:225],a[92:95],v[240:243]
m v[244:247],v[220:223],a[92:95],v[244:247]
s_waitcnt vmcnt(7)
m v[240:243],v[222:225],a[96:99],v[240:243]
m v[244:247],v[220:223],a[96:99],v[244:247]
s_waitcnt vmcnt(6)
m v[240:243],v[222:225],a[100:103],v[240:243]
m v[244:247],v[220:223],a[100:103],v[244:247]
s_waitcnt vmcnt(5)
m v[240:243],v[222:225],a[104:107],v[240:243]
m v[244:247],v[220:223],a[104:107],v[244:247]
s_waitcnt vmcnt(4)
m v[240:243],v[222:225],a[108:111],v[240:243]
m v[244:247],v[220:223],a[108:111],v[244:247]
s_waitcnt vmcnt(3)
m v[240:243],v[222:225],a[112:115],v[240:243]
m v[244:247],v[220:223],a[112:115],v[244:247]
s_waitcnt vmcnt(2)
m v[240:243],v[222:225],a[116:119],v[240:243]
m v[244:247],v[220:223],a[116:119],v[244:247]
s_waitcnt vmcnt(1)
m v[240:243],v[222:225],a[120:123],v[240:243]
m v[244:247],v[220:223],a[120:123],v[244:247]
s_waitcnt vmcnt(0)
m v[240:243],v[222:225],a[124:127],v[240:243]
m v[244:247],v[220:223],a[124:127],v[244:247]
s_nop 6
cv v248,v240,v241
cv v249,v242,v243
cv v250,v244,v245
cv v251,v246,v247
s_mov_b32 s45,0
buffer_store_dwordx2 v[248:249],v229,%10,s45 offen
buffer_store_dwordx2 v[250:251],v229,%10,s45 offen offset:32
s_waitcnt vmcnt(0)
s_endpgm
LC_stub1%=:
s_call_b64 s[50:51],LC_rare0%=
s_branch LC_ret1%=
LC_stub2%=:
s_call_b64 s[50:51],LC_rare1%=
s_branch LC_ret2%=
LC_stub3%=:
s_call_b64 s[50:51],LC_rare0%=
s_branch LC_ret3%=
LC_stub4%=:
s_call_b64 s[50:51],LC_rare1%=
s_branch LC_ret4%=
LC_stub5%=:
s_call_b64 s[50:51],LC_rare0%=
s_branch LC_ret5%=
LC_hs6%=:
s_mov_b32 s44,0x186a0
LC_hl6%=:
buffer_load_dwordx2 v[150:151],v152,%5,%12 offen sc1
s_waitcnt vmcnt(0)
v_mov_b32 v149,v150
buffer_store_dwordx2 v[148:149],v153,%5,%12 offen sc1
v_sub_u32 v155,v151,v148
v_cmp_lt_u32_e32 vcc,7,v155
s_nop 0
s_cmp_eq_u64 vcc,0
s_cselect_b32 s58,1,0
s_cmp_eq_u32 s58,1
s_cbranch_scc1 LC_hr6%=
s_sleep 2
s_sub_u32 s44,s44,1
s_cmp_lg_u32 s44,0
s_cbranch_scc1 LC_hl6%=
s_branch LC_hr6%=
LC_stub7%=:
s_call_b64 s[50:51],LC_rare1%=
s_branch LC_ret7%=
LC_stub8%=:
s_call_b64 s[50:51],LC_rare0%=
s_branch LC_ret8%=
LC_stub9%=:
s_call_b64 s[50:51],LC_rare1%=
s_branch LC_ret9%=
LC_ps10%=:
s_mov_b32 s44,0x186a0
LC_pl11%=:
buffer_load_dword v233,v231,%5,%12 offen sc1
s_waitcnt vmcnt(0)
v_sub_u32 v234,v233,v148
v_add_u32 v234,-2,v234
v_cmp_lt_u32_e32 vcc,5,v234
s_nop 0
s_cbranch_vccz LC_pr10%=
s_sleep 2
s_sub_u32 s44,s44,1
s_cmp_lg_u32 s44,0
s_cbranch_scc1 LC_pl11%=
s_branch LC_pr10%=
LC_rare0%=:
s_nop 7
s_nop 1
v_max3_f32 v220,v4,v5,v6
v_max3_f32 v220,v220,v7,v12
v_max3_f32 v220,v220,v13,v14
v_max_f32 v220,v220,v15
v_mov_b32 v225,v220
s_nop 1
v_permlane32_swap_b32 v220,v225
v_max_f32 v220,v220,v225
v_mov_b32 v225,v220
s_nop 1
v_permlane16_swap_b32 v220,v225
v_max_f32 v220,v220,v225
v_add_f32 v220,0x40e00000,v220
v_max_f32 v220,0,v220
v_sub_f32 v224,0,v220
v_exp_f32 v224,v224
v_sub_f32 v4,v4,v220
v_sub_f32 v5,v5,v220
v_sub_f32 v6,v6,v220
v_sub_f32 v7,v7,v220
v_sub_f32 v12,v12,v220
v_sub_f32 v13,v13,v220
v_sub_f32 v14,v14,v220
v_sub_f32 v15,v15,v220
v_sub_f32 v36,v36,v220
v_sub_f32 v37,v37,v220
v_sub_f32 v38,v38,v220
v_sub_f32 v39,v39,v220
v_sub_f32 v44,v44,v220
v_sub_f32 v45,v45,v220
v_sub_f32 v46,v46,v220
v_sub_f32 v47,v47,v220
v_sub_f32 v84,v84,v220
v_sub_f32 v85,v85,v220
v_sub_f32 v86,v86,v220
v_sub_f32 v87,v87,v220
ar v230,a0
ar v231,a1
ar v232,a2
ar v233,a3
v_mul_f32 v230,v230,v224
v_mul_f32 v231,v231,v224
v_mul_f32 v232,v232,v224
v_mul_f32 v233,v233,v224
v_accvgpr_write_b32 a0,v230
v_accvgpr_write_b32 a1,v231
v_accvgpr_write_b32 a2,v232
v_accvgpr_write_b32 a3,v233
ar v230,a4
ar v231,a5
ar v232,a6
ar v233,a7
v_mul_f32 v230,v230,v224
v_mul_f32 v231,v231,v224
v_mul_f32 v232,v232,v224
v_mul_f32 v233,v233,v224
v_accvgpr_write_b32 a4,v230
v_accvgpr_write_b32 a5,v231
v_accvgpr_write_b32 a6,v232
v_accvgpr_write_b32 a7,v233
ar v230,a8
ar v231,a9
ar v232,a10
ar v233,a11
v_mul_f32 v230,v230,v224
v_mul_f32 v231,v231,v224
v_mul_f32 v232,v232,v224
v_mul_f32 v233,v233,v224
v_accvgpr_write_b32 a8,v230
v_accvgpr_write_b32 a9,v231
v_accvgpr_write_b32 a10,v232
v_accvgpr_write_b32 a11,v233
ar v230,a12
ar v231,a13
ar v232,a14
ar v233,a15
v_mul_f32 v230,v230,v224
v_mul_f32 v231,v231,v224
v_mul_f32 v232,v232,v224
v_mul_f32 v233,v233,v224
v_accvgpr_write_b32 a12,v230
v_accvgpr_write_b32 a13,v231
v_accvgpr_write_b32 a14,v232
v_accvgpr_write_b32 a15,v233
ar v230,a16
ar v231,a17
ar v232,a18
ar v233,a19
v_mul_f32 v230,v230,v224
v_mul_f32 v231,v231,v224
v_mul_f32 v232,v232,v224
v_mul_f32 v233,v233,v224
v_accvgpr_write_b32 a16,v230
v_accvgpr_write_b32 a17,v231
v_accvgpr_write_b32 a18,v232
v_accvgpr_write_b32 a19,v233
ar v230,a20
ar v231,a21
ar v232,a22
ar v233,a23
v_mul_f32 v230,v230,v224
v_mul_f32 v231,v231,v224
v_mul_f32 v232,v232,v224
v_mul_f32 v233,v233,v224
v_accvgpr_write_b32 a20,v230
v_accvgpr_write_b32 a21,v231
v_accvgpr_write_b32 a22,v232
v_accvgpr_write_b32 a23,v233
ar v230,a24
ar v231,a25
ar v232,a26
ar v233,a27
v_mul_f32 v230,v230,v224
v_mul_f32 v231,v231,v224
v_mul_f32 v232,v232,v224
v_mul_f32 v233,v233,v224
v_accvgpr_write_b32 a24,v230
v_accvgpr_write_b32 a25,v231
v_accvgpr_write_b32 a26,v232
v_accvgpr_write_b32 a27,v233
ar v230,a28
ar v231,a29
ar v232,a30
ar v233,a31
v_mul_f32 v230,v230,v224
v_mul_f32 v231,v231,v224
v_mul_f32 v232,v232,v224
v_mul_f32 v233,v233,v224
v_accvgpr_write_b32 a28,v230
v_accvgpr_write_b32 a29,v231
v_accvgpr_write_b32 a30,v232
v_accvgpr_write_b32 a31,v233
ar v230,a32
ar v231,a33
ar v232,a34
ar v233,a35
v_mul_f32 v230,v230,v224
v_mul_f32 v231,v231,v224
v_mul_f32 v232,v232,v224
v_mul_f32 v233,v233,v224
v_accvgpr_write_b32 a32,v230
v_accvgpr_write_b32 a33,v231
v_accvgpr_write_b32 a34,v232
v_accvgpr_write_b32 a35,v233
ar v230,a36
ar v231,a37
ar v232,a38
ar v233,a39
v_mul_f32 v230,v230,v224
v_mul_f32 v231,v231,v224
v_mul_f32 v232,v232,v224
v_mul_f32 v233,v233,v224
v_accvgpr_write_b32 a36,v230
v_accvgpr_write_b32 a37,v231
v_accvgpr_write_b32 a38,v232
v_accvgpr_write_b32 a39,v233
ar v230,a40
ar v231,a41
ar v232,a42
ar v233,a43
v_mul_f32 v230,v230,v224
v_mul_f32 v231,v231,v224
v_mul_f32 v232,v232,v224
v_mul_f32 v233,v233,v224
v_accvgpr_write_b32 a40,v230
v_accvgpr_write_b32 a41,v231
v_accvgpr_write_b32 a42,v232
v_accvgpr_write_b32 a43,v233
ar v230,a44
ar v231,a45
ar v232,a46
ar v233,a47
v_mul_f32 v230,v230,v224
v_mul_f32 v231,v231,v224
v_mul_f32 v232,v232,v224
v_mul_f32 v233,v233,v224
v_accvgpr_write_b32 a44,v230
v_accvgpr_write_b32 a45,v231
v_accvgpr_write_b32 a46,v232
v_accvgpr_write_b32 a47,v233
ar v230,a48
ar v231,a49
ar v232,a50
ar v233,a51
v_mul_f32 v230,v230,v224
v_mul_f32 v231,v231,v224
v_mul_f32 v232,v232,v224
v_mul_f32 v233,v233,v224
v_accvgpr_write_b32 a48,v230
v_accvgpr_write_b32 a49,v231
v_accvgpr_write_b32 a50,v232
v_accvgpr_write_b32 a51,v233
ar v230,a52
ar v231,a53
ar v232,a54
ar v233,a55
v_mul_f32 v230,v230,v224
v_mul_f32 v231,v231,v224
v_mul_f32 v232,v232,v224
v_mul_f32 v233,v233,v224
v_accvgpr_write_b32 a52,v230
v_accvgpr_write_b32 a53,v231
v_accvgpr_write_b32 a54,v232
v_accvgpr_write_b32 a55,v233
ar v230,a56
ar v231,a57
ar v232,a58
ar v233,a59
v_mul_f32 v230,v230,v224
v_mul_f32 v231,v231,v224
v_mul_f32 v232,v232,v224
v_mul_f32 v233,v233,v224
v_accvgpr_write_b32 a56,v230
v_accvgpr_write_b32 a57,v231
v_accvgpr_write_b32 a58,v232
v_accvgpr_write_b32 a59,v233
ar v230,a60
ar v231,a61
ar v232,a62
ar v233,a63
v_mul_f32 v230,v230,v224
v_mul_f32 v231,v231,v224
v_mul_f32 v232,v232,v224
v_mul_f32 v233,v233,v224
v_accvgpr_write_b32 a60,v230
v_accvgpr_write_b32 a61,v231
v_accvgpr_write_b32 a62,v232
v_accvgpr_write_b32 a63,v233
ar v230,a192
ar v231,a193
ar v232,a194
ar v233,a195
v_mul_f32 v230,v230,v224
v_mul_f32 v231,v231,v224
v_mul_f32 v232,v232,v224
v_mul_f32 v233,v233,v224
v_accvgpr_write_b32 a192,v230
v_accvgpr_write_b32 a193,v231
v_accvgpr_write_b32 a194,v232
v_accvgpr_write_b32 a195,v233
v_max3_f32 v221,v20,v21,v22
v_max3_f32 v221,v221,v23,v28
v_max3_f32 v221,v221,v29,v30
v_max_f32 v221,v221,v31
v_mov_b32 v225,v221
s_nop 1
v_permlane32_swap_b32 v221,v225
v_max_f32 v221,v221,v225
v_mov_b32 v225,v221
s_nop 1
v_permlane16_swap_b32 v221,v225
v_max_f32 v221,v221,v225
v_add_f32 v221,0x40e00000,v221
v_max_f32 v221,0,v221
v_sub_f32 v224,0,v221
v_exp_f32 v224,v224
v_sub_f32 v20,v20,v221
v_sub_f32 v21,v21,v221
v_sub_f32 v22,v22,v221
v_sub_f32 v23,v23,v221
v_sub_f32 v28,v28,v221
v_sub_f32 v29,v29,v221
v_sub_f32 v30,v30,v221
v_sub_f32 v31,v31,v221
v_sub_f32 v52,v52,v221
v_sub_f32 v53,v53,v221
v_sub_f32 v54,v54,v221
v_sub_f32 v55,v55,v221
v_sub_f32 v60,v60,v221
v_sub_f32 v61,v61,v221
v_sub_f32 v62,v62,v221
v_sub_f32 v63,v63,v221
v_sub_f32 v92,v92,v221
v_sub_f32 v93,v93,v221
v_sub_f32 v94,v94,v221
v_sub_f32 v95,v95,v221
ar v230,a64
ar v231,a65
ar v232,a66
ar v233,a67
v_mul_f32 v230,v230,v224
v_mul_f32 v231,v231,v224
v_mul_f32 v232,v232,v224
v_mul_f32 v233,v233,v224
v_accvgpr_write_b32 a64,v230
v_accvgpr_write_b32 a65,v231
v_accvgpr_write_b32 a66,v232
v_accvgpr_write_b32 a67,v233
ar v230,a68
ar v231,a69
ar v232,a70
ar v233,a71
v_mul_f32 v230,v230,v224
v_mul_f32 v231,v231,v224
v_mul_f32 v232,v232,v224
v_mul_f32 v233,v233,v224
v_accvgpr_write_b32 a68,v230
v_accvgpr_write_b32 a69,v231
v_accvgpr_write_b32 a70,v232
v_accvgpr_write_b32 a71,v233
ar v230,a72
ar v231,a73
ar v232,a74
ar v233,a75
v_mul_f32 v230,v230,v224
v_mul_f32 v231,v231,v224
v_mul_f32 v232,v232,v224
v_mul_f32 v233,v233,v224
v_accvgpr_write_b32 a72,v230
v_accvgpr_write_b32 a73,v231
v_accvgpr_write_b32 a74,v232
v_accvgpr_write_b32 a75,v233
ar v230,a76
ar v231,a77
ar v232,a78
ar v233,a79
v_mul_f32 v230,v230,v224
v_mul_f32 v231,v231,v224
v_mul_f32 v232,v232,v224
v_mul_f32 v233,v233,v224
v_accvgpr_write_b32 a76,v230
v_accvgpr_write_b32 a77,v231
v_accvgpr_write_b32 a78,v232
v_accvgpr_write_b32 a79,v233
ar v230,a80
ar v231,a81
ar v232,a82
ar v233,a83
v_mul_f32 v230,v230,v224
v_mul_f32 v231,v231,v224
v_mul_f32 v232,v232,v224
v_mul_f32 v233,v233,v224
v_accvgpr_write_b32 a80,v230
v_accvgpr_write_b32 a81,v231
v_accvgpr_write_b32 a82,v232
v_accvgpr_write_b32 a83,v233
ar v230,a84
ar v231,a85
ar v232,a86
ar v233,a87
v_mul_f32 v230,v230,v224
v_mul_f32 v231,v231,v224
v_mul_f32 v232,v232,v224
v_mul_f32 v233,v233,v224
v_accvgpr_write_b32 a84,v230
v_accvgpr_write_b32 a85,v231
v_accvgpr_write_b32 a86,v232
v_accvgpr_write_b32 a87,v233
ar v230,a88
ar v231,a89
ar v232,a90
ar v233,a91
v_mul_f32 v230,v230,v224
v_mul_f32 v231,v231,v224
v_mul_f32 v232,v232,v224
v_mul_f32 v233,v233,v224
v_accvgpr_write_b32 a88,v230
v_accvgpr_write_b32 a89,v231
v_accvgpr_write_b32 a90,v232
v_accvgpr_write_b32 a91,v233
ar v230,a92
ar v231,a93
ar v232,a94
ar v233,a95
v_mul_f32 v230,v230,v224
v_mul_f32 v231,v231,v224
v_mul_f32 v232,v232,v224
v_mul_f32 v233,v233,v224
v_accvgpr_write_b32 a92,v230
v_accvgpr_write_b32 a93,v231
v_accvgpr_write_b32 a94,v232
v_accvgpr_write_b32 a95,v233
ar v230,a96
ar v231,a97
ar v232,a98
ar v233,a99
v_mul_f32 v230,v230,v224
v_mul_f32 v231,v231,v224
v_mul_f32 v232,v232,v224
v_mul_f32 v233,v233,v224
v_accvgpr_write_b32 a96,v230
v_accvgpr_write_b32 a97,v231
v_accvgpr_write_b32 a98,v232
v_accvgpr_write_b32 a99,v233
ar v230,a100
ar v231,a101
ar v232,a102
ar v233,a103
v_mul_f32 v230,v230,v224
v_mul_f32 v231,v231,v224
v_mul_f32 v232,v232,v224
v_mul_f32 v233,v233,v224
v_accvgpr_write_b32 a100,v230
v_accvgpr_write_b32 a101,v231
v_accvgpr_write_b32 a102,v232
v_accvgpr_write_b32 a103,v233
ar v230,a104
ar v231,a105
ar v232,a106
ar v233,a107
v_mul_f32 v230,v230,v224
v_mul_f32 v231,v231,v224
v_mul_f32 v232,v232,v224
v_mul_f32 v233,v233,v224
v_accvgpr_write_b32 a104,v230
v_accvgpr_write_b32 a105,v231
v_accvgpr_write_b32 a106,v232
v_accvgpr_write_b32 a107,v233
ar v230,a108
ar v231,a109
ar v232,a110
ar v233,a111
v_mul_f32 v230,v230,v224
v_mul_f32 v231,v231,v224
v_mul_f32 v232,v232,v224
v_mul_f32 v233,v233,v224
v_accvgpr_write_b32 a108,v230
v_accvgpr_write_b32 a109,v231
v_accvgpr_write_b32 a110,v232
v_accvgpr_write_b32 a111,v233
ar v230,a112
ar v231,a113
ar v232,a114
ar v233,a115
v_mul_f32 v230,v230,v224
v_mul_f32 v231,v231,v224
v_mul_f32 v232,v232,v224
v_mul_f32 v233,v233,v224
v_accvgpr_write_b32 a112,v230
v_accvgpr_write_b32 a113,v231
v_accvgpr_write_b32 a114,v232
v_accvgpr_write_b32 a115,v233
ar v230,a116
ar v231,a117
ar v232,a118
ar v233,a119
v_mul_f32 v230,v230,v224
v_mul_f32 v231,v231,v224
v_mul_f32 v232,v232,v224
v_mul_f32 v233,v233,v224
v_accvgpr_write_b32 a116,v230
v_accvgpr_write_b32 a117,v231
v_accvgpr_write_b32 a118,v232
v_accvgpr_write_b32 a119,v233
ar v230,a120
ar v231,a121
ar v232,a122
ar v233,a123
v_mul_f32 v230,v230,v224
v_mul_f32 v231,v231,v224
v_mul_f32 v232,v232,v224
v_mul_f32 v233,v233,v224
v_accvgpr_write_b32 a120,v230
v_accvgpr_write_b32 a121,v231
v_accvgpr_write_b32 a122,v232
v_accvgpr_write_b32 a123,v233
ar v230,a124
ar v231,a125
ar v232,a126
ar v233,a127
v_mul_f32 v230,v230,v224
v_mul_f32 v231,v231,v224
v_mul_f32 v232,v232,v224
v_mul_f32 v233,v233,v224
v_accvgpr_write_b32 a124,v230
v_accvgpr_write_b32 a125,v231
v_accvgpr_write_b32 a126,v232
v_accvgpr_write_b32 a127,v233
ar v230,a196
ar v231,a197
ar v232,a198
ar v233,a199
v_mul_f32 v230,v230,v224
v_mul_f32 v231,v231,v224
v_mul_f32 v232,v232,v224
v_mul_f32 v233,v233,v224
v_accvgpr_write_b32 a196,v230
v_accvgpr_write_b32 a197,v231
v_accvgpr_write_b32 a198,v232
v_accvgpr_write_b32 a199,v233
v_exp_f32 v220,v4
v_exp_f32 v221,v5
v_exp_f32 v222,v6
v_exp_f32 v223,v7
cv v68,v220,v221
v_exp_f32 v224,v12
v_exp_f32 v225,v13
cv v69,v222,v223
v_exp_f32 v220,v14
v_exp_f32 v221,v15
cv v70,v224,v225
v_exp_f32 v222,v20
v_exp_f32 v223,v21
cv v71,v220,v221
v_exp_f32 v224,v22
v_exp_f32 v225,v23
cv v76,v222,v223
v_exp_f32 v220,v28
v_exp_f32 v221,v29
cv v77,v224,v225
v_exp_f32 v222,v30
v_exp_f32 v223,v31
cv v78,v220,v221
cv v79,v222,v223
v_or3_b32 v228,v68,v69,v70
v_or3_b32 v228,v228,v71,v76
v_or3_b32 v228,v228,v77,v78
v_or_b32 v228,v228,v79
v_and_b32 v228,0x40004000,v228
s_nop 7
s_setpc_b64 s[50:51]
LC_rare1%=:
s_nop 7
s_nop 1
v_max3_f32 v220,v36,v37,v38
v_max3_f32 v220,v220,v39,v44
v_max3_f32 v220,v220,v45,v46
v_max_f32 v220,v220,v47
v_mov_b32 v225,v220
s_nop 1
v_permlane32_swap_b32 v220,v225
v_max_f32 v220,v220,v225
v_mov_b32 v225,v220
s_nop 1
v_permlane16_swap_b32 v220,v225
v_max_f32 v220,v220,v225
v_add_f32 v220,0x40e00000,v220
v_max_f32 v220,0,v220
v_sub_f32 v224,0,v220
v_exp_f32 v224,v224
v_sub_f32 v4,v4,v220
v_sub_f32 v5,v5,v220
v_sub_f32 v6,v6,v220
v_sub_f32 v7,v7,v220
v_sub_f32 v12,v12,v220
v_sub_f32 v13,v13,v220
v_sub_f32 v14,v14,v220
v_sub_f32 v15,v15,v220
v_sub_f32 v36,v36,v220
v_sub_f32 v37,v37,v220
v_sub_f32 v38,v38,v220
v_sub_f32 v39,v39,v220
v_sub_f32 v44,v44,v220
v_sub_f32 v45,v45,v220
v_sub_f32 v46,v46,v220
v_sub_f32 v47,v47,v220
v_sub_f32 v84,v84,v220
v_sub_f32 v85,v85,v220
v_sub_f32 v86,v86,v220
v_sub_f32 v87,v87,v220
ar v230,a0
ar v231,a1
ar v232,a2
ar v233,a3
v_mul_f32 v230,v230,v224
v_mul_f32 v231,v231,v224
v_mul_f32 v232,v232,v224
v_mul_f32 v233,v233,v224
v_accvgpr_write_b32 a0,v230
v_accvgpr_write_b32 a1,v231
v_accvgpr_write_b32 a2,v232
v_accvgpr_write_b32 a3,v233
ar v230,a4
ar v231,a5
ar v232,a6
ar v233,a7
v_mul_f32 v230,v230,v224
v_mul_f32 v231,v231,v224
v_mul_f32 v232,v232,v224
v_mul_f32 v233,v233,v224
v_accvgpr_write_b32 a4,v230
v_accvgpr_write_b32 a5,v231
v_accvgpr_write_b32 a6,v232
v_accvgpr_write_b32 a7,v233
ar v230,a8
ar v231,a9
ar v232,a10
ar v233,a11
v_mul_f32 v230,v230,v224
v_mul_f32 v231,v231,v224
v_mul_f32 v232,v232,v224
v_mul_f32 v233,v233,v224
v_accvgpr_write_b32 a8,v230
v_accvgpr_write_b32 a9,v231
v_accvgpr_write_b32 a10,v232
v_accvgpr_write_b32 a11,v233
ar v230,a12
ar v231,a13
ar v232,a14
ar v233,a15
v_mul_f32 v230,v230,v224
v_mul_f32 v231,v231,v224
v_mul_f32 v232,v232,v224
v_mul_f32 v233,v233,v224
v_accvgpr_write_b32 a12,v230
v_accvgpr_write_b32 a13,v231
v_accvgpr_write_b32 a14,v232
v_accvgpr_write_b32 a15,v233
ar v230,a16
ar v231,a17
ar v232,a18
ar v233,a19
v_mul_f32 v230,v230,v224
v_mul_f32 v231,v231,v224
v_mul_f32 v232,v232,v224
v_mul_f32 v233,v233,v224
v_accvgpr_write_b32 a16,v230
v_accvgpr_write_b32 a17,v231
v_accvgpr_write_b32 a18,v232
v_accvgpr_write_b32 a19,v233
ar v230,a20
ar v231,a21
ar v232,a22
ar v233,a23
v_mul_f32 v230,v230,v224
v_mul_f32 v231,v231,v224
v_mul_f32 v232,v232,v224
v_mul_f32 v233,v233,v224
v_accvgpr_write_b32 a20,v230
v_accvgpr_write_b32 a21,v231
v_accvgpr_write_b32 a22,v232
v_accvgpr_write_b32 a23,v233
ar v230,a24
ar v231,a25
ar v232,a26
ar v233,a27
v_mul_f32 v230,v230,v224
v_mul_f32 v231,v231,v224
v_mul_f32 v232,v232,v224
v_mul_f32 v233,v233,v224
v_accvgpr_write_b32 a24,v230
v_accvgpr_write_b32 a25,v231
v_accvgpr_write_b32 a26,v232
v_accvgpr_write_b32 a27,v233
ar v230,a28
ar v231,a29
ar v232,a30
ar v233,a31
v_mul_f32 v230,v230,v224
v_mul_f32 v231,v231,v224
v_mul_f32 v232,v232,v224
v_mul_f32 v233,v233,v224
v_accvgpr_write_b32 a28,v230
v_accvgpr_write_b32 a29,v231
v_accvgpr_write_b32 a30,v232
v_accvgpr_write_b32 a31,v233
ar v230,a32
ar v231,a33
ar v232,a34
ar v233,a35
v_mul_f32 v230,v230,v224
v_mul_f32 v231,v231,v224
v_mul_f32 v232,v232,v224
v_mul_f32 v233,v233,v224
v_accvgpr_write_b32 a32,v230
v_accvgpr_write_b32 a33,v231
v_accvgpr_write_b32 a34,v232
v_accvgpr_write_b32 a35,v233
ar v230,a36
ar v231,a37
ar v232,a38
ar v233,a39
v_mul_f32 v230,v230,v224
v_mul_f32 v231,v231,v224
v_mul_f32 v232,v232,v224
v_mul_f32 v233,v233,v224
v_accvgpr_write_b32 a36,v230
v_accvgpr_write_b32 a37,v231
v_accvgpr_write_b32 a38,v232
v_accvgpr_write_b32 a39,v233
ar v230,a40
ar v231,a41
ar v232,a42
ar v233,a43
v_mul_f32 v230,v230,v224
v_mul_f32 v231,v231,v224
v_mul_f32 v232,v232,v224
v_mul_f32 v233,v233,v224
v_accvgpr_write_b32 a40,v230
v_accvgpr_write_b32 a41,v231
v_accvgpr_write_b32 a42,v232
v_accvgpr_write_b32 a43,v233
ar v230,a44
ar v231,a45
)ASM"
R"ASM(ar v232,a46
ar v233,a47
v_mul_f32 v230,v230,v224
v_mul_f32 v231,v231,v224
v_mul_f32 v232,v232,v224
v_mul_f32 v233,v233,v224
v_accvgpr_write_b32 a44,v230
v_accvgpr_write_b32 a45,v231
v_accvgpr_write_b32 a46,v232
v_accvgpr_write_b32 a47,v233
ar v230,a48
ar v231,a49
ar v232,a50
ar v233,a51
v_mul_f32 v230,v230,v224
v_mul_f32 v231,v231,v224
v_mul_f32 v232,v232,v224
v_mul_f32 v233,v233,v224
v_accvgpr_write_b32 a48,v230
v_accvgpr_write_b32 a49,v231
v_accvgpr_write_b32 a50,v232
v_accvgpr_write_b32 a51,v233
ar v230,a52
ar v231,a53
ar v232,a54
ar v233,a55
v_mul_f32 v230,v230,v224
v_mul_f32 v231,v231,v224
v_mul_f32 v232,v232,v224
v_mul_f32 v233,v233,v224
v_accvgpr_write_b32 a52,v230
v_accvgpr_write_b32 a53,v231
v_accvgpr_write_b32 a54,v232
v_accvgpr_write_b32 a55,v233
ar v230,a56
ar v231,a57
ar v232,a58
ar v233,a59
v_mul_f32 v230,v230,v224
v_mul_f32 v231,v231,v224
v_mul_f32 v232,v232,v224
v_mul_f32 v233,v233,v224
v_accvgpr_write_b32 a56,v230
v_accvgpr_write_b32 a57,v231
v_accvgpr_write_b32 a58,v232
v_accvgpr_write_b32 a59,v233
ar v230,a60
ar v231,a61
ar v232,a62
ar v233,a63
v_mul_f32 v230,v230,v224
v_mul_f32 v231,v231,v224
v_mul_f32 v232,v232,v224
v_mul_f32 v233,v233,v224
v_accvgpr_write_b32 a60,v230
v_accvgpr_write_b32 a61,v231
v_accvgpr_write_b32 a62,v232
v_accvgpr_write_b32 a63,v233
ar v230,a192
ar v231,a193
ar v232,a194
ar v233,a195
v_mul_f32 v230,v230,v224
v_mul_f32 v231,v231,v224
v_mul_f32 v232,v232,v224
v_mul_f32 v233,v233,v224
v_accvgpr_write_b32 a192,v230
v_accvgpr_write_b32 a193,v231
v_accvgpr_write_b32 a194,v232
v_accvgpr_write_b32 a195,v233
v_max3_f32 v221,v52,v53,v54
v_max3_f32 v221,v221,v55,v60
v_max3_f32 v221,v221,v61,v62
v_max_f32 v221,v221,v63
v_mov_b32 v225,v221
s_nop 1
v_permlane32_swap_b32 v221,v225
v_max_f32 v221,v221,v225
v_mov_b32 v225,v221
s_nop 1
v_permlane16_swap_b32 v221,v225
v_max_f32 v221,v221,v225
v_add_f32 v221,0x40e00000,v221
v_max_f32 v221,0,v221
v_sub_f32 v224,0,v221
v_exp_f32 v224,v224
v_sub_f32 v20,v20,v221
v_sub_f32 v21,v21,v221
v_sub_f32 v22,v22,v221
v_sub_f32 v23,v23,v221
v_sub_f32 v28,v28,v221
v_sub_f32 v29,v29,v221
v_sub_f32 v30,v30,v221
v_sub_f32 v31,v31,v221
v_sub_f32 v52,v52,v221
v_sub_f32 v53,v53,v221
v_sub_f32 v54,v54,v221
v_sub_f32 v55,v55,v221
v_sub_f32 v60,v60,v221
v_sub_f32 v61,v61,v221
v_sub_f32 v62,v62,v221
v_sub_f32 v63,v63,v221
v_sub_f32 v92,v92,v221
v_sub_f32 v93,v93,v221
v_sub_f32 v94,v94,v221
v_sub_f32 v95,v95,v221
ar v230,a64
ar v231,a65
ar v232,a66
ar v233,a67
v_mul_f32 v230,v230,v224
v_mul_f32 v231,v231,v224
v_mul_f32 v232,v232,v224
v_mul_f32 v233,v233,v224
v_accvgpr_write_b32 a64,v230
v_accvgpr_write_b32 a65,v231
v_accvgpr_write_b32 a66,v232
v_accvgpr_write_b32 a67,v233
ar v230,a68
ar v231,a69
ar v232,a70
ar v233,a71
v_mul_f32 v230,v230,v224
v_mul_f32 v231,v231,v224
v_mul_f32 v232,v232,v224
v_mul_f32 v233,v233,v224
v_accvgpr_write_b32 a68,v230
v_accvgpr_write_b32 a69,v231
v_accvgpr_write_b32 a70,v232
v_accvgpr_write_b32 a71,v233
ar v230,a72
ar v231,a73
ar v232,a74
ar v233,a75
v_mul_f32 v230,v230,v224
v_mul_f32 v231,v231,v224
v_mul_f32 v232,v232,v224
v_mul_f32 v233,v233,v224
v_accvgpr_write_b32 a72,v230
v_accvgpr_write_b32 a73,v231
v_accvgpr_write_b32 a74,v232
v_accvgpr_write_b32 a75,v233
ar v230,a76
ar v231,a77
ar v232,a78
ar v233,a79
v_mul_f32 v230,v230,v224
v_mul_f32 v231,v231,v224
v_mul_f32 v232,v232,v224
v_mul_f32 v233,v233,v224
v_accvgpr_write_b32 a76,v230
v_accvgpr_write_b32 a77,v231
v_accvgpr_write_b32 a78,v232
v_accvgpr_write_b32 a79,v233
ar v230,a80
ar v231,a81
ar v232,a82
ar v233,a83
v_mul_f32 v230,v230,v224
v_mul_f32 v231,v231,v224
v_mul_f32 v232,v232,v224
v_mul_f32 v233,v233,v224
v_accvgpr_write_b32 a80,v230
v_accvgpr_write_b32 a81,v231
v_accvgpr_write_b32 a82,v232
v_accvgpr_write_b32 a83,v233
ar v230,a84
ar v231,a85
ar v232,a86
ar v233,a87
v_mul_f32 v230,v230,v224
v_mul_f32 v231,v231,v224
v_mul_f32 v232,v232,v224
v_mul_f32 v233,v233,v224
v_accvgpr_write_b32 a84,v230
v_accvgpr_write_b32 a85,v231
v_accvgpr_write_b32 a86,v232
v_accvgpr_write_b32 a87,v233
ar v230,a88
ar v231,a89
ar v232,a90
ar v233,a91
v_mul_f32 v230,v230,v224
v_mul_f32 v231,v231,v224
v_mul_f32 v232,v232,v224
v_mul_f32 v233,v233,v224
v_accvgpr_write_b32 a88,v230
v_accvgpr_write_b32 a89,v231
v_accvgpr_write_b32 a90,v232
v_accvgpr_write_b32 a91,v233
ar v230,a92
ar v231,a93
ar v232,a94
ar v233,a95
v_mul_f32 v230,v230,v224
v_mul_f32 v231,v231,v224
v_mul_f32 v232,v232,v224
v_mul_f32 v233,v233,v224
v_accvgpr_write_b32 a92,v230
v_accvgpr_write_b32 a93,v231
v_accvgpr_write_b32 a94,v232
v_accvgpr_write_b32 a95,v233
ar v230,a96
ar v231,a97
ar v232,a98
ar v233,a99
v_mul_f32 v230,v230,v224
v_mul_f32 v231,v231,v224
v_mul_f32 v232,v232,v224
v_mul_f32 v233,v233,v224
v_accvgpr_write_b32 a96,v230
v_accvgpr_write_b32 a97,v231
v_accvgpr_write_b32 a98,v232
v_accvgpr_write_b32 a99,v233
ar v230,a100
ar v231,a101
ar v232,a102
ar v233,a103
v_mul_f32 v230,v230,v224
v_mul_f32 v231,v231,v224
v_mul_f32 v232,v232,v224
v_mul_f32 v233,v233,v224
v_accvgpr_write_b32 a100,v230
v_accvgpr_write_b32 a101,v231
v_accvgpr_write_b32 a102,v232
v_accvgpr_write_b32 a103,v233
ar v230,a104
ar v231,a105
ar v232,a106
ar v233,a107
v_mul_f32 v230,v230,v224
v_mul_f32 v231,v231,v224
v_mul_f32 v232,v232,v224
v_mul_f32 v233,v233,v224
v_accvgpr_write_b32 a104,v230
v_accvgpr_write_b32 a105,v231
v_accvgpr_write_b32 a106,v232
v_accvgpr_write_b32 a107,v233
ar v230,a108
ar v231,a109
ar v232,a110
ar v233,a111
v_mul_f32 v230,v230,v224
v_mul_f32 v231,v231,v224
v_mul_f32 v232,v232,v224
v_mul_f32 v233,v233,v224
v_accvgpr_write_b32 a108,v230
v_accvgpr_write_b32 a109,v231
v_accvgpr_write_b32 a110,v232
v_accvgpr_write_b32 a111,v233
ar v230,a112
ar v231,a113
ar v232,a114
ar v233,a115
v_mul_f32 v230,v230,v224
v_mul_f32 v231,v231,v224
v_mul_f32 v232,v232,v224
v_mul_f32 v233,v233,v224
v_accvgpr_write_b32 a112,v230
v_accvgpr_write_b32 a113,v231
v_accvgpr_write_b32 a114,v232
v_accvgpr_write_b32 a115,v233
ar v230,a116
ar v231,a117
ar v232,a118
ar v233,a119
v_mul_f32 v230,v230,v224
v_mul_f32 v231,v231,v224
v_mul_f32 v232,v232,v224
v_mul_f32 v233,v233,v224
v_accvgpr_write_b32 a116,v230
v_accvgpr_write_b32 a117,v231
v_accvgpr_write_b32 a118,v232
v_accvgpr_write_b32 a119,v233
ar v230,a120
ar v231,a121
ar v232,a122
ar v233,a123
v_mul_f32 v230,v230,v224
v_mul_f32 v231,v231,v224
v_mul_f32 v232,v232,v224
v_mul_f32 v233,v233,v224
v_accvgpr_write_b32 a120,v230
v_accvgpr_write_b32 a121,v231
v_accvgpr_write_b32 a122,v232
v_accvgpr_write_b32 a123,v233
ar v230,a124
ar v231,a125
ar v232,a126
ar v233,a127
v_mul_f32 v230,v230,v224
v_mul_f32 v231,v231,v224
v_mul_f32 v232,v232,v224
v_mul_f32 v233,v233,v224
v_accvgpr_write_b32 a124,v230
v_accvgpr_write_b32 a125,v231
v_accvgpr_write_b32 a126,v232
v_accvgpr_write_b32 a127,v233
ar v230,a196
ar v231,a197
ar v232,a198
ar v233,a199
v_mul_f32 v230,v230,v224
v_mul_f32 v231,v231,v224
v_mul_f32 v232,v232,v224
v_mul_f32 v233,v233,v224
v_accvgpr_write_b32 a196,v230
v_accvgpr_write_b32 a197,v231
v_accvgpr_write_b32 a198,v232
v_accvgpr_write_b32 a199,v233
v_exp_f32 v220,v36
v_exp_f32 v221,v37
v_exp_f32 v222,v38
v_exp_f32 v223,v39
cv v68,v220,v221
v_exp_f32 v224,v44
v_exp_f32 v225,v45
cv v69,v222,v223
v_exp_f32 v220,v46
v_exp_f32 v221,v47
cv v70,v224,v225
v_exp_f32 v222,v52
v_exp_f32 v223,v53
cv v71,v220,v221
v_exp_f32 v224,v54
v_exp_f32 v225,v55
cv v76,v222,v223
v_exp_f32 v220,v60
v_exp_f32 v221,v61
cv v77,v224,v225
v_exp_f32 v222,v62
v_exp_f32 v223,v63
cv v78,v220,v221
cv v79,v222,v223
v_or3_b32 v228,v68,v69,v70
v_or3_b32 v228,v228,v71,v76
v_or3_b32 v228,v228,v77,v78
v_or_b32 v228,v228,v79
v_and_b32 v228,0x40004000,v228
s_nop 7
s_setpc_b64 s[50:51]
Lend%=:
.purgem m
.purgem t
.purgem r
.purgem dn
.purgem dt
.purgem ar
.purgem cv

)ASM"
  :: "s"(sx),"s"(swq),"s"(sk),"s"(sv),"s"(swo),"s"(sp),"s"(wid),"s"(b),"s"(hh),"s"(idx),"s"(so),"s"(head),"s"(flg)
  : "v0","v1","v2","v3","v4","v5","v6","v7","v8","v9","v10","v11","v12","v13","v14","v15","v16","v17","v18","v19","v20","v21","v22","v23","v24","v25","v26","v27","v28","v29","v30","v31","v32","v33","v34","v35","v36","v37","v38","v39","v40","v41","v42","v43","v44","v45","v46","v47","v48","v49","v50","v51","v52","v53","v54","v55","v56","v57","v58","v59","v60","v61","v62","v63","v64","v65","v66","v67","v68","v69","v70","v71","v72","v73","v74","v75","v76","v77","v78","v79","v80","v81","v82","v83","v84","v85","v86","v87","v88","v89","v90","v91","v92","v93","v94","v95","v96","v97","v98","v99","v100","v101","v102","v103","v104","v105","v106","v107","v108","v109","v110","v111","v112","v113","v114","v115","v116","v117","v118","v119","v120","v121","v122","v123","v124","v125","v126","v127","v128","v129","v130","v131","v132","v133","v134","v135","v136","v137","v138","v139","v140","v141","v142","v143","v144","v145","v146","v147","v148","v149","v150","v151","v152","v153","v154","v155","v156","v157","v158","v159","v160","v161","v162","v163","v164","v165","v166","v167","v168","v169","v170","v171","v172","v173","v174","v175","v176","v177","v178","v179","v180","v181","v182","v183","v184","v185","v186","v187","v188","v189","v190","v191","v192","v193","v194","v195","v196","v197","v198","v199","v200","v201","v202","v203","v204","v205","v206","v207","v208","v209","v210","v211","v212","v213","v214","v215","v216","v217","v218","v219","v220","v221","v222","v223","v224","v225","v226","v227","v228","v229","v230","v231","v232","v233","v234","v235","v236","v237","v238","v239","v240","v241","v242","v243","v244","v245","v246","v247","v248","v249","v250","v251","v252","v253","v254","v255","a0","a1","a2","a3","a4","a5","a6","a7","a8","a9","a10","a11","a12","a13","a14","a15","a16","a17","a18","a19","a20","a21","a22","a23","a24","a25","a26","a27","a28","a29","a30","a31","a32","a33","a34","a35","a36","a37","a38","a39","a40","a41","a42","a43","a44","a45","a46","a47","a48","a49","a50","a51","a52","a53","a54","a55","a56","a57","a58","a59","a60","a61","a62","a63","a64","a65","a66","a67","a68","a69","a70","a71","a72","a73","a74","a75","a76","a77","a78","a79","a80","a81","a82","a83","a84","a85","a86","a87","a88","a89","a90","a91","a92","a93","a94","a95","a96","a97","a98","a99","a100","a101","a102","a103","a104","a105","a106","a107","a108","a109","a110","a111","a112","a113","a114","a115","a116","a117","a118","a119","a120","a121","a122","a123","a124","a125","a126","a127","a128","a129","a130","a131","a132","a133","a134","a135","a136","a137","a138","a139","a140","a141","a142","a143","a144","a145","a146","a147","a148","a149","a150","a151","a152","a153","a154","a155","a156","a157","a158","a159","a160","a161","a162","a163","a164","a165","a166","a167","a168","a169","a170","a171","a172","a173","a174","a175","a176","a177","a178","a179","a180","a181","a182","a183","a184","a185","a186","a187","a188","a189","a190","a191","a192","a193","a194","a195","a196","a197","a198","a199","a200","a201","a202","a203","a204","a205","a206","a207","a208","a209","a210","a211","a212","a213","a214","a215","a216","a217","a218","a219","a220","a221","a222","a223","a224","a225","a226","a227","a228","a229","a230","a231","a232","a233","a234","a235","a236","a237","a238","a239","a240","a241","a242","a243","a244","a245","a246","a247","a248","a249","a250","a251","a252","a253","a254","a255","s40","s41","s42","s43","s44","s45","s46","s47","s48","s49","s50","s51","s52","s53","s54","s55","s56","s57","s58","s59","s60","s61","s62","s63","s64","s65","s66","s67","s68","s69","s70","s71","s72","s73","s74","s75","s76","s77","s78","s79","s80","s81","s82","s83","s84","s85","s86","s87","s88","s89","s90","s91","s92","s93","s94","s95","vcc","scc","memory");
}


extern "C" void kernel_launch(void* const* d_in, const int* in_sizes, int n_in, void* d_out, int out_size, void* d_ws, size_t ws_size, hipStream_t stream) {
  char* part=(char*)d_ws; char* dbg=part+256*143360+65536;

  hipFuncSetAttribute((const void*)mha_main,hipFuncAttributeMaxDynamicSharedMemorySize,163840);
  mha_main<<<256,256,163840,stream>>>((const char*)d_in[0],(const char*)d_in[1],(const char*)d_in[2],(const char*)d_in[3],(const char*)d_in[4],
     (const char*)d_in[5],(const char*)d_in[6],(const char*)d_in[7],(const char*)d_in[8],(const char*)d_in[9],part,(char*)d_out,dbg);

}
```
